```python
import torch

def _softmax(x, axis=-1):
    return torch.softmax(x, dim=axis)
def _gelu(x):
    return torch.nn.functional.gelu(x, approximate="tanh")
def _silu(x):
    return torch.nn.functional.silu(x)
def _sigmoid(x):
    return torch.sigmoid(x)
def _layernorm(x, axis=-1, eps=1e-5):
    m = x.mean(dim=axis, keepdim=True)
    v = x.var(dim=axis, keepdim=True, unbiased=False)
    return (x - m) / torch.sqrt(v + eps)
def _scatter_add(tgt, idx, src, axis=0):
    return tgt.clone().index_add_(axis, idx, src)
def _segment_sum(src, seg, n):
    out = torch.zeros((n,) + src.shape[1:], dtype=src.dtype)
    return out.index_add_(0, seg, src)
def _pad(x, pw):
    flat = tuple(v for pair in reversed(pw) for v in pair)
    return torch.nn.functional.pad(x, flat)

kernel_name = 'parallel_sum__softmax_full_mha__glu_gated_reglu__prompt'

def setup_inputs():

    _w = (lambda a: (lambda t: t.to(torch.bfloat16) if t.is_floating_point() else t)(torch.as_tensor(a)))
    def _A_setup():
        B, S, D = 1, 2048, 640
        return {
            "x": _w((lambda *s: torch.randn(*s, dtype=torch.bfloat16) * 0.05)(B, S, D)),
            "wqkv": _w((lambda *s: torch.randn(*s, dtype=torch.bfloat16) * 0.05)(D, 3 * D)),
            "wo": _w((lambda *s: torch.randn(*s, dtype=torch.bfloat16) * 0.05)(D, D)),
        }
    _A_in = _A_setup()

    def _B_setup():
        B, S, D, F = 1, 2048, 640, 1536
        return {
            "x": _w((lambda *s: torch.randn(*s, dtype=torch.bfloat16) * 0.05)(B, S, D)),
            "W_gate": _w((lambda *s: torch.randn(*s, dtype=torch.bfloat16) * 0.4)(D, F) * (2.0 / D) ** 0.5),
            "W_up": _w((lambda *s: torch.randn(*s, dtype=torch.bfloat16) * 0.4)(D, F) * (2.0 / D) ** 0.5),
            "W_down": _w((lambda *s: torch.randn(*s, dtype=torch.bfloat16) * 0.4)(F, D) * (1.0 / F) ** 0.5),
        }
    _B_in = _B_setup()

    return {
        "A_x": _A_in["x"],
        "A_wqkv": _A_in["wqkv"],
        "A_wo": _A_in["wo"],
        "B_x": _B_in["x"],
        "B_W_gate": _B_in["W_gate"],
        "B_W_up": _B_in["W_up"],
        "B_W_down": _B_in["W_down"],
    }

def reference(A_x, A_wqkv, A_wo, B_x, B_W_gate, B_W_up, B_W_down):
    def _A_ref(x, wqkv, wo):
        B, S, D = x.shape
        H = 32; Dh = D // H
        qkv = (lambda x, *d: x.permute(*d))((x @ wqkv).reshape(B, S, 3, H, Dh), 2, 0, 3, 1, 4)
        q, k, v = qkv[0], qkv[1], qkv[2]
        scale = Dh ** -0.5
        scores = torch.einsum("bhqd,bhkd->bhqk", q, k) * scale
        p = _softmax(scores, axis=-1)
        o = torch.einsum("bhqk,bhkd->bhqd", p, v)
        return (lambda x, *d: x.permute(*d))(o, 0, 2, 1, 3).reshape(B, S, D) @ wo

    def _B_ref(x, W_gate, W_up, W_down):
        gate = torch.einsum("bsd,df->bsf", x, W_gate)
        up = torch.einsum("bsd,df->bsf", x, W_up)
        gate = gate * (lambda x, dt: x.to(dt))(gate > 0, gate.dtype)
        h = gate * up
        return torch.einsum("bsf,fd->bsd", h, W_down)

    x = A_x
    xn = _layernorm(x, axis=-1)
    return x + _A_ref(xn, A_wqkv, A_wo) + _B_ref(xn, B_W_gate, B_W_up, B_W_down)
```

```cpp
#include <hip/hip_runtime.h>
#include <hip/hip_bf16.h>
#include <stdint.h>

typedef __bf16 bfx4 __attribute__((ext_vector_type(4)));
typedef __bf16 bfx8 __attribute__((ext_vector_type(8)));
typedef __bf16 bfx16 __attribute__((ext_vector_type(16)));
typedef short s16x4 __attribute__((ext_vector_type(4)));
typedef short s16x8 __attribute__((ext_vector_type(8)));
typedef short s16x16 __attribute__((ext_vector_type(16)));
typedef float f32x2 __attribute__((ext_vector_type(2)));
typedef float f32x4 __attribute__((ext_vector_type(4)));
typedef float f32x8 __attribute__((ext_vector_type(8)));
typedef float f32x16 __attribute__((ext_vector_type(16)));
typedef unsigned u32x2 __attribute__((ext_vector_type(2)));
typedef unsigned u32x4 __attribute__((ext_vector_type(4)));
typedef unsigned u32x8 __attribute__((ext_vector_type(8)));
#define LDS_AS __attribute__((address_space(3)))
typedef LDS_AS char* lds_ptr;

constexpr int S_ = 2048, D_ = 640, H_ = 32, F_ = 1536;

constexpr int KV_TILE = 2624, KV_K2 = 1024, KV_V = 1280, KV_VSUB = 672, KV_VHALF = 336;
constexpr size_t WS_QF1 = 0;
constexpr size_t WS_QF2 = WS_QF1 + (size_t)H_ * 64 * 1024;
constexpr size_t WS_KV = WS_QF2 + (size_t)H_ * 64 * 256;
constexpr size_t KV_HEAD = (size_t)64 * KV_TILE;
constexpr size_t WS_A3 = WS_KV + H_ * KV_HEAD + 8192;
constexpr int A3_KS_O = 40, A3_KS = 136;

#define SBAR() __builtin_amdgcn_sched_barrier(0)
#define MFMA32(a, b, c) __builtin_amdgcn_mfma_f32_32x32x16_bf16((bfx8)(a), (bfx8)(b), (c), 0, 0, 0)

__device__ __forceinline__ unsigned cvtpk(float lo, float hi) {
    typedef __bf16 bfx2 __attribute__((ext_vector_type(2)));
    bfx2 r = {(__bf16)lo, (__bf16)hi};
    return __builtin_bit_cast(unsigned, r);
}
__device__ __forceinline__ void glds16(const void* g, unsigned lds_base) {
    unsigned sv;
    asm volatile("s_mov_b32 %0, m0\n\ts_mov_b32 m0, %2\n\ts_nop 0\n\tglobal_load_lds_dwordx4 %1, off\n\ts_mov_b32 m0, %0"
                 : "=&s"(sv) : "v"(g), "s"(lds_base) : "memory");
}
__device__ __forceinline__ s16x4 trread(const LDS_AS char* p) {
    return __builtin_amdgcn_ds_read_tr16_b64_v4i16((LDS_AS s16x4*)p);
}
__device__ __forceinline__ float bf_lo(unsigned u) { return __uint_as_float(u << 16); }
__device__ __forceinline__ float bf_hi(unsigned u) { return __uint_as_float(u & 0xffff0000u); }
__device__ __forceinline__ float swap32_add(float v) {
    auto r = __builtin_amdgcn_permlane32_swap(__float_as_uint(v), __float_as_uint(v), false, false);
    return __uint_as_float(r[0]) + __uint_as_float(r[1]);
}
__device__ __forceinline__ float swap32_max(float v) {
    auto r = __builtin_amdgcn_permlane32_swap(__float_as_uint(v), __float_as_uint(v), false, false);
    return fmaxf(__uint_as_float(r[0]), __uint_as_float(r[1]));
}

constexpr int K1_SLOTS = 5, K1_XB = 8192, K1_WB = 20480, K1_SLOT = K1_XB + K1_WB;
constexpr int K1_STAT = K1_SLOTS * K1_SLOT;
constexpr int K1_LDS = K1_STAT + 2 * 128 * 8 + 4 * 320 * 4;

struct K1Frags { s16x8 x[2]; s16x16 w[5]; };

__device__ __forceinline__ void k1_load(K1Frags& f, const LDS_AS char* xa0, const LDS_AS char* xa1, const LDS_AS char* wa0, const LDS_AS char* wa1, int t0) {
    f.x[0] = *(const LDS_AS s16x8*)xa0;
    f.x[1] = *(const LDS_AS s16x8*)xa1;
#pragma unroll
    for (int t = 0; t < 5; ++t) {
        const LDS_AS char* p = ((t & 1) ? wa1 : wa0) + (t >> 1) * 1024;
        s16x4 a = trread(p), b = trread(p + 512), c = trread(p + 10240), d = trread(p + 10240 + 512);
        f.w[t] = __builtin_shufflevector(__builtin_shufflevector(a, b, 0, 1, 2, 3, 4, 5, 6, 7), __builtin_shufflevector(c, d, 0, 1, 2, 3, 4, 5, 6, 7),
                                         0, 1, 2, 3, 4, 5, 6, 7, 8, 9, 10, 11, 12, 13, 14, 15);
    }
}
template <bool TRANS>
__device__ __forceinline__ void k1_mma(const K1Frags& f, f32x16 (&acc)[5]) {
#pragma unroll
    for (int ks = 0; ks < 2; ++ks)
#pragma unroll
        for (int t = 0; t < 5; ++t) {
            s16x8 w = ks ? __builtin_shufflevector(f.w[t], f.w[t], 8, 9, 10, 11, 12, 13, 14, 15) : __builtin_shufflevector(f.w[t], f.w[t], 0, 1, 2, 3, 4, 5, 6, 7);
            acc[t] = TRANS ? MFMA32(w, f.x[ks], acc[t]) : MFMA32(f.x[ks], w, acc[t]);
        }
}
__device__ __forceinline__ void k1_rowstat(const K1Frags& f, float& s1, float& s2) {
#pragma unroll
    for (int ks = 0; ks < 2; ++ks) {
        u32x4 u = __builtin_bit_cast(u32x4, f.x[ks]);
#pragma unroll
        for (int i = 0; i < 4; ++i) {
            float a = bf_lo(u[i]), b = bf_hi(u[i]);
            s1 += a; s2 = fmaf(a, a, s2); s1 += b; s2 = fmaf(b, b, s2);
        }
    }
}
__device__ __forceinline__ void k1_colsum(const K1Frags& f, f32x4 (&cs)[5], s16x8 ones, int idx) {
#pragma unroll
    for (int t = 0; t < 5; ++t) cs[t] = __builtin_amdgcn_smfmac_f32_16x16x64_bf16((bfx8)ones, (bfx16)f.w[t], cs[t], idx, 0, 0);
}

template <bool TRANS>
__device__ __forceinline__ void k1_mainloop(f32x16 (&acc)[5], f32x4 (&cs)[5], float& s1, float& s2, lds_ptr lds, int wid, int lane,
                                            const char* xsrc, const char* wsrc0, const char* wsrc1, const char* wsrc2, int wstep, int wcol_slot0, int wcol_slot1) {
    const int wm = wid & 3, wn = wid >> 2;
    const unsigned lds0 = (unsigned)(uintptr_t)lds;
    const int row = wm * 32 + (lane & 31), h = lane >> 5;
    const int xoff0 = row * 64 + ((h ^ ((row >> 2) & 3)) << 4), xoff1 = xoff0 ^ 32;
    const int q = (lane >> 2) & 3, p = lane & 3, fsw = 3 * ((q >> 1) & 1);
    const int wbase = K1_XB + h * 5120 + q * 128 + p * 8;
    const int woff0 = wbase + (wcol_slot0 >> 2) * 1024 + (((wcol_slot0 & 3) ^ fsw) << 5);
    const int woff1 = wbase + (wcol_slot1 >> 2) * 1024 + (((wcol_slot1 & 3) ^ fsw) << 5);
    const int r16 = lane & 15;
    const short one = 0x3f80;
    s16x8 ones = {0, 0, 0, 0, 0, 0, 0, 0};
    if (r16 < 2) ones = (s16x8){one, one, one, one, 0, 0, 0, 0};
    else if (r16 < 4) ones = (s16x8){0, 0, 0, 0, one, one, one, one};
    const int idx = (r16 & 1) ? 0xEEEE : 0x4444;

    const bool halfact = lane < 32;
    auto stage = [&](int slot) {
        const unsigned b = lds0 + slot * K1_SLOT;
        glds16(xsrc, b + wid * 1024);
        glds16(wsrc0, b + K1_XB + wid * 2048);
        glds16(wsrc1, b + K1_XB + wid * 2048 + 1024);
        if (halfact) glds16(wsrc2, b + K1_XB + 16384 + wid * 512);
        xsrc += 64; wsrc0 += wstep; wsrc1 += wstep; wsrc2 += wstep;
    };
#pragma unroll
    for (int s = 0; s < K1_SLOTS; ++s) stage(s);
    asm volatile("s_waitcnt vmcnt(16)\n\ts_barrier" ::: "memory");
    K1Frags fa, fb;
    k1_load(fa, lds + xoff0, lds + xoff1, lds + woff0, lds + woff1, 0);
    int slot = 0;
    for (int s = 0; s < 20; s += 2) {
        if (s < 16) asm volatile("s_waitcnt vmcnt(12) lgkmcnt(0)\n\ts_barrier" ::: "memory");
        else if (s == 16) asm volatile("s_waitcnt vmcnt(8) lgkmcnt(0)\n\ts_barrier" ::: "memory");
        else asm volatile("s_waitcnt vmcnt(0) lgkmcnt(0)\n\ts_barrier" ::: "memory");
        if (s + 5 < 20) stage(slot);
        slot = (slot == K1_SLOTS - 1) ? 0 : slot + 1;
        { const lds_ptr b = lds + slot * K1_SLOT; k1_load(fb, b + xoff0, b + xoff1, b + woff0, b + woff1, 0); }
        k1_mma<TRANS>(fa, acc);
        if (wn == 0) k1_rowstat(fa, s1, s2);
        if (wm == (s & 3)) k1_colsum(fa, cs, ones, idx);
        if (s + 2 < 20) {
            if (s < 14) asm volatile("s_waitcnt vmcnt(12) lgkmcnt(0)\n\ts_barrier" ::: "memory");
            else if (s == 14) asm volatile("s_waitcnt vmcnt(12) lgkmcnt(0)\n\ts_barrier" ::: "memory");
            else asm volatile("s_waitcnt vmcnt(4) lgkmcnt(0)\n\ts_barrier" ::: "memory");
            if (s + 6 < 20) stage(slot);
            slot = (slot == K1_SLOTS - 1) ? 0 : slot + 1;
            const lds_ptr b = lds + slot * K1_SLOT; k1_load(fa, b + xoff0, b + xoff1, b + woff0, b + woff1, 0);
        }
        k1_mma<TRANS>(fb, acc);
        if (wn == 1) k1_rowstat(fb, s1, s2);
        if (wm == ((s + 1) & 3)) k1_colsum(fb, cs, ones, idx);
    }
}

__global__ __launch_bounds__(512, 2) void k1_proj(const char* __restrict__ x, const char* __restrict__ wqkv, const char* __restrict__ wg,
                                                  const char* __restrict__ wu, char* __restrict__ ws) {
    extern __shared__ __attribute__((aligned(16))) char smem[];
    lds_ptr lds = (lds_ptr)smem;
    const int tid = threadIdx.x, lane = tid & 63, wid = __builtin_amdgcn_readfirstlane(tid >> 6);
    const int bid = blockIdx.x, xcd = bid & 7, bi = bid >> 3;
    const int panel = (xcd & 1) * 8 + (bi & 7), grp = (xcd >> 1) * 4 + (bi >> 3);
    const int wm = wid & 3, wn = wid >> 2, h = lane >> 5, l31 = lane & 31;
    const bool ffn = grp >= 6;
    const int hc0 = min(160 * (grp - 6), F_ - 160);
    const char* xsrc = x + (size_t)(panel * 128 + wid * 16 + (lane >> 2)) * (D_ * 2) + (((lane & 3) ^ ((lane >> 4) & 3)) << 4);
    auto wptr = [&](int piece, int r, int pos) -> const char* {
        const int rb = piece / 5, cb = piece - rb * 5, c = pos ^ (6 * ((r >> 1) & 1)), k = rb * 8 + r;
        if (!ffn) return wqkv + (size_t)k * (3 * D_ * 2) + (grp * 320 + cb * 64 + c * 8) * 2;
        return (c < 4 ? wg : wu) + (size_t)k * (F_ * 2) + (hc0 + cb * 32 + (c & 3) * 8) * 2;
    };
    const char* wsrc0 = wptr(wid * 2, lane >> 3, lane & 7);
    const char* wsrc1 = wptr(wid * 2 + 1, lane >> 3, lane & 7);
    const char* wsrc2 = wptr(16 + (wid >> 1), (wid & 1) * 4 + ((lane >> 3) & 3), lane & 7);
    const int wstep = 32 * (ffn ? F_ * 2 : 3 * D_ * 2);
    const int blk = (lane >> 4) & 1, T0 = wn * 5, T1 = T0 + 1;
    const int slot0 = ffn ? (T0 >> 1) * 4 + (T0 & 1) + 2 * blk : T0 * 2 + blk;
    const int slot1 = ffn ? (T1 >> 1) * 4 + (T1 & 1) + 2 * blk : T1 * 2 + blk;

    f32x16 acc[5]; f32x4 cs[5];
#pragma unroll
    for (int t = 0; t < 5; ++t) { acc[t] = f32x16{}; cs[t] = f32x4{}; }
    float s1 = 0.f, s2 = 0.f;
    const bool isv = (grp == 4 || grp == 5);
    if (isv) k1_mainloop<false>(acc, cs, s1, s2, lds, wid, lane, xsrc, wsrc0, wsrc1, wsrc2, wstep, slot0, slot1);
    else k1_mainloop<true>(acc, cs, s1, s2, lds, wid, lane, xsrc, wsrc0, wsrc1, wsrc2, wstep, slot0, slot1);

    LDS_AS f32x2* rowp = (LDS_AS f32x2*)(lds + K1_STAT);
    LDS_AS float* colp = (LDS_AS float*)(lds + K1_STAT + 2048);
    s1 = swap32_add(s1); s2 = swap32_add(s2);
    if (h == 0) rowp[wn * 128 + wm * 32 + l31] = f32x2{s1, s2};
    if (lane < 16) {
#pragma unroll
        for (int t = 0; t < 5; ++t) { colp[wm * 320 + wn * 160 + t * 32 + lane] = cs[t][0] + cs[t][1]; colp[wm * 320 + wn * 160 + t * 32 + 16 + lane] = cs[t][2] + cs[t][3]; }
    }
    __syncthreads();
    LDS_AS f32x2* rowf = (LDS_AS f32x2*)(lds);
    LDS_AS float* colf = (LDS_AS float*)(lds + 1024);
    if (tid < 128) {
        const f32x2 a = rowp[tid], b = rowp[128 + tid];
        const float mean = (a[0] + b[0]) * (1.0f / D_);
        const float var = fmaxf((a[1] + b[1]) * (1.0f / D_) - mean * mean, 0.f);
        const float rstd = 1.0f / sqrtf(var + 1e-5f);
        rowf[tid] = f32x2{rstd, -mean * rstd};
    } else if (tid < 448) {
        const int c = tid - 128;
        colf[c] = (colp[c] + colp[320 + c]) + (colp[640 + c] + colp[960 + c]);
    }
    __syncthreads();

    const int ttile = panel * 4 + wm;
    if (!isv) {
        f32x2 st = rowf[wm * 32 + l31];
        if (grp < 2) { const float c = 0.22360679774997896f * 1.4426950408889634f; st[0] *= c; st[1] *= c; }
#pragma unroll
        for (int t = 0; t < 5; ++t)
#pragma unroll
            for (int g = 0; g < 4; ++g) {
                const f32x4 c = *(const LDS_AS f32x4*)(colf + wn * 160 + t * 32 + g * 8 + h * 4);
#pragma unroll
                for (int i = 0; i < 4; ++i) acc[t][g * 4 + i] = fmaf(acc[t][g * 4 + i], st[0], st[1] * c[i]);
            }
        if (ffn) {
#pragma unroll
            for (int t = 0; t < 5; ++t) {
                float hv[8];
#pragma unroll
                for (int r = 0; r < 8; ++r) hv[r] = fmaxf(acc[t][r], 0.f) * acc[t][r + 8];
                const u32x4 o = {cvtpk(hv[0], hv[1]), cvtpk(hv[2], hv[3]), cvtpk(hv[4], hv[5]), cvtpk(hv[6], hv[7])};
                const int ks = A3_KS_O + (hc0 >> 4) + wn * 5 + t;
                *(u32x4*)(ws + WS_A3 + ((size_t)ks * 64 + ttile) * 1024 + lane * 16) = o;
            }
        } else {
            const bool isq = grp < 2;
            const int pos = isq ? l31 : ((l31 & 19) | ((l31 & 4) << 1) | ((l31 & 8) >> 1));
            const int head0 = (grp & 1) * 16 + wn * 8;
            char* f1 = isq ? ws + WS_QF1 + ((size_t)head0 * 64 + ttile) * 1024 : ws + WS_KV + head0 * KV_HEAD + (size_t)ttile * KV_TILE;
            char* f2 = isq ? ws + WS_QF2 + ((size_t)head0 * 64 + ttile) * 256 : f1 + KV_K2;
            const size_t hs1 = isq ? (size_t)64 * 1024 : KV_HEAD, hs2 = isq ? (size_t)64 * 256 : KV_HEAD;
            f1 += (pos + 32 * h) * 16; f2 += pos * 8 + h * hs2;
#define Q4(gam) cvtpk(acc[(gam) >> 2][((gam) & 3) * 4 + 0], acc[(gam) >> 2][((gam) & 3) * 4 + 1]), cvtpk(acc[(gam) >> 2][((gam) & 3) * 4 + 2], acc[(gam) >> 2][((gam) & 3) * 4 + 3])
#pragma unroll
            for (int pp = 0; pp < 4; ++pp) {
                *(u32x4*)(f1 + (2 * pp) * hs1) = u32x4{Q4(5 * pp), Q4(5 * pp + 1)};
                *(u32x2*)(f2 + (2 * pp) * hs2) = u32x2{Q4(5 * pp + 2)};
                *(u32x4*)(f1 + (2 * pp + 1) * hs1) = u32x4{Q4(5 * pp + 3), Q4(5 * pp + 4)};
            }
#undef Q4
        }
    } else {
        f32x2 st[16];
#pragma unroll
        for (int g = 0; g < 4; ++g) {
            const f32x4 a = *(const LDS_AS f32x4*)(rowf + wm * 32 + g * 8 + h * 4), b = *(const LDS_AS f32x4*)(rowf + wm * 32 + g * 8 + h * 4 + 2);
            st[g * 4 + 0] = f32x2{a[0], a[1]}; st[g * 4 + 1] = f32x2{a[2], a[3]}; st[g * 4 + 2] = f32x2{b[0], b[1]}; st[g * 4 + 3] = f32x2{b[2], b[3]};
        }
#pragma unroll
        for (int t = 0; t < 5; ++t) {
            const int col = wn * 160 + t * 32 + l31, hd = col / 20, d = col - hd * 20;
            const float c = colf[col];
            float v[16];
#pragma unroll
            for (int r = 0; r < 16; ++r) v[r] = fmaf(acc[t][r], st[r][0], st[r][1] * c);
            char* dst = ws + WS_KV + ((grp & 1) * 16 + hd) * KV_HEAD + (size_t)ttile * KV_TILE + KV_V + h * KV_VHALF + d * 16;
            *(u32x4*)dst = u32x4{cvtpk(v[0], v[1]), cvtpk(v[2], v[3]), cvtpk(v[4], v[5]), cvtpk(v[6], v[7])};
            *(u32x4*)(dst + KV_VSUB) = u32x4{cvtpk(v[8], v[9]), cvtpk(v[10], v[11]), cvtpk(v[12], v[13]), cvtpk(v[14], v[15])};
        }
        if (lane < 32) {
            char* dst = ws + WS_KV + ((grp & 1) * 16 + wn * 8 + (lane >> 2)) * KV_HEAD + (size_t)ttile * KV_TILE + KV_V + (lane & 3) * KV_VHALF + 20 * 16;
            *(u32x4*)dst = u32x4{0x3f803f80u, 0x3f803f80u, 0x3f803f80u, 0x3f803f80u};
        }
    }
}

constexpr int K2_CHUNK = 8 * KV_TILE, K2_SLOTB = 24576, K2_SLOTS = 3, K2_LDS = K2_SLOTS * K2_SLOTB;

#define SMF32(a, b, c) __builtin_amdgcn_smfmac_f32_32x32x32_bf16((bfx8)(a), __builtin_bit_cast(bfx16, (b)), (c), 0xDDDD, 0, 0)
#define MFMA8(a, b, c) __builtin_amdgcn_mfma_f32_32x32x8bf16_1k((a), (b), (c), 0, 0, 0)
__device__ __forceinline__ f32x8 lo8(const f32x16& v) { return __builtin_shufflevector(v, v, 0, 1, 2, 3, 4, 5, 6, 7); }
__device__ __forceinline__ f32x8 hi8(const f32x16& v) { return __builtin_shufflevector(v, v, 8, 9, 10, 11, 12, 13, 14, 15); }
__device__ __forceinline__ float max16(const f32x16& s) {
    float m = fmaxf(fmaxf(s[0], s[1]), s[2]);
#pragma unroll
    for (int r = 3; r < 15; r += 2) m = fmaxf(fmaxf(m, s[r]), s[r + 1]);
    return swap32_max(fmaxf(m, s[15]));
}

__global__ __launch_bounds__(512, 2) void k2_attn(char* __restrict__ ws) {
    extern __shared__ __attribute__((aligned(16))) char smem[];
    lds_ptr lds = (lds_ptr)smem;
    const unsigned lds0 = (unsigned)(uintptr_t)lds;
    const int tid = threadIdx.x, lane = tid & 63, wid = __builtin_amdgcn_readfirstlane(tid >> 6);
    const int bid = blockIdx.x, head = (bid & 7) * 4 + ((bid >> 3) & 3), qb = bid >> 5, qt = qb * 8 + wid;
    const int h = lane >> 5, l31 = lane & 31;
    const char* kv = ws + WS_KV + head * KV_HEAD;
    const char* src = kv + wid * 1024 + lane * 16;
    auto stage = [&](int slot) {
        const unsigned b = lds0 + slot * K2_SLOTB + wid * 1024;
        glds16(src, b); glds16(src + 8192, b + 8192); glds16(src + 16384, b + 16384);
        src += K2_CHUNK;
    };
    stage(0); stage(1);
    const s16x8 qf1 = *(const s16x8*)(ws + WS_QF1 + ((size_t)head * 64 + qt) * 1024 + lane * 16);
    s16x4 qf2 = *(const s16x4*)(ws + WS_QF2 + ((size_t)head * 64 + qt) * 256 + l31 * 8);
    if (h) qf2 = s16x4{0, 0, 0, 0};
    const int k1off = lane * 16, k2off = KV_K2 + l31 * 8;
    const int vd = l31 <= 20 ? l31 : (l31 < 28 ? 20 : 19), voff = KV_V + h * KV_VHALF + vd * 16;

    f32x16 o = f32x16{}, negm;
    asm volatile("s_waitcnt vmcnt(3)\n\ts_barrier" ::: "memory");
    {
        const s16x8 k1 = *(const LDS_AS s16x8*)(lds + k1off);
        const s16x4 k2 = *(const LDS_AS s16x4*)(lds + k2off);
        f32x16 s = MFMA32(k1, qf1, f32x16{});
        s = MFMA8(k2, qf2, s);
        const float m = -max16(s);
#pragma unroll
        for (int r = 0; r < 16; ++r) negm[r] = m;
    }
    int slot = 0;
    for (int c = 0; c < 8; ++c) {
        if (c) {
            if (c < 7) asm volatile("s_waitcnt vmcnt(3) lgkmcnt(0)\n\ts_barrier" ::: "memory");
            else asm volatile("s_waitcnt vmcnt(0) lgkmcnt(0)\n\ts_barrier" ::: "memory");
        }
        if (c + 2 < 8) stage(slot == 0 ? 2 : slot - 1);
        const lds_ptr b = lds + slot * K2_SLOTB;
#pragma unroll
        for (int t = 0; t < 8; ++t) {
            const s16x8 k1 = *(const LDS_AS s16x8*)(b + k1off + t * KV_TILE);
            const s16x4 k2 = *(const LDS_AS s16x4*)(b + k2off + t * KV_TILE);
            const s16x8 v0 = *(const LDS_AS s16x8*)(b + voff + t * KV_TILE);
            const s16x8 v1 = *(const LDS_AS s16x8*)(b + voff + t * KV_TILE + KV_VSUB);
            f32x16 s = MFMA32(k1, qf1, negm);
            s = MFMA8(k2, qf2, s);
#pragma unroll
            for (int r = 0; r < 16; ++r) s[r] = __builtin_amdgcn_exp2f(s[r]);
            o = SMF32(v0, lo8(s), o);
            o = SMF32(v1, hi8(s), o);
        }
        slot = (slot == K2_SLOTS - 1) ? 0 : slot + 1;
    }
    {
        bool bad = false;
#pragma unroll
        for (int r = 0; r < 9; ++r) bad |= !(fabsf(o[r]) < 1e30f);
        if (!h) {
#pragma unroll
            for (int r = 9; r < 12; ++r) bad |= !(fabsf(o[r]) < 1e30f);
        }
        if (__builtin_expect(__any(bad), 0)) {
            float m = -INFINITY;
            for (int t = 0; t < 64; ++t) {
                const char* g = kv + (size_t)t * KV_TILE;
                f32x16 s = MFMA32(*(const s16x8*)(g + k1off), qf1, f32x16{});
                s = MFMA8(*(const s16x4*)(g + k2off), qf2, s);
                m = fmaxf(m, max16(s));
            }
#pragma unroll
            for (int r = 0; r < 16; ++r) negm[r] = -m;
            o = f32x16{};
            for (int t = 0; t < 64; ++t) {
                const char* g = kv + (size_t)t * KV_TILE;
                f32x16 s = MFMA32(*(const s16x8*)(g + k1off), qf1, negm);
                s = MFMA8(*(const s16x4*)(g + k2off), qf2, s);
#pragma unroll
                for (int r = 0; r < 16; ++r) s[r] = __builtin_amdgcn_exp2f(s[r]);
                o = SMF32(*(const s16x8*)(g + voff), lo8(s), o);
                o = SMF32(*(const s16x8*)(g + voff + KV_VSUB), hi8(s), o);
            }
        }
    }
    auto rr = __builtin_amdgcn_permlane32_swap(__float_as_uint(o[8]), __float_as_uint(o[8]), false, false);
    const float rl = 1.0f / __uint_as_float(rr[1]);
    char* a3 = ws + WS_A3;
    *(u32x4*)(a3 + ((size_t)head * 64 + qt) * 1024 + lane * 16) =
        u32x4{cvtpk(o[0] * rl, o[1] * rl), cvtpk(o[2] * rl, o[3] * rl), cvtpk(o[4] * rl, o[5] * rl), cvtpk(o[6] * rl, o[7] * rl)};
    if (!h) *(u32x2*)(a3 + ((size_t)(32 + (head >> 2)) * 64 + qt) * 1024 + (((head >> 1) & 1) * 32 + l31) * 16 + (head & 1) * 8) =
        u32x2{cvtpk(o[8] * rl, o[9] * rl), cvtpk(o[10] * rl, o[11] * rl)};
}

constexpr int K3_SLOTS = 4, K3_WAVE = K3_SLOTS * 5120, K3_LDS = 8 * K3_WAVE;

__global__ __launch_bounds__(512, 2) void k3_out(const char* __restrict__ x, const char* __restrict__ wo, const char* __restrict__ wd,
                                                 const char* __restrict__ ws, char* __restrict__ out) {
    extern __shared__ __attribute__((aligned(16))) char smem[];
    lds_ptr lds = (lds_ptr)smem;
    const unsigned lds0 = (unsigned)(uintptr_t)lds;
    const int tid = threadIdx.x, lane = tid & 63, wid = __builtin_amdgcn_readfirstlane(tid >> 6);
    const int bid = blockIdx.x, tt = (bid & 7) * 8 + ((bid >> 3) & 7), cg = bid >> 6;
    const int h = lane >> 5, l31 = lane & 31;
    const int r = lane >> 2, hh = r >> 3, j = r & 7;
    auto wrow = [&](int ks) -> const char* {
        if (ks < 32) return wo + (size_t)(ks * 20 + (j & 3) + 8 * (j >> 2) + 4 * hh) * (D_ * 2);
        if (ks < 40) return wo + (size_t)(((ks - 32) * 4 + hh * 2 + (j >> 2)) * 20 + 16 + (j & 3)) * (D_ * 2);
        return wd + (size_t)((ks - 40) * 16 + (j & 3) + 8 * (j >> 2) + 4 * hh) * (D_ * 2);
    };
    const int wcol = (cg * 160 + (lane & 3) * 8) * 2;
    auto stage = [&](int i) {
        const int ks = i * 8 + wid;
        const char* p = wrow(ks) + wcol;
        const unsigned b = lds0 + wid * K3_WAVE + (i & 3) * 5120;
#pragma unroll
        for (int t = 0; t < 5; ++t) glds16(p + t * 64, b + t * 1024);
    };
    const char* a3 = ws + WS_A3 + ((size_t)wid * 64 + tt) * 1024 + lane * 16;
    s16x8 af[17];
#pragma unroll
    for (int i = 0; i < 17; ++i) af[i] = *(const s16x8*)(a3 + (size_t)i * 8 * 64 * 1024);
    asm volatile("s_waitcnt vmcnt(0)" ::: "memory");
#pragma unroll
    for (int i = 0; i < 17; ++i) asm volatile("" : "+v"(af[i]));
    stage(0); stage(1); stage(2);
    f32x16 acc[5];
#pragma unroll
    for (int t = 0; t < 5; ++t) acc[t] = f32x16{};
    const lds_ptr rb = lds + wid * K3_WAVE + h * 512 + ((lane >> 2) & 3) * 64 + ((lane >> 4) & 1) * 32 + (lane & 3) * 8;
#pragma unroll
    for (int i = 0; i < 17; ++i) {
        if (i + 3 < 17) { asm volatile("s_waitcnt lgkmcnt(0)" ::: "memory"); stage(i + 3); asm volatile("s_waitcnt vmcnt(15)" ::: "memory"); }
        else if (i + 3 == 17) asm volatile("s_waitcnt vmcnt(10)" ::: "memory");
        else if (i + 2 == 17) asm volatile("s_waitcnt vmcnt(5)" ::: "memory");
        else asm volatile("s_waitcnt vmcnt(0)" ::: "memory");
#pragma unroll
        for (int t = 0; t < 5; ++t) {
            const lds_ptr p = rb + (i & 3) * 5120 + t * 1024;
            const s16x4 a = trread(p), b = trread(p + 256);
            acc[t] = MFMA32(__builtin_shufflevector(a, b, 0, 1, 2, 3, 4, 5, 6, 7), af[i], acc[t]);
        }
    }
    __syncthreads();
#pragma unroll
    for (int t = 0; t < 5; ++t)
#pragma unroll
        for (int g = 0; g < 4; ++g)
            *(LDS_AS f32x4*)(lds + wid * K3_WAVE + (t * 4 + g) * 1024 + lane * 16) = f32x4{acc[t][g * 4], acc[t][g * 4 + 1], acc[t][g * 4 + 2], acc[t][g * 4 + 3]};
    __syncthreads();
    for (int u = wid; u < 20; u += 8) {
        f32x4 s = *(const LDS_AS f32x4*)(lds + u * 1024 + lane * 16);
#pragma unroll
        for (int w = 1; w < 8; ++w) s += *(const LDS_AS f32x4*)(lds + w * K3_WAVE + u * 1024 + lane * 16);
        const size_t off = (size_t)(tt * 32 + l31) * (D_ * 2) + (cg * 160 + u * 8 + h * 4) * 2;
        const u32x2 xv = *(const u32x2*)(x + off);
        *(u32x2*)(out + off) = u32x2{cvtpk(s[0] + bf_lo(xv[0]), s[1] + bf_hi(xv[0])), cvtpk(s[2] + bf_lo(xv[1]), s[3] + bf_hi(xv[1]))};
    }
}

extern "C" void kernel_launch(void* const* d_in, const int* in_sizes, int n_in, void* d_out, int out_size, void* d_ws, size_t ws_size,
                              hipStream_t stream) {
    const char* x = (const char*)d_in[0];
    const char* wqkv = (const char*)d_in[1];
    const char* wo = (const char*)d_in[2];
    const char* wg = (const char*)d_in[4];
    const char* wu = (const char*)d_in[5];
    const char* wd = (const char*)d_in[6];
    char* ws = (char*)d_ws;
    (void)hipFuncSetAttribute((const void*)k1_proj, hipFuncAttributeMaxDynamicSharedMemorySize, K1_LDS);
    (void)hipFuncSetAttribute((const void*)k2_attn, hipFuncAttributeMaxDynamicSharedMemorySize, K2_LDS);
    (void)hipFuncSetAttribute((const void*)k3_out, hipFuncAttributeMaxDynamicSharedMemorySize, K3_LDS);
    k1_proj<<<256, 512, K1_LDS, stream>>>(x, wqkv, wg, wu, ws);
    k2_attn<<<256, 512, K2_LDS, stream>>>(ws);
    k3_out<<<256, 512, K3_LDS, stream>>>(x, wo, wd, ws, (char*)d_out);
}
```

```cpp
#include <hip/hip_runtime.h>
#include <hip/hip_bf16.h>
#include <stdint.h>

typedef __bf16 bfx4 __attribute__((ext_vector_type(4)));
typedef __bf16 bfx8 __attribute__((ext_vector_type(8)));
typedef __bf16 bfx16 __attribute__((ext_vector_type(16)));
typedef short s16x4 __attribute__((ext_vector_type(4)));
typedef short s16x8 __attribute__((ext_vector_type(8)));
typedef short s16x16 __attribute__((ext_vector_type(16)));
typedef float f32x2 __attribute__((ext_vector_type(2)));
typedef float f32x4 __attribute__((ext_vector_type(4)));
typedef float f32x8 __attribute__((ext_vector_type(8)));
typedef float f32x16 __attribute__((ext_vector_type(16)));
typedef unsigned u32x2 __attribute__((ext_vector_type(2)));
typedef unsigned u32x4 __attribute__((ext_vector_type(4)));
typedef unsigned u32x8 __attribute__((ext_vector_type(8)));
#define LDS_AS __attribute__((address_space(3)))
typedef LDS_AS char* lds_ptr;

#define ABL 0
#define K1_ROT 0
constexpr int S_ = 2048, D_ = 640, H_ = 32, F_ = 1536;

constexpr int KV_TILE = 2624, KV_K2 = 1024, KV_V = 1280, KV_VSUB = 672, KV_VHALF = 336;
constexpr size_t WS_QF1 = 0;
constexpr size_t WS_QF2 = WS_QF1 + (size_t)H_ * 64 * 1024;
constexpr size_t WS_KV = WS_QF2 + (size_t)H_ * 64 * 256;
constexpr size_t KV_HEAD = (size_t)64 * KV_TILE;
constexpr size_t WS_O3 = WS_KV + H_ * KV_HEAD + 8192;
constexpr size_t WS_H3 = WS_O3 + (size_t)64 * 40 * 1024;
constexpr size_t WS_BD = WS_H3 + (size_t)64 * 96 * 1024;

#define STAMP_ARG
#define STAMP_PASS
#define STAMP_DECL do {} while (0)
#define STAMP(i) do {} while (0)
#define STAMP_FLUSH(kern) do {} while (0)
#define SBAR() __builtin_amdgcn_sched_barrier(0)
#define MFMA32(a, b, c) __builtin_amdgcn_mfma_f32_32x32x16_bf16((bfx8)(a), (bfx8)(b), (c), 0, 0, 0)

__device__ __forceinline__ unsigned cvtpk(float lo, float hi) {
    typedef __bf16 bfx2 __attribute__((ext_vector_type(2)));
    bfx2 r = {(__bf16)lo, (__bf16)hi};
    return __builtin_bit_cast(unsigned, r);
}
__device__ __forceinline__ void glds16(const void* g, unsigned lds_base) {
    unsigned sv;
    asm volatile("s_mov_b32 %0, m0\n\ts_mov_b32 m0, %2\n\ts_nop 0\n\tglobal_load_lds_dwordx4 %1, off\n\ts_mov_b32 m0, %0"
                 : "=&s"(sv) : "v"(g), "s"(lds_base) : "memory");
}
__device__ __forceinline__ s16x4 trread(const LDS_AS char* p) {
    return __builtin_amdgcn_ds_read_tr16_b64_v4i16((LDS_AS s16x4*)p);
}
__device__ __forceinline__ float rbf(float v) { return (float)(__bf16)v; }
__device__ __forceinline__ float bf_lo(unsigned u) { return __uint_as_float(u << 16); }
__device__ __forceinline__ float bf_hi(unsigned u) { return __uint_as_float(u & 0xffff0000u); }
__device__ __forceinline__ float swap32_add(float v) {
    auto r = __builtin_amdgcn_permlane32_swap(__float_as_uint(v), __float_as_uint(v), false, false);
    return __uint_as_float(r[0]) + __uint_as_float(r[1]);
}
__device__ __forceinline__ float swap32_max(float v) {
    auto r = __builtin_amdgcn_permlane32_swap(__float_as_uint(v), __float_as_uint(v), false, false);
    return fmaxf(__uint_as_float(r[0]), __uint_as_float(r[1]));
}

constexpr int K1_WSLOT = 20480, K1_X0 = 4 * K1_WSLOT, K1_XSLOT = 20480, K1_STAT = K1_X0 + 3 * K1_XSLOT;
constexpr int K1_LDS = K1_STAT + 160 * 8 + 320 * 4;

#define PIN(x) asm volatile("" : "+v"(x))
#define MEMFENCE() asm volatile("" ::: "memory")
#define WAIT_BAR(N) do { MEMFENCE(); __builtin_amdgcn_s_waitcnt(0x70 | ((N) & 15) | (((N) >> 4) << 14)); __builtin_amdgcn_s_barrier(); MEMFENCE(); SBAR(); } while (0)
#define LGKM_BAR() do { MEMFENCE(); __builtin_amdgcn_s_waitcnt(0xC07F); __builtin_amdgcn_s_barrier(); MEMFENCE(); SBAR(); } while (0)
#define SMF16(a, b, c, i) __builtin_amdgcn_smfmac_f32_16x16x64_bf16((bfx8)(a), (bfx16)(b), (c), (i), 0, 0)
#define CAT8(a, b) __builtin_shufflevector(a, b, 0, 1, 2, 3, 4, 5, 6, 7)
#define CAT16(a, b) __builtin_shufflevector(a, b, 0, 1, 2, 3, 4, 5, 6, 7, 8, 9, 10, 11, 12, 13, 14, 15)

__device__ __forceinline__ void glds16s(const char* sbase, unsigned voff, unsigned lds_base) {
    unsigned sv;
    asm volatile("s_mov_b32 %0, m0\n\ts_mov_b32 m0, %3\n\ts_nop 0\n\tglobal_load_lds_dwordx4 %1, %2\n\ts_mov_b32 m0, %0"
                 : "=&s"(sv) : "v"(voff), "s"(sbase), "s"(lds_base) : "memory");
}

template <int MODE> struct K1F { s16x8 x[MODE == 2 ? 5 : 2]; s16x4 w[MODE == 2 ? 2 : 5][2]; };

template <int MODE>
__device__ __forceinline__ void k1_read(K1F<MODE>& n, lds_ptr xa, lds_ptr w0, lds_ptr w1, int wimm, int i) {
    if (MODE != 2) {
        if (i < 2) n.x[i] = *(const LDS_AS s16x8*)(xa + i * 4096);
        else if (i < 12) { const int j = i - 2, t = j >> 1; n.w[t][j & 1] = trread(((t & 1) ? w1 : w0) + wimm + (t >> 1) * 1024 + (j & 1) * 512); }
    } else {
        if (i < 4) n.w[i >> 1][i & 1] = trread(((i >> 1) ? w1 : w0) + wimm + (i & 1) * 512);
        else if (i < 9) n.x[i - 4] = *(const LDS_AS s16x8*)(xa + (i - 4) * 4096);
    }
}
struct K1S { u32x4 u[2]; float s1[5], s2[5]; };
template <int MODE, bool RD, int J0, int J1, int R0, int R1>
__device__ __forceinline__ void k1_step(const K1F<MODE>& f, K1F<MODE>& n, f32x16 (&acc)[10], lds_ptr xa, lds_ptr w0, lds_ptr w1, int wimm, K1S& st, lds_ptr sp) {
#pragma unroll
    for (int g = 0; g < 10; ++g) {
        const int xi = MODE == 2 ? g >> 1 : g & 1, wi = MODE == 2 ? g & 1 : g >> 1;
        const s16x8 w = CAT8(f.w[wi][0], f.w[wi][1]);
#if !(ABL & 1)
        acc[g] = MODE == 1 ? MFMA32(f.x[xi], w, acc[g]) : MFMA32(w, f.x[xi], acc[g]);
#else
        { s16x8 x_ = f.x[xi]; asm volatile("" :: "v"(w), "v"(x_)); }
#endif
        PIN(acc[g]); SBAR();
        if (RD) { k1_read<MODE>(n, xa, w0, w1, wimm, 2 * g); k1_read<MODE>(n, xa, w0, w1, wimm, 2 * g + 1); }
        if (g < 8) {
            const int J = g < 4 ? J0 : J1;
            if (J >= 0 && !(ABL & 8)) {
                const unsigned v = st.u[g >> 2][g & 3];
                const float a = bf_lo(v), c = bf_hi(v);
                st.s1[J < 0 ? 0 : J] += a + c; st.s2[J < 0 ? 0 : J] = fmaf(a, a, fmaf(c, c, st.s2[J < 0 ? 0 : J]));
                PIN(st.s1[J < 0 ? 0 : J]); PIN(st.s2[J < 0 ? 0 : J]);
            }
        }
        if (g == 6 && R0 >= 0) st.u[0] = *(const LDS_AS u32x4*)(sp + R0 * 4096);
        if (g == 8 && R1 >= 0) st.u[1] = *(const LDS_AS u32x4*)(sp + R1 * 4096);
        SBAR();
    }
}
__device__ __forceinline__ float sum8(float v) {
    v += __uint_as_float(__builtin_amdgcn_update_dpp(0, __float_as_uint(v), 0xB1, 0xF, 0xF, true));
    v += __uint_as_float(__builtin_amdgcn_update_dpp(0, __float_as_uint(v), 0x4E, 0xF, 0xF, true));
    v += __uint_as_float(__builtin_amdgcn_update_dpp(0, __float_as_uint(v), 0x141, 0xF, 0xF, true));
    return v;
}
template <int MODE>
__device__ __forceinline__ void k1_compute(f32x16 (&acc)[10], lds_ptr lds, int wid, int lane) {
    constexpr int NCB = MODE == 2 ? 4 : 5, NXP = MODE == 2 ? 5 : 4, J4 = MODE == 2 ? 4 : -1;
    const int h = lane >> 5, l31 = lane & 31, blk = (lane >> 4) & 1, q = (lane >> 2) & 3, p = lane & 3, fsw = 3 * (q >> 1);
    const int xrow = (MODE == 2 ? 0 : (wid & 1) * 64) + l31, xo = K1_X0 + xrow * 128 + ((h ^ ((xrow >> 1) & 7)) << 4);
    lds_ptr xa0 = lds + xo, xa1 = lds + (xo ^ 32), xa2 = lds + (xo ^ 64), xa3 = lds + (xo ^ 96), sp = lds + K1_X0 + wid * 1024 + lane * 16;
    const int wb = h * NCB * 1024 + q * 128 + p * 8;
    lds_ptr w0, w1;
    if (MODE == 2) { const int T = wid * 2; w0 = lds + wb + ((T >> 2) + 2 * blk) * 1024 + (((T & 3) ^ fsw ^ blk) << 5); w1 = lds + wb + ((T >> 2) + 2 * blk) * 1024 + ((((T + 1) & 3) ^ fsw ^ blk) << 5); }
    else { const int s0 = (wid >> 1) * 10 + blk, s1 = s0 + 2; w0 = lds + wb + (s0 >> 2) * 1024 + (((s0 & 3) ^ fsw) << 5); w1 = lds + wb + (s1 >> 2) * 1024 + (((s1 & 3) ^ fsw) << 5); }
    K1F<MODE> fa, fb = {};
    K1S st = {};
    int xs = 0;
    for (int u = 0; u < 10; ++u) {
        LGKM_BAR();
        k1_step<MODE, true, -1, -1, 0, 1>(fb, fa, acc, xa0, w0, w1, 0, st, sp);
        k1_step<MODE, true, 0, 1, 2, -1>(fa, fb, acc, xa1, w0, w1, 2 * NCB * 1024, st, sp);
        LGKM_BAR();
        k1_step<MODE, true, 2, -1, 3, J4>(fb, fa, acc, xa2, w0, w1, K1_WSLOT, st, sp);
        k1_step<MODE, true, 3, J4, -1, -1>(fa, fb, acc, xa3, w0, w1, K1_WSLOT + 2 * NCB * 1024, st, sp);
        const int dw = (u & 1) ? -2 * K1_WSLOT : 2 * K1_WSLOT, dx = (xs == 2) ? -2 * K1_XSLOT : K1_XSLOT;
        xs = (xs == 2) ? 0 : xs + 1;
        w0 += dw; w1 += dw; xa0 += dx; xa1 += dx; xa2 += dx; xa3 += dx; sp += dx;
    }
#pragma unroll
    for (int g = 0; g < 10; ++g) {
        const int xi = MODE == 2 ? g >> 1 : g & 1, wi = MODE == 2 ? g & 1 : g >> 1;
        const s16x8 w = CAT8(fb.w[wi][0], fb.w[wi][1]);
        acc[g] = MODE == 1 ? MFMA32(fb.x[xi], w, acc[g]) : MFMA32(w, fb.x[xi], acc[g]);
    }
    const int pos = lane & 7;
    float a = 0.f, b = 0.f;
#pragma unroll
    for (int j = 0; j < NXP; ++j) { const float sa = sum8(st.s1[j]), sb = sum8(st.s2[j]); a = pos == j ? sa : a; b = pos == j ? sb : b; }
    const float mean = a * (1.0f / D_), var = fmaxf(b * (1.0f / D_) - mean * mean, 0.f);
    const float rstd = __builtin_amdgcn_rcpf(rbf(__builtin_amdgcn_sqrtf(rbf(rbf(var) + 1e-5f))));
    if (pos < NXP) ((LDS_AS f32x2*)(lds + K1_STAT))[pos * 32 + wid * 8 + (lane >> 3)] = f32x2{rstd, -rbf(mean) * rstd};
}

template <bool FFN>
__device__ __forceinline__ void k1_helper(lds_ptr lds, int hw, int lane, const char* xp, const char* wp0, const char* wp1, int rot STAMP_ARG) {
    constexpr int NCB = FFN ? 4 : 5, WROW = FFN ? F_ * 2 : 3 * D_ * 2;
    const unsigned lds0 = (unsigned)(uintptr_t)lds;
    const int r = lane >> 3, pos = lane & 7;
    const unsigned xvo = (hw * 8 + r) * (D_ * 2) + ((pos ^ ((hw & 1) * 4 + (r >> 1))) << 4);
    const unsigned wvo0 = (hw * 8 + r) * WROW + ((pos ^ (6 * ((r >> 1) & 1))) << 4), wvo1 = wvo0 ^ 32;
    auto issue = [&](int t) {
        if (t >= -3 && t <= 16) {
            const int sl = t + 3; int ks = sl + 2 * rot; ks = ks >= 20 ? ks - 20 : ks;
            const unsigned d = lds0 + (sl & 3) * K1_WSLOT + hw * NCB * 1024;
            const char* a = wp0 + (unsigned)(ks * 32 * WROW);
            if (FFN) { const char* b = wp1 + (unsigned)(ks * 32 * WROW); glds16s(a, wvo0, d); glds16s(a + 128, wvo0, d + 1024); glds16s(b, wvo1, d + 2048); glds16s(b + 128, wvo1, d + 3072); }
            else {
#pragma unroll
                for (int j = 0; j < 5; ++j) glds16s(a + j * 128, wvo0, d + j * 1024);
            }
        }
        if (t <= 15) {
            const int un = (t + 4) >> 1; int ku = un + rot; ku = ku >= 10 ? ku - 10 : ku;
            const unsigned d = lds0 + K1_X0 + (un % 3) * K1_XSLOT + hw * 1024;
            const char* a = xp + ku * 128;
            if (!(t & 1)) { glds16s(a, xvo, d); glds16s(a + 32 * D_ * 2, xvo, d + 4096); if (FFN) glds16s(a + 4 * 32 * D_ * 2, xvo, d + 4 * 4096); }
            else { glds16s(a + 2 * 32 * D_ * 2, xvo, d + 2 * 4096); glds16s(a + 3 * 32 * D_ * 2, xvo, d + 3 * 4096); }
        }
    };
    STAMP(4);
    issue(-4); issue(-3); issue(-2);
    const int r16 = lane & 15, h = lane >> 5, blk = (lane >> 4) & 1, q = (lane >> 2) & 3, fsw = 3 * (q >> 1);
    const unsigned olo = r16 < 2 ? 0x3f803f80u : 0u, ohi = (r16 >> 1) == 1 ? 0x3f803f80u : 0u;
    const s16x8 ones = __builtin_bit_cast(s16x8, (u32x4{olo, olo, ohi, ohi}));
    const int idx = (r16 & 1) ? 0xEEEE : 0x4444;
    const int cs0 = hw * 2 + blk;
    const int wrd = h * NCB * 1024 + q * 128 + (lane & 3) * 8 + (FFN ? 2 * blk * 1024 + ((hw ^ fsw ^ blk) << 5) : (cs0 >> 2) * 1024 + (((cs0 & 3) ^ fsw) << 5));
    f32x4 cs[3] = {f32x4{}, f32x4{}, f32x4{}};
    const int j2 = (FFN || hw >= 2) ? 0 : 2 * 2048;
    for (int sl = 0; sl < 20; ++sl) {
        if (sl == 0) WAIT_BAR(7); else if (sl <= 16) WAIT_BAR(FFN ? 13 : 14); else if (sl == 17) WAIT_BAR(FFN ? 10 : 12); else if (sl == 18) WAIT_BAR(NCB); else WAIT_BAR(0);
        if (sl == 0) STAMP(5);
        const lds_ptr b = lds + (sl & 3) * K1_WSLOT + wrd;
        s16x16 wv[3];
#pragma unroll
        for (int j = 0; j < (FFN ? 2 : 3); ++j) {
            const lds_ptr pp = b + (j == 2 ? j2 : j * (FFN ? 1024 : 2048));
            wv[j] = CAT16(CAT8(trread(pp), trread(pp + 512)), CAT8(trread(pp + 2 * NCB * 1024), trread(pp + 2 * NCB * 1024 + 512)));
        }
        if (!(ABL & 4)) { if (sl == 0) issue(-1);
        issue(sl); }
#pragma unroll
        for (int j = 0; j < (FFN ? 2 : 3); ++j) if (!(ABL & 16) && (j < 2 || hw < 2)) cs[j] = SMF16(ones, wv[j], cs[j], idx);
    }
    LDS_AS float* colf = (LDS_AS float*)(lds + K1_STAT + 1280);
    if (lane < 16) {
#pragma unroll
        for (int j = 0; j < (FFN ? 2 : 3); ++j)
            if (j < 2 || hw < 2) { colf[(hw + 4 * j) * 32 + lane] = cs[j][0] + cs[j][1]; colf[(hw + 4 * j) * 32 + 16 + lane] = cs[j][2] + cs[j][3]; }
    }
}

__global__ __launch_bounds__(512, 2) void k1_proj(const char* __restrict__ x, const char* __restrict__ wqkv, const char* __restrict__ wg,
                                                  const char* __restrict__ wu, char* __restrict__ ws) {
    extern __shared__ __attribute__((aligned(16))) char smem[];
    lds_ptr lds = (lds_ptr)smem;
    STAMP_DECL; STAMP(0);
    const int tid = threadIdx.x, lane = tid & 63, wid = __builtin_amdgcn_readfirstlane(tid >> 6);
    const int bid = blockIdx.x, xcd = bid & 7, bi = bid >> 3;
    const bool ffn = xcd >= 3;
    int panel, grp;
    if (!ffn) { if (xcd < 2) { panel = xcd * 8 + (bi & 7); grp = bi >> 3; } else { panel = bi & 15; grp = 4 + (bi >> 4); } }
    else { const int i = (xcd - 3) * 32 + bi; if (i >= 156) return; grp = i / 13; panel = i - grp * 13; }
    const int tt0 = ffn ? min(panel * 5, 59) : panel * 4;
    if (wid >= 4) {
        const char* xp = x + (size_t)tt0 * 32 * (D_ * 2);
        const int rot = K1_ROT ? (bi * 5) >> 4 : 0;
        if (ffn) k1_helper<true>(lds, wid - 4, lane, xp, wg + grp * 256, wu + grp * 256, rot STAMP_PASS);
        else k1_helper<false>(lds, wid - 4, lane, xp, wqkv + grp * 640, nullptr, rot STAMP_PASS);
        STAMP(1);
        LGKM_BAR();
        STAMP_FLUSH(0);
        return;
    }
    __builtin_amdgcn_s_setprio(2);
    const int h = lane >> 5, l31 = lane & 31;
    f32x16 acc[10];
#pragma unroll
    for (int t = 0; t < 10; ++t) acc[t] = f32x16{};
    const LDS_AS f32x2* rowf = (const LDS_AS f32x2*)(lds + K1_STAT);
    const LDS_AS float* colf = (const LDS_AS float*)(lds + K1_STAT + 1280);
    if (ffn) {
        k1_compute<2>(acc, lds, wid, lane);
        STAMP(1);
        LGKM_BAR();
        STAMP(2);
        f32x4 cv[2][4];
#pragma unroll
        for (int c = 0; c < 2; ++c)
#pragma unroll
            for (int g = 0; g < 4; ++g) cv[c][g] = *(const LDS_AS f32x4*)(colf + (wid * 2 + c) * 32 + g * 8 + h * 4);
#pragma unroll
        for (int m = 0; m < 5; ++m) {
            const f32x2 rs = rowf[m * 32 + l31];
#pragma unroll
            for (int c = 0; c < 2; ++c) {
                const f32x16& a = acc[m * 2 + c];
                float hv[8];
#pragma unroll
                for (int r = 0; r < 8; ++r) hv[r] = fmaxf(fmaf(a[r], rs[0], rs[1] * cv[c][r >> 2][r & 3]), 0.f) * fmaf(a[r + 8], rs[0], rs[1] * cv[c][2 + (r >> 2)][r & 3]);
                const int ks = grp * 8 + wid * 2 + c;
                *(u32x4*)(ws + WS_H3 + ((size_t)(tt0 + m) * 96 + ks) * 1024 + lane * 16) = u32x4{cvtpk(hv[0], hv[1]), cvtpk(hv[2], hv[3]), cvtpk(hv[4], hv[5]), cvtpk(hv[6], hv[7])};
            }
        }
    } else if (grp < 4) {
        k1_compute<0>(acc, lds, wid, lane);
        STAMP(1);
        LGKM_BAR();
        STAMP(2);
        const int wm = wid & 1, wn = wid >> 1;
        const bool isq = grp < 2;
        const int pos = isq ? l31 : ((l31 & 19) | ((l31 & 4) << 1) | ((l31 & 8) >> 1));
        const int head0 = (grp & 1) * 16 + wn * 8, tt = tt0 + wm * 2;
        char* f1 = isq ? ws + WS_QF1 + ((size_t)head0 * 64 + tt) * 1024 : ws + WS_KV + head0 * KV_HEAD + (size_t)tt * KV_TILE;
        char* f2 = isq ? ws + WS_QF2 + ((size_t)head0 * 64 + tt) * 256 : f1 + KV_K2;
        const size_t hs1 = isq ? (size_t)64 * 1024 : KV_HEAD, hs2 = isq ? (size_t)64 * 256 : KV_HEAD;
        const int ts1 = isq ? 1024 : KV_TILE, ts2 = isq ? 256 : KV_TILE;
        f1 += (pos + 32 * h) * 16; f2 += pos * 8 + h * hs2;
        f32x2 rs[2] = {rowf[wm * 64 + l31], rowf[wm * 64 + 32 + l31]};
        if (isq) { const float qs = 0.22360679774997896f * 1.4426950408889634f; rs[0] *= qs; rs[1] *= qs; }
#pragma unroll
        for (int t = 0; t < 5; ++t)
#pragma unroll
            for (int g = 0; g < 4; ++g) {
                const f32x4 cv = *(const LDS_AS f32x4*)(colf + wn * 160 + t * 32 + g * 8 + h * 4);
#pragma unroll
                for (int m = 0; m < 2; ++m)
#pragma unroll
                    for (int i = 0; i < 4; ++i) acc[t * 2 + m][g * 4 + i] = fmaf(acc[t * 2 + m][g * 4 + i], rs[m][0], rs[m][1] * cv[i]);
            }
#define Q4(gam) cvtpk(acc[((gam) >> 2) * 2 + m][((gam) & 3) * 4 + 0], acc[((gam) >> 2) * 2 + m][((gam) & 3) * 4 + 1]), cvtpk(acc[((gam) >> 2) * 2 + m][((gam) & 3) * 4 + 2], acc[((gam) >> 2) * 2 + m][((gam) & 3) * 4 + 3])
#pragma unroll
        for (int pp = 0; pp < 4; ++pp)
#pragma unroll
            for (int m = 0; m < 2; ++m) {
                *(u32x4*)(f1 + (2 * pp) * hs1 + m * ts1) = u32x4{Q4(5 * pp), Q4(5 * pp + 1)};
                *(u32x2*)(f2 + (2 * pp) * hs2 + m * ts2) = u32x2{Q4(5 * pp + 2)};
                *(u32x4*)(f1 + (2 * pp + 1) * hs1 + m * ts1) = u32x4{Q4(5 * pp + 3), Q4(5 * pp + 4)};
            }
#undef Q4
    } else {
        k1_compute<1>(acc, lds, wid, lane);
        STAMP(1);
        LGKM_BAR();
        STAMP(2);
        const int wm = wid & 1, wn = wid >> 1;
#pragma unroll
        for (int m = 0; m < 2; ++m) {
            const int tt = tt0 + wm * 2 + m;
            f32x2 rs[16];
#pragma unroll
            for (int g = 0; g < 4; ++g) {
                const LDS_AS f32x2* rp = rowf + wm * 64 + m * 32 + g * 8 + h * 4;
                const f32x4 a = *(const LDS_AS f32x4*)rp, b = *(const LDS_AS f32x4*)(rp + 2);
                rs[g * 4 + 0] = f32x2{a[0], a[1]}; rs[g * 4 + 1] = f32x2{a[2], a[3]}; rs[g * 4 + 2] = f32x2{b[0], b[1]}; rs[g * 4 + 3] = f32x2{b[2], b[3]};
            }
#pragma unroll
            for (int t = 0; t < 5; ++t) {
                const int col = wn * 160 + t * 32 + l31, hd = col / 20, d = col - hd * 20;
                const float cv = colf[col];
                float v[16];
#pragma unroll
                for (int r = 0; r < 16; ++r) v[r] = fmaf(acc[t * 2 + m][r], rs[r][0], rs[r][1] * cv);
                char* dst = ws + WS_KV + ((grp & 1) * 16 + hd) * KV_HEAD + (size_t)tt * KV_TILE + KV_V + h * KV_VHALF + d * 16;
                *(u32x4*)dst = u32x4{cvtpk(v[0], v[1]), cvtpk(v[2], v[3]), cvtpk(v[4], v[5]), cvtpk(v[6], v[7])};
                *(u32x4*)(dst + KV_VSUB) = u32x4{cvtpk(v[8], v[9]), cvtpk(v[10], v[11]), cvtpk(v[12], v[13]), cvtpk(v[14], v[15])};
            }
        }
        char* dst = ws + WS_KV + ((grp & 1) * 16 + wn * 8 + (l31 >> 2)) * KV_HEAD + (size_t)(tt0 + wm * 2 + h) * KV_TILE + KV_V + (lane & 3) * KV_VHALF + 20 * 16;
        *(u32x4*)dst = u32x4{0x3f803f80u, 0x3f803f80u, 0x3f803f80u, 0x3f803f80u};
    }
    STAMP(3); STAMP_FLUSH(0);
}

constexpr int K2_CHUNK = 8 * KV_TILE, K2_SLOTB = 24576, K2_SLOTS = 3, K2_LDS = K2_SLOTS * K2_SLOTB;

#define SMF32(a, b, c) __builtin_amdgcn_smfmac_f32_32x32x32_bf16((bfx8)(a), __builtin_bit_cast(bfx16, (b)), (c), 0xDDDD, 0, 0)
#define MFMA8(a, b, c) __builtin_amdgcn_mfma_f32_32x32x8bf16_1k((a), (b), (c), 0, 0, 0)
__device__ __forceinline__ f32x8 lo8(const f32x16& v) { return __builtin_shufflevector(v, v, 0, 1, 2, 3, 4, 5, 6, 7); }
__device__ __forceinline__ f32x8 hi8(const f32x16& v) { return __builtin_shufflevector(v, v, 8, 9, 10, 11, 12, 13, 14, 15); }
__device__ __forceinline__ float max16(const f32x16& s) {
    float m = fmaxf(fmaxf(s[0], s[1]), s[2]);
#pragma unroll
    for (int r = 3; r < 15; r += 2) m = fmaxf(fmaxf(m, s[r]), s[r + 1]);
    return swap32_max(fmaxf(m, s[15]));
}

__device__ __forceinline__ void k2_down(lds_ptr lds, int gi, int wid, int lane, const char* __restrict__ wd, char* __restrict__ ws) {
    const unsigned lds0 = (unsigned)(uintptr_t)lds;
    const int li = gi >> 3, tt = ((gi & 7) * 2 + (li >= 10)) * 4 + wid, cg = li >= 10 ? li - 10 : li;
    const int h = lane >> 5, l31 = lane & 31, j = lane >> 3, blk = (lane >> 4) & 1, q = (lane >> 2) & 3, fsw = 3 * (q >> 1);
    const unsigned wvo = ((j & 3) + 8 * (j >> 2)) * (D_ * 2) + (((lane & 7) ^ (6 * ((j >> 1) & 1))) << 4);
    const char* wp = wd + cg * 128 + wid * 32 * (D_ * 2);
    const char* hp = ws + WS_H3 + (size_t)tt * 96 * 1024 + lane * 16;
    s16x8 ha[8], hb[8];
    auto fetch = [&](int st, s16x8 (&hf)[8]) {
        const char* a = wp + (unsigned)(st * 128 * D_ * 2);
        const unsigned d = lds0 + (st & 1) * 16384 + wid * 4096;
        glds16s(a, wvo, d); glds16s(a + 4 * D_ * 2, wvo, d + 1024); glds16s(a + 16 * D_ * 2, wvo, d + 2048); glds16s(a + 20 * D_ * 2, wvo, d + 3072);
#pragma unroll
        for (int i = 0; i < 8; ++i) hf[i] = *(const s16x8*)(hp + (st * 8 + i) * 1024);
    };
    f32x16 acc[2] = {f32x16{}, f32x16{}};
    const lds_ptr r0 = lds + h * 1024 + q * 128 + (lane & 3) * 8 + ((blk ^ fsw) << 5), r1 = lds + h * 1024 + q * 128 + (lane & 3) * 8 + (((2 + blk) ^ fsw) << 5);
    auto compute = [&](int slot, const s16x8 (&hf)[8]) {
#pragma unroll
        for (int i = 0; i < 8; ++i) {
            const int o = slot * 16384 + i * 2048;
            acc[0] = MFMA32(CAT8(trread(r0 + o), trread(r0 + o + 512)), hf[i], acc[0]);
            acc[1] = MFMA32(CAT8(trread(r1 + o), trread(r1 + o + 512)), hf[i], acc[1]);
        }
    };
    fetch(0, ha);
    for (int st = 0; st < 12; st += 2) {
        WAIT_BAR(0);
        fetch(st + 1, hb);
        compute(0, ha);
        WAIT_BAR(0);
        if (st + 2 < 12) fetch(st + 2, ha);
        compute(1, hb);
    }
    char* dst = ws + WS_BD + (size_t)(tt * 32 + l31) * (D_ * 2) + cg * 128 + h * 8;
#pragma unroll
    for (int t = 0; t < 2; ++t)
#pragma unroll
        for (int g = 0; g < 4; ++g)
            *(u32x2*)(dst + t * 64 + g * 16) = u32x2{cvtpk(acc[t][g * 4], acc[t][g * 4 + 1]), cvtpk(acc[t][g * 4 + 2], acc[t][g * 4 + 3])};
}

__global__ __launch_bounds__(512, 4) void k2_attn(const char* __restrict__ wd, char* __restrict__ ws) {
    extern __shared__ __attribute__((aligned(16))) char smem[];
    lds_ptr lds = (lds_ptr)smem;
    const unsigned lds0 = (unsigned)(uintptr_t)lds;
    const int tid = threadIdx.x, lane = tid & 63, wid = __builtin_amdgcn_readfirstlane(tid >> 6);
    STAMP_DECL; STAMP(0);
    if (blockIdx.x >= 256) {
        if (wid < 4) { k2_down(lds, blockIdx.x - 256, wid, lane, wd, ws); STAMP(4); STAMP_FLUSH(1); }
        return;
    }
    const int bid = blockIdx.x, head = (bid & 7) * 4 + ((bid >> 3) & 3), qb = bid >> 5, qt = qb * 8 + wid;
    const int h = lane >> 5, l31 = lane & 31;
    const char* kv = ws + WS_KV + head * KV_HEAD;
    const char* src = kv + wid * 1024 + lane * 16;
    auto stage = [&](int slot) {
        const unsigned b = lds0 + slot * K2_SLOTB + wid * 1024;
        glds16(src, b); glds16(src + 8192, b + 8192); glds16(src + 16384, b + 16384);
        src += K2_CHUNK;
    };
    stage(0); stage(1); stage(2);
    const s16x8 qf1 = *(const s16x8*)(ws + WS_QF1 + ((size_t)head * 64 + qt) * 1024 + lane * 16);
    s16x4 qf2 = *(const s16x4*)(ws + WS_QF2 + ((size_t)head * 64 + qt) * 256 + l31 * 8);
    if (h) qf2 = s16x4{0, 0, 0, 0};
    const int k1off = lane * 16, k2off = h ? KV_V + 20 * 16 : KV_K2 + l31 * 8;
    const int vd = l31 <= 20 ? l31 : (l31 < 28 ? 20 : 19), voff = KV_V + h * KV_VHALF + vd * 16;

    f32x16 o = f32x16{}, sc[2];
    s16x8 k1[2], v0[4], v1[4]; s16x4 k2[2];
    lds_ptr pk1 = lds + k1off, pk2 = lds + k2off, pv = lds + voff;
#define RD_K(t, j) do { k1[(t) & 1] = *(const LDS_AS s16x8*)(pk1 + (j) * KV_TILE); k2[(t) & 1] = *(const LDS_AS s16x4*)(pk2 + (j) * KV_TILE); } while (0)
#define RD_V(t, j) do { v0[(t) & 3] = *(const LDS_AS s16x8*)(pv + (j) * KV_TILE); v1[(t) & 3] = *(const LDS_AS s16x8*)(pv + (j) * KV_TILE + KV_VSUB); } while (0)
    asm volatile("s_waitcnt vmcnt(6)\n\ts_barrier" ::: "memory");
    RD_K(0, 0); RD_K(1, 1); RD_V(0, 0); RD_V(1, 1);
    {
        f32x16 s = MFMA32(k1[0], qf1, f32x16{});
        s = MFMA8(k2[0], qf2, s);
        const float m = rbf(-max16(s));
#pragma unroll
        for (int r = 0; r < 16; ++r) sc[0][r] = s[r] + m;
        if (h) qf2[0] = (short)(__float_as_uint(m) >> 16);
        sc[1] = f32x16{}; v1[3] = s16x8{};
    }
    int slot = 0;
    STAMP(1);
#define EXP4(S, i) do { S[i] = __builtin_amdgcn_exp2f(S[i]); S[i + 1] = __builtin_amdgcn_exp2f(S[i + 1]); S[i + 2] = __builtin_amdgcn_exp2f(S[i + 2]); S[i + 3] = __builtin_amdgcn_exp2f(S[i + 3]); PIN(S); SBAR(); } while (0)
    for (int c = 0; c < 8; ++c) {
#pragma unroll
        for (int j = 0; j < 8; ++j) {
            if (j == 6) {
                if (c < 6) asm volatile("s_waitcnt vmcnt(3) lgkmcnt(0)\n\ts_barrier" ::: "memory");
                else asm volatile("s_waitcnt vmcnt(0) lgkmcnt(0)\n\ts_barrier" ::: "memory");
                if (c + 3 < 8) stage(slot);
                const int d = (slot == K2_SLOTS - 1) ? -(K2_SLOTS - 1) * K2_SLOTB : K2_SLOTB;
                slot = (slot == K2_SLOTS - 1) ? 0 : slot + 1;
                pk1 += d; pk2 += d; pv += d;
                SBAR();
            }
            o = SMF32(v1[(j + 3) & 3], hi8(sc[(j + 1) & 1]), o); PIN(o); SBAR();
            EXP4(sc[j & 1], 0);
            sc[(j + 1) & 1] = MFMA32(k1[(j + 1) & 1], qf1, f32x16{}); PIN(sc[(j + 1) & 1]); SBAR();
            RD_K(j, (j + 2) & 7);
            EXP4(sc[j & 1], 4);
            sc[(j + 1) & 1] = MFMA8(k2[(j + 1) & 1], qf2, sc[(j + 1) & 1]); PIN(sc[(j + 1) & 1]); SBAR();
            EXP4(sc[j & 1], 8);
            o = SMF32(v0[j & 3], lo8(sc[j & 1]), o); PIN(o); SBAR();
            EXP4(sc[j & 1], 12);
            RD_V(j + 2, (j + 2) & 7);
            SBAR();
        }
    }
    o = SMF32(v1[3], hi8(sc[1]), o);
    STAMP(2);
    {
        bool bad = false;
#pragma unroll
        for (int r = 0; r < 9; ++r) bad |= !(fabsf(o[r]) < 1e30f);
        if (!h) {
#pragma unroll
            for (int r = 9; r < 12; ++r) bad |= !(fabsf(o[r]) < 1e30f);
        } else bad |= !(o[8] > 1e-30f);
        if (__builtin_expect(__any(bad), 0)) {
            float m = -INFINITY;
            f32x16 negm;
            if (h) qf2[0] = 0;
#pragma unroll 1
            for (int t = 0; t < 64; ++t) {
                const char* g = kv + (size_t)t * KV_TILE;
                f32x16 s = MFMA32(*(const s16x8*)(g + k1off), qf1, f32x16{});
                s = MFMA8(*(const s16x4*)(g + k2off), qf2, s);
                m = fmaxf(m, max16(s));
            }
#pragma unroll
            for (int r = 0; r < 16; ++r) negm[r] = -m;
            o = f32x16{};
#pragma unroll 1
            for (int t = 0; t < 64; ++t) {
                const char* g = kv + (size_t)t * KV_TILE;
                f32x16 s = MFMA32(*(const s16x8*)(g + k1off), qf1, negm);
                s = MFMA8(*(const s16x4*)(g + k2off), qf2, s);
#pragma unroll
                for (int r = 0; r < 16; ++r) s[r] = __builtin_amdgcn_exp2f(s[r]);
                o = SMF32(*(const s16x8*)(g + voff), lo8(s), o);
                o = SMF32(*(const s16x8*)(g + voff + KV_VSUB), hi8(s), o);
            }
        }
    }
    auto rr = __builtin_amdgcn_permlane32_swap(__float_as_uint(o[8]), __float_as_uint(o[8]), false, false);
    const float rl = 1.0f / __uint_as_float(rr[1]);
    char* a3 = ws + WS_O3 + (size_t)qt * 40 * 1024;
    *(u32x4*)(a3 + head * 1024 + lane * 16) =
        u32x4{cvtpk(o[0] * rl, o[1] * rl), cvtpk(o[2] * rl, o[3] * rl), cvtpk(o[4] * rl, o[5] * rl), cvtpk(o[6] * rl, o[7] * rl)};
    if (!h) *(u32x2*)(a3 + (32 + (head >> 2)) * 1024 + (((head >> 1) & 1) * 32 + l31) * 16 + (head & 1) * 8) =
        u32x2{cvtpk(o[8] * rl, o[9] * rl), cvtpk(o[10] * rl, o[11] * rl)};
    STAMP(3); STAMP_FLUSH(1);
}

constexpr int K3_PART = 81920, K3_PROW = 272, K3_LDS = K3_PART + 2 * 128 * K3_PROW;

__global__ __launch_bounds__(512, 2) void k3_out(const char* __restrict__ x, const char* __restrict__ wo, const char* __restrict__ ws, char* __restrict__ out) {
    extern __shared__ __attribute__((aligned(16))) char smem[];
    lds_ptr lds = (lds_ptr)smem;
    const unsigned lds0 = (unsigned)(uintptr_t)lds;
    const int tid = threadIdx.x, lane = tid & 63, wid = __builtin_amdgcn_readfirstlane(tid >> 6);
    STAMP_DECL; STAMP(0);
    const int bid = blockIdx.x, li = bid >> 3, panel = (bid & 7) * 2 + (li >= 10), cg = li >= 10 ? li - 10 : li;
    const int m = wid & 3, kh = wid >> 2;
    const int h = lane >> 5, l31 = lane & 31, j = lane >> 3, blk = (lane >> 4) & 1, q = (lane >> 2) & 3, fsw = 3 * (q >> 1);
    const unsigned sw = ((lane & 7) ^ (6 * ((j >> 1) & 1))) << 4;
    const unsigned voa = ((j & 3) + 8 * (j >> 2)) * (D_ * 2) + sw, vob = ((j >> 2) * 20 + 16 + (j & 3)) * (D_ * 2) + sw;
    const char* wp = wo + cg * 128;
    const char* op = ws + WS_O3 + ((size_t)(panel * 4 + m) * 40 + kh * 20) * 1024 + lane * 16;
    s16x8 of[20];
#pragma unroll
    for (int i = 0; i < 5; ++i) {
        const int ks = kh * 20 + i * 4 + m;
        const unsigned d = lds0 + ks * 2048;
        if (ks < 32) { const char* a = wp + (unsigned)(ks * 20 * D_ * 2); glds16s(a, voa, d); glds16s(a + 4 * D_ * 2, voa, d + 1024); }
        else { const char* a = wp + (unsigned)((ks - 32) * 80 * D_ * 2); glds16s(a, vob, d); glds16s(a + 40 * D_ * 2, vob, d + 1024); }
#pragma unroll
        for (int k = 0; k < 4; ++k) of[i * 4 + k] = *(const s16x8*)(op + (i * 4 + k) * 1024);
        MEMFENCE();
    }
    const size_t eoff = (size_t)(panel * 128 + wid * 16 + j) * (D_ * 2) + cg * 128 + (lane & 7) * 16;
    u32x4 xv[2], bv[2];
#pragma unroll
    for (int i = 0; i < 2; ++i) { xv[i] = *(const u32x4*)(x + eoff + i * 8 * D_ * 2); bv[i] = *(const u32x4*)(ws + WS_BD + eoff + i * 8 * D_ * 2); }
    STAMP(1);
    f32x16 acc[2] = {f32x16{}, f32x16{}};
    const int rb = kh * 20 * 2048 + h * 1024 + q * 128 + (lane & 3) * 8;
    const lds_ptr r0 = lds + rb + ((blk ^ fsw) << 5), r1 = lds + rb + (((2 + blk) ^ fsw) << 5);
#pragma unroll
    for (int i = 0; i < 5; ++i) {
        if (i == 0) WAIT_BAR(28); else if (i == 1) WAIT_BAR(22); else if (i == 2) WAIT_BAR(16); else if (i == 3) WAIT_BAR(10); else WAIT_BAR(4);
#pragma unroll
        for (int k = 0; k < 4; ++k) {
            const int o = (i * 4 + k) * 2048;
            acc[0] = MFMA32(CAT8(trread(r0 + o), trread(r0 + o + 512)), of[i * 4 + k], acc[0]);
            acc[1] = MFMA32(CAT8(trread(r1 + o), trread(r1 + o + 512)), of[i * 4 + k], acc[1]);
        }
    }
    STAMP(2);
    const lds_ptr pw = lds + K3_PART + (kh * 128 + m * 32 + l31) * K3_PROW + h * 16;
#pragma unroll
    for (int t = 0; t < 2; ++t)
#pragma unroll
        for (int g = 0; g < 4; ++g) *(LDS_AS f32x4*)(pw + t * 128 + g * 32) = f32x4{acc[t][g * 4], acc[t][g * 4 + 1], acc[t][g * 4 + 2], acc[t][g * 4 + 3]};
    __syncthreads();
#pragma unroll
    for (int i = 0; i < 2; ++i) {
        const lds_ptr pr = lds + K3_PART + (wid * 16 + i * 8 + j) * K3_PROW + (lane & 7) * 32;
        const f32x4 a0 = *(const LDS_AS f32x4*)pr + *(const LDS_AS f32x4*)(pr + 128 * K3_PROW);
        const f32x4 a1 = *(const LDS_AS f32x4*)(pr + 16) + *(const LDS_AS f32x4*)(pr + 128 * K3_PROW + 16);
        u32x4 o;
#pragma unroll
        for (int e = 0; e < 4; ++e) {
            const float lo = e < 2 ? a0[2 * e] : a1[2 * e - 4], hi = e < 2 ? a0[2 * e + 1] : a1[2 * e - 3];
            o[e] = cvtpk(rbf(bf_lo(xv[i][e]) + rbf(lo)) + bf_lo(bv[i][e]), rbf(bf_hi(xv[i][e]) + rbf(hi)) + bf_hi(bv[i][e]));
        }
        *(u32x4*)(out + eoff + i * 8 * D_ * 2) = o;
    }
    STAMP(3); STAMP_FLUSH(2);
}

extern "C" void kernel_launch(void* const* d_in, const int* in_sizes, int n_in, void* d_out, int out_size, void* d_ws, size_t ws_size,
                              hipStream_t stream) {
    const char* x = (const char*)d_in[0];
    const char* wqkv = (const char*)d_in[1];
    const char* wo = (const char*)d_in[2];
    const char* wg = (const char*)d_in[4];
    const char* wu = (const char*)d_in[5];
    const char* wd = (const char*)d_in[6];
    char* ws = (char*)d_ws;
    (void)hipFuncSetAttribute((const void*)k1_proj, hipFuncAttributeMaxDynamicSharedMemorySize, K1_LDS);
    (void)hipFuncSetAttribute((const void*)k2_attn, hipFuncAttributeMaxDynamicSharedMemorySize, K2_LDS);
    (void)hipFuncSetAttribute((const void*)k3_out, hipFuncAttributeMaxDynamicSharedMemorySize, K3_LDS);
    k1_proj<<<256, 512, K1_LDS, stream>>>(x, wqkv, wg, wu, ws);
    k2_attn<<<416, 512, K2_LDS, stream>>>(wd, ws);
    k3_out<<<160, 512, K3_LDS, stream>>>(x, wo, ws, (char*)d_out);
}
```

```cpp
#include <hip/hip_runtime.h>
#include <hip/hip_bf16.h>
#include <stdint.h>

typedef __bf16 bfx4 __attribute__((ext_vector_type(4)));
typedef __bf16 bfx8 __attribute__((ext_vector_type(8)));
typedef __bf16 bfx16 __attribute__((ext_vector_type(16)));
typedef short s16x4 __attribute__((ext_vector_type(4)));
typedef short s16x8 __attribute__((ext_vector_type(8)));
typedef short s16x16 __attribute__((ext_vector_type(16)));
typedef float f32x2 __attribute__((ext_vector_type(2)));
typedef float f32x4 __attribute__((ext_vector_type(4)));
typedef float f32x8 __attribute__((ext_vector_type(8)));
typedef float f32x16 __attribute__((ext_vector_type(16)));
typedef unsigned u32x2 __attribute__((ext_vector_type(2)));
typedef unsigned u32x4 __attribute__((ext_vector_type(4)));
typedef unsigned u32x8 __attribute__((ext_vector_type(8)));
#define LDS_AS __attribute__((address_space(3)))
typedef LDS_AS char* lds_ptr;

#define ABL 0
#define DPRIO 1
#define K1_ROT 0
constexpr int S_ = 2048, D_ = 640, H_ = 32, F_ = 1536;

constexpr int KV_TILE = 2624, KV_K2 = 1024, KV_V = 1280, KV_VSUB = 672, KV_VHALF = 336;
constexpr size_t WS_QF1 = 0;
constexpr size_t WS_QF2 = WS_QF1 + (size_t)H_ * 64 * 1024;
constexpr size_t WS_KV = WS_QF2 + (size_t)H_ * 64 * 256;
constexpr size_t KV_HEAD = (size_t)64 * KV_TILE;
constexpr size_t WS_O16 = WS_KV + H_ * KV_HEAD + 8192;
constexpr size_t WS_H16 = WS_O16 + (size_t)128 * 20 * 1024;
constexpr size_t WS_WP = WS_H16 + (size_t)128 * 48 * 1024;
constexpr size_t WS_CNT = WS_WP + (size_t)8 * 68 * 5120 + 8192;

#define STAMP_ARG
#define STAMP_PASS
#define STAMP_DECL do {} while (0)
#define STAMP(i) do {} while (0)
#define STAMP_FLUSH(kern) do {} while (0)
#define SBAR() __builtin_amdgcn_sched_barrier(0)
#define MFMA32(a, b, c) __builtin_amdgcn_mfma_f32_32x32x16_bf16((bfx8)(a), (bfx8)(b), (c), 0, 0, 0)

__device__ __forceinline__ unsigned cvtpk(float lo, float hi) {
    typedef __bf16 bfx2 __attribute__((ext_vector_type(2)));
    bfx2 r = {(__bf16)lo, (__bf16)hi};
    return __builtin_bit_cast(unsigned, r);
}
__device__ __forceinline__ void glds16(const void* g, unsigned lds_base) {
    unsigned sv;
    asm volatile("s_mov_b32 %0, m0\n\ts_mov_b32 m0, %2\n\ts_nop 0\n\tglobal_load_lds_dwordx4 %1, off\n\ts_mov_b32 m0, %0"
                 : "=&s"(sv) : "v"(g), "s"(lds_base) : "memory");
}
__device__ __forceinline__ s16x4 trread(const LDS_AS char* p) {
    return __builtin_amdgcn_ds_read_tr16_b64_v4i16((LDS_AS s16x4*)p);
}
__device__ __forceinline__ float rbf(float v) { return (float)(__bf16)v; }
__device__ __forceinline__ float bf_lo(unsigned u) { return __uint_as_float(u << 16); }
__device__ __forceinline__ float bf_hi(unsigned u) { return __uint_as_float(u & 0xffff0000u); }
__device__ __forceinline__ float swap32_add(float v) {
    auto r = __builtin_amdgcn_permlane32_swap(__float_as_uint(v), __float_as_uint(v), false, false);
    return __uint_as_float(r[0]) + __uint_as_float(r[1]);
}
__device__ __forceinline__ float swap32_max(float v) {
    auto r = __builtin_amdgcn_permlane32_swap(__float_as_uint(v), __float_as_uint(v), false, false);
    return fmaxf(__uint_as_float(r[0]), __uint_as_float(r[1]));
}

constexpr int K1_WSLOT = 20480, K1_X0 = 4 * K1_WSLOT, K1_XSLOT = 20480, K1_STAT = K1_X0 + 3 * K1_XSLOT;
constexpr int K1_LDS = K1_STAT + 160 * 8 + 320 * 4;

#define PIN(x) asm volatile("" : "+v"(x))
#define MEMFENCE() asm volatile("" ::: "memory")
#define WAIT_BAR(N) do { MEMFENCE(); __builtin_amdgcn_s_waitcnt(0x70 | ((N) & 15) | (((N) >> 4) << 14)); __builtin_amdgcn_s_barrier(); MEMFENCE(); SBAR(); } while (0)
#define LGKM_BAR() do { MEMFENCE(); __builtin_amdgcn_s_waitcnt(0xC07F); __builtin_amdgcn_s_barrier(); MEMFENCE(); SBAR(); } while (0)
#define SMF16(a, b, c, i) __builtin_amdgcn_smfmac_f32_16x16x64_bf16((bfx8)(a), (bfx16)(b), (c), (i), 0, 0)
#define CAT8(a, b) __builtin_shufflevector(a, b, 0, 1, 2, 3, 4, 5, 6, 7)
#define CAT16(a, b) __builtin_shufflevector(a, b, 0, 1, 2, 3, 4, 5, 6, 7, 8, 9, 10, 11, 12, 13, 14, 15)

__device__ __forceinline__ void glds16s(const char* sbase, unsigned voff, unsigned lds_base) {
    unsigned sv;
    asm volatile("s_mov_b32 %0, m0\n\ts_mov_b32 m0, %3\n\ts_nop 0\n\tglobal_load_lds_dwordx4 %1, %2\n\ts_mov_b32 m0, %0"
                 : "=&s"(sv) : "v"(voff), "s"(sbase), "s"(lds_base) : "memory");
}

template <int MODE> struct K1F { s16x8 x[MODE == 2 ? 5 : 2]; s16x4 w[MODE == 2 ? 2 : 5][2]; };

template <int MODE>
__device__ __forceinline__ void k1_read(K1F<MODE>& n, lds_ptr xa, lds_ptr w0, lds_ptr w1, int wimm, int i) {
    if (MODE != 2) {
        if (i < 2) n.x[i] = *(const LDS_AS s16x8*)(xa + i * 4096);
        else if (i < 12) { const int j = i - 2, t = j >> 1; n.w[t][j & 1] = trread(((t & 1) ? w1 : w0) + wimm + (t >> 1) * 1024 + (j & 1) * 512); }
    } else {
        if (i < 4) n.w[i >> 1][i & 1] = trread(((i >> 1) ? w1 : w0) + wimm + (i & 1) * 512);
        else if (i < 9) n.x[i - 4] = *(const LDS_AS s16x8*)(xa + (i - 4) * 4096);
    }
}
struct K1S { u32x4 u[2]; float s1[5], s2[5]; };
template <int MODE, bool RD, int J0, int J1, int R0, int R1>
__device__ __forceinline__ void k1_step(const K1F<MODE>& f, K1F<MODE>& n, f32x16 (&acc)[10], lds_ptr xa, lds_ptr w0, lds_ptr w1, int wimm, K1S& st, lds_ptr sp) {
#pragma unroll
    for (int g = 0; g < 10; ++g) {
        const int xi = MODE == 2 ? g >> 1 : g & 1, wi = MODE == 2 ? g & 1 : g >> 1;
        const s16x8 w = CAT8(f.w[wi][0], f.w[wi][1]);
#if !(ABL & 1)
        acc[g] = MODE == 1 ? MFMA32(f.x[xi], w, acc[g]) : MFMA32(w, f.x[xi], acc[g]);
#else
        { s16x8 x_ = f.x[xi]; asm volatile("" :: "v"(w), "v"(x_)); }
#endif
        PIN(acc[g]); SBAR();
        if (RD) { k1_read<MODE>(n, xa, w0, w1, wimm, 2 * g); k1_read<MODE>(n, xa, w0, w1, wimm, 2 * g + 1); }
        if (g < 8) {
            const int J = g < 4 ? J0 : J1;
            if (J >= 0 && !(ABL & 8)) {
                const unsigned v = st.u[g >> 2][g & 3];
                const float a = bf_lo(v), c = bf_hi(v);
                st.s1[J < 0 ? 0 : J] += a + c; st.s2[J < 0 ? 0 : J] = fmaf(a, a, fmaf(c, c, st.s2[J < 0 ? 0 : J]));
                PIN(st.s1[J < 0 ? 0 : J]); PIN(st.s2[J < 0 ? 0 : J]);
            }
        }
        if (g == 6 && R0 >= 0) st.u[0] = *(const LDS_AS u32x4*)(sp + R0 * 4096);
        if (g == 8 && R1 >= 0) st.u[1] = *(const LDS_AS u32x4*)(sp + R1 * 4096);
        SBAR();
    }
}
__device__ __forceinline__ float sum8(float v) {
    v += __uint_as_float(__builtin_amdgcn_update_dpp(0, __float_as_uint(v), 0xB1, 0xF, 0xF, true));
    v += __uint_as_float(__builtin_amdgcn_update_dpp(0, __float_as_uint(v), 0x4E, 0xF, 0xF, true));
    v += __uint_as_float(__builtin_amdgcn_update_dpp(0, __float_as_uint(v), 0x141, 0xF, 0xF, true));
    return v;
}
template <int MODE>
__device__ __forceinline__ void k1_compute(f32x16 (&acc)[10], lds_ptr lds, int wid, int lane) {
    constexpr int NCB = MODE == 2 ? 4 : 5, NXP = MODE == 2 ? 5 : 4, J4 = MODE == 2 ? 4 : -1;
    const int h = lane >> 5, l31 = lane & 31, blk = (lane >> 4) & 1, q = (lane >> 2) & 3, p = lane & 3, fsw = 3 * (q >> 1);
    const int xrow = (MODE == 2 ? 0 : (wid & 1) * 64) + l31, xo = K1_X0 + xrow * 128 + ((h ^ ((xrow >> 1) & 7)) << 4);
    lds_ptr xa0 = lds + xo, xa1 = lds + (xo ^ 32), xa2 = lds + (xo ^ 64), xa3 = lds + (xo ^ 96), sp = lds + K1_X0 + wid * 1024 + lane * 16;
    const int wb = h * NCB * 1024 + q * 128 + p * 8;
    lds_ptr w0, w1;
    if (MODE == 2) { const int T = wid * 2; w0 = lds + wb + ((T >> 2) + 2 * blk) * 1024 + (((T & 3) ^ fsw ^ blk) << 5); w1 = lds + wb + ((T >> 2) + 2 * blk) * 1024 + ((((T + 1) & 3) ^ fsw ^ blk) << 5); }
    else { const int s0 = (wid >> 1) * 10 + blk, s1 = s0 + 2; w0 = lds + wb + (s0 >> 2) * 1024 + (((s0 & 3) ^ fsw) << 5); w1 = lds + wb + (s1 >> 2) * 1024 + (((s1 & 3) ^ fsw) << 5); }
    K1F<MODE> fa, fb = {};
    K1S st = {};
    int xs = 0;
    for (int u = 0; u < 10; ++u) {
        LGKM_BAR();
        k1_step<MODE, true, -1, -1, 0, 1>(fb, fa, acc, xa0, w0, w1, 0, st, sp);
        k1_step<MODE, true, 0, 1, 2, -1>(fa, fb, acc, xa1, w0, w1, 2 * NCB * 1024, st, sp);
        LGKM_BAR();
        k1_step<MODE, true, 2, -1, 3, J4>(fb, fa, acc, xa2, w0, w1, K1_WSLOT, st, sp);
        k1_step<MODE, true, 3, J4, -1, -1>(fa, fb, acc, xa3, w0, w1, K1_WSLOT + 2 * NCB * 1024, st, sp);
        const int dw = (u & 1) ? -2 * K1_WSLOT : 2 * K1_WSLOT, dx = (xs == 2) ? -2 * K1_XSLOT : K1_XSLOT;
        xs = (xs == 2) ? 0 : xs + 1;
        w0 += dw; w1 += dw; xa0 += dx; xa1 += dx; xa2 += dx; xa3 += dx; sp += dx;
    }
#pragma unroll
    for (int g = 0; g < 10; ++g) {
        const int xi = MODE == 2 ? g >> 1 : g & 1, wi = MODE == 2 ? g & 1 : g >> 1;
        const s16x8 w = CAT8(fb.w[wi][0], fb.w[wi][1]);
        acc[g] = MODE == 1 ? MFMA32(fb.x[xi], w, acc[g]) : MFMA32(w, fb.x[xi], acc[g]);
    }
    const int pos = lane & 7;
    float a = 0.f, b = 0.f;
#pragma unroll
    for (int j = 0; j < NXP; ++j) { const float sa = sum8(st.s1[j]), sb = sum8(st.s2[j]); a = pos == j ? sa : a; b = pos == j ? sb : b; }
    const float mean = a * (1.0f / D_), var = fmaxf(b * (1.0f / D_) - mean * mean, 0.f);
    const float rstd = __builtin_amdgcn_rcpf(rbf(__builtin_amdgcn_sqrtf(rbf(rbf(var) + 1e-5f))));
    if (pos < NXP) ((LDS_AS f32x2*)(lds + K1_STAT))[pos * 32 + wid * 8 + (lane >> 3)] = f32x2{rstd, -rbf(mean) * rstd};
}

template <bool FFN>
__device__ __forceinline__ void k1_helper(lds_ptr lds, int hw, int lane, const char* xp, const char* wp0, const char* wp1, int rot,
                                          const char* wd, const char* wo, char* wpk, int job STAMP_ARG) {
    constexpr int NCB = FFN ? 4 : 5, WROW = FFN ? F_ * 2 : 3 * D_ * 2;
    const unsigned lds0 = (unsigned)(uintptr_t)lds;
    const int r = lane >> 3, pos = lane & 7;
    const unsigned xvo = (hw * 8 + r) * (D_ * 2) + ((pos ^ ((hw & 1) * 4 + (r >> 1))) << 4);
    const unsigned wvo0 = (hw * 8 + r) * WROW + ((pos ^ (6 * ((r >> 1) & 1))) << 4), wvo1 = wvo0 ^ 32;
    auto issue = [&](int t) {
        if (t >= -3 && t <= 16) {
            const int sl = t + 3; int ks = sl + 2 * rot; ks = ks >= 20 ? ks - 20 : ks;
            const unsigned d = lds0 + (sl & 3) * K1_WSLOT + hw * NCB * 1024;
            const char* a = wp0 + (unsigned)(ks * 32 * WROW);
            if (FFN) { const char* b = wp1 + (unsigned)(ks * 32 * WROW); glds16s(a, wvo0, d); glds16s(a + 128, wvo0, d + 1024); glds16s(b, wvo1, d + 2048); glds16s(b + 128, wvo1, d + 3072); }
            else {
#pragma unroll
                for (int j = 0; j < 5; ++j) glds16s(a + j * 128, wvo0, d + j * 1024);
            }
        }
        if (t <= 15) {
            const int un = (t + 4) >> 1; int ku = un + rot; ku = ku >= 10 ? ku - 10 : ku;
            const unsigned d = lds0 + K1_X0 + (un % 3) * K1_XSLOT + hw * 1024;
            const char* a = xp + ku * 128;
            if (!(t & 1)) { glds16s(a, xvo, d); glds16s(a + 32 * D_ * 2, xvo, d + 4096); if (FFN) glds16s(a + 4 * 32 * D_ * 2, xvo, d + 4 * 4096); }
            else { glds16s(a + 2 * 32 * D_ * 2, xvo, d + 2 * 4096); glds16s(a + 3 * 32 * D_ * 2, xvo, d + 3 * 4096); }
        }
    };
    STAMP(4);
    issue(-4); issue(-3); issue(-2);
    const int r16 = lane & 15, h = lane >> 5, blk = (lane >> 4) & 1, q = (lane >> 2) & 3, fsw = 3 * (q >> 1);
    const unsigned olo = r16 < 2 ? 0x3f803f80u : 0u, ohi = (r16 >> 1) == 1 ? 0x3f803f80u : 0u;
    const s16x8 ones = __builtin_bit_cast(s16x8, (u32x4{olo, olo, ohi, ohi}));
    const int idx = (r16 & 1) ? 0xEEEE : 0x4444;
    const int cs0 = hw * 2 + blk;
    const int wrd = h * NCB * 1024 + q * 128 + (lane & 3) * 8 + (FFN ? 2 * blk * 1024 + ((hw ^ fsw ^ blk) << 5) : (cs0 >> 2) * 1024 + (((cs0 & 3) ^ fsw) << 5));
    f32x4 cs[3] = {f32x4{}, f32x4{}, f32x4{}};
    const int j2 = (FFN || hw >= 2) ? 0 : 2 * 2048;
    for (int sl = 0; sl < 20; ++sl) {
        if (sl == 0) WAIT_BAR(7); else if (sl <= 16) WAIT_BAR(FFN ? 13 : 14); else if (sl == 17) WAIT_BAR(FFN ? 10 : 12); else if (sl == 18) WAIT_BAR(NCB + 4); else WAIT_BAR(4);
        if (sl == 0) STAMP(5);
        const lds_ptr b = lds + (sl & 3) * K1_WSLOT + wrd;
        s16x16 wv[3];
#pragma unroll
        for (int j = 0; j < (FFN ? 2 : 3); ++j) {
            const lds_ptr pp = b + (j == 2 ? j2 : j * (FFN ? 1024 : 2048));
            wv[j] = CAT16(CAT8(trread(pp), trread(pp + 512)), CAT8(trread(pp + 2 * NCB * 1024), trread(pp + 2 * NCB * 1024 + 512)));
        }
        if (!(ABL & 4)) { if (sl == 0) issue(-1);
        issue(sl); }
        if (sl == 17) {
            const int jb = job < 680 ? job : 0, step = jb / 10, cb = jb - step * 10;
            const unsigned va = ((r & 3) + 8 * (r >> 2)) * (D_ * 2) + pos * 16, vb = ((r >> 2) * 20 + 16 + (r & 3)) * (D_ * 2) + pos * 16;
#pragma unroll
            for (int g = 0; g < 4; ++g) {
                const int ks = (step - 48) * 2 + (g >> 1), hh = g & 1;
                const char* a = step < 48 ? wd + (step * 32 + (g >> 1) * 16 + hh * 4) * (D_ * 2) : ks < 32 ? wo + (ks * 20 + hh * 4) * (D_ * 2) : wo + ((ks - 32) * 4 + hh * 2) * 20 * (D_ * 2);
                glds16s(a + cb * 128, step < 64 ? va : vb, lds0 + hw * 4096 + g * 1024);
            }
        }
#pragma unroll
        for (int j = 0; j < (FFN ? 2 : 3); ++j) if (!(ABL & 16) && (j < 2 || hw < 2)) cs[j] = SMF16(ones, wv[j], cs[j], idx);
    }
    LDS_AS float* colf = (LDS_AS float*)(lds + K1_STAT + 1280);
    if (lane < 16) {
#pragma unroll
        for (int j = 0; j < (FFN ? 2 : 3); ++j)
            if (j < 2 || hw < 2) { colf[(hw + 4 * j) * 32 + lane] = cs[j][0] + cs[j][1]; colf[(hw + 4 * j) * 32 + 16 + lane] = cs[j][2] + cs[j][3]; }
    }
    STAMP(1);
    LGKM_BAR();
    MEMFENCE(); __builtin_amdgcn_s_waitcnt(0xF70); MEMFENCE();
    if (job < 680) {
        const int step = job / 10, cb = job - step * 10;
        const lds_ptr pr = lds + hw * 4096 + (lane >> 4) * 1024 + q * 128 + (lane & 3) * 8;
#pragma unroll
        for (int nt = 0; nt < 4; ++nt) {
            const int N = cb * 4 + nt, cg = N / 5;
            *(s16x8*)(wpk + ((cg * 68 + step) * 5 + N - cg * 5) * 1024 + lane * 16) = CAT8(trread(pr + nt * 32), trread(pr + nt * 32 + 512));
        }
    }
}

__global__ __launch_bounds__(512, 2) void k1_proj(const char* __restrict__ x, const char* __restrict__ wqkv, const char* __restrict__ wg,
                                                  const char* __restrict__ wu, const char* __restrict__ wd, const char* __restrict__ wo, char* __restrict__ ws) {
    extern __shared__ __attribute__((aligned(16))) char smem[];
    lds_ptr lds = (lds_ptr)smem;
    STAMP_DECL; STAMP(0);
    const int tid = threadIdx.x, lane = tid & 63, wid = __builtin_amdgcn_readfirstlane(tid >> 6);
    const int bid = blockIdx.x, xcd = bid & 7, bi = bid >> 3;
    const bool ffn = xcd >= 3;
    int panel, grp;
    if (!ffn) { if (xcd < 2) { panel = xcd * 8 + (bi & 7); grp = bi >> 3; } else { panel = bi & 15; grp = 4 + (bi >> 4); } }
    else {
        const int i = (xcd - 3) * 32 + bi;
        if (i >= 156) { if (i == 156 && tid < 8) *(unsigned*)(ws + WS_CNT + tid * 128) = 0u; return; }
        grp = i / 13; panel = i - grp * 13;
    }
    const int tt0 = ffn ? min(panel * 5, 59) : panel * 4;
    if (wid >= 4) {
        const char* xp = x + (size_t)tt0 * 32 * (D_ * 2);
        const int rot = K1_ROT ? (bi * 5) >> 4 : 0;
        const int job = (wid - 4) * 252 + xcd * 32 + bi;
        if (ffn) k1_helper<true>(lds, wid - 4, lane, xp, wg + grp * 256, wu + grp * 256, rot, wd, wo, ws + WS_WP, job STAMP_PASS);
        else k1_helper<false>(lds, wid - 4, lane, xp, wqkv + grp * 640, nullptr, rot, wd, wo, ws + WS_WP, job STAMP_PASS);
        STAMP(3);
        STAMP_FLUSH(0);
        return;
    }
    __builtin_amdgcn_s_setprio(2);
    const int h = lane >> 5, l31 = lane & 31;
    f32x16 acc[10];
#pragma unroll
    for (int t = 0; t < 10; ++t) acc[t] = f32x16{};
    const LDS_AS f32x2* rowf = (const LDS_AS f32x2*)(lds + K1_STAT);
    const LDS_AS float* colf = (const LDS_AS float*)(lds + K1_STAT + 1280);
    if (ffn) {
        k1_compute<2>(acc, lds, wid, lane);
        STAMP(1);
        LGKM_BAR();
        STAMP(2);
        f32x4 cv[2][4];
#pragma unroll
        for (int c = 0; c < 2; ++c)
#pragma unroll
            for (int g = 0; g < 4; ++g) cv[c][g] = *(const LDS_AS f32x4*)(colf + (wid * 2 + c) * 32 + g * 8 + h * 4);
#pragma unroll
        for (int m = 0; m < 5; ++m) {
            const f32x2 rs = rowf[m * 32 + l31];
#pragma unroll
            for (int c = 0; c < 2; ++c) {
                const f32x16& a = acc[m * 2 + c];
                float hv[8];
#pragma unroll
                for (int r = 0; r < 8; ++r) hv[r] = fmaxf(fmaf(a[r], rs[0], rs[1] * cv[c][r >> 2][r & 3]), 0.f) * fmaf(a[r + 8], rs[0], rs[1] * cv[c][2 + (r >> 2)][r & 3]);
                *(u32x4*)(ws + WS_H16 + ((size_t)((tt0 + m) * 2 + (l31 >> 4)) * 48 + grp * 4 + wid) * 1024 + ((c * 2 + h) * 16 + (l31 & 15)) * 16) = u32x4{cvtpk(hv[0], hv[1]), cvtpk(hv[2], hv[3]), cvtpk(hv[4], hv[5]), cvtpk(hv[6], hv[7])};
            }
        }
    } else if (grp < 4) {
        k1_compute<0>(acc, lds, wid, lane);
        STAMP(1);
        LGKM_BAR();
        STAMP(2);
        const int wm = wid & 1, wn = wid >> 1;
        const bool isq = grp < 2;
        const int pos = isq ? l31 : ((l31 & 19) | ((l31 & 4) << 1) | ((l31 & 8) >> 1));
        const int head0 = (grp & 1) * 16 + wn * 8, tt = tt0 + wm * 2;
        char* f1 = isq ? ws + WS_QF1 + ((size_t)head0 * 64 + tt) * 1024 : ws + WS_KV + head0 * KV_HEAD + (size_t)tt * KV_TILE;
        char* f2 = isq ? ws + WS_QF2 + ((size_t)head0 * 64 + tt) * 256 : f1 + KV_K2;
        const size_t hs1 = isq ? (size_t)64 * 1024 : KV_HEAD, hs2 = isq ? (size_t)64 * 256 : KV_HEAD;
        const int ts1 = isq ? 1024 : KV_TILE, ts2 = isq ? 256 : KV_TILE;
        f1 += (pos + 32 * h) * 16; f2 += pos * 8 + h * hs2;
        f32x2 rs[2] = {rowf[wm * 64 + l31], rowf[wm * 64 + 32 + l31]};
        if (isq) { const float qs = 0.22360679774997896f * 1.4426950408889634f; rs[0] *= qs; rs[1] *= qs; }
#pragma unroll
        for (int t = 0; t < 5; ++t)
#pragma unroll
            for (int g = 0; g < 4; ++g) {
                const f32x4 cv = *(const LDS_AS f32x4*)(colf + wn * 160 + t * 32 + g * 8 + h * 4);
#pragma unroll
                for (int m = 0; m < 2; ++m)
#pragma unroll
                    for (int i = 0; i < 4; ++i) acc[t * 2 + m][g * 4 + i] = fmaf(acc[t * 2 + m][g * 4 + i], rs[m][0], rs[m][1] * cv[i]);
            }
#define Q4(gam) cvtpk(acc[((gam) >> 2) * 2 + m][((gam) & 3) * 4 + 0], acc[((gam) >> 2) * 2 + m][((gam) & 3) * 4 + 1]), cvtpk(acc[((gam) >> 2) * 2 + m][((gam) & 3) * 4 + 2], acc[((gam) >> 2) * 2 + m][((gam) & 3) * 4 + 3])
#pragma unroll
        for (int pp = 0; pp < 4; ++pp)
#pragma unroll
            for (int m = 0; m < 2; ++m) {
                *(u32x4*)(f1 + (2 * pp) * hs1 + m * ts1) = u32x4{Q4(5 * pp), Q4(5 * pp + 1)};
                *(u32x2*)(f2 + (2 * pp) * hs2 + m * ts2) = u32x2{Q4(5 * pp + 2)};
                *(u32x4*)(f1 + (2 * pp + 1) * hs1 + m * ts1) = u32x4{Q4(5 * pp + 3), Q4(5 * pp + 4)};
            }
#undef Q4
    } else {
        k1_compute<1>(acc, lds, wid, lane);
        STAMP(1);
        LGKM_BAR();
        STAMP(2);
        const int wm = wid & 1, wn = wid >> 1;
#pragma unroll
        for (int m = 0; m < 2; ++m) {
            const int tt = tt0 + wm * 2 + m;
            f32x2 rs[16];
#pragma unroll
            for (int g = 0; g < 4; ++g) {
                const LDS_AS f32x2* rp = rowf + wm * 64 + m * 32 + g * 8 + h * 4;
                const f32x4 a = *(const LDS_AS f32x4*)rp, b = *(const LDS_AS f32x4*)(rp + 2);
                rs[g * 4 + 0] = f32x2{a[0], a[1]}; rs[g * 4 + 1] = f32x2{a[2], a[3]}; rs[g * 4 + 2] = f32x2{b[0], b[1]}; rs[g * 4 + 3] = f32x2{b[2], b[3]};
            }
#pragma unroll
            for (int t = 0; t < 5; ++t) {
                const int col = wn * 160 + t * 32 + l31, hd = col / 20, d = col - hd * 20;
                const float cv = colf[col];
                float v[16];
#pragma unroll
                for (int r = 0; r < 16; ++r) v[r] = fmaf(acc[t * 2 + m][r], rs[r][0], rs[r][1] * cv);
                char* dst = ws + WS_KV + ((grp & 1) * 16 + hd) * KV_HEAD + (size_t)tt * KV_TILE + KV_V + h * KV_VHALF + d * 16;
                *(u32x4*)dst = u32x4{cvtpk(v[0], v[1]), cvtpk(v[2], v[3]), cvtpk(v[4], v[5]), cvtpk(v[6], v[7])};
                *(u32x4*)(dst + KV_VSUB) = u32x4{cvtpk(v[8], v[9]), cvtpk(v[10], v[11]), cvtpk(v[12], v[13]), cvtpk(v[14], v[15])};
            }
        }
        char* dst = ws + WS_KV + ((grp & 1) * 16 + wn * 8 + (l31 >> 2)) * KV_HEAD + (size_t)(tt0 + wm * 2 + h) * KV_TILE + KV_V + (lane & 3) * KV_VHALF + 20 * 16;
        *(u32x4*)dst = u32x4{0x3f803f80u, 0x3f803f80u, 0x3f803f80u, 0x3f803f80u};
    }
    STAMP(3); STAMP_FLUSH(0);
}

constexpr int K2_CHUNK = 8 * KV_TILE, K2_SLOTB = 24576, K2_SLOTS = 3, K2_KV0 = 81920, K2_LDS = K2_KV0 + K2_SLOTS * K2_SLOTB;
constexpr int K2_XCH = 2 * K2_SLOTB;

#define SMF32(a, b, c) __builtin_amdgcn_smfmac_f32_32x32x32_bf16((bfx8)(a), __builtin_bit_cast(bfx16, (b)), (c), 0xDDDD, 0, 0)
#define MFMA8(a, b, c) __builtin_amdgcn_mfma_f32_32x32x8bf16_1k((a), (b), (c), 0, 0, 0)
__device__ __forceinline__ f32x8 lo8(const f32x16& v) { return __builtin_shufflevector(v, v, 0, 1, 2, 3, 4, 5, 6, 7); }
__device__ __forceinline__ f32x8 hi8(const f32x16& v) { return __builtin_shufflevector(v, v, 8, 9, 10, 11, 12, 13, 14, 15); }
__device__ __forceinline__ float max16(const f32x16& s) {
    float m = fmaxf(fmaxf(s[0], s[1]), s[2]);
#pragma unroll
    for (int r = 3; r < 15; r += 2) m = fmaxf(fmaxf(m, s[r]), s[r + 1]);
    return swap32_max(fmaxf(m, s[15]));
}

#define MFMA16(a, b, c) __builtin_amdgcn_mfma_f32_16x16x32_bf16((bfx8)(a), (bfx8)(b), (c), 0, 0, 0)
typedef __attribute__((address_space(1))) unsigned gu32;
typedef __attribute__((address_space(1))) unsigned long long gu64;

struct K2B { lds_ptr r0, r1; const char* wsrc; unsigned wdst; int lane; };
__device__ __forceinline__ void k2_wpiece(const K2B& c, int st, int i) {
    glds16s(c.wsrc + st * 40960 + i * 8192, c.lane * 16, c.wdst + (st % 3) * 40960 + i * 8192);
}
template <int I, int D, int NP>
__device__ __forceinline__ void k2_bstep(const K2B& c, f32x4 (&acc)[10], const s16x8 (&w)[5], s16x8 (&n)[5], const s16x8& a0, const s16x8& a1) {
    constexpr int O = ((I + 1) % 6) * 20480;
#pragma unroll
    for (int i = 0; i < 10; ++i) {
        if (I < 16 && i < 5) n[i] = *(const LDS_AS s16x8*)((O < 61440 ? c.r0 : c.r1) + O % 61440 + i * 1024);
        acc[i] = MFMA16(w[i >> 1], (i & 1) ? a1 : a0, acc[i]);
        PIN(acc[i]);
        if (NP > 5 ? i < NP : ((i & 1) && (i >> 1) < NP)) { const int k = NP > 5 ? i : i >> 1; k2_wpiece(c, D + k / 5, k % 5); }
        SBAR();
    }
}

__global__ __launch_bounds__(512, 2) void k2_attn(const char* __restrict__ x, char* __restrict__ ws, char* __restrict__ out) {
    extern __shared__ __attribute__((aligned(16))) char smem[];
    lds_ptr lds = (lds_ptr)smem + K2_KV0;
    const unsigned lds0 = (unsigned)(uintptr_t)lds;
    const int tid = threadIdx.x, lane = tid & 63, wid = __builtin_amdgcn_readfirstlane(tid >> 6);
    STAMP_DECL; STAMP(0);
    const int tp = ((blockIdx.x & 7) >> 1) * 8 + ((blockIdx.x >> 3) & 7), cg = (blockIdx.x & 1) * 4 + (blockIdx.x >> 6);
    K2B kb;
    kb.wsrc = ws + WS_WP + (size_t)cg * 68 * 5120 + wid * 1024; kb.wdst = (unsigned)(uintptr_t)(lds_ptr)smem + wid * 1024; kb.lane = lane;
    const char* hsrc = ws + WS_H16 + ((size_t)(tp * 4 + (wid & 1) * 2) * 48 + (wid >> 1)) * 1024;
    s16x8 hf[24];
#pragma unroll
    for (int i = 0; i < 24; ++i) asm volatile("" : "=v"(hf[i]));
    const int bid = blockIdx.x, head = (bid & 7) * 4 + ((bid >> 3) & 3), par = wid >> 2, qt0 = (bid >> 5) * 8 + (wid & 3) * 2;
    const int h = lane >> 5, l31 = lane & 31;
    const char* kv = ws + WS_KV + head * KV_HEAD;
    const char* src = kv + wid * 1024;
    auto stage = [&](int slot) {
        const unsigned b = lds0 + slot * K2_SLOTB + wid * 1024;
        glds16s(src, lane * 16, b); glds16s(src + 8192, lane * 16, b + 8192); glds16s(src + 16384, lane * 16, b + 16384);
        src += K2_CHUNK;
    };
    stage(0);
    s16x8 q1[2]; s16x4 q2[2];
#pragma unroll
    for (int b = 0; b < 2; ++b) {
        q1[b] = *(const s16x8*)(ws + WS_QF1 + ((size_t)head * 64 + qt0 + b) * 1024 + lane * 16);
        q2[b] = *(const s16x4*)(ws + WS_QF2 + ((size_t)head * 64 + qt0 + b) * 256 + l31 * 8);
    }
    const int k1off = lane * 16, k2off = h ? KV_V + 20 * 16 : KV_K2 + l31 * 8;
    const int vd = l31 <= 20 ? l31 : (l31 < 28 ? 20 : 19), voff = KV_V + h * KV_VHALF + vd * 16;

    f32x16 oa[2] = {f32x16{}, f32x16{}}, sc[2];
    s16x8 k1[2], v0[2], v1[2]; s16x4 k2[2];
    lds_ptr pk1 = lds + k1off + par * KV_TILE, pk2 = lds + k2off + par * KV_TILE, pv = lds + voff + par * KV_TILE;
#define RD_K(t, j) do { k1[(t) & 1] = *(const LDS_AS s16x8*)(pk1 + (j) * 2 * KV_TILE); k2[(t) & 1] = *(const LDS_AS s16x4*)(pk2 + (j) * 2 * KV_TILE); } while (0)
#define RD_V(t, j) do { v0[(t) & 1] = *(const LDS_AS s16x8*)(pv + (j) * 2 * KV_TILE); v1[(t) & 1] = *(const LDS_AS s16x8*)(pv + (j) * 2 * KV_TILE + KV_VSUB); } while (0)
    WAIT_BAR(0);
    stage(1); stage(2);
    {
        const s16x8 ka = *(const LDS_AS s16x8*)(lds + k1off);
        const s16x4 kb = *(const LDS_AS s16x4*)(lds + k2off);
        RD_K(0, 0); RD_V(0, 0);
#pragma unroll
        for (int b = 0; b < 2; ++b) {
            if (h) q2[b] = s16x4{0, 0, 0, 0};
            f32x16 s = MFMA32(ka, q1[b], f32x16{});
            s = MFMA8(kb, q2[b], s);
            const float m = rbf(-max16(s));
            if (h) q2[b][0] = (short)(__float_as_uint(m) >> 16);
        }
        sc[0] = MFMA32(k1[0], q1[0], f32x16{});
        sc[0] = MFMA8(k2[0], q2[0], sc[0]);
        sc[1] = f32x16{}; v1[1] = s16x8{};
    }
    int slot = 0;
#define EXP4(S, i) do { S[i] = __builtin_amdgcn_exp2f(S[i]); S[i + 1] = __builtin_amdgcn_exp2f(S[i + 1]); S[i + 2] = __builtin_amdgcn_exp2f(S[i + 2]); S[i + 3] = __builtin_amdgcn_exp2f(S[i + 3]); PIN(S); SBAR(); } while (0)
    const char* sA = src; unsigned dA = 0; int nA = 0;
    const char* sB = kb.wsrc; unsigned dB = kb.wdst;
    const int hvo = k1off + 4096;
#define DMA_A(i) do { if (nA > (i)) glds16s(sA + (i) * 8192, k1off, dA + (i) * 8192); SBAR(); } while (0)
#define DMA_B() do { if (c < 5) { glds16s(sB, k1off, dB); sB += 8192; dB += 8192; } SBAR(); } while (0)
    for (int cc = 0; cc < 8; ++cc) {
        int c = cc;
        asm volatile("" : "+s"(c));
#pragma unroll
        for (int j = 0; j < 4; ++j) {
            if (j == 3) {
                WAIT_BAR(6);
                __builtin_amdgcn_s_setprio(2);
                if (c < 5) { sA = src; src += K2_CHUNK; dA = lds0 + slot * K2_SLOTB + wid * 1024; nA = 3; }
                else { sA = sB + (c - 6) * 24576; dA = dB + (c - 6) * 24576; nA = c == 5 ? 0 : 9 - c; }
                const int d = (slot == K2_SLOTS - 1) ? -(K2_SLOTS - 1) * K2_SLOTB : K2_SLOTB;
                slot = (slot == K2_SLOTS - 1) ? 0 : slot + 1;
                pk1 += d; pk2 += d; pv += d;
                SBAR();
            }
            RD_K(j + 1, (j + 1) & 3); SBAR();
            oa[1] = SMF32(v1[(j + 1) & 1], hi8(sc[1]), oa[1]); PIN(oa[1]); SBAR();
            EXP4(sc[0], 0);
            sc[1] = MFMA32(k1[j & 1], q1[1], f32x16{}); PIN(sc[1]); SBAR();
            if (j == 3) DMA_A(0);
            if (j == 0) DMA_A(2);
            if (j == 1) DMA_B();
            if (j == 2) {
                asm volatile("s_getpc_b64 vcc\n\ts_add_u32 vcc_lo, vcc_lo, %26\n\ts_addc_u32 vcc_hi, vcc_hi, 0\n\ts_setpc_b64 vcc\n\t"
                    "global_load_dwordx4 %0, %24, %25 offset:-4096\n\tglobal_load_dwordx4 %1, %24, %25\n\tglobal_load_dwordx4 %2, %24, %27\n\ts_branch .Lhf_end%=\n\t"
                    "global_load_dwordx4 %3, %24, %25 offset:-4096\n\tglobal_load_dwordx4 %4, %24, %25\n\tglobal_load_dwordx4 %5, %24, %27\n\ts_branch .Lhf_end%=\n\t"
                    "global_load_dwordx4 %6, %24, %25 offset:-4096\n\tglobal_load_dwordx4 %7, %24, %25\n\tglobal_load_dwordx4 %8, %24, %27\n\ts_branch .Lhf_end%=\n\t"
                    "global_load_dwordx4 %9, %24, %25 offset:-4096\n\tglobal_load_dwordx4 %10, %24, %25\n\tglobal_load_dwordx4 %11, %24, %27\n\ts_branch .Lhf_end%=\n\t"
                    "global_load_dwordx4 %12, %24, %25 offset:-4096\n\tglobal_load_dwordx4 %13, %24, %25\n\tglobal_load_dwordx4 %14, %24, %27\n\ts_branch .Lhf_end%=\n\t"
                    "global_load_dwordx4 %15, %24, %25 offset:-4096\n\tglobal_load_dwordx4 %16, %24, %25\n\tglobal_load_dwordx4 %17, %24, %27\n\ts_branch .Lhf_end%=\n\t"
                    "global_load_dwordx4 %18, %24, %25 offset:-4096\n\tglobal_load_dwordx4 %19, %24, %25\n\tglobal_load_dwordx4 %20, %24, %27\n\ts_branch .Lhf_end%=\n\t"
                    "global_load_dwordx4 %21, %24, %25 offset:-4096\n\tglobal_load_dwordx4 %22, %24, %25\n\tglobal_load_dwordx4 %23, %24, %27\n\ts_branch .Lhf_end%=\n\t"
                    ".Lhf_end%=:"
                    : "+v"(hf[0]), "+v"(hf[1]), "+v"(hf[2]), "+v"(hf[3]), "+v"(hf[4]), "+v"(hf[5]), "+v"(hf[6]), "+v"(hf[7]), "+v"(hf[8]), "+v"(hf[9]), "+v"(hf[10]), "+v"(hf[11]), "+v"(hf[12]), "+v"(hf[13]), "+v"(hf[14]), "+v"(hf[15]), "+v"(hf[16]), "+v"(hf[17]), "+v"(hf[18]), "+v"(hf[19]), "+v"(hf[20]), "+v"(hf[21]), "+v"(hf[22]), "+v"(hf[23])
                    : "v"(hvo), "s"(hsrc), "s"(c * 28 + 12), "s"(hsrc + 4096) : "vcc", "scc", "memory");
                hsrc += 3 * 4096;
                MEMFENCE(); SBAR();
            }
            EXP4(sc[0], 4);
            sc[1] = MFMA8(k2[j & 1], q2[1], sc[1]); PIN(sc[1]); SBAR();
            EXP4(sc[0], 8);
            oa[0] = SMF32(v0[j & 1], lo8(sc[0]), oa[0]); PIN(oa[0]); SBAR();
            EXP4(sc[0], 12);
            RD_V(j + 1, (j + 1) & 3);
            if (j == 1) __builtin_amdgcn_s_setprio(1);
            if (j == 2) __builtin_amdgcn_s_setprio(0);
            SBAR();
            oa[0] = SMF32(v1[j & 1], hi8(sc[0]), oa[0]); PIN(oa[0]); SBAR();
            EXP4(sc[1], 0);
            sc[0] = MFMA32(k1[(j + 1) & 1], q1[0], f32x16{}); PIN(sc[0]); SBAR();
            if (j == 3) DMA_A(1);
            if (j == 0) DMA_B();
            EXP4(sc[1], 4);
            sc[0] = MFMA8(k2[(j + 1) & 1], q2[0], sc[0]); PIN(sc[0]); SBAR();
            EXP4(sc[1], 8);
            oa[1] = SMF32(v0[j & 1], lo8(sc[1]), oa[1]); PIN(oa[1]); SBAR();
            EXP4(sc[1], 12);
        }
    }
    oa[1] = SMF32(v1[1], hi8(sc[1]), oa[1]);
    int ln = __builtin_amdgcn_mbcnt_hi(~0u, __builtin_amdgcn_mbcnt_lo(~0u, 0u));
    PIN(ln);
    const int h2 = ln >> 5, l2 = ln & 31;
    f32x16 o = par ? oa[1] : oa[0];
    {
        const f32x16 g = par ? oa[0] : oa[1];
        const lds_ptr xw = lds + K2_XCH + wid * 3072 + ln * 16, xr = lds + K2_XCH + (wid ^ 4) * 3072 + ln * 16;
#pragma unroll
        for (int i = 0; i < 3; ++i) *(LDS_AS f32x4*)(xw + i * 1024) = f32x4{g[4 * i], g[4 * i + 1], g[4 * i + 2], g[4 * i + 3]};
        WAIT_BAR(0);
#pragma unroll
        for (int i = 0; i < 3; ++i) { const f32x4 t = *(const LDS_AS f32x4*)(xr + i * 1024); o[4 * i] += t[0]; o[4 * i + 1] += t[1]; o[4 * i + 2] += t[2]; o[4 * i + 3] += t[3]; }
    }
    const int qt = qt0 + par;
    {
        float big = fabsf(o[0]);
#pragma unroll
        for (int r = 1; r < 9; r += 2) big = fabsf(o[r]) + fabsf(o[r + 1]) + big;
        const float big2 = big + fabsf(o[9]) + fabsf(o[10]) + fabsf(o[11]);
        const bool bad = h2 ? !(big < 1e30f && o[8] > 1e-30f) : !(big2 < 1e30f);
        if (__builtin_expect(__any(bad), 0)) {
            const s16x8 qf1 = par ? q1[1] : q1[0];
            s16x4 qf2 = par ? q2[1] : q2[0];
            if (h2) qf2[0] = 0;
            const int g1 = ln * 16, g2 = KV_K2 + l2 * 8, gv = KV_V + h2 * KV_VHALF + min(l2, 20) * 16;
            float m = -INFINITY;
            f32x16 negm;
#pragma unroll 1
            for (int t = 0; t < 64; ++t) {
                const char* g = kv + (size_t)t * KV_TILE;
                f32x16 s = MFMA32(*(const s16x8*)(g + g1), qf1, f32x16{});
                s = MFMA8(*(const s16x4*)(g + g2), qf2, s);
                m = fmaxf(m, max16(s));
            }
#pragma unroll
            for (int r = 0; r < 16; ++r) negm[r] = -m;
            o = f32x16{};
#pragma unroll 1
            for (int t = 0; t < 64; ++t) {
                const char* g = kv + (size_t)t * KV_TILE;
                f32x16 s = MFMA32(*(const s16x8*)(g + g1), qf1, negm);
                s = MFMA8(*(const s16x4*)(g + g2), qf2, s);
#pragma unroll
                for (int r = 0; r < 16; ++r) s[r] = __builtin_amdgcn_exp2f(s[r]);
                o = SMF32(*(const s16x8*)(g + gv), lo8(s), o);
                o = SMF32(*(const s16x8*)(g + gv + KV_VSUB), hi8(s), o);
            }
        }
    }
    auto rr = __builtin_amdgcn_permlane32_swap(__float_as_uint(o[8]), __float_as_uint(o[8]), false, false);
    const float rl = __builtin_amdgcn_rcpf(__uint_as_float(rr[1]));
    const __amdgpu_buffer_rsrc_t ors = __builtin_amdgcn_make_buffer_rsrc(ws + WS_O16, 0, 128 * 20 * 1024, 0x00020000);
    const int ob = (qt * 2 + (l2 >> 4)) * 20 * 1024 + (l2 & 15) * 16;
    __builtin_amdgcn_raw_buffer_store_b128(u32x4{cvtpk(o[0] * rl, o[1] * rl), cvtpk(o[2] * rl, o[3] * rl), cvtpk(o[4] * rl, o[5] * rl), cvtpk(o[6] * rl, o[7] * rl)},
                                           ors, ob + (head >> 1) * 1024 + ((head & 1) * 2 + h2) * 256, 0, 16);
    if (!h2) __builtin_amdgcn_raw_buffer_store_b64(u32x2{cvtpk(o[8] * rl, o[9] * rl), cvtpk(o[10] * rl, o[11] * rl)},
                                                   ors, ob + (16 + (head >> 3)) * 1024 + ((head >> 1) & 3) * 256 + (head & 1) * 8, 0, 16);
    STAMP(3);
    __builtin_amdgcn_s_setprio(0);
    const int mt = wid & 1, kq = wid >> 1;
    kb.lane = ln; kb.r0 = (lds_ptr)smem + kq * 5120 + ln * 16; kb.r1 = kb.r0 + 61440;
    asm volatile("" : "+v"(kb.r1));
    const int ch0 = wid * 128 + ln, ch1 = ch0 + 64, er0 = ch0 / 10, er1 = ch1 / 10, ec0 = ch0 - er0 * 10, ec1 = ch1 - er1 * 10;
    const size_t eb = (size_t)tp * 64 * (D_ * 2) + cg * 160;
    const unsigned eo0 = er0 * (D_ * 2) + ec0 * 16, eo1 = er1 * (D_ * 2) + ec1 * 16;
    u32x4 xv0 = u32x4{}, xv1 = u32x4{};
    if (wid < 5) { xv0 = *(const u32x4*)(x + eb + eo0); xv1 = *(const u32x4*)(x + eb + eo1); }
    f32x4 acc[10];
#pragma unroll
    for (int i = 0; i < 10; ++i) acc[i] = f32x4{};
    gu32* cnt = (gu32*)(ws + WS_CNT);
    s16x8 wa[5], wb[5];
#pragma unroll
    for (int i = 0; i < 5; ++i) wa[i] = *(const LDS_AS s16x8*)(kb.r0 + i * 1024);
    k2_bstep<0, 0, 0>(kb, acc, wa, wb, hf[0], hf[12]);
    k2_bstep<1, 0, 0>(kb, acc, wb, wa, hf[1], hf[13]);
    k2_bstep<2, 0, 0>(kb, acc, wa, wb, hf[2], hf[14]);
    STAMP(1);
    asm volatile("s_waitcnt vmcnt(0)" ::: "memory");
    LGKM_BAR();
    STAMP(2);
    if (tid == 0) __hip_atomic_fetch_add(cnt + (blockIdx.x >> 5) * 32, 1u, __ATOMIC_RELAXED, __HIP_MEMORY_SCOPE_AGENT);
    k2_bstep<3, 3, 5>(kb, acc, wb, wa, hf[3], hf[15]);
    k2_bstep<4, 4, 5>(kb, acc, wa, wb, hf[4], hf[16]);
    WAIT_BAR(5);
    k2_bstep<5, 5, 5>(kb, acc, wb, wa, hf[5], hf[17]);
    k2_bstep<6, 0, 0>(kb, acc, wa, wb, hf[6], hf[18]);
    WAIT_BAR(5);
    k2_bstep<7, 6, 5>(kb, acc, wb, wa, hf[7], hf[19]);
    unsigned seen = 0;
    const char* cw = ws + WS_CNT + (tp >> 2) * 128;
    if (wid == 0) asm volatile("global_load_dword %0, %1, %2 sc1" : "+v"(seen) : "v"(0u), "s"(cw) : "memory");
    SBAR();
    k2_bstep<8, 0, 0>(kb, acc, wa, wb, hf[8], hf[20]);
    STAMP(6);
    if (wid == 0) {
        asm volatile("s_waitcnt vmcnt(0)" : "+v"(seen) :: "memory");
#pragma unroll 1
        for (unsigned spin = 0; spin < (1u << 22) && __builtin_amdgcn_readfirstlane(seen) < 32u; ++spin) {
            __builtin_amdgcn_s_sleep(2);
            asm volatile("global_load_dword %0, %1, %2 sc1\n\ts_waitcnt vmcnt(0)" : "=&v"(seen) : "v"(0u), "s"(cw) : "memory");
        }
    }
    WAIT_BAR(5);
    STAMP(4);
    s16x8 of[10];
#pragma unroll
    for (int i = 0; i < 10; ++i)
        of[i] = __builtin_bit_cast(s16x8, __builtin_amdgcn_raw_buffer_load_b128(ors, ln * 16, ((tp * 4 + mt * 2 + (i & 1)) * 20 + (i >> 1) * 4 + kq) * 1024, 16));
    MEMFENCE(); SBAR();
    k2_bstep<9, 7, 5>(kb, acc, wb, wa, hf[9], hf[21]);
    k2_bstep<10, 0, 0>(kb, acc, wa, wb, hf[10], hf[22]);
    WAIT_BAR(15);
    k2_bstep<11, 8, 3>(kb, acc, wb, wa, hf[11], hf[23]);
    STAMP(7);
    k2_bstep<12, 0, 0>(kb, acc, wa, wb, of[0], of[1]);
    WAIT_BAR(3);
    k2_bstep<13, 0, 0>(kb, acc, wb, wa, of[2], of[3]);
    k2_bstep<14, 0, 0>(kb, acc, wa, wb, of[4], of[5]);
    WAIT_BAR(0);
    k2_bstep<15, 0, 0>(kb, acc, wb, wa, of[6], of[7]);
    k2_bstep<16, 0, 0>(kb, acc, wa, wb, of[8], of[9]);
    {
        const int tk = ln & 15;
        const lds_ptr pw = (lds_ptr)smem + kq * 20480 + (mt * 32 + tk) * 320 + (((ln >> 4) ^ ((tk >> 1) & 3)) << 4);
#pragma unroll
        for (int i = 0; i < 10; ++i) *(LDS_AS f32x4*)(pw + (i & 1) * 16 * 320 + (i >> 1) * 64) = acc[i];
        LGKM_BAR();
        if (wid < 5) {
#pragma unroll
            for (int e = 0; e < 2; ++e) {
                const int er = e ? er1 : er0, ec = e ? ec1 : ec0, sw = (er >> 1) & 3;
                const lds_ptr p0 = (lds_ptr)smem + er * 320 + (((2 * ec) ^ sw) << 4), p1 = (lds_ptr)smem + er * 320 + (((2 * ec + 1) ^ sw) << 4);
                f32x4 a = *(const LDS_AS f32x4*)p0, b = *(const LDS_AS f32x4*)p1;
#pragma unroll
                for (int k = 1; k < 4; ++k) { a += *(const LDS_AS f32x4*)(p0 + k * 20480); b += *(const LDS_AS f32x4*)(p1 + k * 20480); }
                const u32x4 xv = e ? xv1 : xv0;
                *(u32x4*)(out + eb + (e ? eo1 : eo0)) = u32x4{cvtpk(a[0] + bf_lo(xv[0]), a[1] + bf_hi(xv[0])), cvtpk(a[2] + bf_lo(xv[1]), a[3] + bf_hi(xv[1])),
                                                              cvtpk(b[0] + bf_lo(xv[2]), b[1] + bf_hi(xv[2])), cvtpk(b[2] + bf_lo(xv[3]), b[3] + bf_hi(xv[3]))};
            }
        }
    }
    STAMP(5); STAMP_FLUSH(1);
}

extern "C" void kernel_launch(void* const* d_in, const int* in_sizes, int n_in, void* d_out, int out_size, void* d_ws, size_t ws_size,
                              hipStream_t stream) {
    const char* x = (const char*)d_in[0];
    const char* wqkv = (const char*)d_in[1];
    const char* wo = (const char*)d_in[2];
    const char* wg = (const char*)d_in[4];
    const char* wu = (const char*)d_in[5];
    const char* wd = (const char*)d_in[6];
    char* ws = (char*)d_ws;
    (void)hipFuncSetAttribute((const void*)k1_proj, hipFuncAttributeMaxDynamicSharedMemorySize, K1_LDS);
    (void)hipFuncSetAttribute((const void*)k2_attn, hipFuncAttributeMaxDynamicSharedMemorySize, K2_LDS);
    k1_proj<<<256, 512, K1_LDS, stream>>>(x, wqkv, wg, wu, wd, wo, ws);
    k2_attn<<<256, 512, K2_LDS, stream>>>(x, ws, (char*)d_out);
}
```

```cpp
#include <hip/hip_runtime.h>
#include <hip/hip_bf16.h>
#include <stdint.h>

typedef __bf16 bfx4 __attribute__((ext_vector_type(4)));
typedef __bf16 bfx8 __attribute__((ext_vector_type(8)));
typedef __bf16 bfx16 __attribute__((ext_vector_type(16)));
typedef short s16x4 __attribute__((ext_vector_type(4)));
typedef short s16x8 __attribute__((ext_vector_type(8)));
typedef short s16x16 __attribute__((ext_vector_type(16)));
typedef float f32x2 __attribute__((ext_vector_type(2)));
typedef float f32x4 __attribute__((ext_vector_type(4)));
typedef float f32x8 __attribute__((ext_vector_type(8)));
typedef float f32x16 __attribute__((ext_vector_type(16)));
typedef unsigned u32x2 __attribute__((ext_vector_type(2)));
typedef unsigned u32x4 __attribute__((ext_vector_type(4)));
typedef unsigned u32x8 __attribute__((ext_vector_type(8)));
#define LDS_AS __attribute__((address_space(3)))
typedef LDS_AS char* lds_ptr;

#define ABL 0
#define DPRIO 1
#define WARM 0
#define K1_ROT 0
constexpr int S_ = 2048, D_ = 640, H_ = 32, F_ = 1536;

constexpr int Q_TILE = 1280, Q_HEAD = 64 * Q_TILE, KV_TILE = 2816, KV_V = 1280, KV_HEAD = 64 * KV_TILE;
constexpr int WS_Q = 0;
constexpr int WS_KV = WS_Q + H_ * Q_HEAD;
constexpr int WS_O16 = WS_KV + H_ * KV_HEAD + 8192;
constexpr int WS_H16 = WS_O16 + 128 * 20 * 1024;
constexpr int WS_WP = WS_H16 + 128 * 48 * 1024;
constexpr int WS_EPOCH = WS_WP + 8 * 68 * 5120 + 8192;
constexpr int WS_FA = WS_EPOCH + 256, WS_FB = WS_FA + 2048, WS_FC = WS_FB + 2048, WS_END = WS_FC + 2048;

#define PSTAMP(i) do {} while (0)
#define PFLUSH() do {} while (0)
#define STAMP_ARG
#define STAMP_PASS
#define STAMP_DECL do {} while (0)
#define STAMP(i) do {} while (0)
#define STAMP_FLUSH(kern) do {} while (0)
#define SBAR() __builtin_amdgcn_sched_barrier(0)
#define MFMA32(a, b, c) __builtin_amdgcn_mfma_f32_32x32x16_bf16((bfx8)(a), (bfx8)(b), (c), 0, 0, 0)

__device__ __forceinline__ unsigned cvtpk(float lo, float hi) {
    typedef __bf16 bfx2 __attribute__((ext_vector_type(2)));
    bfx2 r = {(__bf16)lo, (__bf16)hi};
    return __builtin_bit_cast(unsigned, r);
}
__device__ __forceinline__ void glds16(const void* g, unsigned lds_base) {
    unsigned sv;
    asm volatile("s_mov_b32 %0, m0\n\ts_mov_b32 m0, %2\n\ts_nop 0\n\tglobal_load_lds_dwordx4 %1, off\n\ts_mov_b32 m0, %0"
                 : "=&s"(sv) : "v"(g), "s"(lds_base) : "memory");
}
__device__ __forceinline__ s16x4 trread(const LDS_AS char* p) {
    return __builtin_amdgcn_ds_read_tr16_b64_v4i16((LDS_AS s16x4*)p);
}
__device__ __forceinline__ float rbf(float v) { return (float)(__bf16)v; }
__device__ __forceinline__ float bf_lo(unsigned u) { return __uint_as_float(u << 16); }
__device__ __forceinline__ float bf_hi(unsigned u) { return __uint_as_float(u & 0xffff0000u); }
__device__ __forceinline__ float swap32_add(float v) {
    auto r = __builtin_amdgcn_permlane32_swap(__float_as_uint(v), __float_as_uint(v), false, false);
    return __uint_as_float(r[0]) + __uint_as_float(r[1]);
}
__device__ __forceinline__ float swap32_max(float v) {
    auto r = __builtin_amdgcn_permlane32_swap(__float_as_uint(v), __float_as_uint(v), false, false);
    return fmaxf(__uint_as_float(r[0]), __uint_as_float(r[1]));
}

constexpr int P1_WSLOT = 24576, P1_X0 = 3 * P1_WSLOT, P1_XSLOT = 16384, P1_STAT = P1_X0 + 5 * P1_XSLOT;
constexpr int P1_COLA = P1_STAT + 1024, P1_COLB = P1_COLA + 512, LDS_JUNK = 161792, LDS_BYTES = 163840;
constexpr int K2_CHUNK = 8 * KV_TILE, K2_SLOTB = 24576, K2_SLOTS = 3, K2_KV0 = 81920;
typedef __attribute__((address_space(1))) unsigned long long gu64;

#define PIN(x) asm volatile("" : "+v"(x))
#define MEMFENCE() asm volatile("" ::: "memory")
#define WAIT_BAR(N) do { MEMFENCE(); __builtin_amdgcn_s_waitcnt(0x70 | ((N) & 15) | (((N) >> 4) << 14)); __builtin_amdgcn_s_barrier(); MEMFENCE(); SBAR(); } while (0)
#define LGKM_BAR() do { MEMFENCE(); __builtin_amdgcn_s_waitcnt(0xC07F); __builtin_amdgcn_s_barrier(); MEMFENCE(); SBAR(); } while (0)
#define MFMA16(a, b, c) __builtin_amdgcn_mfma_f32_16x16x32_bf16((bfx8)(a), (bfx8)(b), (c), 0, 0, 0)
#define SMF16(a, b, c, i) __builtin_amdgcn_smfmac_f32_16x16x64_bf16((bfx8)(a), (bfx16)(b), (c), (i), 0, 0)
#define CAT8(a, b) __builtin_shufflevector(a, b, 0, 1, 2, 3, 4, 5, 6, 7)
#define CAT16(a, b) __builtin_shufflevector(a, b, 0, 1, 2, 3, 4, 5, 6, 7, 8, 9, 10, 11, 12, 13, 14, 15)

__device__ __forceinline__ void glds16s(const char* sbase, unsigned voff, unsigned lds_base) {
    unsigned sv;
    asm volatile("s_mov_b32 %0, m0\n\ts_mov_b32 m0, %3\n\ts_nop 0\n\tglobal_load_lds_dwordx4 %1, %2\n\ts_mov_b32 m0, %0"
                 : "=&s"(sv) : "v"(voff), "s"(sbase), "s"(lds_base) : "memory");
}

__device__ __forceinline__ void glds16c(const char* sbase, unsigned voff, unsigned lds_base) {
    unsigned sv;
    asm volatile("s_mov_b32 %0, m0\n\ts_mov_b32 m0, %3\n\ts_nop 0\n\tglobal_load_lds_dwordx4 %1, %2 sc1\n\ts_mov_b32 m0, %0"
                 : "=&s"(sv) : "v"(voff), "s"(sbase), "s"(lds_base) : "memory");
}
__device__ __forceinline__ void warm1(const char* p, unsigned junk) {
    unsigned sv;
    asm volatile("s_mov_b32 %0, m0\n\ts_mov_b32 m0, %2\n\ts_nop 0\n\tglobal_load_lds_dword %1, off\n\ts_mov_b32 m0, %0" : "=&s"(sv) : "v"(p), "s"(junk) : "memory");
}
typedef __amdgpu_buffer_rsrc_t rsrc_t;
#define ST16(rs, v, voff, soff) __builtin_amdgcn_raw_buffer_store_b128((v), (rs), (voff) + (soff), 0, 16)
#define ST8(rs, v, voff, soff) __builtin_amdgcn_raw_buffer_store_b64((v), (rs), (voff), (soff), 16)
#define LD16(rs, voff, soff) __builtin_bit_cast(s16x8, __builtin_amdgcn_raw_buffer_load_b128((rs), (voff), (soff), 16))
#define LD8(rs, voff, soff) __builtin_bit_cast(s16x4, __builtin_amdgcn_raw_buffer_load_b64((rs), (voff), (soff), 16))
__device__ __forceinline__ f32x2 pk(const f32x16& a, int i) { return f32x2{a[i], a[i + 1]}; }
__device__ __forceinline__ f32x2 pk4(const f32x4& a, int i) { return f32x2{a[i], a[i + 1]}; }
__device__ __forceinline__ f32x2 pfma(f32x2 a, f32x2 b, f32x2 c) { return __builtin_elementwise_fma(a, b, c); }
__device__ __forceinline__ unsigned cvt2(f32x2 v) { return cvtpk(v[0], v[1]); }

__device__ __forceinline__ void raise_flag(char* ws, int arr, unsigned long long tag) {
    __hip_atomic_store((gu64*)(ws + arr) + blockIdx.x, tag, __ATOMIC_RELAXED, __HIP_MEMORY_SCOPE_AGENT);
}
struct Poll { u32x4 a, b; };
__device__ __forceinline__ void poll_request(Poll& p, const char* arr, unsigned voff) {
    asm volatile("global_load_dwordx4 %0, %2, %3 sc1\n\tglobal_load_dwordx4 %1, %2, %3 offset:1024 sc1" : "+v"(p.a), "+v"(p.b) : "v"(voff), "s"(arr) : "memory");
}
__device__ __forceinline__ void poll_wait(Poll& p, const char* arr, unsigned voff, unsigned long long tag) {
    const unsigned tl = (unsigned)tag, th = (unsigned)(tag >> 32);
#pragma unroll 1
    for (unsigned spin = 0; spin < (1u << 18); ++spin) {
        asm volatile("s_waitcnt vmcnt(0)" : "+v"(p.a), "+v"(p.b) :: "memory");
        if (__all(p.a[0] == tl && p.a[1] == th && p.a[2] == tl && p.a[3] == th && p.b[0] == tl && p.b[1] == th && p.b[2] == tl && p.b[3] == th)) break;
        __builtin_amdgcn_s_sleep(1);
        poll_request(p, arr, voff);
    }
    asm volatile("s_waitcnt vmcnt(0)" : "+v"(p.a), "+v"(p.b) :: "memory");
}

template <int MODE> struct P1F { s16x8 x[2]; s16x4 w[MODE == 2 ? 3 : 2][2]; };

template <int MODE>
__device__ __forceinline__ void p1_read(P1F<MODE>& n, lds_ptr xa, lds_ptr w0, lds_ptr w1, int wimm, int i) {
    constexpr int TS = MODE == 2 ? 256 : 512;
    if (i == 0) { n.x[0] = *(const LDS_AS s16x8*)xa; n.w[0][0] = trread(w0 + wimm); n.w[0][1] = trread(w0 + wimm + TS); }
    if (i == 1) { n.x[1] = *(const LDS_AS s16x8*)(xa + 4096); n.w[1][0] = trread(w1 + wimm); n.w[1][1] = trread(w1 + wimm + TS); }
    if (i == 2 && MODE == 2) { n.w[MODE == 2 ? 2 : 0][0] = trread(w0 + wimm + 1024); n.w[MODE == 2 ? 2 : 0][1] = trread(w0 + wimm + 1024 + TS); }
}
template <int MODE, int NR>
__device__ __forceinline__ void p1_step(const P1F<MODE>& f, f32x16 (&acc)[MODE == 2 ? 6 : 4], P1F<MODE>& n0, lds_ptr xa0, int wi0, P1F<MODE>& n1, lds_ptr xa1, int wi1,
                                        lds_ptr w0, lds_ptr w1) {
    constexpr int NM = MODE == 2 ? 6 : 4;
#pragma unroll
    for (int g = 0; g < NM; ++g) {
        const s16x8 w = CAT8(f.w[g >> 1][0], f.w[g >> 1][1]);
#if !(ABL & 1)
        acc[g] = MODE == 1 ? MFMA32(f.x[g & 1], w, acc[g]) : MFMA32(w, f.x[g & 1], acc[g]);
#else
        { s16x8 x_ = f.x[g & 1]; asm volatile("" :: "v"(w), "v"(x_)); }
#endif
        PIN(acc[g]); SBAR();
        if (NR >= 1 && g < NM / 2) p1_read<MODE>(n0, xa0, w0, w1, wi0, g);
        if (NR >= 2 && g >= NM / 2) p1_read<MODE>(n1, xa1, w0, w1, wi1, g - NM / 2);
        SBAR();
    }
}
struct P1X { lds_ptr a0, a1, a2, a3; };
template <int MODE>
__device__ __forceinline__ void p1_first(P1F<MODE> (&f)[4], const P1X& xa, lds_ptr w0, lds_ptr w1) {
    constexpr int KS = MODE == 2 ? 6144 : 4096;
#pragma unroll
    for (int i = 0; i < 3; ++i) p1_read<MODE>(f[0], xa.a0, w0, w1, 0, i);
#pragma unroll
    for (int i = 0; i < 3; ++i) p1_read<MODE>(f[1], xa.a1, w0, w1, KS, i);
#pragma unroll
    for (int i = 0; i < 3; ++i) p1_read<MODE>(f[2], xa.a2, w0, w1, 2 * KS, i);
    SBAR();
}
template <int MODE>
__device__ __forceinline__ void p1_lo(P1F<MODE> (&f)[4], f32x16 (&acc)[MODE == 2 ? 6 : 4], const P1X& xa, lds_ptr w0, lds_ptr w1) {
    constexpr int KS = MODE == 2 ? 6144 : 4096;
    p1_step<MODE, 1>(f[0], acc, f[3], xa.a3, 3 * KS, f[3], xa.a3, 0, w0, w1);
    p1_step<MODE, 0>(f[1], acc, f[3], xa.a3, 0, f[3], xa.a3, 0, w0, w1);
}
template <int MODE>
__device__ __forceinline__ void p1_hi(P1F<MODE> (&f)[4], f32x16 (&acc)[MODE == 2 ? 6 : 4], const P1X& xa, lds_ptr w0, lds_ptr w1) {
    constexpr int KS = MODE == 2 ? 6144 : 4096;
    p1_step<MODE, 2>(f[2], acc, f[0], xa.a0, 0, f[1], xa.a1, KS, w0, w1);
    p1_step<MODE, 1>(f[3], acc, f[2], xa.a2, 2 * KS, f[2], xa.a2, 0, w0, w1);
}
__device__ __forceinline__ P1X p1_xaddr(lds_ptr lds, int wid, int lane) {
    const int xrow = (wid & 1) * 64 + (lane & 31), xo = P1_X0 + xrow * 128 + (((lane >> 5) ^ ((xrow >> 1) & 7)) << 4);
    return P1X{lds + xo, lds + (xo ^ 32), lds + (xo ^ 64), lds + (xo ^ 96)};
}
template <int MODE>
__device__ __forceinline__ void p1_five(P1F<MODE> (&f)[4], f32x16 (&acc)[MODE == 2 ? 6 : 4], P1X& xa, lds_ptr& w0, lds_ptr& w1, int& ws3, int ps) {
#pragma unroll 1
    for (int n = 0; n < 5; ++n) {
        const int dw = ws3 == 2 ? -2 * P1_WSLOT : P1_WSLOT, dx = n == 4 ? -4 * P1_XSLOT : P1_XSLOT;
        ws3 = ws3 == 2 ? 0 : ws3 + 1;
        p1_lo<MODE>(f, acc, xa, w0, w1);
        w0 += dw; w1 += dw; xa.a0 += dx; xa.a1 += dx; xa.a2 += dx; xa.a3 += dx;
        PSTAMP(ps + 2 * n);
        LGKM_BAR();
        PSTAMP(ps + 2 * n + 1);
        p1_hi<MODE>(f, acc, xa, w0, w1);
    }
}

struct Pack { const char *wd, *wo; int job; const char* warm; };
__device__ __forceinline__ void pack_fetch(const Pack& pk_, lds_ptr lds, int wid, int lane) {
    const int step = pk_.job / 10, cb = pk_.job - step * 10, r = lane >> 3;
    const unsigned va = ((r & 3) + 8 * (r >> 2)) * (D_ * 2) + (lane & 7) * 16, vb = ((r >> 2) * 20 + 16 + (r & 3)) * (D_ * 2) + (lane & 7) * 16;
#pragma unroll
    for (int g = 0; g < 4; ++g) {
        const int ks = (step - 48) * 2 + (g >> 1), hh = g & 1;
        const char* a = step < 48 ? pk_.wd + (step * 32 + (g >> 1) * 16 + hh * 4) * (D_ * 2) : ks < 32 ? pk_.wo + (ks * 20 + hh * 4) * (D_ * 2) : pk_.wo + ((ks - 32) * 4 + hh * 2) * 20 * (D_ * 2);
        glds16s(a + cb * 128, step < 64 ? va : vb, (unsigned)(uintptr_t)lds + wid * P1_WSLOT + 16384 + g * 1024);
    }
}
__device__ __forceinline__ void pack_read(s16x8 (&pf)[4], lds_ptr lds, int wid, int lane) {
    asm volatile("s_waitcnt vmcnt(0)" ::: "memory");
    const lds_ptr pr = lds + wid * P1_WSLOT + 16384 + (lane >> 4) * 1024 + ((lane >> 2) & 3) * 128 + (lane & 3) * 8;
#pragma unroll
    for (int nt = 0; nt < 4; ++nt) pf[nt] = CAT8(trread(pr + nt * 32), trread(pr + nt * 32 + 512));
}
__device__ __forceinline__ void pack_store(const Pack& pk_, const s16x8 (&pf)[4], int lane, rsrc_t wsr) {
    const int step = pk_.job / 10, cb = pk_.job - step * 10;
#pragma unroll
    for (int nt = 0; nt < 4; ++nt) {
        const int N = cb * 4 + nt, cg = N / 5;
        ST16(wsr, __builtin_bit_cast(u32x4, pf[nt]), lane * 16, WS_WP + ((cg * 68 + step) * 5 + N - cg * 5) * 1024);
    }
}

template <int MODE>
__device__ __forceinline__ void p1a_compute(f32x16 (&acc)[4], lds_ptr lds, int wid, int lane, const Pack& pk_, s16x8 (&pf)[4]) {
    const int h = lane >> 5, blk = (lane >> 4) & 1, q = (lane >> 2) & 3, fsw = 3 * (q >> 1);
    P1X xa = p1_xaddr(lds, wid, lane);
    const int wb = h * 2048 + (wid >> 1) * 1024 + q * 128 + (lane & 3) * 8;
    lds_ptr w0 = lds + wb + ((blk ^ fsw) << 5), w1 = lds + wb + (((2 + blk) ^ fsw) << 5);
    P1F<MODE> f[4];
    int ws3 = 0;
    PSTAMP(0);
    LGKM_BAR();
    PSTAMP(1);
    p1_first<MODE>(f, xa, w0, w1);
#pragma unroll 1
    for (int hf_ = 0; hf_ < 2; ++hf_) {
        p1_five<MODE>(f, acc, xa, w0, w1, ws3, 2 + 10 * hf_);
        if (hf_ == 0) {
            if (pk_.job < 680) pack_read(pf, lds, wid, lane);
        }
    }
}

__device__ __forceinline__ void p1_compute(lds_ptr lds, int wid, int lane, int panel, int grp, rsrc_t wsr, const Pack& pk_ STAMP_ARG) {
    const int h = lane >> 5, l31 = lane & 31, wm = wid & 1, wn = wid >> 1, tt = panel * 4 + wm * 2;
    const LDS_AS f32x2* rowf = (const LDS_AS f32x2*)(lds + P1_STAT);
    if (WARM && wid < 3) warm1(pk_.warm, (unsigned)(uintptr_t)lds + LDS_JUNK + wid * 256);
    if (pk_.job < 680) { __builtin_amdgcn_s_sleep(6); pack_fetch(pk_, lds, wid, lane); }
    {
        f32x16 acc[4] = {f32x16{}, f32x16{}, f32x16{}, f32x16{}};
        s16x8 pf[4] = {s16x8{}, s16x8{}, s16x8{}, s16x8{}};
        const LDS_AS float* colf = (const LDS_AS float*)(lds + P1_COLA) + wn * 64;
        const int sec = grp / 5, q0 = (grp - sec * 5) * 32 + wn * 16;
        if (sec < 2) {
            p1a_compute<0>(acc, lds, wid, lane, pk_, pf);
            STAMP(1);
            const int pos = sec ? ((l31 & 19) | ((l31 & 4) << 1) | ((l31 & 8) >> 1)) : l31;
            const int hs = sec ? KV_HEAD : Q_HEAD, ts = sec ? KV_TILE : Q_TILE, so = (sec ? WS_KV : WS_Q) + tt * ts;
            const float qs = sec ? 1.0f : 0.22360679774997896f * 1.4426950408889634f;
            f32x2 nm[2], rr[2];
#pragma unroll
            for (int m = 0; m < 2; ++m) { const f32x2 rs = rowf[wm * 64 + m * 32 + l31]; nm[m] = f32x2{rs[1], rs[1]}; rr[m] = f32x2{rs[0] * qs, rs[0] * qs}; }
            const bool odd = lane & 1;
#pragma unroll
            for (int gam = 0; gam < 8; gam += 2) {
                const int qd = q0 + 2 * (gam + (lane & 1)) + h, hd = (qd * 205) >> 10, vo = hd * hs + (qd - hd * 5) * 256 + (pos & ~1) * 8;
                const f32x4 ca = *(const LDS_AS f32x4*)(colf + gam * 8 + h * 4), cb = *(const LDS_AS f32x4*)(colf + gam * 8 + 8 + h * 4);
#pragma unroll
                for (int m = 0; m < 2; ++m) {
                    const f32x16& a = acc[(gam >> 2) * 2 + m];
                    const int r0 = (gam & 3) * 4;
                    const unsigned a0 = cvt2(pfma(pk4(ca, 0), nm[m], pk(a, r0)) * rr[m]), a1 = cvt2(pfma(pk4(ca, 2), nm[m], pk(a, r0 + 2)) * rr[m]);
                    const unsigned b0 = cvt2(pfma(pk4(cb, 0), nm[m], pk(a, r0 + 4)) * rr[m]), b1 = cvt2(pfma(pk4(cb, 2), nm[m], pk(a, r0 + 6)) * rr[m]);
                    const unsigned g0 = __builtin_amdgcn_update_dpp(0, odd ? a0 : b0, 0xB1, 0xF, 0xF, true), g1 = __builtin_amdgcn_update_dpp(0, odd ? a1 : b1, 0xB1, 0xF, 0xF, true);
                    ST16(wsr, (odd ? u32x4{g0, g1, b0, b1} : u32x4{a0, a1, g0, g1}), vo, so + m * ts);
                }
            }
        } else {
            p1a_compute<1>(acc, lds, wid, lane, pk_, pf);
            STAMP(1);
            if (sec == 2) {
                const int so = WS_KV + tt * KV_TILE + KV_V;
#pragma unroll
                for (int m = 0; m < 2; ++m) {
                    f32x2 rr[8], nm[8];
#pragma unroll
                    for (int g = 0; g < 4; ++g) {
                        const LDS_AS f32x2* rp = rowf + wm * 64 + m * 32 + g * 8 + h * 4;
                        const f32x4 a = *(const LDS_AS f32x4*)rp, b = *(const LDS_AS f32x4*)(rp + 2);
                        rr[2 * g] = f32x2{a[0], a[2]}; nm[2 * g] = f32x2{a[1], a[3]}; rr[2 * g + 1] = f32x2{b[0], b[2]}; nm[2 * g + 1] = f32x2{b[1], b[3]};
                    }
#pragma unroll
                    for (int t = 0; t < 2; ++t) {
                        const int qd = q0 + t * 8 + (l31 >> 2), hd = (qd * 205) >> 10, pl = qd - hd * 5;
                        const int vo = hd * KV_HEAD + pl * 256 + ((h ^ (pl & 3)) << 6) + (l31 & 3) * 16;
                        const float c1 = colf[t * 32 + l31];
                        const f32x2 c2 = {c1, c1};
                        unsigned w[8];
#pragma unroll
                        for (int i = 0; i < 8; ++i) w[i] = cvt2(pfma(nm[i], c2, pk(acc[t * 2 + m], 2 * i)) * rr[i]);
                        ST16(wsr, (u32x4{w[0], w[1], w[2], w[3]}), vo, so + m * KV_TILE);
                        ST16(wsr, (u32x4{w[4], w[5], w[6], w[7]}), vo ^ 128, so + m * KV_TILE);
                    }
                }
                const int hd = q0 / 5 + (lane >> 4);
                const unsigned ov = (lane & 3) ? 0u : 0x3f803f80u;
                if (hd * 5 + 4 < q0 + 16) {
                    ST16(wsr, (u32x4{ov, ov, ov, ov}), hd * KV_HEAD + 5 * 256 + (lane & 15) * 16, so);
                    ST16(wsr, (u32x4{ov, ov, ov, ov}), hd * KV_HEAD + 5 * 256 + (lane & 15) * 16, so + KV_TILE);
                }
            }
        }
        if (pk_.job < 680) pack_store(pk_, pf, lane, wsr);
    }
    STAMP(2);
    f32x16 acc[6];
#pragma unroll
    for (int t = 0; t < 6; ++t) acc[t] = f32x16{};
    {
        const int blk = (lane >> 4) & 1, T0 = wn * 3, T1 = T0 + 1;
        P1X xa = p1_xaddr(lds, wid, lane);
        const int wb = P1_WSLOT + 3 * blk * 1024 + h * 512 + ((lane >> 2) & 3) * 64 + (lane & 3) * 8;
        lds_ptr w0 = lds + wb + (T0 >> 1) * 1024 + (((T0 & 1) ^ blk) << 5), w1 = lds + wb + (T1 >> 1) * 1024 + (((T1 & 1) ^ blk) << 5);
        P1F<2> f[4];
        p1_first<2>(f, xa, w0, w1);
        int ws3 = 1;
#pragma unroll 1
        for (int hf_ = 0; hf_ < 2; ++hf_) {
            p1_five<2>(f, acc, xa, w0, w1, ws3, 22 + 10 * hf_);
            asm volatile("s_waitcnt vmcnt(0)" ::: "memory");
        }
    }
    STAMP(3);
    {
        const LDS_AS float* colf = (const LDS_AS float*)(lds + P1_COLB) + wn * 96;
        const int vo = (l31 >> 4) * 48 * 1024 + (h * 16 + (l31 & 15)) * 16;
        f32x2 nm[2], r2[2];
#pragma unroll
        for (int m = 0; m < 2; ++m) { const f32x2 rs = rowf[wm * 64 + m * 32 + l31]; nm[m] = f32x2{rs[1], rs[1]}; r2[m] = f32x2{rs[0] * rs[0], rs[0] * rs[0]}; }
#pragma unroll
        for (int c = 0; c < 3; ++c) {
            f32x4 cv[4];
#pragma unroll
            for (int g = 0; g < 4; ++g) cv[g] = *(const LDS_AS f32x4*)(colf + c * 32 + g * 8 + h * 4);
            if (c >= 1 && wn) {
#pragma unroll
                for (int g = 0; g < 4; ++g) cv[g] += *(const LDS_AS f32x4*)(colf + 64 + c * 32 + g * 8 + h * 4);
            }
            const int so = WS_H16 + tt * 2 * 48 * 1024 + (grp * 6 + wn * 3 + c) * 512;
#pragma unroll
            for (int m = 0; m < 2; ++m) {
                const f32x16& a = acc[c * 2 + m];
                f32x2 g[4], u[4];
#pragma unroll
                for (int i = 0; i < 4; ++i) { g[i] = pfma(pk4(cv[i >> 1], (i & 1) * 2), nm[m], pk(a, 2 * i)); u[i] = pfma(pk4(cv[2 + (i >> 1)], (i & 1) * 2), nm[m], pk(a, 8 + 2 * i)); }
#pragma unroll
                for (int i = 0; i < 4; ++i) { g[i] = f32x2{fmaxf(g[i][0], 0.f), fmaxf(g[i][1], 0.f)}; u[i] *= r2[m]; }
                ST16(wsr, (u32x4{cvt2(g[0] * u[0]), cvt2(g[1] * u[1]), cvt2(g[2] * u[2]), cvt2(g[3] * u[3])}), vo, so + m * 2 * 48 * 1024);
            }
        }
    }
}

__device__ __forceinline__ void glds_x4(const char* sbase, const unsigned (&v)[4], unsigned lds_base) {
    unsigned sv;
    asm volatile("s_mov_b32 %0, m0\n\ts_mov_b32 m0, %6\n\ts_nop 0\n\tglobal_load_lds_dwordx4 %1, %5\n\tglobal_load_lds_dwordx4 %2, %5 offset:1024\n\t"
                 "global_load_lds_dwordx4 %3, %5 offset:2048\n\tglobal_load_lds_dwordx4 %4, %5 offset:3072\n\ts_mov_b32 m0, %0"
                 : "=&s"(sv) : "v"(v[0]), "v"(v[1]), "v"(v[2]), "v"(v[3]), "s"(sbase), "s"(lds_base) : "memory");
}
__device__ __forceinline__ void glds_x6(const char* sa, const char* sb, const unsigned (&v)[6], unsigned lds_base) {
    unsigned sv;
    asm volatile("s_mov_b32 %0, m0\n\ts_mov_b32 m0, %9\n\ts_nop 0\n\tglobal_load_lds_dwordx4 %1, %7\n\tglobal_load_lds_dwordx4 %2, %7 offset:1024\n\t"
                 "global_load_lds_dwordx4 %3, %7 offset:2048\n\ts_mov_b32 m0, %10\n\ts_nop 0\n\tglobal_load_lds_dwordx4 %4, %8\n\tglobal_load_lds_dwordx4 %5, %8 offset:1024\n\t"
                 "global_load_lds_dwordx4 %6, %8 offset:2048\n\ts_mov_b32 m0, %0"
                 : "=&s"(sv) : "v"(v[0]), "v"(v[1]), "v"(v[2]), "v"(v[3]), "v"(v[4]), "v"(v[5]), "s"(sa), "s"(sb), "s"(lds_base), "s"(lds_base + 3072) : "memory");
}

__device__ __forceinline__ unsigned long long p1_helper(lds_ptr lds, int hw, int lane, const char* xp, const char* wq, const char* wgp, const char* wup, int rot,
                                          char* ws, const char* kv, const char* qsrc, int pq, int gq) {
    unsigned long long ep = 0;
    asm volatile("global_load_dwordx2 %0, %1, %2 sc1" : "+v"(ep) : "v"(0u), "s"(ws + WS_EPOCH) : "memory");
    const unsigned lds0 = (unsigned)(uintptr_t)lds;
    const int r = lane >> 3, pos = lane & 7;
    unsigned xv[4], wa[4], wb[6];
#pragma unroll
    for (int j = 0; j < 4; ++j) {
        xv[j] = 4096 + (hw * 32 + j * 8 + r) * (D_ * 2) + ((pos ^ ((j & 1) * 4 + (r >> 1))) << 4) - j * 1024;
        wa[j] = 4096 + (hw * 16 + (j >> 1) * 8 + r) * (3 * D_ * 2) + (j & 1) * 128 + ((pos ^ (6 * ((r >> 1) & 1))) << 4) - j * 1024;
    }
#pragma unroll
    for (int j = 0; j < 6; ++j) wb[j] = 4096 + (hw * 16 + (lane >> 2)) * (F_ * 2) + (j % 3) * 64 + (((lane & 3) ^ (j < 3 ? 0 : 2)) << 4) - (j % 3) * 1024;
    const char* wpa = (lane & 8) ? wq + (size_t)(pq * 8 + hw * 2 + ((lane >> 1) & 1)) * (3 * D_ * 2) + (lane & 1) * 128 : xp + (size_t)(gq * 32 + hw * 8 + (lane & 7)) * (D_ * 2);
    const unsigned wsa = (lane & 8) ? 64 * 3 * D_ * 2 : 128;
    const char* wpb = (const char*)((uintptr_t)((lane & 4) ? wup : wgp) + (size_t)(pq * 8 + hw * 2 + ((lane >> 1) & 1)) * (F_ * 2) + (lane & 1) * 128);
    auto warm = [&](int p) {
        if (!WARM) return;
        if (p < 10) warm1(wpa + (size_t)p * wsa, lds0 + LDS_JUNK + 1024 + hw * 256);
        else warm1(wpb + (unsigned)((p < 15 ? p - 5 : p - 15) * 64 * F_ * 2), lds0 + LDS_JUNK + 1024 + hw * 256);
    };
    xp -= 4096; wq -= 4096; wgp -= 4096; wup -= 4096;
    int ku = rot, dws = 0, dxs = 0;
    auto issue = [&](bool pa, bool xx) {
        if (pa) glds_x4(wq + (unsigned)(ku * 64 * 3 * D_ * 2), wa, lds0 + dws * P1_WSLOT + hw * 4096);
        else glds_x6(wgp + (unsigned)(ku * 64 * F_ * 2), wup + (unsigned)(ku * 64 * F_ * 2), wb, lds0 + dws * P1_WSLOT + hw * 6144);
        ku = ku == 9 ? 0 : ku + 1; dws = dws == 2 ? 0 : dws + 1; dxs = dxs == 4 ? 0 : dxs + 1;
        if (xx) glds_x4(xp + ku * 128, xv, lds0 + P1_X0 + dxs * P1_XSLOT + hw * 4096);
    };
    glds_x4(xp + ku * 128, xv, lds0 + P1_X0 + hw * 4096);
    issue(true, true);
    __builtin_amdgcn_s_setprio(3);
    const int r16 = lane & 15, h = lane >> 5, blk = (lane >> 4) & 1, q = (lane >> 2) & 3, fsw = 3 * (q >> 1);
    const unsigned olo = r16 < 2 ? 0x3f803f80u : 0u, ohi = (r16 >> 1) == 1 ? 0x3f803f80u : 0u;
    const s16x8 ones = __builtin_bit_cast(s16x8, (u32x4{olo, olo, ohi, ohi}));
    const int idx = (r16 & 1) ? 0xEEEE : 0x4444;
    int ws3 = 0;
    {
        lds_ptr b = lds + h * 2048 + (hw >> 1) * 1024 + q * 128 + (lane & 3) * 8 + ((((hw & 1) * 2 + blk) ^ fsw) << 5);
        const int so = P1_X0 + (hw * 32 + r16) * 128 + (((lane >> 4) ^ (r16 >> 1)) << 4);
        lds_ptr sp0 = lds + so, sp1 = lds + (so ^ 64);
        const s16x8 ones2 = __builtin_bit_cast(s16x8, (u32x4{olo, olo, olo, olo}));
        f32x4 cs = f32x4{}, s1[2] = {f32x4{}, f32x4{}}, s2[2] = {f32x4{}, f32x4{}};
        auto colsum = [&]() {
            const s16x16 wv0 = CAT16(CAT8(trread(b), trread(b + 512)), CAT8(trread(b + 4096), trread(b + 4096 + 512)));
            const s16x16 wv1 = CAT16(CAT8(trread(b + 8192), trread(b + 8192 + 512)), CAT8(trread(b + 12288), trread(b + 12288 + 512)));
            cs = SMF16(ones, wv0, cs, idx);
            cs = SMF16(ones, wv1, cs, idx);
            b += ws3 == 2 ? -2 * P1_WSLOT : P1_WSLOT; ws3 = ws3 == 2 ? 0 : ws3 + 1;
        };
        auto rowstat = [&](int dx) {
            s16x8 xa[2], xb[2];
#pragma unroll
            for (int t = 0; t < 2; ++t) { xa[t] = *(const LDS_AS s16x8*)(sp0 + t * 2048); xb[t] = *(const LDS_AS s16x8*)(sp1 + t * 2048); }
#pragma unroll
            for (int t = 0; t < 2; ++t) { s2[t] = MFMA16(xa[t], xa[t], s2[t]); s1[t] = SMF16(ones2, CAT16(xa[t], xb[t]), s1[t], idx); }
#pragma unroll
            for (int t = 0; t < 2; ++t) s2[t] = MFMA16(xb[t], xb[t], s2[t]);
            sp0 += dx; sp1 += dx;
        };
        for (int nn = 0; nn < 9; ++nn) {
            int n = nn;
            asm volatile("" : "+s"(n));
            PSTAMP(2 * n);
            if (n == 0) WAIT_BAR(4); else if (n < 8) WAIT_BAR(12 + WARM); else WAIT_BAR(8 + 2 * WARM);
            PSTAMP(2 * n + 1);
            if (n == 0) issue(true, true);
            if (n == 8) ku = ku >= 5 ? ku - 5 : ku + 5;
            if (!(ABL & 4)) issue(n < 8, n < 7);
            warm(n + 5);
            colsum();
            rowstat(n == 4 ? -4 * P1_XSLOT : P1_XSLOT);
        }
        MEMFENCE(); __builtin_amdgcn_s_waitcnt(0xF70 | (10 + 3 * WARM)); MEMFENCE();
        rowstat(0);
        {
            const int t = blk;
            const float d0 = (lane & 2) ? ((lane & 1) ? s2[0][3] : s2[0][2]) : ((lane & 1) ? s2[0][1] : s2[0][0]);
            const float d1 = (lane & 2) ? ((lane & 1) ? s2[1][3] : s2[1][2]) : ((lane & 1) ? s2[1][1] : s2[1][0]);
            const float a0 = __shfl(s1[0][0] + s1[0][1], r16), a1 = __shfl(s1[1][0] + s1[1][1], r16), c0 = __shfl(d0, r16 + 16 * q), c1 = __shfl(d1, r16 + 16 * q);
            const float a = t ? a1 : a0, c = t ? c1 : c0;
            const float mean = a * (1.0f / D_), var = fmaxf(c * (1.0f / D_) - mean * mean, 0.f);
            const float rstd = __builtin_amdgcn_rcpf(rbf(__builtin_amdgcn_sqrtf(rbf(rbf(var) + 1e-5f))));
            if (lane < 32) ((LDS_AS f32x2*)(lds + P1_STAT))[hw * 32 + lane] = f32x2{rstd, -rbf(mean)};
        }
        PSTAMP(18);
        WAIT_BAR(6 + 2 * WARM);
        PSTAMP(19);
        if (!(ABL & 4)) issue(false, false);
        warm(14);
        colsum();
        LDS_AS float* colf = (LDS_AS float*)(lds + P1_COLA) + hw * 32;
        if (lane < 16) { colf[lane] = cs[0] + cs[1]; colf[16 + lane] = cs[2] + cs[3]; }
    }
    asm volatile("" : "+v"(ep));
    const unsigned long long tag = (((unsigned long long)(unsigned)__builtin_amdgcn_readfirstlane((unsigned)(ep >> 32)) << 32) | (unsigned)__builtin_amdgcn_readfirstlane((unsigned)ep)) + 1ull;
    {
        const int rb = P1_WSLOT + 3 * blk * 1024 + h * 512 + q * 64 + (lane & 3) * 8 + (((hw & 1) ^ blk) << 5);
        lds_ptr b = lds + rb + (hw >> 1) * 1024, c = lds + rb + 2048 + (hw >> 1) * 12288;
        f32x4 cs = f32x4{}, ct = f32x4{};
        Poll pl = {};
        for (int nn = 10; nn < 20; ++nn) {
            int n = nn;
            asm volatile("" : "+s"(n));
            PSTAMP(2 * n);
            if (n < 13) WAIT_BAR(6 + 2 * WARM); else if (n == 13) WAIT_BAR(10 + 2 * WARM); else if (n == 18) WAIT_BAR(10); else if (n == 19) WAIT_BAR(0); else WAIT_BAR(14);
            PSTAMP(2 * n + 1);
            if (n == 16 && hw == 0 && lane == 0) raise_flag(ws, WS_FA, tag);
            if (n < 18 && !(ABL & 4)) issue(false, n >= 12 && n < 17);
            if (n < 15) warm(n + 5);
            if (n == 19) poll_request(pl, ws + WS_FA, lane * 16);
            const s16x16 wv0 = CAT16(CAT8(trread(b), trread(b + 256)), CAT8(trread(b + 6144), trread(b + 6144 + 256)));
            const s16x16 wv2 = CAT16(CAT8(trread(c), trread(c + 256)), CAT8(trread(c + 6144), trread(c + 6144 + 256)));
            const s16x16 wv1 = CAT16(CAT8(trread(b + 12288), trread(b + 12288 + 256)), CAT8(trread(b + 18432), trread(b + 18432 + 256)));
            cs = SMF16(ones, wv0, cs, idx);
            ct = SMF16(ones, wv2, ct, idx);
            cs = SMF16(ones, wv1, cs, idx);
            const int dw = ws3 == 2 ? -2 * P1_WSLOT : P1_WSLOT;
            b += dw; c += dw; ws3 = ws3 == 2 ? 0 : ws3 + 1;
        }
        LDS_AS float* colf = (LDS_AS float*)(lds + P1_COLB);
        if (lane < 16) { colf[hw * 32 + lane] = cs[0] + cs[1]; colf[hw * 32 + 16 + lane] = cs[2] + cs[3]; }
        if (lane < 16) { colf[128 + hw * 32 + lane] = ct[0] + ct[1]; colf[128 + hw * 32 + 16 + lane] = ct[2] + ct[3]; }
        LGKM_BAR();
        poll_wait(pl, ws + WS_FA, lane * 16, tag);
        if (blockIdx.x == 0 && hw == 0 && lane == 0) __hip_atomic_store((gu64*)(ws + WS_EPOCH), tag, __ATOMIC_RELAXED, __HIP_MEMORY_SCOPE_AGENT);
#pragma unroll
        for (int j = 0; j < 3; ++j) glds16c(qsrc + (hw + 4 * j) * 1024, lane * 16, lds0 + (hw + 4 * j) * 1024);
#pragma unroll
        for (int j = 0; j < 12; ++j) glds16c(kv + (j / 6) * K2_CHUNK + (hw + 4 * (j % 6)) * 1024, lane * 16, lds0 + K2_KV0 + (j / 6) * K2_SLOTB + (hw + 4 * (j % 6)) * 1024);
        MEMFENCE(); __builtin_amdgcn_s_waitcnt(0xF70 | 6);
    }
    return tag;
}

constexpr int K2_XCH = 2 * K2_SLOTB;

#define SMF32(a, b, c) __builtin_amdgcn_smfmac_f32_32x32x32_bf16((bfx8)(a), __builtin_bit_cast(bfx16, (b)), (c), 0xDDDD, 0, 0)
#define MFMA8(a, b, c) __builtin_amdgcn_mfma_f32_32x32x8bf16_1k((a), (b), (c), 0, 0, 0)
__device__ __forceinline__ f32x8 lo8(const f32x16& v) { return __builtin_shufflevector(v, v, 0, 1, 2, 3, 4, 5, 6, 7); }
__device__ __forceinline__ f32x8 hi8(const f32x16& v) { return __builtin_shufflevector(v, v, 8, 9, 10, 11, 12, 13, 14, 15); }
__device__ __forceinline__ float max16(const f32x16& s) {
    float m = fmaxf(fmaxf(s[0], s[1]), s[2]);
#pragma unroll
    for (int r = 3; r < 15; r += 2) m = fmaxf(fmaxf(m, s[r]), s[r + 1]);
    return swap32_max(fmaxf(m, s[15]));
}

struct K2B { lds_ptr r0, r1; const char* wsrc; unsigned wdst; int lane; };
__device__ __forceinline__ void k2_wpiece(const K2B& c, int st, int i) {
    glds16c(c.wsrc + st * 40960 + i * 8192, c.lane * 16, c.wdst + (st % 3) * 40960 + i * 8192);
}
template <int I, int D, int NP>
__device__ __forceinline__ void k2_bstep(const K2B& c, f32x4 (&acc)[10], const s16x8 (&w)[5], s16x8 (&n)[5], const s16x8& a0, const s16x8& a1) {
    constexpr int O = ((I + 1) % 6) * 20480;
#pragma unroll
    for (int i = 0; i < 10; ++i) {
        if (I < 16 && i < 5) n[i] = *(const LDS_AS s16x8*)((O < 61440 ? c.r0 : c.r1) + O % 61440 + i * 1024);
        acc[i] = MFMA16(w[i >> 1], (i & 1) ? a1 : a0, acc[i]);
        PIN(acc[i]);
        if (NP > 5 ? i < NP : ((i & 1) && (i >> 1) < NP)) { const int k = NP > 5 ? i : i >> 1; k2_wpiece(c, D + k / 5, k % 5); }
        SBAR();
    }
}

__global__ __launch_bounds__(512, 2) void fused_block(const char* __restrict__ x, const char* __restrict__ wqkv, const char* __restrict__ wg, const char* __restrict__ wu,
                                                      const char* __restrict__ wd, const char* __restrict__ wo, char* __restrict__ ws, char* __restrict__ out) {
    extern __shared__ __attribute__((aligned(16))) char smem[];
    const int wid = __builtin_amdgcn_readfirstlane(threadIdx.x >> 6);
    STAMP_DECL; STAMP(0); PSTAMP(63);
    unsigned long long tag = 0;
    const rsrc_t wsr = __builtin_amdgcn_make_buffer_rsrc(ws, 0, (int)WS_END, 0x00020000);
    const int bid = blockIdx.x, head = (bid & 7) * 4 + ((bid >> 3) & 3), par = wid >> 2, qt0 = (bid >> 5) * 8 + (wid & 3) * 2;
    const char* kv = ws + WS_KV + head * KV_HEAD;
    {
        const int bi = bid >> 3, panel = (bid & 1) * 8 + (bi & 7), grp = ((bid & 7) >> 1) * 4 + (bi >> 3), l0 = threadIdx.x & 63;
        if (wid < 4) p1_compute((lds_ptr)smem, wid, l0, panel, grp, wsr, Pack{wd, wo, wid * 256 + bid,
                                 l0 < 32 ? x + (size_t)(panel * 128 + (bi >> 3) * 32 + l0) * (D_ * 2) + (2 + wid) * 128
                                         : wqkv + (size_t)((2 + wid) * 64 + (bi & 7) * 8 + ((l0 >> 1) & 7)) * (3 * D_ * 2) + min(grp, 14) * 256 + (l0 & 1) * 128} STAMP_PASS);
        else {
            const int rot = K1_ROT ? ((5 * (bi & 7)) / 4 + (5 * (bi >> 3)) / 2) % 10 : 0;
            tag = p1_helper((lds_ptr)smem, wid - 4, l0, x + (size_t)panel * 128 * (D_ * 2), wqkv + min(grp, 14) * 256, wg + grp * 192, wu + grp * 192, rot,
                            ws, kv, ws + WS_Q + head * Q_HEAD + (bid >> 5) * 8 * Q_TILE, bi & 7, bi >> 3);
        }
    }
    int lane = __builtin_amdgcn_mbcnt_hi(~0u, __builtin_amdgcn_mbcnt_lo(~0u, 0u));
    PIN(lane);
    __builtin_amdgcn_s_setprio(0);
    LGKM_BAR();
    STAMP(4);
    lds_ptr lds = (lds_ptr)smem + K2_KV0;
    const unsigned lds0 = (unsigned)(uintptr_t)lds;
    const int tp = ((blockIdx.x & 7) >> 1) * 8 + ((blockIdx.x >> 3) & 7), cg = (blockIdx.x & 1) * 4 + (blockIdx.x >> 6);
    K2B kb;
    kb.wsrc = ws + WS_WP + cg * 68 * 5120 + wid * 1024; kb.wdst = (unsigned)(uintptr_t)(lds_ptr)smem + wid * 1024; kb.lane = lane;
    const char* hsrc = ws + WS_H16 + ((tp * 4 + (wid & 1) * 2) * 48 + (wid >> 1)) * 1024;
    s16x8 hf[24];
#pragma unroll
    for (int i = 0; i < 24; ++i) asm volatile("" : "=v"(hf[i]));
    const int h = lane >> 5, l31 = lane & 31, l16 = lane * 16;
    const char* src = kv + wid * 1024 + 2 * K2_CHUNK;
#pragma unroll
    for (int i = 0; i < 3; ++i) glds16c(src + i * 8192, l16, lds0 + 2 * K2_SLOTB + wid * 1024 + i * 8192);
    src += K2_CHUNK;
    const int k1off = h * 512 + l31 * 8, k2off = h ? KV_V + 5 * 256 : 1024 + l31 * 8;
    const int vq = min(l31 >> 2, 5), voff = KV_V + vq * 256 + ((h ^ (vq & 3)) << 6) + (l31 < 24 ? l31 & 3 : 1) * 16;

    f32x16 oa[2] = {f32x16{}, f32x16{}}, sc[2];
    s16x8 k1[2], v0[2], v1[2]; s16x4 k2[2];
    lds_ptr pk1 = lds + k1off + par * KV_TILE, pk2 = lds + k2off + par * KV_TILE, pv = lds + voff + par * KV_TILE, pw = lds + (voff ^ 128) + par * KV_TILE;
#define LDS8(p) (*(const LDS_AS s16x4*)(p))
#define RD_K(t, j) do { k1[(t) & 1] = CAT8(LDS8(pk1 + (j) * 2 * KV_TILE), LDS8(pk1 + (j) * 2 * KV_TILE + 256)); k2[(t) & 1] = LDS8(pk2 + (j) * 2 * KV_TILE); } while (0)
#define RD_V(t, j) do { v0[(t) & 1] = *(const LDS_AS s16x8*)(pv + (j) * 2 * KV_TILE); v1[(t) & 1] = *(const LDS_AS s16x8*)(pw + (j) * 2 * KV_TILE); } while (0)
    s16x8 q1[2]; s16x4 q2[2];
#pragma unroll
    for (int b = 0; b < 2; ++b) {
        const lds_ptr qp = (lds_ptr)smem + ((wid & 3) * 2 + b) * Q_TILE;
        q1[b] = CAT8(LDS8(qp + k1off), LDS8(qp + k1off + 256)); q2[b] = LDS8(qp + 1024 + l31 * 8);
    }
    {
        const s16x8 ka = CAT8(LDS8(lds + k1off), LDS8(lds + k1off + 256));
        const s16x4 kb = LDS8(lds + k2off);
        RD_K(0, 0); RD_V(0, 0);
#pragma unroll
        for (int b = 0; b < 2; ++b) {
            if (h) q2[b] = s16x4{0, 0, 0, 0};
            f32x16 s = MFMA32(ka, q1[b], f32x16{});
            s = MFMA8(kb, q2[b], s);
            const float m = rbf(-max16(s));
            if (h) q2[b][0] = (short)(__float_as_uint(m) >> 16);
        }
        sc[0] = MFMA32(k1[0], q1[0], f32x16{});
        sc[0] = MFMA8(k2[0], q2[0], sc[0]);
        sc[1] = f32x16{}; v1[1] = s16x8{};
    }
    int slot = 0;
    STAMP(5);
#define EXP4(S, i) do { S[i] = __builtin_amdgcn_exp2f(S[i]); S[i + 1] = __builtin_amdgcn_exp2f(S[i + 1]); S[i + 2] = __builtin_amdgcn_exp2f(S[i + 2]); S[i + 3] = __builtin_amdgcn_exp2f(S[i + 3]); PIN(S); SBAR(); } while (0)
    const char* sA = src; unsigned dA = 0; int nA = 0;
    const char* sB = kb.wsrc; unsigned dB = kb.wdst;
    const int hvo = l16 + 4096;
    Poll pl = {};
#define DMA_A(i) do { if (nA > (i)) glds16c(sA + (i) * 8192, l16, dA + (i) * 8192); SBAR(); } while (0)
#define DMA_B() do { if (c >= 3) { glds16c(sB, l16, dB); sB += 8192; dB += 8192; } SBAR(); } while (0)
#define LOAD_H(e) do { if (c >= 3 && c < 7) { asm volatile("s_getpc_b64 vcc\n\ts_add_u32 vcc_lo, vcc_lo, %26\n\ts_addc_u32 vcc_hi, vcc_hi, 0\n\ts_setpc_b64 vcc\n\t" \
        "global_load_dwordx4 %0, %24, %25 offset:-4096 sc1\n\tglobal_load_dwordx4 %1, %24, %25 sc1\n\tglobal_load_dwordx4 %2, %24, %27 sc1\n\ts_branch .Lhf_end%=\n\t" \
        "global_load_dwordx4 %3, %24, %25 offset:-4096 sc1\n\tglobal_load_dwordx4 %4, %24, %25 sc1\n\tglobal_load_dwordx4 %5, %24, %27 sc1\n\ts_branch .Lhf_end%=\n\t" \
        "global_load_dwordx4 %6, %24, %25 offset:-4096 sc1\n\tglobal_load_dwordx4 %7, %24, %25 sc1\n\tglobal_load_dwordx4 %8, %24, %27 sc1\n\ts_branch .Lhf_end%=\n\t" \
        "global_load_dwordx4 %9, %24, %25 offset:-4096 sc1\n\tglobal_load_dwordx4 %10, %24, %25 sc1\n\tglobal_load_dwordx4 %11, %24, %27 sc1\n\ts_branch .Lhf_end%=\n\t" \
        "global_load_dwordx4 %12, %24, %25 offset:-4096 sc1\n\tglobal_load_dwordx4 %13, %24, %25 sc1\n\tglobal_load_dwordx4 %14, %24, %27 sc1\n\ts_branch .Lhf_end%=\n\t" \
        "global_load_dwordx4 %15, %24, %25 offset:-4096 sc1\n\tglobal_load_dwordx4 %16, %24, %25 sc1\n\tglobal_load_dwordx4 %17, %24, %27 sc1\n\ts_branch .Lhf_end%=\n\t" \
        "global_load_dwordx4 %18, %24, %25 offset:-4096 sc1\n\tglobal_load_dwordx4 %19, %24, %25 sc1\n\tglobal_load_dwordx4 %20, %24, %27 sc1\n\ts_branch .Lhf_end%=\n\t" \
        "global_load_dwordx4 %21, %24, %25 offset:-4096 sc1\n\tglobal_load_dwordx4 %22, %24, %25 sc1\n\tglobal_load_dwordx4 %23, %24, %27 sc1\n\ts_branch .Lhf_end%=\n\t" \
        ".Lhf_end%=:" : "+v"(hf[0]), "+v"(hf[1]), "+v"(hf[2]), "+v"(hf[3]), "+v"(hf[4]), "+v"(hf[5]), "+v"(hf[6]), "+v"(hf[7]), "+v"(hf[8]), "+v"(hf[9]), "+v"(hf[10]), "+v"(hf[11]), "+v"(hf[12]), "+v"(hf[13]), "+v"(hf[14]), "+v"(hf[15]), "+v"(hf[16]), "+v"(hf[17]), "+v"(hf[18]), "+v"(hf[19]), "+v"(hf[20]), "+v"(hf[21]), "+v"(hf[22]), "+v"(hf[23]) \
        : "v"(hvo), "s"(hsrc), "s"((e) * 28 + 12), "s"(hsrc + 4096) : "vcc", "scc", "memory"); hsrc += 3 * 4096; } SBAR(); } while (0)
    for (int cc = 0; cc < 8; ++cc) {
        int c = cc;
        asm volatile("" : "+s"(c));
#pragma unroll
        for (int j = 0; j < 4; ++j) {
            if (j == 3) {
                if (c == 2 && wid == 4) poll_wait(pl, ws + WS_FB, l16, tag);
                if (c < 3) { asm volatile("s_waitcnt vmcnt(0)" ::: "memory"); LGKM_BAR(); } else WAIT_BAR(8);
                __builtin_amdgcn_s_setprio(2);
                if (c == 0 && wid == 4 && lane == 0) raise_flag(ws, WS_FB, tag);
                if (c < 5) { sA = src; src += K2_CHUNK; dA = lds0 + slot * K2_SLOTB + wid * 1024; nA = 3; }
                else { sA = kb.wsrc + 81920 + (c - 6) * 24576; dA = kb.wdst + 81920 + (c - 6) * 24576; nA = c == 5 ? 0 : 9 - c; }
                const int d = (slot == K2_SLOTS - 1) ? -(K2_SLOTS - 1) * K2_SLOTB : K2_SLOTB;
                slot = (slot == K2_SLOTS - 1) ? 0 : slot + 1;
                pk1 += d; pk2 += d; pv += d; pw += d;
                SBAR();
            }
            RD_K(j + 1, (j + 1) & 3); SBAR();
            oa[1] = SMF32(v1[(j + 1) & 1], hi8(sc[1]), oa[1]); PIN(oa[1]); SBAR();
            EXP4(sc[0], 0);
            sc[1] = MFMA32(k1[j & 1], q1[1], f32x16{}); PIN(sc[1]); SBAR();
            if (j == 3) DMA_A(0);
            if (j == 0) DMA_A(2);
            if (j == 1) DMA_B();
            if (j == 2) LOAD_H(2 * (c - 3) + 1);
            EXP4(sc[0], 4);
            sc[1] = MFMA8(k2[j & 1], q2[1], sc[1]); PIN(sc[1]); SBAR();
            EXP4(sc[0], 8);
            oa[0] = SMF32(v0[j & 1], lo8(sc[0]), oa[0]); PIN(oa[0]); SBAR();
            EXP4(sc[0], 12);
            RD_V(j + 1, (j + 1) & 3);
            if (j == 1) __builtin_amdgcn_s_setprio(1);
            if (j == 2) __builtin_amdgcn_s_setprio(0);
            SBAR();
            oa[0] = SMF32(v1[j & 1], hi8(sc[0]), oa[0]); PIN(oa[0]); SBAR();
            EXP4(sc[1], 0);
            sc[0] = MFMA32(k1[(j + 1) & 1], q1[0], f32x16{}); PIN(sc[0]); SBAR();
            if (j == 3) DMA_A(1);
            if (j == 0) DMA_B();
            if (j == 1) { if (c == 2 && wid == 4) poll_request(pl, ws + WS_FB, l16); LOAD_H(2 * (c - 3)); }
            EXP4(sc[1], 4);
            sc[0] = MFMA8(k2[(j + 1) & 1], q2[0], sc[0]); PIN(sc[0]); SBAR();
            EXP4(sc[1], 8);
            oa[1] = SMF32(v0[j & 1], lo8(sc[1]), oa[1]); PIN(oa[1]); SBAR();
            EXP4(sc[1], 12);
        }
    }
    oa[1] = SMF32(v1[1], hi8(sc[1]), oa[1]);
    int ln = __builtin_amdgcn_mbcnt_hi(~0u, __builtin_amdgcn_mbcnt_lo(~0u, 0u));
    PIN(ln);
    const int h2 = ln >> 5, l2 = ln & 31;
    f32x16 o = par ? oa[1] : oa[0];
    {
        const f32x16 g = par ? oa[0] : oa[1];
        const lds_ptr xw = lds + K2_XCH + wid * 3072 + ln * 16, xr = lds + K2_XCH + (wid ^ 4) * 3072 + ln * 16;
#pragma unroll
        for (int i = 0; i < 3; ++i) *(LDS_AS f32x4*)(xw + i * 1024) = f32x4{g[4 * i], g[4 * i + 1], g[4 * i + 2], g[4 * i + 3]};
        WAIT_BAR(0);
#pragma unroll
        for (int i = 0; i < 3; ++i) { const f32x4 t = *(const LDS_AS f32x4*)(xr + i * 1024); o[4 * i] += t[0]; o[4 * i + 1] += t[1]; o[4 * i + 2] += t[2]; o[4 * i + 3] += t[3]; }
    }
    const int qt = qt0 + par;
    {
        float big = fabsf(o[0]);
#pragma unroll
        for (int r = 1; r < 9; r += 2) big = fabsf(o[r]) + fabsf(o[r + 1]) + big;
        const float big2 = big + fabsf(o[9]) + fabsf(o[10]) + fabsf(o[11]);
        const bool bad = h2 ? !(big < 1e30f && o[8] > 1e-30f) : !(big2 < 1e30f);
        if (__builtin_expect(__any(bad), 0)) {
            const s16x8 qf1 = par ? q1[1] : q1[0];
            s16x4 qf2 = par ? q2[1] : q2[0];
            if (h2) qf2[0] = 0;
            const int g1 = h2 * 512 + l2 * 8, g2 = 1024 + l2 * 8, gq = min(l2 >> 2, 5), gv = KV_V + gq * 256 + ((h2 ^ (gq & 3)) << 6) + (l2 & 3) * 16;
            float m = -INFINITY;
            f32x16 negm;
#pragma unroll 1
            for (int t = 0; t < 64; ++t) {
                const int g = WS_KV + head * KV_HEAD + t * KV_TILE;
                f32x16 s = MFMA32(CAT8(LD8(wsr, g1, g), LD8(wsr, g1 + 256, g)), qf1, f32x16{});
                s = MFMA8(LD8(wsr, g2, g), qf2, s);
                m = fmaxf(m, max16(s));
            }
#pragma unroll
            for (int r = 0; r < 16; ++r) negm[r] = -m;
            o = f32x16{};
#pragma unroll 1
            for (int t = 0; t < 64; ++t) {
                const int g = WS_KV + head * KV_HEAD + t * KV_TILE;
                f32x16 s = MFMA32(CAT8(LD8(wsr, g1, g), LD8(wsr, g1 + 256, g)), qf1, negm);
                s = MFMA8(LD8(wsr, g2, g), qf2, s);
#pragma unroll
                for (int r = 0; r < 16; ++r) s[r] = __builtin_amdgcn_exp2f(s[r]);
                o = SMF32(LD16(wsr, gv, g), lo8(s), o);
                o = SMF32(LD16(wsr, gv ^ 128, g), hi8(s), o);
            }
        }
    }
    auto rr = __builtin_amdgcn_permlane32_swap(__float_as_uint(o[8]), __float_as_uint(o[8]), false, false);
    const float rl = __builtin_amdgcn_rcpf(__uint_as_float(rr[1]));
    const int ob = (l2 >> 4) * 20 * 1024 + (l2 & 15) * 16, os = (int)WS_O16 + qt * 2 * 20 * 1024;
    ST16(wsr, (u32x4{cvtpk(o[0] * rl, o[1] * rl), cvtpk(o[2] * rl, o[3] * rl), cvtpk(o[4] * rl, o[5] * rl), cvtpk(o[6] * rl, o[7] * rl)}),
         ob + h2 * 256, os + (head >> 1) * 1024 + (head & 1) * 512);
    if (!h2) ST8(wsr, (u32x2{cvtpk(o[8] * rl, o[9] * rl), cvtpk(o[10] * rl, o[11] * rl)}), ob, os + (16 + (head >> 3)) * 1024 + ((head >> 1) & 3) * 256 + (head & 1) * 8);
    STAMP(6);
    __builtin_amdgcn_s_setprio(0);
    const int mt = wid & 1, kq = wid >> 1;
    kb.lane = ln; kb.r0 = (lds_ptr)smem + kq * 5120 + ln * 16; kb.r1 = kb.r0 + 61440;
    asm volatile("" : "+v"(kb.r1));
    const int ch0 = wid * 128 + ln, ch1 = ch0 + 64, er0 = ch0 / 10, er1 = ch1 / 10, ec0 = ch0 - er0 * 10, ec1 = ch1 - er1 * 10;
    const size_t eb = (size_t)tp * 64 * (D_ * 2) + cg * 160;
    const unsigned eo0 = er0 * (D_ * 2) + ec0 * 16, eo1 = er1 * (D_ * 2) + ec1 * 16;
    u32x4 xv0 = u32x4{}, xv1 = u32x4{};
    if (wid < 5) { xv0 = *(const u32x4*)(x + eb + eo0); xv1 = *(const u32x4*)(x + eb + eo1); }
    f32x4 acc[10];
#pragma unroll
    for (int i = 0; i < 10; ++i) acc[i] = f32x4{};
    s16x8 wa[5], wb[5];
#pragma unroll
    for (int i = 0; i < 5; ++i) wa[i] = *(const LDS_AS s16x8*)(kb.r0 + i * 1024);
    k2_bstep<0, 0, 0>(kb, acc, wa, wb, hf[0], hf[12]);
    k2_bstep<1, 0, 0>(kb, acc, wb, wa, hf[1], hf[13]);
    k2_bstep<2, 0, 0>(kb, acc, wa, wb, hf[2], hf[14]);
    asm volatile("s_waitcnt vmcnt(0)" ::: "memory");
    LGKM_BAR();
    if (wid == 4 && ln == 0) raise_flag(ws, WS_FC, tag);
    k2_bstep<3, 3, 5>(kb, acc, wb, wa, hf[3], hf[15]);
    k2_bstep<4, 4, 5>(kb, acc, wa, wb, hf[4], hf[16]);
    WAIT_BAR(5);
    k2_bstep<5, 5, 5>(kb, acc, wb, wa, hf[5], hf[17]);
    k2_bstep<6, 0, 0>(kb, acc, wa, wb, hf[6], hf[18]);
    WAIT_BAR(5);
    k2_bstep<7, 6, 5>(kb, acc, wb, wa, hf[7], hf[19]);
    unsigned long long seen = 0;
    const char* cw = ws + WS_FC + (tp >> 2) * 256;
    const unsigned cvo = l2 * 8;
    if (wid == 4) asm volatile("global_load_dwordx2 %0, %1, %2 sc1" : "+v"(seen) : "v"(cvo), "s"(cw) : "memory");
    SBAR();
    k2_bstep<8, 0, 0>(kb, acc, wa, wb, hf[8], hf[20]);
    if (wid == 4) {
        asm volatile("s_waitcnt vmcnt(0)" : "+v"(seen) :: "memory");
#pragma unroll 1
        for (unsigned spin = 0; spin < (1u << 20) && !__all(seen == tag); ++spin) {
            __builtin_amdgcn_s_sleep(1);
            asm volatile("global_load_dwordx2 %0, %1, %2 sc1\n\ts_waitcnt vmcnt(0)" : "=&v"(seen) : "v"(cvo), "s"(cw) : "memory");
        }
    }
    WAIT_BAR(5);
    s16x8 of[10];
#pragma unroll
    for (int i = 0; i < 10; ++i)
        of[i] = LD16(wsr, ln * 16, (int)WS_O16 + ((tp * 4 + mt * 2 + (i & 1)) * 20 + (i >> 1) * 4 + kq) * 1024);
    MEMFENCE(); SBAR();
    k2_bstep<9, 7, 5>(kb, acc, wb, wa, hf[9], hf[21]);
    k2_bstep<10, 0, 0>(kb, acc, wa, wb, hf[10], hf[22]);
    WAIT_BAR(15);
    k2_bstep<11, 8, 3>(kb, acc, wb, wa, hf[11], hf[23]);
    k2_bstep<12, 0, 0>(kb, acc, wa, wb, of[0], of[1]);
    WAIT_BAR(3);
    k2_bstep<13, 0, 0>(kb, acc, wb, wa, of[2], of[3]);
    k2_bstep<14, 0, 0>(kb, acc, wa, wb, of[4], of[5]);
    WAIT_BAR(0);
    k2_bstep<15, 0, 0>(kb, acc, wb, wa, of[6], of[7]);
    k2_bstep<16, 0, 0>(kb, acc, wa, wb, of[8], of[9]);
    {
        const int tk = ln & 15;
        const lds_ptr pp = (lds_ptr)smem + kq * 20480 + (mt * 32 + tk) * 320 + (((ln >> 4) ^ ((tk >> 1) & 3)) << 4);
#pragma unroll
        for (int i = 0; i < 10; ++i) *(LDS_AS f32x4*)(pp + (i & 1) * 16 * 320 + (i >> 1) * 64) = acc[i];
        LGKM_BAR();
        if (wid < 5) {
#pragma unroll
            for (int e = 0; e < 2; ++e) {
                const int er = e ? er1 : er0, ec = e ? ec1 : ec0, sw = (er >> 1) & 3;
                const lds_ptr p0 = (lds_ptr)smem + er * 320 + (((2 * ec) ^ sw) << 4), p1 = (lds_ptr)smem + er * 320 + (((2 * ec + 1) ^ sw) << 4);
                f32x4 a = *(const LDS_AS f32x4*)p0, b = *(const LDS_AS f32x4*)p1;
#pragma unroll
                for (int k = 1; k < 4; ++k) { a += *(const LDS_AS f32x4*)(p0 + k * 20480); b += *(const LDS_AS f32x4*)(p1 + k * 20480); }
                const u32x4 xv = e ? xv1 : xv0;
                *(u32x4*)(out + eb + (e ? eo1 : eo0)) = u32x4{cvtpk(a[0] + bf_lo(xv[0]), a[1] + bf_hi(xv[0])), cvtpk(a[2] + bf_lo(xv[1]), a[3] + bf_hi(xv[1])),
                                                              cvtpk(b[0] + bf_lo(xv[2]), b[1] + bf_hi(xv[2])), cvtpk(b[2] + bf_lo(xv[3]), b[3] + bf_hi(xv[3]))};
            }
        }
    }
    STAMP(7); STAMP_FLUSH(0); PFLUSH();
}

extern "C" void kernel_launch(void* const* d_in, const int* in_sizes, int n_in, void* d_out, int out_size, void* d_ws, size_t ws_size,
                              hipStream_t stream) {
    const char* x = (const char*)d_in[0];
    const char* wqkv = (const char*)d_in[1];
    const char* wo = (const char*)d_in[2];
    const char* wg = (const char*)d_in[4];
    const char* wu = (const char*)d_in[5];
    const char* wd = (const char*)d_in[6];
    char* ws = (char*)d_ws;
    (void)hipFuncSetAttribute((const void*)fused_block, hipFuncAttributeMaxDynamicSharedMemorySize, LDS_BYTES);
    fused_block<<<256, 512, LDS_BYTES, stream>>>(x, wqkv, wg, wu, wd, wo, ws, (char*)d_out);
}
```

```cpp
#include <hip/hip_runtime.h>
#include <hip/hip_bf16.h>
#include <stdint.h>

typedef __bf16 bfx4 __attribute__((ext_vector_type(4)));
typedef __bf16 bfx8 __attribute__((ext_vector_type(8)));
typedef __bf16 bfx16 __attribute__((ext_vector_type(16)));
typedef short s16x4 __attribute__((ext_vector_type(4)));
typedef short s16x8 __attribute__((ext_vector_type(8)));
typedef short s16x16 __attribute__((ext_vector_type(16)));
typedef float f32x2 __attribute__((ext_vector_type(2)));
typedef float f32x4 __attribute__((ext_vector_type(4)));
typedef float f32x8 __attribute__((ext_vector_type(8)));
typedef float f32x16 __attribute__((ext_vector_type(16)));
typedef unsigned u32x2 __attribute__((ext_vector_type(2)));
typedef unsigned u32x4 __attribute__((ext_vector_type(4)));
typedef unsigned u32x8 __attribute__((ext_vector_type(8)));
#define LDS_AS __attribute__((address_space(3)))
typedef LDS_AS char* lds_ptr;

#define ABL 0
#define DPRIO 1
#define CODEWARM 1
#define EVEN 1
#define HPRIO 3
#define CPRIO 0
#define WPWARM 0
#define UNR2 1
#define HUNR 1
#define K1_ROT 1
constexpr int S_ = 2048, D_ = 640, H_ = 32, F_ = 1536;

constexpr int Q_TILE = 1280, Q_HEAD = 64 * Q_TILE, KV_TILE = 2816, KV_V = 1280, KV_HEAD = 64 * KV_TILE;
constexpr int WS_Q = 0;
constexpr int WS_KV = WS_Q + H_ * Q_HEAD;
constexpr int WS_O16 = WS_KV + H_ * KV_HEAD + 8192;
constexpr int WS_H16 = WS_O16 + 128 * 20 * 1024;
constexpr int WS_WP = WS_H16 + 128 * 48 * 1024;
constexpr int WS_EPOCH = WS_WP + 8 * 68 * 5120 + 8192;
constexpr int WS_FA = WS_EPOCH + 256, WS_FB = WS_FA + 2048, WS_FC = WS_FB + 2048, WS_END = WS_FC + 8192;

#define PSTAMP(i) do {} while (0)
#define PFLUSH() do {} while (0)
#define DIAG_PF2 0
#define QSTAMP(i) do {} while (0)
#define STAMP_ARG
#define STAMP_PASS
#define STAMP_DECL do {} while (0)
#define STAMP(i) do {} while (0)
#define STAMP_FLUSH(kern) do {} while (0)
#define SBAR() __builtin_amdgcn_sched_barrier(0)
#define MFMA32(a, b, c) __builtin_amdgcn_mfma_f32_32x32x16_bf16((bfx8)(a), (bfx8)(b), (c), 0, 0, 0)

__device__ __forceinline__ unsigned cvtpk(float lo, float hi) {
    typedef __bf16 bfx2 __attribute__((ext_vector_type(2)));
    bfx2 r = {(__bf16)lo, (__bf16)hi};
    return __builtin_bit_cast(unsigned, r);
}
__device__ __forceinline__ void glds16(const void* g, unsigned lds_base) {
    unsigned sv;
    asm volatile("s_mov_b32 %0, m0\n\ts_mov_b32 m0, %2\n\ts_nop 0\n\tglobal_load_lds_dwordx4 %1, off\n\ts_mov_b32 m0, %0"
                 : "=&s"(sv) : "v"(g), "s"(lds_base) : "memory");
}
__device__ __forceinline__ s16x4 trread(const LDS_AS char* p) {
    return __builtin_amdgcn_ds_read_tr16_b64_v4i16((LDS_AS s16x4*)p);
}
__device__ __forceinline__ float rbf(float v) { return (float)(__bf16)v; }
__device__ __forceinline__ float bf_lo(unsigned u) { return __uint_as_float(u << 16); }
__device__ __forceinline__ float bf_hi(unsigned u) { return __uint_as_float(u & 0xffff0000u); }
__device__ __forceinline__ float swap32_add(float v) {
    auto r = __builtin_amdgcn_permlane32_swap(__float_as_uint(v), __float_as_uint(v), false, false);
    return __uint_as_float(r[0]) + __uint_as_float(r[1]);
}
__device__ __forceinline__ float swap32_max(float v) {
    auto r = __builtin_amdgcn_permlane32_swap(__float_as_uint(v), __float_as_uint(v), false, false);
    return fmaxf(__uint_as_float(r[0]), __uint_as_float(r[1]));
}

constexpr int P1_WSLOT = 24576, P1_X0 = 3 * P1_WSLOT, P1_XSLOT = 16384, P1_STAT = P1_X0 + 5 * P1_XSLOT;
constexpr int P1_COLA = P1_STAT + 1024, P1_COLB = P1_COLA + 512, P1_TAG = P1_COLB + 1024, LDS_JUNK = 161792, LDS_BYTES = 163840;
constexpr int K2_CHUNK = 8 * KV_TILE, K2_SLOTB = 24576, K2_SLOTS = 3, K2_KV0 = 81920, K2_Q0 = 65536;
typedef __attribute__((address_space(1))) unsigned long long gu64;

#define PIN(x) asm volatile("" : "+v"(x))
#define MEMFENCE() asm volatile("" ::: "memory")
#define WAIT_BAR(N) do { MEMFENCE(); __builtin_amdgcn_s_waitcnt(0x70 | ((N) & 15) | (((N) >> 4) << 14)); __builtin_amdgcn_s_barrier(); MEMFENCE(); SBAR(); } while (0)
#define LGKM_BAR() do { MEMFENCE(); __builtin_amdgcn_s_waitcnt(0xC07F); __builtin_amdgcn_s_barrier(); MEMFENCE(); SBAR(); } while (0)
#define MFMA16(a, b, c) __builtin_amdgcn_mfma_f32_16x16x32_bf16((bfx8)(a), (bfx8)(b), (c), 0, 0, 0)
#define SMF16(a, b, c, i) __builtin_amdgcn_smfmac_f32_16x16x64_bf16((bfx8)(a), (bfx16)(b), (c), (i), 0, 0)
#define CAT8(a, b) __builtin_shufflevector(a, b, 0, 1, 2, 3, 4, 5, 6, 7)
#define CAT16(a, b) __builtin_shufflevector(a, b, 0, 1, 2, 3, 4, 5, 6, 7, 8, 9, 10, 11, 12, 13, 14, 15)

__device__ __forceinline__ void glds16s(const char* sbase, unsigned voff, unsigned lds_base) {
    unsigned sv;
    asm volatile("s_mov_b32 %0, m0\n\ts_mov_b32 m0, %3\n\ts_nop 0\n\tglobal_load_lds_dwordx4 %1, %2\n\ts_mov_b32 m0, %0"
                 : "=&s"(sv) : "v"(voff), "s"(sbase), "s"(lds_base) : "memory");
}

__device__ __forceinline__ void glds16c(const char* sbase, unsigned voff, unsigned lds_base) {
    asm volatile("s_mov_b32 m0, %2\n\ts_nop 0\n\tglobal_load_lds_dwordx4 %0, %1 sc1" :: "v"(voff), "s"(sbase), "s"(lds_base) : "memory");
}
typedef __amdgpu_buffer_rsrc_t rsrc_t;
#define ST16(rs, v, voff, soff) __builtin_amdgcn_raw_buffer_store_b128((v), (rs), (voff) + (soff), 0, (ABL & 32) ? 0 : 16)
#define ST8(rs, v, voff, soff) __builtin_amdgcn_raw_buffer_store_b64((v), (rs), (voff), (soff), 16)
#define LD16(rs, voff, soff) __builtin_bit_cast(s16x8, __builtin_amdgcn_raw_buffer_load_b128((rs), (voff), (soff), 16))
#define LD8(rs, voff, soff) __builtin_bit_cast(s16x4, __builtin_amdgcn_raw_buffer_load_b64((rs), (voff), (soff), 16))
__device__ __forceinline__ f32x2 pk(const f32x16& a, int i) { return f32x2{a[i], a[i + 1]}; }
__device__ __forceinline__ f32x2 pk4(const f32x4& a, int i) { return f32x2{a[i], a[i + 1]}; }
__device__ __forceinline__ f32x2 pfma(f32x2 a, f32x2 b, f32x2 c) { return __builtin_elementwise_fma(a, b, c); }
__device__ __forceinline__ unsigned cvt2(f32x2 v) { return cvtpk(v[0], v[1]); }
__device__ __forceinline__ float add1(float a, float b) { float r = a + b; asm volatile("" : "+v"(r)); return r; }
__device__ __forceinline__ float mul1(float a, float b) { float r = a * b; asm volatile("" : "+v"(r)); return r; }

__device__ __forceinline__ void raise_flag(char* ws, int arr, unsigned long long tag) {
    __hip_atomic_store((gu64*)(ws + arr) + blockIdx.x, tag, __ATOMIC_RELAXED, __HIP_MEMORY_SCOPE_AGENT);
}
struct Poll { u32x4 a, b; };
__device__ __forceinline__ void poll_request(Poll& p, const char* arr, unsigned voff) {
    asm volatile("global_load_dwordx4 %0, %2, %3 sc1\n\tglobal_load_dwordx4 %1, %2, %3 offset:1024 sc1" : "+v"(p.a), "+v"(p.b) : "v"(voff), "s"(arr) : "memory");
}
__device__ __forceinline__ void poll_wait(Poll& p, const char* arr, unsigned voff, unsigned long long tag) {
    const unsigned tl = (unsigned)tag, th = (unsigned)(tag >> 32);
#pragma unroll 1
    for (unsigned spin = 0; spin < (1u << 18); ++spin) {
        asm volatile("s_waitcnt vmcnt(0)" : "+v"(p.a), "+v"(p.b) :: "memory");
        if (__all(p.a[0] == tl && p.a[1] == th && p.a[2] == tl && p.a[3] == th && p.b[0] == tl && p.b[1] == th && p.b[2] == tl && p.b[3] == th)) break;
        __builtin_amdgcn_s_sleep(1);
        poll_request(p, arr, voff);
    }
    asm volatile("s_waitcnt vmcnt(0)" : "+v"(p.a), "+v"(p.b) :: "memory");
}

template <int MODE> struct P1F { s16x8 x[2]; s16x4 w[MODE == 2 ? 3 : 2][2]; };

template <int MODE>
__device__ __forceinline__ void p1_read(P1F<MODE>& n, lds_ptr xa, lds_ptr w0, lds_ptr w1, int wimm, int i) {
    constexpr int TS = MODE == 2 ? 256 : 512;
    if (i == 0) { n.x[0] = *(const LDS_AS s16x8*)xa; n.w[0][0] = trread(w0 + wimm); n.w[0][1] = trread(w0 + wimm + TS); }
    if (i == 1) { n.x[1] = *(const LDS_AS s16x8*)(xa + 4096); n.w[1][0] = trread(w1 + wimm); n.w[1][1] = trread(w1 + wimm + TS); }
    if (i == 2 && MODE == 2) { n.w[MODE == 2 ? 2 : 0][0] = trread(w0 + wimm + 1024); n.w[MODE == 2 ? 2 : 0][1] = trread(w0 + wimm + 1024 + TS); }
}
template <int MODE, int NR>
__device__ __forceinline__ void p1_step(const P1F<MODE>& f, f32x16 (&acc)[MODE == 2 ? 6 : 4], P1F<MODE>& n0, lds_ptr xa0, int wi0, P1F<MODE>& n1, lds_ptr xa1, int wi1,
                                        lds_ptr w0, lds_ptr w1) {
    constexpr int NM = MODE == 2 ? 6 : 4;
#pragma unroll
    for (int g = 0; g < NM; ++g) {
        const s16x8 w = CAT8(f.w[g >> 1][0], f.w[g >> 1][1]);
#if !(ABL & 1)
        acc[g] = MODE == 1 ? MFMA32(f.x[g & 1], w, acc[g]) : MFMA32(w, f.x[g & 1], acc[g]);
#else
        { s16x8 x_ = f.x[g & 1]; asm volatile("" :: "v"(w), "v"(x_)); }
#endif
        PIN(acc[g]); SBAR();
        if (NR >= 1 && g < NM / 2) p1_read<MODE>(n0, xa0, w0, w1, wi0, g);
        if (NR >= 2 && g >= NM / 2) p1_read<MODE>(n1, xa1, w0, w1, wi1, g - NM / 2);
        SBAR();
    }
}
struct P1X { lds_ptr a0, a1, a2, a3; };
template <int MODE>
__device__ __forceinline__ void p1_first(P1F<MODE> (&f)[4], const P1X& xa, lds_ptr w0, lds_ptr w1) {
    constexpr int KS = MODE == 2 ? 6144 : 4096;
#pragma unroll
    for (int i = 0; i < 3; ++i) p1_read<MODE>(f[0], xa.a0, w0, w1, 0, i);
#pragma unroll
    for (int i = 0; i < 3; ++i) p1_read<MODE>(f[1], xa.a1, w0, w1, KS, i);
    SBAR();
}
template <int MODE>
__device__ __forceinline__ void p1_lo(P1F<MODE> (&f)[4], f32x16 (&acc)[MODE == 2 ? 6 : 4], const P1X& xa, lds_ptr w0, lds_ptr w1) {
    constexpr int KS = MODE == 2 ? 6144 : 4096;
    p1_step<MODE, 1>(f[0], acc, f[2], xa.a2, 2 * KS, f[2], xa.a2, 0, w0, w1);
    p1_step<MODE, 1>(f[1], acc, f[3], xa.a3, 3 * KS, f[3], xa.a3, 0, w0, w1);
}
template <int MODE>
__device__ __forceinline__ void p1_hi(P1F<MODE> (&f)[4], f32x16 (&acc)[MODE == 2 ? 6 : 4], const P1X& xa, lds_ptr w0, lds_ptr w1) {
    constexpr int KS = MODE == 2 ? 6144 : 4096;
    p1_step<MODE, 1>(f[2], acc, f[0], xa.a0, 0, f[0], xa.a0, 0, w0, w1);
    p1_step<MODE, 1>(f[3], acc, f[1], xa.a1, KS, f[1], xa.a1, 0, w0, w1);
}
__device__ __forceinline__ P1X p1_xaddr(lds_ptr lds, int wid, int lane) {
    const int xrow = (wid & 1) * 64 + (lane & 31), xo = P1_X0 + xrow * 128 + (((lane >> 5) ^ ((xrow >> 1) & 7)) << 4);
    return P1X{lds + xo, lds + (xo ^ 32), lds + (xo ^ 64), lds + (xo ^ 96)};
}
template <int MODE>
__device__ __forceinline__ void p1_five(P1F<MODE> (&f)[4], f32x16 (&acc)[MODE == 2 ? 6 : 4], P1X& xa, lds_ptr& w0, lds_ptr& w1, int& ws3, int wrap, int ps) {
#pragma unroll
    for (int n = 0; n < 5; ++n) {
        const int dw = ws3 == 2 ? -2 * P1_WSLOT : P1_WSLOT, dx = n == wrap ? -4 * P1_XSLOT : P1_XSLOT;
        ws3 = ws3 == 2 ? 0 : ws3 + 1;
        p1_lo<MODE>(f, acc, xa, w0, w1);
        w0 += dw; w1 += dw; xa.a0 += dx; xa.a1 += dx; xa.a2 += dx; xa.a3 += dx;
        asm volatile("" : "+v"(w0), "+v"(w1), "+v"(xa.a0), "+v"(xa.a1), "+v"(xa.a2), "+v"(xa.a3));
        PSTAMP(ps + 2 * n);
        LGKM_BAR();
        PSTAMP(ps + 2 * n + 1);
        p1_hi<MODE>(f, acc, xa, w0, w1);
    }
}

struct Pack { const char *wd, *wo; int job; };
__device__ __forceinline__ void pack_fetch(const Pack& pk_, lds_ptr lds, int wid, int lane) {
    const int step = pk_.job / 10, cb = pk_.job - step * 10, r = lane >> 3;
    const unsigned va = ((r & 3) + 8 * (r >> 2)) * (D_ * 2) + (lane & 7) * 16, vb = ((r >> 2) * 20 + 16 + (r & 3)) * (D_ * 2) + (lane & 7) * 16;
#pragma unroll
    for (int g = 0; g < 4; ++g) {
        const int ks = (step - 48) * 2 + (g >> 1), hh = g & 1;
        const char* a = step < 48 ? pk_.wd + (step * 32 + (g >> 1) * 16 + hh * 4) * (D_ * 2) : ks < 32 ? pk_.wo + (ks * 20 + hh * 4) * (D_ * 2) : pk_.wo + ((ks - 32) * 4 + hh * 2) * 20 * (D_ * 2);
        glds16s(a + cb * 128, step < 64 ? va : vb, (unsigned)(uintptr_t)lds + wid * P1_WSLOT + 16384 + g * 1024);
    }
}
__device__ __forceinline__ void pack_read(s16x8 (&pf)[4], lds_ptr lds, int wid, int lane) {
    asm volatile("s_waitcnt vmcnt(0)" ::: "memory");
    const lds_ptr pr = lds + wid * P1_WSLOT + 16384 + (lane >> 4) * 1024 + ((lane >> 2) & 3) * 128 + (lane & 3) * 8;
#pragma unroll
    for (int nt = 0; nt < 4; ++nt) pf[nt] = CAT8(trread(pr + nt * 32), trread(pr + nt * 32 + 512));
}
__device__ __forceinline__ void pack_store(const Pack& pk_, const s16x8 (&pf)[4], int lane, rsrc_t wsr) {
    const int step = pk_.job / 10, cb = pk_.job - step * 10;
#pragma unroll
    for (int nt = 0; nt < 4; ++nt) {
        const int N = cb * 4 + nt, cg = N / 5;
        ST16(wsr, __builtin_bit_cast(u32x4, pf[nt]), lane * 16, WS_WP + ((cg * 68 + step) * 5 + N - cg * 5) * 1024);
    }
}

template <int MODE>
__device__ __forceinline__ void p1a_compute(f32x16 (&acc)[4], lds_ptr lds, int wid, int lane, const Pack& pk_, s16x8 (&pf)[4]) {
    const int h = lane >> 5, blk = (lane >> 4) & 1, q = (lane >> 2) & 3, fsw = 3 * (q >> 1);
    P1X xa = p1_xaddr(lds, wid, lane);
    const int wb = h * 2048 + (wid >> 1) * 1024 + q * 128 + (lane & 3) * 8;
    lds_ptr w0 = lds + wb + ((blk ^ fsw) << 5), w1 = lds + wb + (((2 + blk) ^ fsw) << 5);
    P1F<MODE> f[4];
    int ws3 = 0;
    PSTAMP(0);
    LGKM_BAR();
    PSTAMP(1);
    p1_first<MODE>(f, xa, w0, w1);
#pragma unroll UNR2
    for (int hf_ = 0; hf_ < 2; ++hf_) {
        p1_five<MODE>(f, acc, xa, w0, w1, ws3, 4, 2 + 10 * hf_);
        if (hf_ == 0) {
            if (pk_.job < 680) pack_read(pf, lds, wid, lane);
        }
    }
}

__device__ __forceinline__ void p1_compute(lds_ptr lds, int wid, int lane, int panel, int grp, int rb, rsrc_t wsr, const Pack& pk_ STAMP_ARG) {
    const int h = lane >> 5, l31 = lane & 31, wm = wid & 1, wn = wid >> 1, tt = panel * 4 + wm * 2;
    const LDS_AS f32x2* rowf = (const LDS_AS f32x2*)(lds + P1_STAT);
    __builtin_amdgcn_s_setprio(CPRIO);
    if (pk_.job < 680) pack_fetch(pk_, lds, wid, lane);
    {
        f32x16 acc[4] = {f32x16{}, f32x16{}, f32x16{}, f32x16{}};
        s16x8 pf[4] = {s16x8{}, s16x8{}, s16x8{}, s16x8{}};
        const LDS_AS float* colf = (const LDS_AS float*)(lds + P1_COLA) + wn * 64;
        const int sec = grp / 5, q0 = (grp - sec * 5) * 32 + wn * 16;
        if (sec < 2) {
            p1a_compute<0>(acc, lds, wid, lane, pk_, pf);
            STAMP(1);
            const int pos = sec ? ((l31 & 19) | ((l31 & 4) << 1) | ((l31 & 8) >> 1)) : l31;
            const int hs = sec ? KV_HEAD : Q_HEAD, ts = sec ? KV_TILE : Q_TILE, so = (sec ? WS_KV : WS_Q) + tt * ts;
            const float qs = sec ? 1.0f : 0.22360679774997896f * 1.4426950408889634f;
            f32x2 nm[2], rr[2];
#pragma unroll
            for (int m = 0; m < 2; ++m) { const f32x2 rs = rowf[wm * 64 + m * 32 + l31]; nm[m] = f32x2{rs[1], rs[1]}; rr[m] = f32x2{rs[0] * qs, rs[0] * qs}; }
            const bool odd = lane & 1;
#pragma unroll
            for (int gam = 0; gam < 8; gam += 2) {
                const int qd = q0 + 2 * (gam + (lane & 1)) + h, hd = (qd * 205) >> 10, vo = hd * hs + (qd - hd * 5) * 256 + (pos & ~1) * 8;
                const f32x4 ca = *(const LDS_AS f32x4*)(colf + gam * 8 + h * 4), cb = *(const LDS_AS f32x4*)(colf + gam * 8 + 8 + h * 4);
#pragma unroll
                for (int m = 0; m < 2; ++m) {
                    const f32x16& a = acc[(gam >> 2) * 2 + m];
                    const int r0 = (gam & 3) * 4;
                    const unsigned a0 = cvt2(pfma(pk4(ca, 0), nm[m], pk(a, r0)) * rr[m]), a1 = cvt2(pfma(pk4(ca, 2), nm[m], pk(a, r0 + 2)) * rr[m]);
                    const unsigned b0 = cvt2(pfma(pk4(cb, 0), nm[m], pk(a, r0 + 4)) * rr[m]), b1 = cvt2(pfma(pk4(cb, 2), nm[m], pk(a, r0 + 6)) * rr[m]);
                    const unsigned g0 = __builtin_amdgcn_update_dpp(0, odd ? a0 : b0, 0xB1, 0xF, 0xF, true), g1 = __builtin_amdgcn_update_dpp(0, odd ? a1 : b1, 0xB1, 0xF, 0xF, true);
                    ST16(wsr, (odd ? u32x4{g0, g1, b0, b1} : u32x4{a0, a1, g0, g1}), vo, so + m * ts);
                }
            }
        } else {
            p1a_compute<1>(acc, lds, wid, lane, pk_, pf);
            STAMP(1);
            if (sec == 2) {
                const int so = WS_KV + tt * KV_TILE + KV_V;
#pragma unroll
                for (int m = 0; m < 2; ++m) {
                    f32x2 rr[8], nm[8];
#pragma unroll
                    for (int g = 0; g < 4; ++g) {
                        const LDS_AS f32x2* rp = rowf + wm * 64 + m * 32 + g * 8 + h * 4;
                        const f32x4 a = *(const LDS_AS f32x4*)rp, b = *(const LDS_AS f32x4*)(rp + 2);
                        rr[2 * g] = f32x2{a[0], a[2]}; nm[2 * g] = f32x2{a[1], a[3]}; rr[2 * g + 1] = f32x2{b[0], b[2]}; nm[2 * g + 1] = f32x2{b[1], b[3]};
                    }
#pragma unroll
                    for (int t = 0; t < 2; ++t) {
                        const int qd = q0 + t * 8 + (l31 >> 2), hd = (qd * 205) >> 10, pl = qd - hd * 5;
                        const int vo = hd * KV_HEAD + pl * 256 + ((h ^ (pl & 3)) << 6) + (l31 & 3) * 16;
                        const float c1 = colf[t * 32 + l31];
                        const f32x2 c2 = {c1, c1};
                        unsigned w[8];
#pragma unroll
                        for (int i = 0; i < 8; ++i) w[i] = cvt2(pfma(nm[i], c2, pk(acc[t * 2 + m], 2 * i)) * rr[i]);
                        ST16(wsr, (u32x4{w[0], w[1], w[2], w[3]}), vo, so + m * KV_TILE);
                        ST16(wsr, (u32x4{w[4], w[5], w[6], w[7]}), vo ^ 128, so + m * KV_TILE);
                    }
                }
                const int hd = q0 / 5 + (lane >> 4);
                const unsigned ov = (lane & 3) ? 0u : 0x3f803f80u;
                if (hd * 5 + 4 < q0 + 16) {
                    ST16(wsr, (u32x4{ov, ov, ov, ov}), hd * KV_HEAD + 5 * 256 + (lane & 15) * 16, so);
                    ST16(wsr, (u32x4{ov, ov, ov, ov}), hd * KV_HEAD + 5 * 256 + (lane & 15) * 16, so + KV_TILE);
                }
            }
        }
        if (pk_.job < 680) pack_store(pk_, pf, lane, wsr);
    }
    STAMP(6);
    f32x16 acc[6];
#pragma unroll
    for (int t = 0; t < 6; ++t) acc[t] = f32x16{};
    {
        const int blk = (lane >> 4) & 1, T0 = wn * 3, T1 = T0 + 1;
        P1X xa = p1_xaddr(lds + rb * P1_XSLOT, wid, lane);
        const int wb = P1_WSLOT + 3 * blk * 1024 + h * 512 + ((lane >> 2) & 3) * 64 + (lane & 3) * 8;
        lds_ptr w0 = lds + wb + (T0 >> 1) * 1024 + (((T0 & 1) ^ blk) << 5), w1 = lds + wb + (T1 >> 1) * 1024 + (((T1 & 1) ^ blk) << 5);
        P1F<2> f[4];
        p1_first<2>(f, xa, w0, w1);
        int ws3 = 1;
#pragma unroll UNR2
        for (int hf_ = 0; hf_ < 2; ++hf_) {
            p1_five<2>(f, acc, xa, w0, w1, ws3, 4 - rb, 22 + 10 * hf_);
            if (!(ABL & 16)) asm volatile("s_waitcnt vmcnt(0)" ::: "memory");
        }
    }
    STAMP(3);
    {
        const LDS_AS float* colf = (const LDS_AS float*)(lds + P1_COLB) + wn * 96;
        const int vo = (l31 >> 4) * 48 * 1024 + (h * 16 + (l31 & 15)) * 16;
        f32x2 nm[2], r2[2];
#pragma unroll
        for (int m = 0; m < 2; ++m) { const f32x2 rs = rowf[wm * 64 + m * 32 + l31]; nm[m] = f32x2{rs[1], rs[1]}; r2[m] = f32x2{rs[0] * rs[0], rs[0] * rs[0]}; }
#pragma unroll
        for (int c = 0; c < 3; ++c) {
            f32x4 cv[4];
#pragma unroll
            for (int g = 0; g < 4; ++g) cv[g] = *(const LDS_AS f32x4*)(colf + c * 32 + g * 8 + h * 4);
            if (c >= 1 && wn) {
#pragma unroll
                for (int g = 0; g < 4; ++g) cv[g] += *(const LDS_AS f32x4*)(colf + 64 + c * 32 + g * 8 + h * 4);
            }
            const int so = WS_H16 + tt * 2 * 48 * 1024 + (grp * 6 + wn * 3 + c) * 512;
#pragma unroll
            for (int m = 0; m < 2; ++m) {
                const f32x16& a = acc[c * 2 + m];
                f32x2 g[4], u[4];
#pragma unroll
                for (int i = 0; i < 4; ++i) { g[i] = pfma(pk4(cv[i >> 1], (i & 1) * 2), nm[m], pk(a, 2 * i)); u[i] = pfma(pk4(cv[2 + (i >> 1)], (i & 1) * 2), nm[m], pk(a, 8 + 2 * i)); }
#pragma unroll
                for (int i = 0; i < 4; ++i) { g[i] = f32x2{fmaxf(g[i][0], 0.f), fmaxf(g[i][1], 0.f)}; u[i] *= r2[m]; }
                ST16(wsr, (u32x4{cvt2(g[0] * u[0]), cvt2(g[1] * u[1]), cvt2(g[2] * u[2]), cvt2(g[3] * u[3])}), vo, so + m * 2 * 48 * 1024);
            }
        }
    }
}

__device__ __forceinline__ void glds_x4(const char* sbase, const unsigned (&v)[4], unsigned lds_base) {
    asm volatile("s_mov_b32 m0, %5\n\ts_nop 0\n\tglobal_load_lds_dwordx4 %0, %4\n\tglobal_load_lds_dwordx4 %1, %4 offset:1024\n\t"
                 "global_load_lds_dwordx4 %2, %4 offset:2048\n\tglobal_load_lds_dwordx4 %3, %4 offset:3072"
                 :: "v"(v[0]), "v"(v[1]), "v"(v[2]), "v"(v[3]), "s"(sbase), "s"(lds_base) : "memory");
}
__device__ __forceinline__ void glds_x3(const char* sbase, const unsigned* v, unsigned lds_base) {
    asm volatile("s_mov_b32 m0, %4\n\ts_nop 0\n\tglobal_load_lds_dwordx4 %0, %3\n\tglobal_load_lds_dwordx4 %1, %3 offset:1024\n\tglobal_load_lds_dwordx4 %2, %3 offset:2048"
                 :: "v"(v[0]), "v"(v[1]), "v"(v[2]), "s"(sbase), "s"(lds_base) : "memory");
}

__device__ __forceinline__ unsigned long long p1_helper(lds_ptr lds, int hw, int lane, const char* xp, const char* wq, const char* wgp, const char* wup, int rb,
                                          char* ws, const char* kv, const char* qsrc) {
    const unsigned lds0 = (unsigned)(uintptr_t)lds;
    const int r = lane >> 3, pos = lane & 7;
    unsigned xv[4], wa[4], wb[6];
#pragma unroll
    for (int j = 0; j < 4; ++j) {
        xv[j] = 4096 + (hw * 32 + j * 8 + r) * (D_ * 2) + ((pos ^ ((j & 1) * 4 + (r >> 1))) << 4) - j * 1024;
        wa[j] = 4096 + (hw * 16 + (j >> 1) * 8 + r) * (3 * D_ * 2) + (j & 1) * 128 + ((pos ^ (6 * ((r >> 1) & 1))) << 4) - j * 1024;
    }
#pragma unroll
    for (int j = 0; j < 6; ++j) wb[j] = 4096 + (hw * 16 + (lane >> 2)) * (F_ * 2) + (j % 3) * 64 + (((lane & 3) ^ (j < 3 ? 0 : 2)) << 4) - (j % 3) * 1024;
    xp -= 4096; wq -= 4096; wgp -= 4096; wup -= 4096;
    int kb_ = 0, xs = 0, dws = 0, left = 5;
    auto issue1 = [&](bool pa) {
        if (ABL & 4) return;
        if (pa) glds_x4(wq + (unsigned)((kb_ + xs) * 64 * 3 * D_ * 2), wa, lds0 + dws * P1_WSLOT + hw * 4096);
        else glds_x3(wgp + (unsigned)((kb_ + xs) * 64 * F_ * 2), wb, lds0 + dws * P1_WSLOT + hw * 6144);
        SBAR();
    };
    auto issue2 = [&](bool pa, bool xx) {
        SBAR();
        if (ABL & 4) return;
        if (!pa) glds_x3(wup + (unsigned)((kb_ + xs) * 64 * F_ * 2), wb + 3, lds0 + dws * P1_WSLOT + hw * 6144 + 3072);
        xs = xs == 4 ? 0 : xs + 1; dws = dws == 2 ? 0 : dws + 1; --left; kb_ = left == 0 ? 5 - kb_ : kb_; left = left == 0 ? 5 : left;
        if (xx) glds_x4(xp + (kb_ + xs) * 128, xv, lds0 + P1_X0 + xs * P1_XSLOT + hw * 4096);
    };
    glds_x4(xp, xv, lds0 + P1_X0 + hw * 4096);
    issue1(true); issue2(true, true);
    __builtin_amdgcn_s_barrier();
    unsigned long long ep = 0;
    asm volatile("global_load_dwordx2 %0, %1, %2 sc1" : "+v"(ep) : "v"(0u), "s"(ws + WS_EPOCH) : "memory");
    __builtin_amdgcn_s_setprio(HPRIO);
    const int r16 = lane & 15, h = lane >> 5, blk = (lane >> 4) & 1, q = (lane >> 2) & 3, fsw = 3 * (q >> 1);
    const unsigned olo = r16 < 2 ? 0x3f803f80u : 0u, ohi = (r16 >> 1) == 1 ? 0x3f803f80u : 0u;
    const s16x8 ones = __builtin_bit_cast(s16x8, (u32x4{olo, olo, ohi, ohi}));
    const int idx = (r16 & 1) ? 0xEEEE : 0x4444;
    int ws3 = 0;
    {
        lds_ptr b = lds + h * 2048 + (hw >> 1) * 1024 + q * 128 + (lane & 3) * 8 + ((((hw & 1) * 2 + blk) ^ fsw) << 5);
        const int so = P1_X0 + (hw * 32 + r16) * 128 + (((lane >> 4) ^ (r16 >> 1)) << 4);
        lds_ptr sp0 = lds + so, sp1 = lds + (so ^ 64);
        const s16x8 ones2 = __builtin_bit_cast(s16x8, (u32x4{olo, olo, olo, olo}));
        f32x4 cs = f32x4{}, s1[2] = {f32x4{}, f32x4{}}, s2[2] = {f32x4{}, f32x4{}};
        s16x16 wv0, wv1; s16x8 xa[2], xb[2];
        auto col_rd = [&]() {
            wv0 = CAT16(CAT8(trread(b), trread(b + 512)), CAT8(trread(b + 4096), trread(b + 4096 + 512)));
            wv1 = CAT16(CAT8(trread(b + 8192), trread(b + 8192 + 512)), CAT8(trread(b + 12288), trread(b + 12288 + 512)));
            b += ws3 == 2 ? -2 * P1_WSLOT : P1_WSLOT; ws3 = ws3 == 2 ? 0 : ws3 + 1;
        };
        auto row_rd = [&](int dx) {
#pragma unroll
            for (int t = 0; t < 2; ++t) { xa[t] = *(const LDS_AS s16x8*)(sp0 + t * 2048); xb[t] = *(const LDS_AS s16x8*)(sp1 + t * 2048); }
            sp0 += dx; sp1 += dx;
        };
        auto row_mm = [&]() {
#pragma unroll
            for (int t = 0; t < 2; ++t) { s2[t] = MFMA16(xa[t], xa[t], s2[t]); s1[t] = SMF16(ones2, CAT16(xa[t], xb[t]), s1[t], idx); }
#pragma unroll
            for (int t = 0; t < 2; ++t) { s2[t] = MFMA16(xb[t], xb[t], s2[t]); PIN(s1[t]); PIN(s2[t]); }
        };
#pragma unroll
        for (int nn = 0; nn < 9; ++nn) {
            int n = nn;
            if (!HUNR) asm volatile("" : "+s"(n));
            PSTAMP(2 * n);
            if (n == 0) WAIT_BAR(5); else if (n < 8) WAIT_BAR(12); else WAIT_BAR(8);
            PSTAMP(2 * n + 1);
            if (!(ABL & 8) || n == 0) { col_rd(); row_rd(n == 4 ? -4 * P1_XSLOT : P1_XSLOT); }
            if (n == 0) { issue1(true); issue2(true, true); }
            if (n == 8) { kb_ = 5; xs = rb; }
            issue1(n < 8);
            if (!(ABL & 8)) {
            cs = SMF16(ones, wv0, cs, idx);
            row_mm();
            cs = SMF16(ones, wv1, cs, idx); PIN(cs); }
            issue2(n < 8, n < 7);
        }
        MEMFENCE(); __builtin_amdgcn_s_waitcnt(0xF70 | 10); MEMFENCE();
        row_rd(0); row_mm();
        {
            const int t = blk;
            const float d0 = (lane & 2) ? ((lane & 1) ? s2[0][3] : s2[0][2]) : ((lane & 1) ? s2[0][1] : s2[0][0]);
            const float d1 = (lane & 2) ? ((lane & 1) ? s2[1][3] : s2[1][2]) : ((lane & 1) ? s2[1][1] : s2[1][0]);
            const float a0 = __shfl(s1[0][0] + s1[0][1], r16), a1 = __shfl(s1[1][0] + s1[1][1], r16), c0 = __shfl(d0, r16 + 16 * q), c1 = __shfl(d1, r16 + 16 * q);
            const float a = t ? a1 : a0, c = t ? c1 : c0;
            const float mean = a * (1.0f / D_), var = fmaxf(c * (1.0f / D_) - mean * mean, 0.f);
            const float rstd = __builtin_amdgcn_rcpf(rbf(__builtin_amdgcn_sqrtf(rbf(rbf(var) + 1e-5f))));
            if (lane < 32) ((LDS_AS f32x2*)(lds + P1_STAT))[hw * 32 + lane] = f32x2{rstd, -rbf(mean)};
        }
        PSTAMP(18);
        WAIT_BAR(6);
        PSTAMP(19);
        col_rd();
        issue1(false);
        cs = SMF16(ones, wv0, cs, idx);
        cs = SMF16(ones, wv1, cs, idx); PIN(cs);
        issue2(false, false);
        LDS_AS float* colf = (LDS_AS float*)(lds + P1_COLA) + hw * 32;
        if (lane < 16) { colf[lane] = cs[0] + cs[1]; colf[16 + lane] = cs[2] + cs[3]; }
    }
    asm volatile("" : "+v"(ep));
    const unsigned long long tag = (((unsigned long long)(unsigned)__builtin_amdgcn_readfirstlane((unsigned)(ep >> 32)) << 32) | (unsigned)__builtin_amdgcn_readfirstlane((unsigned)ep)) + 1ull;
    if (hw == 0 && lane == 0) *(LDS_AS unsigned long long*)(lds + P1_TAG) = tag;
    {
        const int ro = P1_WSLOT + 3 * blk * 1024 + h * 512 + q * 64 + (lane & 3) * 8 + (((hw & 1) ^ blk) << 5);
        lds_ptr b = lds + ro + (hw >> 1) * 1024, c = lds + ro + 2048 + (hw >> 1) * 12288;
        f32x4 cs = f32x4{}, ct = f32x4{};
        Poll pl = {};
#pragma unroll
        for (int nn = 10; nn < 20; ++nn) {
            int n = nn;
            if (!HUNR) asm volatile("" : "+s"(n));
            PSTAMP(2 * n);
            if (n < 13) WAIT_BAR(6); else if (n == 13 || n == 18) WAIT_BAR(10); else if (n == 19) WAIT_BAR(0); else WAIT_BAR(14);
            PSTAMP(2 * n + 1);
            const s16x16 wv0 = CAT16(CAT8(trread(b), trread(b + 256)), CAT8(trread(b + 6144), trread(b + 6144 + 256)));
            const s16x16 wv2 = CAT16(CAT8(trread(c), trread(c + 256)), CAT8(trread(c + 6144), trread(c + 6144 + 256)));
            const s16x16 wv1 = CAT16(CAT8(trread(b + 12288), trread(b + 12288 + 256)), CAT8(trread(b + 18432), trread(b + 18432 + 256)));
            if (n == 16 && hw == 0 && lane == 0) raise_flag(ws, WS_FA, tag);
            if (n < 18) issue1(false);
            if (n == 19) poll_request(pl, ws + WS_FA, lane * 16);
            cs = SMF16(ones, wv0, cs, idx);
            ct = SMF16(ones, wv2, ct, idx);
            cs = SMF16(ones, wv1, cs, idx); PIN(cs); PIN(ct);
            if (n < 18) issue2(false, n >= 12 && n < 17);
            const int dw = ws3 == 2 ? -2 * P1_WSLOT : P1_WSLOT;
            b += dw; c += dw; ws3 = ws3 == 2 ? 0 : ws3 + 1;
        }
        LDS_AS float* colf = (LDS_AS float*)(lds + P1_COLB);
        if (lane < 16) { colf[hw * 32 + lane] = cs[0] + cs[1]; colf[hw * 32 + 16 + lane] = cs[2] + cs[3]; }
        if (lane < 16) { colf[128 + hw * 32 + lane] = ct[0] + ct[1]; colf[128 + hw * 32 + 16 + lane] = ct[2] + ct[3]; }
        LGKM_BAR();
        poll_wait(pl, ws + WS_FA, lane * 16, tag);
        if (blockIdx.x == 0 && hw == 0 && lane == 0) __hip_atomic_store((gu64*)(ws + WS_EPOCH), tag, __ATOMIC_RELAXED, __HIP_MEMORY_SCOPE_AGENT);
#pragma unroll
        for (int j = 0; j < 3; ++j) glds16c(qsrc + (hw + 4 * j) * 1024, lane * 16, lds0 + K2_Q0 + (hw + 4 * j) * 1024);
#pragma unroll
        for (int j = 0; j < 12; ++j) glds16c(kv + (j / 6) * K2_CHUNK + (hw + 4 * (j % 6)) * 1024, lane * 16, lds0 + K2_KV0 + (j / 6) * K2_SLOTB + (hw + 4 * (j % 6)) * 1024);
        MEMFENCE(); __builtin_amdgcn_s_waitcnt(0xF70 | 6);
    }
    return tag;
}

constexpr int K2_XCH = 2 * K2_SLOTB;

#define SMF32(a, b, c) __builtin_amdgcn_smfmac_f32_32x32x32_bf16((bfx8)(a), __builtin_bit_cast(bfx16, (b)), (c), 0xDDDD, 0, 0)
#define MFMA8(a, b, c) __builtin_amdgcn_mfma_f32_32x32x8bf16_1k((a), (b), (c), 0, 0, 0)
__device__ __forceinline__ f32x8 lo8(const f32x16& v) { return __builtin_shufflevector(v, v, 0, 1, 2, 3, 4, 5, 6, 7); }
__device__ __forceinline__ f32x8 hi8(const f32x16& v) { return __builtin_shufflevector(v, v, 8, 9, 10, 11, 12, 13, 14, 15); }
__device__ __forceinline__ float max16(const f32x16& s) {
    float m = fmaxf(fmaxf(s[0], s[1]), s[2]);
#pragma unroll
    for (int r = 3; r < 15; r += 2) m = fmaxf(fmaxf(m, s[r]), s[r + 1]);
    return swap32_max(fmaxf(m, s[15]));
}

struct K2B { lds_ptr r0, r1, r2; const char* wsrc; unsigned wdst; int lane, osoff; rsrc_t wsr; };
constexpr int k2_slot(int st) { return st < 3 ? st : st == 3 ? 0 : st == 4 ? 3 : st == 5 ? 1 : st == 6 ? 2 : st == 7 ? 0 : 3; }
__device__ __forceinline__ void k2_wpiece(const K2B& c, int st, int i) {
    glds16c(c.wsrc + st * 40960 + i * 8192, c.lane * 16, c.wdst + k2_slot(st) * 40960 + i * 8192);
}
template <int I, int D, int NP, int OF>
__device__ __forceinline__ void k2_bstep(const K2B& c, f32x4 (&acc)[10], const s16x8 (&w)[5], s16x8 (&n)[5], const s16x8& a0, const s16x8& a1, s16x8 (&of)[10]) {
    constexpr int O = k2_slot((I + 1) >> 1) * 40960 + ((I + 1) & 1) * 20480;
#pragma unroll
    for (int i = 0; i < 10; ++i) {
        if (I > 0 && I < 16 && i < 5) n[i] = *(const LDS_AS s16x8*)((O < 61440 ? c.r0 : O < 122880 ? c.r1 : c.r2) + O % 61440 + i * 1024);
        acc[i] = MFMA16(w[i >> 1], (i & 1) ? a1 : a0, acc[i]);
        PIN(acc[i]);
        if (OF >= 0 && !(i & 1)) { const int k = (OF < 0 ? 0 : OF) + (i >> 1); of[k] = LD16(c.wsr, c.lane * 16, c.osoff + ((k & 1) * 20 + (k >> 1) * 4) * 1024); MEMFENCE(); }
        if ((i & 1) && (i >> 1) < NP) k2_wpiece(c, D, i >> 1);
        SBAR();
    }
}

__global__ __launch_bounds__(512, 2) void fused_block(const char* __restrict__ x, const char* __restrict__ wqkv, const char* __restrict__ wg, const char* __restrict__ wu,
                                                      const char* __restrict__ wd, const char* __restrict__ wo, char* __restrict__ ws, char* __restrict__ out) {
    extern __shared__ __attribute__((aligned(16))) char smem[];
    asm volatile("" :: "s"(x), "s"(wqkv), "s"(wg), "s"(wu), "s"(wd), "s"(wo), "s"(ws), "s"(out));
    const int wid = __builtin_amdgcn_readfirstlane(threadIdx.x >> 6);
    STAMP_DECL; STAMP(0); PSTAMP(63);
    unsigned long long tag = 0;
    if (wid < 4) {
        __builtin_amdgcn_s_barrier();
        if (CODEWARM && wid == 3) {
            unsigned off = ((blockIdx.x >> 3) * 12 + min(threadIdx.x & 63, 11)) * 128;
            asm volatile("s_getpc_b64 vcc\n.Lfb_begin:\n\tv_min_u32 %0, .Lfb_end-.Lfb_begin, %0\n\ts_mov_b32 m0, %1\n\ts_nop 0\n\tglobal_load_lds_dword %0, vcc"
                         : "+v"(off) : "s"((unsigned)(uintptr_t)(lds_ptr)smem + LDS_JUNK) : "vcc", "memory");
        }
    }
    const rsrc_t wsr = __builtin_amdgcn_make_buffer_rsrc(ws, 0, (int)WS_END, 0x00020000);
    const int bid = blockIdx.x, head = (bid & 7) * 4 + ((bid >> 3) & 3), par = wid >> 2, qt0 = (bid >> 5) * 8 + (wid & 3) * 2;
    const char* kv = ws + WS_KV + head * KV_HEAD;
    {
        const int bi = bid >> 3, panel = (bid & 1) * 8 + (bi & 7), grp = ((bid & 7) >> 1) * 4 + (bi >> 3), l0 = threadIdx.x & 63;
        const int rb = K1_ROT ? (5 * (bi & 7)) >> 3 : 0;
        if (wid < 4) {
            p1_compute((lds_ptr)smem, wid, l0, panel, grp, rb, wsr, Pack{wd, wo, wid * 256 + bid} STAMP_PASS);
            const u32x2 e = *(const LDS_AS u32x2*)((lds_ptr)smem + P1_TAG);
            tag = ((unsigned long long)(unsigned)__builtin_amdgcn_readfirstlane(e[1]) << 32) | (unsigned)__builtin_amdgcn_readfirstlane(e[0]);
        } else {
            tag = p1_helper((lds_ptr)smem, wid - 4, l0, x + (size_t)panel * 128 * (D_ * 2), wqkv + min(grp, 14) * 256, wg + grp * 192, wu + grp * 192, rb,
                            ws, kv, ws + WS_Q + head * Q_HEAD + (bid >> 5) * 8 * Q_TILE);
        }
    }
    int lane = __builtin_amdgcn_mbcnt_hi(~0u, __builtin_amdgcn_mbcnt_lo(~0u, 0u));
    PIN(lane);
    __builtin_amdgcn_s_setprio(2);
    LGKM_BAR();
    STAMP(4);
    lds_ptr lds = (lds_ptr)smem + K2_KV0;
    const unsigned lds0 = (unsigned)(uintptr_t)lds;
    const int tp = ((blockIdx.x & 7) >> 1) * 8 + ((blockIdx.x >> 3) & 7), cg = (blockIdx.x & 1) * 4 + (blockIdx.x >> 6);
    K2B kb;
    kb.wsrc = ws + WS_WP + cg * 68 * 5120 + wid * 1024; kb.wdst = (unsigned)(uintptr_t)(lds_ptr)smem + wid * 1024; kb.lane = lane;
    const int hoff = WS_H16 + ((tp * 4 + (wid & 1) * 2) * 48 + (wid >> 1)) * 1024;
    s16x8 hf[24];
    const int h = lane >> 5, l31 = lane & 31, l16 = lane * 16;
    const char* src = kv + wid * 1024 + 2 * K2_CHUNK;
#pragma unroll
    for (int i = 0; i < 3; ++i) glds16c(src + i * 8192, l16, lds0 + 2 * K2_SLOTB + wid * 1024 + i * 8192);
    src += K2_CHUNK;
    const int k1off = h * 512 + l31 * 8, k2off = h ? KV_V + 5 * 256 : 1024 + l31 * 8;
    const int vq = min(l31 >> 2, 5), voff = KV_V + vq * 256 + ((h ^ (vq & 3)) << 6) + (l31 < 24 ? l31 & 3 : 1) * 16;

    f32x16 oa[2] = {f32x16{}, f32x16{}}, sc[2];
    s16x8 k1[2], v0[2], v1[2]; s16x4 k2[2];
    lds_ptr pk1 = lds + k1off + par * KV_TILE, pk2 = lds + k2off + par * KV_TILE, pv = lds + voff + par * KV_TILE, pw = lds + (voff ^ 128) + par * KV_TILE;
    lds_ptr pk3 = pk1 + 256;
    asm volatile("" : "+v"(pk3));
#define LDS8(p) (*(const LDS_AS s16x4*)(p))
#define RD_K(t, j) do { k1[(t) & 1] = CAT8(LDS8(pk1 + (j) * 2 * KV_TILE), LDS8(pk3 + (j) * 2 * KV_TILE)); k2[(t) & 1] = LDS8(pk2 + (j) * 2 * KV_TILE); } while (0)
#define RD_V(t, j) do { v0[(t) & 1] = *(const LDS_AS s16x8*)(pv + (j) * 2 * KV_TILE); v1[(t) & 1] = *(const LDS_AS s16x8*)(pw + (j) * 2 * KV_TILE); } while (0)
    s16x8 q1[2]; s16x4 q2[2];
#pragma unroll
    for (int b = 0; b < 2; ++b) {
        const lds_ptr qp = (lds_ptr)smem + K2_Q0 + ((wid & 3) * 2 + b) * Q_TILE;
        q1[b] = CAT8(LDS8(qp + k1off), LDS8(qp + k1off + 256)); q2[b] = LDS8(qp + 1024 + l31 * 8);
    }
    {
        const s16x8 ka = CAT8(LDS8(lds + k1off), LDS8(lds + k1off + 256));
        const s16x4 kb = LDS8(lds + k2off);
        RD_K(0, 0); RD_V(0, 0);
#pragma unroll
        for (int b = 0; b < 2; ++b) {
            if (h) q2[b] = s16x4{0, 0, 0, 0};
            f32x16 s = MFMA32(ka, q1[b], f32x16{});
            s = MFMA8(kb, q2[b], s);
            const float m = rbf(-max16(s));
            if (h) q2[b][0] = (short)(__float_as_uint(m) >> 16);
        }
        sc[0] = MFMA32(k1[0], q1[0], f32x16{});
        sc[0] = MFMA8(k2[0], q2[0], sc[0]);
        sc[1] = f32x16{}; v1[1] = s16x8{};
    }
    STAMP(5); PSTAMP(42); PSTAMP(43);
#define EXP4(S, i) do { S[i] = __builtin_amdgcn_exp2f(S[i]); S[i + 1] = __builtin_amdgcn_exp2f(S[i + 1]); S[i + 2] = __builtin_amdgcn_exp2f(S[i + 2]); S[i + 3] = __builtin_amdgcn_exp2f(S[i + 3]); PIN(S); SBAR(); } while (0)
    Poll pl = {};
    auto request = [&](int hs) {
        const int p = (hs - 6) >> 3, r = (hs - 6) & 7;
        const int w = hs < 3 ? hs : (hs >= 9 && hs < 14) ? hs - 6 : (hs == 17 || hs == 18) ? hs - 9 : (hs >= 54 && hs < 57) ? hs - 44 : hs >= 62 ? hs - 49 : -1;
        const int e = (p >= 2 && p <= 4 && r >= 3) ? (p - 2) * 5 + r - 3 : p == 5 ? 15 + r : hs == 57 ? 23 : -1;
        if (hs >= 6 && p < 5 && r < 3) glds16c(src + p * K2_CHUNK + r * 8192, l16, lds0 + (p % 3) * K2_SLOTB + wid * 1024 + r * 8192);
        else if (w >= 0) glds16c(kb.wsrc + w * 8192, l16, kb.wdst + w * 8192);
        else if (e >= 0) { hf[e] = LD16(wsr, l16, hoff + e * 4096); MEMFENCE(); }
        if (hs == 6 && wid == 4 && lane == 0) raise_flag(ws, WS_FB, tag);
        if (hs == 19 && wid == 4) poll_request(pl, ws + WS_FB, l16);
        SBAR();
    };
#pragma unroll
    for (int c = 0; c < 8; ++c) {
#pragma unroll
        for (int j = 0; j < 4; ++j) {
            if (j == 3) {
                PSTAMP(44 + 2 * c);
                if (c == 2 && wid == 4) poll_wait(pl, ws + WS_FB, l16, tag);
                if (c == 0) { asm volatile("s_waitcnt vmcnt(0)" ::: "memory"); LGKM_BAR(); } else if (c == 7) WAIT_BAR(0); else WAIT_BAR(7);
                PSTAMP(45 + 2 * c);
                __builtin_amdgcn_s_setprio(2);
                const int dsl = (c % 3 == 2) ? -2 * K2_SLOTB : K2_SLOTB;
                pk1 += dsl; pk2 += dsl; pk3 += dsl; pv += dsl; pw += dsl;
                asm volatile("" : "+v"(pk1), "+v"(pk2), "+v"(pk3), "+v"(pv), "+v"(pw));
                SBAR();
            }
            RD_K(j + 1, (j + 1) & 3); SBAR();
            oa[1] = SMF32(v1[(j + 1) & 1], hi8(sc[1]), oa[1]); PIN(oa[1]); SBAR();
            EXP4(sc[0], 0);
            sc[1] = MFMA32(k1[j & 1], q1[1], f32x16{}); PIN(sc[1]); SBAR();
            request(8 * c + 2 * j);
            EXP4(sc[0], 4);
            sc[1] = MFMA8(k2[j & 1], q2[1], sc[1]); PIN(sc[1]); SBAR();
            EXP4(sc[0], 8);
            oa[0] = SMF32(v0[j & 1], lo8(sc[0]), oa[0]); PIN(oa[0]); SBAR();
            EXP4(sc[0], 12);
            RD_V(j + 1, (j + 1) & 3);
            if (j == 1) __builtin_amdgcn_s_setprio(1);
            if (j == 2) __builtin_amdgcn_s_setprio(0);
            SBAR();
            oa[0] = SMF32(v1[j & 1], hi8(sc[0]), oa[0]); PIN(oa[0]); SBAR();
            EXP4(sc[1], 0);
            sc[0] = MFMA32(k1[(j + 1) & 1], q1[0], f32x16{}); PIN(sc[0]); SBAR();
            request(8 * c + 2 * j + 1);
            EXP4(sc[1], 4);
            sc[0] = MFMA8(k2[(j + 1) & 1], q2[0], sc[0]); PIN(sc[0]); SBAR();
            EXP4(sc[1], 8);
            oa[1] = SMF32(v0[j & 1], lo8(sc[1]), oa[1]); PIN(oa[1]); SBAR();
            EXP4(sc[1], 12);
        }
    }
    oa[1] = SMF32(v1[1], hi8(sc[1]), oa[1]);
    unsigned qs_ = 0; (void)qs_;
    STAMP(2);
    PSTAMP(60); QSTAMP(60);
    int ln = __builtin_amdgcn_mbcnt_hi(~0u, __builtin_amdgcn_mbcnt_lo(~0u, 0u));
    PIN(ln);
    const int h2 = ln >> 5, l2 = ln & 31;
    const int mt = wid & 1, kq = wid >> 1;
    kb.lane = ln; kb.r0 = (lds_ptr)smem + kq * 5120 + ln * 16; kb.r1 = kb.r0 + 61440; kb.r2 = kb.r0 + 122880;
    asm volatile("" : "+v"(kb.r1), "+v"(kb.r2));
    kb.wsr = wsr; kb.osoff = WS_O16 + ((tp * 4 + mt * 2) * 20 + kq) * 1024;
    s16x8 wa[5], wb[5];
#pragma unroll
    for (int i = 0; i < 5; ++i) { wa[i] = *(const LDS_AS s16x8*)(kb.r0 + i * 1024); wb[i] = *(const LDS_AS s16x8*)(kb.r0 + 20480 + i * 1024); }
    {
        const lds_ptr xp_ = lds + K2_XCH + (wid & 3) * 6144 + ln * 16;
        if (par) {
#pragma unroll
            for (int i = 0; i < 6; ++i) { const f32x16& g = oa[i / 3]; const int r = 4 * (i % 3); *(LDS_AS f32x4*)(xp_ + i * 1024) = f32x4{g[r], g[r + 1], g[r + 2], g[r + 3]}; }
        }
        LGKM_BAR();
        QSTAMP(21);
        if (!par) {
            __builtin_amdgcn_s_setprio(3);
            f32x4 tt[6];
#pragma unroll
            for (int i = 0; i < 6; ++i) tt[i] = *(const LDS_AS f32x4*)(xp_ + i * 1024);
            SBAR();
#pragma unroll
            for (int i = 0; i < 6; ++i) { const f32x4 t = tt[i]; f32x16& g = oa[i / 3]; const int r = 4 * (i % 3); g[r] = add1(g[r], t[0]); g[r + 1] = add1(g[r + 1], t[1]); g[r + 2] = add1(g[r + 2], t[2]); g[r + 3] = add1(g[r + 3], t[3]); }
            const int ob = (l2 >> 4) * 20 * 1024 + (l2 & 15) * 16, os = WS_O16 + qt0 * 2 * 20 * 1024;
            const int o1 = ob + h2 * 256 + os + (head >> 1) * 1024 + (head & 1) * 512, o2 = os + (16 + (head >> 3)) * 1024 + ((head >> 1) & 3) * 256 + (head & 1) * 8;
            auto put = [&](const f32x16& o, int b, u32x2& e) {
                const float l = __uint_as_float(__builtin_amdgcn_permlane32_swap(__float_as_uint(o[8]), __float_as_uint(o[8]), false, false)[1]);
                const float rl = __builtin_amdgcn_rcpf(l);
                unsigned w[6];
#pragma unroll
                for (int i = 0; i < 6; ++i) w[i] = cvtpk(mul1(o[2 * i], rl), mul1(o[2 * i + 1], rl));
                ST16(wsr, (u32x4{w[0], w[1], w[2], w[3]}), o1, b * 40 * 1024);
                e = u32x2{w[4], w[5]};
                return l;
            };
            u32x2 e0, e1;
            QSTAMP(22);
            const float l0 = put(oa[0], 0, e0), l1 = put(oa[1], 1, e1);
            ST8(wsr, (u32x2{__builtin_amdgcn_permlane32_swap(e0[0], e1[0], false, false)[0], __builtin_amdgcn_permlane32_swap(e0[1], e1[1], false, false)[0]}), ob + h2 * 40 * 1024, o2);
            MEMFENCE(); SBAR();
            QSTAMP(23);
            float big = 0.f;
#pragma unroll
            for (int r = 0; r < 12; ++r) big = add1(big, add1(fabsf(oa[0][r]), fabsf(oa[1][r])));
            if (__builtin_expect(__any(!(big < 1e30f) | !(fminf(l0, l1) > 1e-30f)), 0)) {
                const int g1 = h2 * 512 + l2 * 8, g2 = 1024 + l2 * 8, gq = min(l2 >> 2, 5), gv = KV_V + gq * 256 + ((h2 ^ (gq & 3)) << 6) + (l2 & 3) * 16;
                asm volatile("s_waitcnt vmcnt(0)" ::: "memory");
#pragma unroll 1
                for (int b = 0; b < 2; ++b) {
                    const s16x8 qf1 = b ? q1[1] : q1[0];
                    s16x4 qf2 = b ? q2[1] : q2[0];
                    if (h2) qf2[0] = 0;
                    float m = -INFINITY;
                    f32x16 negm, o = f32x16{};
#pragma unroll 1
                    for (int t = 0; t < 64; ++t) {
                        const int g = WS_KV + head * KV_HEAD + t * KV_TILE;
                        f32x16 s = MFMA32(CAT8(LD8(wsr, g1, g), LD8(wsr, g1 + 256, g)), qf1, f32x16{});
                        s = MFMA8(LD8(wsr, g2, g), qf2, s);
                        m = fmaxf(m, max16(s));
                    }
#pragma unroll
                    for (int r = 0; r < 16; ++r) negm[r] = -m;
#pragma unroll 1
                    for (int t = 0; t < 64; ++t) {
                        const int g = WS_KV + head * KV_HEAD + t * KV_TILE;
                        f32x16 s = MFMA32(CAT8(LD8(wsr, g1, g), LD8(wsr, g1 + 256, g)), qf1, negm);
                        s = MFMA8(LD8(wsr, g2, g), qf2, s);
#pragma unroll
                        for (int r = 0; r < 16; ++r) s[r] = __builtin_amdgcn_exp2f(s[r]);
                        o = SMF32(LD16(wsr, gv, g), lo8(s), o);
                        o = SMF32(LD16(wsr, gv ^ 128, g), hi8(s), o);
                    }
                    u32x2 e;
                    put(o, b, e);
                    if (!h2) ST8(wsr, e, ob + b * 40 * 1024, o2);
                }
            }
        }
    }
    PSTAMP(61); QSTAMP(61);
    if (DIAG_PF2) { PFLUSH(); }
    __builtin_amdgcn_s_setprio(0);
    const int ch0 = wid * 128 + ln, ch1 = ch0 + 64, er0 = ch0 / 10, er1 = ch1 / 10, ec0 = ch0 - er0 * 10, ec1 = ch1 - er1 * 10;
    const size_t eb = (size_t)tp * 64 * (D_ * 2) + cg * 160;
    const unsigned eo0 = er0 * (D_ * 2) + ec0 * 16, eo1 = er1 * (D_ * 2) + ec1 * 16;
    f32x4 acc[10];
#pragma unroll
    for (int i = 0; i < 10; ++i) acc[i] = f32x4{};
    s16x8 of[10];
    QSTAMP(0); k2_bstep<0, 0, 0, -1>(kb, acc, wa, wb, hf[0], hf[12], of);
    QSTAMP(1); k2_bstep<1, 0, 0, -1>(kb, acc, wb, wa, hf[1], hf[13], of);
    QSTAMP(2); k2_bstep<2, 0, 0, -1>(kb, acc, wa, wb, hf[2], hf[14], of);
    if (!par) {
        asm volatile("s_waitcnt vmcnt(0)" ::: "memory");
        if (ln == 0) __hip_atomic_store((gu64*)(ws + WS_FC) + (qt0 >> 1) * 32 + head, tag, __ATOMIC_RELAXED, __HIP_MEMORY_SCOPE_AGENT);
    } else {
#pragma unroll
        for (int i = 0; i < 10; ++i) glds16c(kb.wsrc + 3 * 40960 - 4096 + i * 4096, ln * 16, kb.wdst - 4096 + i * 4096);
    }
    WAIT_BAR(10);
    QSTAMP(3); k2_bstep<3, 4, 5, -1>(kb, acc, wb, wa, hf[3], hf[15], of);
    QSTAMP(4); k2_bstep<4, 5, 5, -1>(kb, acc, wa, wb, hf[4], hf[16], of);
    WAIT_BAR(10);
    QSTAMP(5); k2_bstep<5, 6, 5, -1>(kb, acc, wb, wa, hf[5], hf[17], of);
    unsigned long long seen = 0;
    const char* cw = ws + WS_FC + tp * 256;
    const unsigned cvo = l2 * 8;
    if (wid == 4) asm volatile("global_load_dwordx2 %0, %1, %2 sc1" : "+v"(seen) : "v"(cvo), "s"(cw) : "memory");
    SBAR();
    QSTAMP(6); k2_bstep<6, 0, 0, -1>(kb, acc, wa, wb, hf[6], hf[18], of);
    if (wid == 4) {
        asm volatile("s_waitcnt vmcnt(0)" : "+v"(seen) :: "memory");
#pragma unroll 1
        for (unsigned spin = 0; spin < (1u << 18) && !__all(seen == tag); ++spin) {
            __builtin_amdgcn_s_sleep(1);
            asm volatile("global_load_dwordx2 %0, %1, %2 sc1\n\ts_waitcnt vmcnt(0)" : "=&v"(seen) : "v"(cvo), "s"(cw) : "memory");
        }
    }
    WAIT_BAR(10);
    QSTAMP(7); k2_bstep<7, 0, 0, 0>(kb, acc, wb, wa, hf[7], hf[19], of);
    QSTAMP(8); k2_bstep<8, 0, 0, 5>(kb, acc, wa, wb, hf[8], hf[20], of);
    WAIT_BAR(15);
    QSTAMP(9); k2_bstep<9, 7, 5, -1>(kb, acc, wb, wa, hf[9], hf[21], of);
    QSTAMP(10); k2_bstep<10, 8, 3, -1>(kb, acc, wa, wb, hf[10], hf[22], of);
    WAIT_BAR(8);
    QSTAMP(11); k2_bstep<11, 0, 0, -1>(kb, acc, wb, wa, hf[11], hf[23], of);
    QSTAMP(12); k2_bstep<12, 0, 0, -1>(kb, acc, wa, wb, of[0], of[1], of);
    WAIT_BAR(3);
    QSTAMP(13); k2_bstep<13, 0, 0, -1>(kb, acc, wb, wa, of[2], of[3], of);
    QSTAMP(14); k2_bstep<14, 0, 0, -1>(kb, acc, wa, wb, of[4], of[5], of);
    WAIT_BAR(0);
    u32x4 xv0 = u32x4{}, xv1 = u32x4{};
    if (wid < 5) { xv0 = *(const u32x4*)(x + eb + eo0); xv1 = *(const u32x4*)(x + eb + eo1); }
    MEMFENCE(); SBAR();
    QSTAMP(15); k2_bstep<15, 0, 0, -1>(kb, acc, wb, wa, of[6], of[7], of);
    QSTAMP(16); k2_bstep<16, 0, 0, -1>(kb, acc, wa, wb, of[8], of[9], of);
    QSTAMP(17);
    {
        const int tk = ln & 15;
        const lds_ptr pp = (lds_ptr)smem + kq * 20480 + (mt * 32 + tk) * 320 + (((ln >> 4) ^ ((tk >> 1) & 3)) << 4);
#pragma unroll
        for (int i = 0; i < 10; ++i) *(LDS_AS f32x4*)(pp + (i & 1) * 16 * 320 + (i >> 1) * 64) = acc[i];
        QSTAMP(18);
        LGKM_BAR();
        QSTAMP(19);
        if (wid < 5) {
#pragma unroll
            for (int e = 0; e < 2; ++e) {
                const int er = e ? er1 : er0, ec = e ? ec1 : ec0, sw = (er >> 1) & 3;
                const lds_ptr p0 = (lds_ptr)smem + er * 320 + (((2 * ec) ^ sw) << 4), p1 = (lds_ptr)smem + er * 320 + (((2 * ec + 1) ^ sw) << 4);
                f32x4 a = *(const LDS_AS f32x4*)p0, b = *(const LDS_AS f32x4*)p1;
#pragma unroll
                for (int k = 1; k < 4; ++k) { a += *(const LDS_AS f32x4*)(p0 + k * 20480); b += *(const LDS_AS f32x4*)(p1 + k * 20480); }
                const u32x4 xv = e ? xv1 : xv0;
                *(u32x4*)(out + eb + (e ? eo1 : eo0)) = u32x4{cvtpk(a[0] + bf_lo(xv[0]), a[1] + bf_hi(xv[0])), cvtpk(a[2] + bf_lo(xv[1]), a[3] + bf_hi(xv[1])),
                                                              cvtpk(b[0] + bf_lo(xv[2]), b[1] + bf_hi(xv[2])), cvtpk(b[2] + bf_lo(xv[3]), b[3] + bf_hi(xv[3]))};
            }
        }
    }
    QSTAMP(20);
    if (CODEWARM) asm volatile(".Lfb_end:");
    STAMP(7); STAMP_FLUSH(0); if (!DIAG_PF2) { PFLUSH(); }
}

extern "C" void kernel_launch(void* const* d_in, const int* in_sizes, int n_in, void* d_out, int out_size, void* d_ws, size_t ws_size,
                              hipStream_t stream) {
    const char* x = (const char*)d_in[0];
    const char* wqkv = (const char*)d_in[1];
    const char* wo = (const char*)d_in[2];
    const char* wg = (const char*)d_in[4];
    const char* wu = (const char*)d_in[5];
    const char* wd = (const char*)d_in[6];
    char* ws = (char*)d_ws;
    (void)hipFuncSetAttribute((const void*)fused_block, hipFuncAttributeMaxDynamicSharedMemorySize, LDS_BYTES);
    fused_block<<<256, 512, LDS_BYTES, stream>>>(x, wqkv, wg, wu, wd, wo, ws, (char*)d_out);
}
```

```cpp
#include <hip/hip_runtime.h>
#include <hip/hip_bf16.h>
#include <stdint.h>

typedef __bf16 bfx4 __attribute__((ext_vector_type(4)));
typedef __bf16 bfx8 __attribute__((ext_vector_type(8)));
typedef __bf16 bfx16 __attribute__((ext_vector_type(16)));
typedef short s16x4 __attribute__((ext_vector_type(4)));
typedef short s16x8 __attribute__((ext_vector_type(8)));
typedef short s16x16 __attribute__((ext_vector_type(16)));
typedef float f32x2 __attribute__((ext_vector_type(2)));
typedef float f32x4 __attribute__((ext_vector_type(4)));
typedef float f32x8 __attribute__((ext_vector_type(8)));
typedef float f32x16 __attribute__((ext_vector_type(16)));
typedef unsigned u32x2 __attribute__((ext_vector_type(2)));
typedef unsigned u32x4 __attribute__((ext_vector_type(4)));
typedef unsigned u32x8 __attribute__((ext_vector_type(8)));
#define LDS_AS __attribute__((address_space(3)))
typedef LDS_AS char* lds_ptr;

#define ABL 0
#define DPRIO 1
#define CODEWARM 1
#define EVEN 1
#define HPRIO 3
#define CPRIO 0
#define WPWARM 0
#define UNR2 2
#define HUNR 1
#define FA_POS 14
#define ROTA 0
#define K1_ROT 1
constexpr int S_ = 2048, D_ = 640, H_ = 32, F_ = 1536;

constexpr int Q_TILE = 1280, Q_HEAD = 64 * Q_TILE, KV_TILE = 2816, KV_V = 1280, KV_HEAD = 64 * KV_TILE;
constexpr int WS_Q = 0;
constexpr int WS_KV = WS_Q + H_ * Q_HEAD;
constexpr int WS_O16 = WS_KV + H_ * KV_HEAD + 8192;
constexpr int WS_H16 = WS_O16 + 128 * 20 * 1024;
constexpr int WS_WP = WS_H16 + 128 * 48 * 1024;
constexpr int WS_EPOCH = WS_WP + 8 * 68 * 5120 + 8192;
constexpr int WS_FA = WS_EPOCH + 256, WS_FB = WS_FA + 2048, WS_FC = WS_FB + 2048, WS_END = WS_FC + 8192;

#define PSTAMP(i) do {} while (0)
#define PFLUSH() do {} while (0)
#define DIAG_PF2 0
#define QSTAMP(i) do {} while (0)
#define STAMP_ARG
#define STAMP_PASS
#define STAMP_DECL do {} while (0)
#define STAMP(i) do {} while (0)
#define STAMP_FLUSH(kern) do {} while (0)
#define SBAR() __builtin_amdgcn_sched_barrier(0)
#define MFMA32(a, b, c) __builtin_amdgcn_mfma_f32_32x32x16_bf16((bfx8)(a), (bfx8)(b), (c), 0, 0, 0)

__device__ __forceinline__ unsigned cvtpk(float lo, float hi) {
    typedef __bf16 bfx2 __attribute__((ext_vector_type(2)));
    bfx2 r = {(__bf16)lo, (__bf16)hi};
    return __builtin_bit_cast(unsigned, r);
}
__device__ __forceinline__ void glds16(const void* g, unsigned lds_base) {
    unsigned sv;
    asm volatile("s_mov_b32 %0, m0\n\ts_mov_b32 m0, %2\n\ts_nop 0\n\tglobal_load_lds_dwordx4 %1, off\n\ts_mov_b32 m0, %0"
                 : "=&s"(sv) : "v"(g), "s"(lds_base) : "memory");
}
__device__ __forceinline__ s16x4 trread(const LDS_AS char* p) {
    return __builtin_amdgcn_ds_read_tr16_b64_v4i16((LDS_AS s16x4*)p);
}
__device__ __forceinline__ float rbf(float v) { return (float)(__bf16)v; }
__device__ __forceinline__ float bf_lo(unsigned u) { return __uint_as_float(u << 16); }
__device__ __forceinline__ float bf_hi(unsigned u) { return __uint_as_float(u & 0xffff0000u); }
__device__ __forceinline__ float swap32_add(float v) {
    auto r = __builtin_amdgcn_permlane32_swap(__float_as_uint(v), __float_as_uint(v), false, false);
    return __uint_as_float(r[0]) + __uint_as_float(r[1]);
}
__device__ __forceinline__ float swap32_max(float v) {
    auto r = __builtin_amdgcn_permlane32_swap(__float_as_uint(v), __float_as_uint(v), false, false);
    return fmaxf(__uint_as_float(r[0]), __uint_as_float(r[1]));
}

constexpr int P1_WSLOT = 24576, P1_X0 = 3 * P1_WSLOT, P1_XSLOT = 16384, P1_STAT = P1_X0 + 5 * P1_XSLOT;
constexpr int P1_COLA = P1_STAT + 1024, P1_COLB = P1_COLA + 512, P1_TAG = P1_COLB + 1024, LDS_JUNK = 161792, LDS_BYTES = 163840;
constexpr int K2_CHUNK = 8 * KV_TILE, K2_SLOTB = 24576, K2_SLOTS = 3, K2_KV0 = 81920, K2_C0 = 2 * P1_WSLOT, K2_Q0 = 0;
typedef __attribute__((address_space(1))) unsigned long long gu64;

#define PIN(x) asm volatile("" : "+v"(x))
#define MEMFENCE() asm volatile("" ::: "memory")
#define WAIT_BAR(N) do { MEMFENCE(); __builtin_amdgcn_s_waitcnt(0x70 | ((N) & 15) | (((N) >> 4) << 14)); __builtin_amdgcn_s_barrier(); MEMFENCE(); SBAR(); } while (0)
#define LGKM_BAR() do { MEMFENCE(); __builtin_amdgcn_s_waitcnt(0xC07F); __builtin_amdgcn_s_barrier(); MEMFENCE(); SBAR(); } while (0)
#define MFMA16(a, b, c) __builtin_amdgcn_mfma_f32_16x16x32_bf16((bfx8)(a), (bfx8)(b), (c), 0, 0, 0)
#define SMF16(a, b, c, i) __builtin_amdgcn_smfmac_f32_16x16x64_bf16((bfx8)(a), (bfx16)(b), (c), (i), 0, 0)
#define CAT8(a, b) __builtin_shufflevector(a, b, 0, 1, 2, 3, 4, 5, 6, 7)
#define CAT16(a, b) __builtin_shufflevector(a, b, 0, 1, 2, 3, 4, 5, 6, 7, 8, 9, 10, 11, 12, 13, 14, 15)

__device__ __forceinline__ void glds16s(const char* sbase, unsigned voff, unsigned lds_base) {
    unsigned sv;
    asm volatile("s_mov_b32 %0, m0\n\ts_mov_b32 m0, %3\n\ts_nop 0\n\tglobal_load_lds_dwordx4 %1, %2\n\ts_mov_b32 m0, %0"
                 : "=&s"(sv) : "v"(voff), "s"(sbase), "s"(lds_base) : "memory");
}

__device__ __forceinline__ void glds16c(const char* sbase, unsigned voff, unsigned lds_base) {
    asm volatile("s_mov_b32 m0, %2\n\ts_nop 0\n\tglobal_load_lds_dwordx4 %0, %1 sc1" :: "v"(voff), "s"(sbase), "s"(lds_base) : "memory");
}
typedef __amdgpu_buffer_rsrc_t rsrc_t;
#define ST16(rs, v, voff, soff) __builtin_amdgcn_raw_buffer_store_b128((v), (rs), (voff) + (soff), 0, (ABL & 32) ? 0 : 16)
#define ST8(rs, v, voff, soff) __builtin_amdgcn_raw_buffer_store_b64((v), (rs), (voff), (soff), 16)
#define LD16(rs, voff, soff) __builtin_bit_cast(s16x8, __builtin_amdgcn_raw_buffer_load_b128((rs), (voff), (soff), 16))
#define LD8(rs, voff, soff) __builtin_bit_cast(s16x4, __builtin_amdgcn_raw_buffer_load_b64((rs), (voff), (soff), 16))
__device__ __forceinline__ f32x2 pk(const f32x16& a, int i) { return f32x2{a[i], a[i + 1]}; }
__device__ __forceinline__ f32x2 pk4(const f32x4& a, int i) { return f32x2{a[i], a[i + 1]}; }
__device__ __forceinline__ f32x2 pfma(f32x2 a, f32x2 b, f32x2 c) { return __builtin_elementwise_fma(a, b, c); }
__device__ __forceinline__ unsigned cvt2(f32x2 v) { return cvtpk(v[0], v[1]); }
__device__ __forceinline__ float add1(float a, float b) { float r = a + b; asm volatile("" : "+v"(r)); return r; }
__device__ __forceinline__ float mul1(float a, float b) { float r = a * b; asm volatile("" : "+v"(r)); return r; }

__device__ __forceinline__ void raise_flag(char* ws, int arr, unsigned long long tag) {
    __hip_atomic_store((gu64*)(ws + arr) + blockIdx.x, tag, __ATOMIC_RELAXED, __HIP_MEMORY_SCOPE_AGENT);
}
struct Poll { u32x4 a, b; };
__device__ __forceinline__ void poll_request(Poll& p, const char* arr, unsigned voff) {
    asm volatile("global_load_dwordx4 %0, %2, %3 sc1\n\tglobal_load_dwordx4 %1, %2, %3 offset:1024 sc1" : "+v"(p.a), "+v"(p.b) : "v"(voff), "s"(arr) : "memory");
}
__device__ __forceinline__ void poll_wait(Poll& p, const char* arr, unsigned voff, unsigned long long tag) {
    const unsigned tl = (unsigned)tag, th = (unsigned)(tag >> 32);
#pragma unroll 1
    for (unsigned spin = 0; spin < (1u << 18); ++spin) {
        asm volatile("s_waitcnt vmcnt(0)" : "+v"(p.a), "+v"(p.b) :: "memory");
        if (__all(p.a[0] == tl && p.a[1] == th && p.a[2] == tl && p.a[3] == th && p.b[0] == tl && p.b[1] == th && p.b[2] == tl && p.b[3] == th)) break;
        __builtin_amdgcn_s_sleep(1);
        poll_request(p, arr, voff);
    }
    asm volatile("s_waitcnt vmcnt(0)" : "+v"(p.a), "+v"(p.b) :: "memory");
}

template <int MODE> struct P1F { s16x8 x[2]; s16x4 w[MODE == 2 ? 3 : 2][2]; };

template <int MODE>
__device__ __forceinline__ void p1_read(P1F<MODE>& n, lds_ptr xa, lds_ptr w0, lds_ptr w1, int wimm, int i) {
    constexpr int TS = MODE == 2 ? 256 : 512;
    if (i == 0) { n.x[0] = *(const LDS_AS s16x8*)xa; n.w[0][0] = trread(w0 + wimm); n.w[0][1] = trread(w0 + wimm + TS); }
    if (i == 1) { n.x[1] = *(const LDS_AS s16x8*)(xa + 4096); n.w[1][0] = trread(w1 + wimm); n.w[1][1] = trread(w1 + wimm + TS); }
    if (i == 2 && MODE == 2) { n.w[MODE == 2 ? 2 : 0][0] = trread(w0 + wimm + 1024); n.w[MODE == 2 ? 2 : 0][1] = trread(w0 + wimm + 1024 + TS); }
}
template <int MODE, int NR>
__device__ __forceinline__ void p1_step(const P1F<MODE>& f, f32x16 (&acc)[MODE == 2 ? 6 : 4], P1F<MODE>& n0, lds_ptr xa0, int wi0, P1F<MODE>& n1, lds_ptr xa1, int wi1,
                                        lds_ptr w0, lds_ptr w1) {
    constexpr int NM = MODE == 2 ? 6 : 4;
#pragma unroll
    for (int g = 0; g < NM; ++g) {
        const s16x8 w = CAT8(f.w[g >> 1][0], f.w[g >> 1][1]);
#if !(ABL & 1)
        acc[g] = MODE == 1 ? MFMA32(f.x[g & 1], w, acc[g]) : MFMA32(w, f.x[g & 1], acc[g]);
#else
        { s16x8 x_ = f.x[g & 1]; asm volatile("" :: "v"(w), "v"(x_)); }
#endif
        PIN(acc[g]); SBAR();
        if (NR >= 1 && g < NM / 2) p1_read<MODE>(n0, xa0, w0, w1, wi0, g);
        if (NR >= 2 && g >= NM / 2) p1_read<MODE>(n1, xa1, w0, w1, wi1, g - NM / 2);
        SBAR();
    }
}
struct P1X { lds_ptr a0, a1, a2, a3; };
template <int MODE>
__device__ __forceinline__ void p1_first(P1F<MODE> (&f)[4], const P1X& xa, lds_ptr w0, lds_ptr w1) {
    constexpr int KS = MODE == 2 ? 6144 : 4096;
#pragma unroll
    for (int i = 0; i < 3; ++i) p1_read<MODE>(f[0], xa.a0, w0, w1, 0, i);
#pragma unroll
    for (int i = 0; i < 3; ++i) p1_read<MODE>(f[1], xa.a1, w0, w1, KS, i);
    SBAR();
}
template <int MODE>
__device__ __forceinline__ void p1_lo(P1F<MODE> (&f)[4], f32x16 (&acc)[MODE == 2 ? 6 : 4], const P1X& xa, lds_ptr w0, lds_ptr w1) {
    constexpr int KS = MODE == 2 ? 6144 : 4096;
    p1_step<MODE, 1>(f[0], acc, f[2], xa.a2, 2 * KS, f[2], xa.a2, 0, w0, w1);
    p1_step<MODE, 1>(f[1], acc, f[3], xa.a3, 3 * KS, f[3], xa.a3, 0, w0, w1);
}
template <int MODE>
__device__ __forceinline__ void p1_hi(P1F<MODE> (&f)[4], f32x16 (&acc)[MODE == 2 ? 6 : 4], const P1X& xa, lds_ptr w0, lds_ptr w1) {
    constexpr int KS = MODE == 2 ? 6144 : 4096;
    p1_step<MODE, 1>(f[2], acc, f[0], xa.a0, 0, f[0], xa.a0, 0, w0, w1);
    p1_step<MODE, 1>(f[3], acc, f[1], xa.a1, KS, f[1], xa.a1, 0, w0, w1);
}
__device__ __forceinline__ P1X p1_xaddr(lds_ptr lds, int wid, int lane) {
    const int xrow = (wid & 1) * 64 + (lane & 31), xo = P1_X0 + xrow * 128 + (((lane >> 5) ^ ((xrow >> 1) & 7)) << 4);
    return P1X{lds + xo, lds + (xo ^ 32), lds + (xo ^ 64), lds + (xo ^ 96)};
}
template <int MODE>
__device__ __forceinline__ void p1_five(P1F<MODE> (&f)[4], f32x16 (&acc)[MODE == 2 ? 6 : 4], P1X& xa, lds_ptr& w0, lds_ptr& w1, int& ws3, int wrap, int drain, int ps) {
#pragma unroll
    for (int n = 0; n < 5; ++n) {
        const int dw = ws3 == 2 ? -2 * P1_WSLOT : P1_WSLOT, dx = n == wrap ? -4 * P1_XSLOT : P1_XSLOT;
        ws3 = ws3 == 2 ? 0 : ws3 + 1;
        p1_lo<MODE>(f, acc, xa, w0, w1);
        w0 += dw; w1 += dw; xa.a0 += dx; xa.a1 += dx; xa.a2 += dx; xa.a3 += dx;
        asm volatile("" : "+v"(w0), "+v"(w1), "+v"(xa.a0), "+v"(xa.a1), "+v"(xa.a2), "+v"(xa.a3));
        if (n == drain) asm volatile("s_waitcnt vmcnt(0)" ::: "memory");
        PSTAMP(ps + 2 * n);
        LGKM_BAR();
        PSTAMP(ps + 2 * n + 1);
        p1_hi<MODE>(f, acc, xa, w0, w1);
    }
}

struct Pack { const char *wd, *wo; int job; };
__device__ __forceinline__ void pack_fetch(const Pack& pk_, lds_ptr lds, int wid, int lane) {
    const int step = pk_.job / 10, cb = pk_.job - step * 10, r = lane >> 3;
    const unsigned va = ((r & 3) + 8 * (r >> 2)) * (D_ * 2) + (lane & 7) * 16, vb = ((r >> 2) * 20 + 16 + (r & 3)) * (D_ * 2) + (lane & 7) * 16;
#pragma unroll
    for (int g = 0; g < 4; ++g) {
        const int ks = (step - 48) * 2 + (g >> 1), hh = g & 1;
        const char* a = step < 48 ? pk_.wd + (step * 32 + (g >> 1) * 16 + hh * 4) * (D_ * 2) : ks < 32 ? pk_.wo + (ks * 20 + hh * 4) * (D_ * 2) : pk_.wo + ((ks - 32) * 4 + hh * 2) * 20 * (D_ * 2);
        glds16s(a + cb * 128, step < 64 ? va : vb, (unsigned)(uintptr_t)lds + wid * P1_WSLOT + 16384 + g * 1024);
    }
}
__device__ __forceinline__ void pack_read(s16x8 (&pf)[4], lds_ptr lds, int wid, int lane) {
    asm volatile("s_waitcnt vmcnt(0)" ::: "memory");
    const lds_ptr pr = lds + wid * P1_WSLOT + 16384 + (lane >> 4) * 1024 + ((lane >> 2) & 3) * 128 + (lane & 3) * 8;
#pragma unroll
    for (int nt = 0; nt < 4; ++nt) pf[nt] = CAT8(trread(pr + nt * 32), trread(pr + nt * 32 + 512));
}
__device__ __forceinline__ void pack_store(const Pack& pk_, const s16x8 (&pf)[4], int lane, rsrc_t wsr) {
    const int step = pk_.job / 10, cb = pk_.job - step * 10;
#pragma unroll
    for (int nt = 0; nt < 4; ++nt) {
        const int N = cb * 4 + nt, cg = N / 5;
        ST16(wsr, __builtin_bit_cast(u32x4, pf[nt]), lane * 16, WS_WP + ((cg * 68 + step) * 5 + N - cg * 5) * 1024);
    }
}

template <int MODE>
__device__ __forceinline__ void p1a_compute(f32x16 (&acc)[4], lds_ptr lds, int wid, int lane, int ra, const Pack& pk_, s16x8 (&pf)[4]) {
    const int h = lane >> 5, blk = (lane >> 4) & 1, q = (lane >> 2) & 3, fsw = 3 * (q >> 1);
    P1X xa = p1_xaddr(lds + ra * P1_XSLOT, wid, lane);
    const int wb = h * 2048 + (wid >> 1) * 1024 + q * 128 + (lane & 3) * 8;
    lds_ptr w0 = lds + wb + ((blk ^ fsw) << 5), w1 = lds + wb + (((2 + blk) ^ fsw) << 5);
    P1F<MODE> f[4];
    int ws3 = 0;
    PSTAMP(0);
    LGKM_BAR();
    PSTAMP(1);
    p1_first<MODE>(f, xa, w0, w1);
#pragma unroll UNR2
    for (int hf_ = 0; hf_ < 2; ++hf_) {
        p1_five<MODE>(f, acc, xa, w0, w1, ws3, 4 - ra, -1, 2 + 10 * hf_);
        if (hf_ == 0) {
            if (pk_.job < 680) pack_read(pf, lds, wid, lane);
        }
    }
}

__device__ __forceinline__ void p1_compute(lds_ptr lds, int wid, int lane, int panel, int grp, int ra, int rb, rsrc_t wsr, const Pack& pk_ STAMP_ARG) {
    const int h = lane >> 5, l31 = lane & 31, wm = wid & 1, wn = wid >> 1, tt = panel * 4 + wm * 2;
    const LDS_AS f32x2* rowf = (const LDS_AS f32x2*)(lds + P1_STAT);
    __builtin_amdgcn_s_setprio(CPRIO);
    if (pk_.job < 680) pack_fetch(pk_, lds, wid, lane);
    {
        f32x16 acc[4] = {f32x16{}, f32x16{}, f32x16{}, f32x16{}};
        s16x8 pf[4] = {s16x8{}, s16x8{}, s16x8{}, s16x8{}};
        const LDS_AS float* colf = (const LDS_AS float*)(lds + P1_COLA) + wn * 64;
        const int sec = grp / 5, q0 = (grp - sec * 5) * 32 + wn * 16;
        if (sec < 2) {
            p1a_compute<0>(acc, lds, wid, lane, ra, pk_, pf);
            STAMP(1);
            const int pos = sec ? ((l31 & 19) | ((l31 & 4) << 1) | ((l31 & 8) >> 1)) : l31;
            const int hs = sec ? KV_HEAD : Q_HEAD, ts = sec ? KV_TILE : Q_TILE, so = (sec ? WS_KV : WS_Q) + tt * ts;
            const float qs = sec ? 1.0f : 0.22360679774997896f * 1.4426950408889634f;
            f32x2 nm[2], rr[2];
#pragma unroll
            for (int m = 0; m < 2; ++m) { const f32x2 rs = rowf[wm * 64 + m * 32 + l31]; nm[m] = f32x2{rs[1], rs[1]}; rr[m] = f32x2{rs[0] * qs, rs[0] * qs}; }
            const bool odd = lane & 1;
#pragma unroll
            for (int gam = 0; gam < 8; gam += 2) {
                const int qd = q0 + 2 * (gam + (lane & 1)) + h, hd = (qd * 205) >> 10, vo = hd * hs + (qd - hd * 5) * 256 + (pos & ~1) * 8;
                const f32x4 ca = *(const LDS_AS f32x4*)(colf + gam * 8 + h * 4), cb = *(const LDS_AS f32x4*)(colf + gam * 8 + 8 + h * 4);
#pragma unroll
                for (int m = 0; m < 2; ++m) {
                    const f32x16& a = acc[(gam >> 2) * 2 + m];
                    const int r0 = (gam & 3) * 4;
                    const unsigned a0 = cvt2(pfma(pk4(ca, 0), nm[m], pk(a, r0)) * rr[m]), a1 = cvt2(pfma(pk4(ca, 2), nm[m], pk(a, r0 + 2)) * rr[m]);
                    const unsigned b0 = cvt2(pfma(pk4(cb, 0), nm[m], pk(a, r0 + 4)) * rr[m]), b1 = cvt2(pfma(pk4(cb, 2), nm[m], pk(a, r0 + 6)) * rr[m]);
                    const unsigned g0 = __builtin_amdgcn_update_dpp(0, odd ? a0 : b0, 0xB1, 0xF, 0xF, true), g1 = __builtin_amdgcn_update_dpp(0, odd ? a1 : b1, 0xB1, 0xF, 0xF, true);
                    ST16(wsr, (odd ? u32x4{g0, g1, b0, b1} : u32x4{a0, a1, g0, g1}), vo, so + m * ts);
                }
            }
        } else {
            p1a_compute<1>(acc, lds, wid, lane, ra, pk_, pf);
            STAMP(1);
            if (sec == 2) {
                const int so = WS_KV + tt * KV_TILE + KV_V;
#pragma unroll
                for (int m = 0; m < 2; ++m) {
                    f32x2 rr[8], nm[8];
#pragma unroll
                    for (int g = 0; g < 4; ++g) {
                        const LDS_AS f32x2* rp = rowf + wm * 64 + m * 32 + g * 8 + h * 4;
                        const f32x4 a = *(const LDS_AS f32x4*)rp, b = *(const LDS_AS f32x4*)(rp + 2);
                        rr[2 * g] = f32x2{a[0], a[2]}; nm[2 * g] = f32x2{a[1], a[3]}; rr[2 * g + 1] = f32x2{b[0], b[2]}; nm[2 * g + 1] = f32x2{b[1], b[3]};
                    }
#pragma unroll
                    for (int t = 0; t < 2; ++t) {
                        const int qd = q0 + t * 8 + (l31 >> 2), hd = (qd * 205) >> 10, pl = qd - hd * 5;
                        const int vo = hd * KV_HEAD + pl * 256 + ((h ^ (pl & 3)) << 6) + (l31 & 3) * 16;
                        const float c1 = colf[t * 32 + l31];
                        const f32x2 c2 = {c1, c1};
                        unsigned w[8];
#pragma unroll
                        for (int i = 0; i < 8; ++i) w[i] = cvt2(pfma(nm[i], c2, pk(acc[t * 2 + m], 2 * i)) * rr[i]);
                        ST16(wsr, (u32x4{w[0], w[1], w[2], w[3]}), vo, so + m * KV_TILE);
                        ST16(wsr, (u32x4{w[4], w[5], w[6], w[7]}), vo ^ 128, so + m * KV_TILE);
                    }
                }
                const int hd = q0 / 5 + (lane >> 4);
                const unsigned ov = (lane & 3) ? 0u : 0x3f803f80u;
                if (hd * 5 + 4 < q0 + 16) {
                    ST16(wsr, (u32x4{ov, ov, ov, ov}), hd * KV_HEAD + 5 * 256 + (lane & 15) * 16, so);
                    ST16(wsr, (u32x4{ov, ov, ov, ov}), hd * KV_HEAD + 5 * 256 + (lane & 15) * 16, so + KV_TILE);
                }
            }
        }
        if (pk_.job < 680) pack_store(pk_, pf, lane, wsr);
    }
    STAMP(6);
    f32x16 acc[6];
#pragma unroll
    for (int t = 0; t < 6; ++t) acc[t] = f32x16{};
    {
        const int blk = (lane >> 4) & 1, T0 = wn * 3, T1 = T0 + 1;
        P1X xa = p1_xaddr(lds + rb * P1_XSLOT, wid, lane);
        const int wb = P1_WSLOT + 3 * blk * 1024 + h * 512 + ((lane >> 2) & 3) * 64 + (lane & 3) * 8;
        lds_ptr w0 = lds + wb + (T0 >> 1) * 1024 + (((T0 & 1) ^ blk) << 5), w1 = lds + wb + (T1 >> 1) * 1024 + (((T1 & 1) ^ blk) << 5);
        P1F<2> f[4];
        p1_first<2>(f, xa, w0, w1);
        int ws3 = 1;
#pragma unroll UNR2
        for (int hf_ = 0; hf_ < 2; ++hf_) {
            p1_five<2>(f, acc, xa, w0, w1, ws3, 4 - rb, hf_ == 0 ? FA_POS - 11 : -1, 22 + 10 * hf_);
        }
    }
    STAMP(3);
    {
        const LDS_AS float* colf = (const LDS_AS float*)(lds + P1_COLB) + wn * 96;
        const int vo = (l31 >> 4) * 48 * 1024 + (h * 16 + (l31 & 15)) * 16;
        f32x2 nm[2], r2[2];
#pragma unroll
        for (int m = 0; m < 2; ++m) { const f32x2 rs = rowf[wm * 64 + m * 32 + l31]; nm[m] = f32x2{rs[1], rs[1]}; r2[m] = f32x2{rs[0] * rs[0], rs[0] * rs[0]}; }
#pragma unroll
        for (int c = 0; c < 3; ++c) {
            f32x4 cv[4];
#pragma unroll
            for (int g = 0; g < 4; ++g) cv[g] = *(const LDS_AS f32x4*)(colf + c * 32 + g * 8 + h * 4);
            if (c >= 1 && wn) {
#pragma unroll
                for (int g = 0; g < 4; ++g) cv[g] += *(const LDS_AS f32x4*)(colf + 64 + c * 32 + g * 8 + h * 4);
            }
            const int so = WS_H16 + tt * 2 * 48 * 1024 + (grp * 6 + wn * 3 + c) * 512;
#pragma unroll
            for (int m = 0; m < 2; ++m) {
                const f32x16& a = acc[c * 2 + m];
                f32x2 g[4], u[4];
#pragma unroll
                for (int i = 0; i < 4; ++i) { g[i] = pfma(pk4(cv[i >> 1], (i & 1) * 2), nm[m], pk(a, 2 * i)); u[i] = pfma(pk4(cv[2 + (i >> 1)], (i & 1) * 2), nm[m], pk(a, 8 + 2 * i)); }
#pragma unroll
                for (int i = 0; i < 4; ++i) { g[i] = f32x2{fmaxf(g[i][0], 0.f), fmaxf(g[i][1], 0.f)}; u[i] *= r2[m]; }
                ST16(wsr, (u32x4{cvt2(g[0] * u[0]), cvt2(g[1] * u[1]), cvt2(g[2] * u[2]), cvt2(g[3] * u[3])}), vo, so + m * 2 * 48 * 1024);
            }
        }
    }
}

__device__ __forceinline__ void glds_x4(const char* sbase, const unsigned (&v)[4], unsigned lds_base) {
    asm volatile("s_mov_b32 m0, %5\n\ts_nop 0\n\tglobal_load_lds_dwordx4 %0, %4\n\tglobal_load_lds_dwordx4 %1, %4 offset:1024\n\t"
                 "global_load_lds_dwordx4 %2, %4 offset:2048\n\tglobal_load_lds_dwordx4 %3, %4 offset:3072"
                 :: "v"(v[0]), "v"(v[1]), "v"(v[2]), "v"(v[3]), "s"(sbase), "s"(lds_base) : "memory");
}
__device__ __forceinline__ void glds_x3(const char* sbase, const unsigned* v, unsigned lds_base) {
    asm volatile("s_mov_b32 m0, %4\n\ts_nop 0\n\tglobal_load_lds_dwordx4 %0, %3\n\tglobal_load_lds_dwordx4 %1, %3 offset:1024\n\tglobal_load_lds_dwordx4 %2, %3 offset:2048"
                 :: "v"(v[0]), "v"(v[1]), "v"(v[2]), "s"(sbase), "s"(lds_base) : "memory");
}

__device__ __forceinline__ unsigned long long p1_helper(lds_ptr lds, int hw, int lane, const char* xp, const char* wq, const char* wgp, const char* wup, int ra, int rb,
                                          char* ws, const char* kv, const char* qsrc) {
    const unsigned lds0 = (unsigned)(uintptr_t)lds;
    const int r = lane >> 3, pos = lane & 7;
    unsigned xv[4], wa[4], wb[6];
#pragma unroll
    for (int j = 0; j < 4; ++j) {
        xv[j] = 4096 + (hw * 32 + j * 8 + r) * (D_ * 2) + ((pos ^ ((j & 1) * 4 + (r >> 1))) << 4) - j * 1024;
        wa[j] = 4096 + (hw * 16 + (j >> 1) * 8 + r) * (3 * D_ * 2) + (j & 1) * 128 + ((pos ^ (6 * ((r >> 1) & 1))) << 4) - j * 1024;
    }
#pragma unroll
    for (int j = 0; j < 6; ++j) wb[j] = 4096 + (hw * 16 + (lane >> 2)) * (F_ * 2) + (j % 3) * 64 + (((lane & 3) ^ (j < 3 ? 0 : 2)) << 4) - (j % 3) * 1024;
    xp -= 4096; wq -= 4096; wgp -= 4096; wup -= 4096;
    int kb_ = 0, xs = ra, dws = 0, left = 5;
    auto issue1 = [&](bool pa) {
        if (ABL & 4) return;
        if (pa) glds_x4(wq + (unsigned)((kb_ + xs) * 64 * 3 * D_ * 2), wa, lds0 + dws * P1_WSLOT + hw * 4096);
        else glds_x3(wgp + (unsigned)((kb_ + xs) * 64 * F_ * 2), wb, lds0 + dws * P1_WSLOT + hw * 6144);
        SBAR();
    };
    auto issue2 = [&](bool pa, bool xx) {
        SBAR();
        if (ABL & 4) return;
        if (!pa) glds_x3(wup + (unsigned)((kb_ + xs) * 64 * F_ * 2), wb + 3, lds0 + dws * P1_WSLOT + hw * 6144 + 3072);
        xs = xs == 4 ? 0 : xs + 1; dws = dws == 2 ? 0 : dws + 1; --left; kb_ = left == 0 ? 5 - kb_ : kb_; left = left == 0 ? 5 : left;
        if (xx) glds_x4(xp + (kb_ + xs) * 128, xv, lds0 + P1_X0 + xs * P1_XSLOT + hw * 4096);
    };
    glds_x4(xp + ra * 128, xv, lds0 + P1_X0 + ra * P1_XSLOT + hw * 4096);
    issue1(true); issue2(true, true);
    __builtin_amdgcn_s_barrier();
    unsigned long long ep = 0;
    asm volatile("global_load_dwordx2 %0, %1, %2 sc1" : "+v"(ep) : "v"(0u), "s"(ws + WS_EPOCH) : "memory");
    __builtin_amdgcn_s_setprio(HPRIO);
    const int r16 = lane & 15, h = lane >> 5, blk = (lane >> 4) & 1, q = (lane >> 2) & 3, fsw = 3 * (q >> 1);
    const unsigned olo = r16 < 2 ? 0x3f803f80u : 0u, ohi = (r16 >> 1) == 1 ? 0x3f803f80u : 0u;
    const s16x8 ones = __builtin_bit_cast(s16x8, (u32x4{olo, olo, ohi, ohi}));
    const int idx = (r16 & 1) ? 0xEEEE : 0x4444;
    int ws3 = 0;
    {
        lds_ptr b = lds + h * 2048 + (hw >> 1) * 1024 + q * 128 + (lane & 3) * 8 + ((((hw & 1) * 2 + blk) ^ fsw) << 5);
        const int so = P1_X0 + ra * P1_XSLOT + (hw * 32 + r16) * 128 + (((lane >> 4) ^ (r16 >> 1)) << 4);
        lds_ptr sp0 = lds + so, sp1 = lds + (so ^ 64);
        const s16x8 ones2 = __builtin_bit_cast(s16x8, (u32x4{olo, olo, olo, olo}));
        f32x4 cs = f32x4{}, s1[2] = {f32x4{}, f32x4{}}, s2[2] = {f32x4{}, f32x4{}};
        s16x16 wv0, wv1; s16x8 xa[2], xb[2];
        auto col_rd = [&]() {
            wv0 = CAT16(CAT8(trread(b), trread(b + 512)), CAT8(trread(b + 4096), trread(b + 4096 + 512)));
            wv1 = CAT16(CAT8(trread(b + 8192), trread(b + 8192 + 512)), CAT8(trread(b + 12288), trread(b + 12288 + 512)));
            b += ws3 == 2 ? -2 * P1_WSLOT : P1_WSLOT; ws3 = ws3 == 2 ? 0 : ws3 + 1;
        };
        auto row_rd = [&](int dx) {
#pragma unroll
            for (int t = 0; t < 2; ++t) { xa[t] = *(const LDS_AS s16x8*)(sp0 + t * 2048); xb[t] = *(const LDS_AS s16x8*)(sp1 + t * 2048); }
            sp0 += dx; sp1 += dx;
        };
        auto row_mm = [&]() {
#pragma unroll
            for (int t = 0; t < 2; ++t) { s2[t] = MFMA16(xa[t], xa[t], s2[t]); s1[t] = SMF16(ones2, CAT16(xa[t], xb[t]), s1[t], idx); }
#pragma unroll
            for (int t = 0; t < 2; ++t) { s2[t] = MFMA16(xb[t], xb[t], s2[t]); PIN(s1[t]); PIN(s2[t]); }
        };
#pragma unroll
        for (int nn = 0; nn < 9; ++nn) {
            int n = nn;
            if (!HUNR) asm volatile("" : "+s"(n));
            PSTAMP(2 * n);
            if (n == 0) WAIT_BAR(5); else if (n < 8) WAIT_BAR(12); else WAIT_BAR(8);
            PSTAMP(2 * n + 1);
            if (!(ABL & 8) || n == 0) { col_rd(); row_rd(n % 5 == 4 - ra ? -4 * P1_XSLOT : P1_XSLOT); }
            if (n == 0) { issue1(true); issue2(true, true); }
            if (n == 8) { kb_ = 5; xs = rb; }
            issue1(n < 8);
            if (!(ABL & 8)) {
            cs = SMF16(ones, wv0, cs, idx);
            row_mm();
            cs = SMF16(ones, wv1, cs, idx); PIN(cs); }
            issue2(n < 8, n < 7);
        }
        MEMFENCE(); __builtin_amdgcn_s_waitcnt(0xF70 | 10); MEMFENCE();
        row_rd(0); row_mm();
        {
            const int t = blk;
            const float d0 = (lane & 2) ? ((lane & 1) ? s2[0][3] : s2[0][2]) : ((lane & 1) ? s2[0][1] : s2[0][0]);
            const float d1 = (lane & 2) ? ((lane & 1) ? s2[1][3] : s2[1][2]) : ((lane & 1) ? s2[1][1] : s2[1][0]);
            const float a0 = __shfl(s1[0][0] + s1[0][1], r16), a1 = __shfl(s1[1][0] + s1[1][1], r16), c0 = __shfl(d0, r16 + 16 * q), c1 = __shfl(d1, r16 + 16 * q);
            const float a = t ? a1 : a0, c = t ? c1 : c0;
            const float mean = a * (1.0f / D_), var = fmaxf(c * (1.0f / D_) - mean * mean, 0.f);
            const float rstd = __builtin_amdgcn_rcpf(rbf(__builtin_amdgcn_sqrtf(rbf(rbf(var) + 1e-5f))));
            if (lane < 32) ((LDS_AS f32x2*)(lds + P1_STAT))[hw * 32 + lane] = f32x2{rstd, -rbf(mean)};
        }
        PSTAMP(18);
        WAIT_BAR(6);
        PSTAMP(19);
        col_rd();
        issue1(false);
        cs = SMF16(ones, wv0, cs, idx);
        cs = SMF16(ones, wv1, cs, idx); PIN(cs);
        issue2(false, false);
        LDS_AS float* colf = (LDS_AS float*)(lds + P1_COLA) + hw * 32;
        if (lane < 16) { colf[lane] = cs[0] + cs[1]; colf[16 + lane] = cs[2] + cs[3]; }
    }
    asm volatile("" : "+v"(ep));
    const unsigned long long tag = (((unsigned long long)(unsigned)__builtin_amdgcn_readfirstlane((unsigned)(ep >> 32)) << 32) | (unsigned)__builtin_amdgcn_readfirstlane((unsigned)ep)) + 1ull;
    if (hw == 0 && lane == 0) *(LDS_AS unsigned long long*)(lds + P1_TAG) = tag;
    {
        const int ro = P1_WSLOT + 3 * blk * 1024 + h * 512 + q * 64 + (lane & 3) * 8 + (((hw & 1) ^ blk) << 5);
        lds_ptr b = lds + ro + (hw >> 1) * 1024, c = lds + ro + 2048 + (hw >> 1) * 12288;
        f32x4 cs = f32x4{}, ct = f32x4{};
        Poll pl = {};
#pragma unroll
        for (int nn = 10; nn < 20; ++nn) {
            int n = nn;
            if (!HUNR) asm volatile("" : "+s"(n));
            PSTAMP(2 * n);
            if (n < 13 || n == 19) WAIT_BAR(6); else if (n == 13) WAIT_BAR(10); else if (n == 18) WAIT_BAR(12); else WAIT_BAR(14);
            PSTAMP(2 * n + 1);
            const s16x16 wv0 = CAT16(CAT8(trread(b), trread(b + 256)), CAT8(trread(b + 6144), trread(b + 6144 + 256)));
            const s16x16 wv2 = CAT16(CAT8(trread(c), trread(c + 256)), CAT8(trread(c + 6144), trread(c + 6144 + 256)));
            const s16x16 wv1 = CAT16(CAT8(trread(b + 12288), trread(b + 12288 + 256)), CAT8(trread(b + 18432), trread(b + 18432 + 256)));
            if (n == FA_POS && hw == 0 && lane == 0) raise_flag(ws, WS_FA, tag);
            if (n < 18) issue1(false);
            cs = SMF16(ones, wv0, cs, idx);
            ct = SMF16(ones, wv2, ct, idx);
            cs = SMF16(ones, wv1, cs, idx); PIN(cs); PIN(ct);
            if (n < 18) issue2(false, n >= 12 && n < 17);
            if (n == 17) poll_request(pl, ws + WS_FA, lane * 16);
            if (n == 18) {
                poll_wait(pl, ws + WS_FA, lane * 16, tag);
#pragma unroll
                for (int j = 0; j < 6; ++j) glds16c(kv + (hw + 4 * j) * 1024, lane * 16, lds0 + K2_C0 + (hw + 4 * j) * 1024);
            }
            if (n == 19) {
#pragma unroll
                for (int j = 0; j < 3; ++j) glds16c(qsrc + (hw + 4 * j) * 1024, lane * 16, lds0 + K2_Q0 + (hw + 4 * j) * 1024);
            }
            const int dw = ws3 == 2 ? -2 * P1_WSLOT : P1_WSLOT;
            b += dw; c += dw; ws3 = ws3 == 2 ? 0 : ws3 + 1;
        }
        LDS_AS float* colf = (LDS_AS float*)(lds + P1_COLB);
        if (lane < 16) { colf[hw * 32 + lane] = cs[0] + cs[1]; colf[hw * 32 + 16 + lane] = cs[2] + cs[3]; }
        if (lane < 16) { colf[128 + hw * 32 + lane] = ct[0] + ct[1]; colf[128 + hw * 32 + 16 + lane] = ct[2] + ct[3]; }
        WAIT_BAR(0);
        if (blockIdx.x == 0 && hw == 0 && lane == 0) __hip_atomic_store((gu64*)(ws + WS_EPOCH), tag, __ATOMIC_RELAXED, __HIP_MEMORY_SCOPE_AGENT);
#pragma unroll
        for (int j = 0; j < 6; ++j) glds16c(kv + K2_CHUNK + (hw + 4 * j) * 1024, lane * 16, lds0 + K2_KV0 + K2_SLOTB + (hw + 4 * j) * 1024);
    }
    return tag;
}

constexpr int K2_XCH = 2 * K2_SLOTB;

#define SMF32(a, b, c) __builtin_amdgcn_smfmac_f32_32x32x32_bf16((bfx8)(a), __builtin_bit_cast(bfx16, (b)), (c), 0xDDDD, 0, 0)
#define MFMA8(a, b, c) __builtin_amdgcn_mfma_f32_32x32x8bf16_1k((a), (b), (c), 0, 0, 0)
__device__ __forceinline__ f32x8 lo8(const f32x16& v) { return __builtin_shufflevector(v, v, 0, 1, 2, 3, 4, 5, 6, 7); }
__device__ __forceinline__ f32x8 hi8(const f32x16& v) { return __builtin_shufflevector(v, v, 8, 9, 10, 11, 12, 13, 14, 15); }
__device__ __forceinline__ float max16(const f32x16& s) {
    float m = fmaxf(fmaxf(s[0], s[1]), s[2]);
#pragma unroll
    for (int r = 3; r < 15; r += 2) m = fmaxf(fmaxf(m, s[r]), s[r + 1]);
    return swap32_max(fmaxf(m, s[15]));
}

struct K2B { lds_ptr r0, r1, r2; const char* wsrc; unsigned wdst; int lane, osoff; rsrc_t wsr; };
constexpr int k2_slot(int st) { return st < 3 ? st : st == 3 ? 0 : st == 4 ? 3 : st == 5 ? 1 : st == 6 ? 2 : st == 7 ? 0 : 3; }
__device__ __forceinline__ void k2_wpiece(const K2B& c, int st, int i) {
    glds16c(c.wsrc + st * 40960 + i * 8192, c.lane * 16, c.wdst + k2_slot(st) * 40960 + i * 8192);
}
template <int I, int D, int NP, int OF>
__device__ __forceinline__ void k2_bstep(const K2B& c, f32x4 (&acc)[10], const s16x8 (&w)[5], s16x8 (&n)[5], const s16x8& a0, const s16x8& a1, s16x8 (&of)[10]) {
    constexpr int O = k2_slot((I + 1) >> 1) * 40960 + ((I + 1) & 1) * 20480;
#pragma unroll
    for (int i = 0; i < 10; ++i) {
        if (I > 0 && I < 16 && i < 5) n[i] = *(const LDS_AS s16x8*)((O < 61440 ? c.r0 : O < 122880 ? c.r1 : c.r2) + O % 61440 + i * 1024);
        acc[i] = MFMA16(w[i >> 1], (i & 1) ? a1 : a0, acc[i]);
        PIN(acc[i]);
        if (OF >= 0 && !(i & 1)) { const int k = (OF < 0 ? 0 : OF) + (i >> 1); of[k] = LD16(c.wsr, c.lane * 16, c.osoff + ((k & 1) * 20 + (k >> 1) * 4) * 1024); MEMFENCE(); }
        if ((i & 1) && (i >> 1) < NP) k2_wpiece(c, D, i >> 1);
        SBAR();
    }
}

__global__ __launch_bounds__(512, 2) void fused_block(const char* __restrict__ x, const char* __restrict__ wqkv, const char* __restrict__ wg, const char* __restrict__ wu,
                                                      const char* __restrict__ wd, const char* __restrict__ wo, char* __restrict__ ws, char* __restrict__ out) {
    extern __shared__ __attribute__((aligned(16))) char smem[];
    asm volatile("" :: "s"(x), "s"(wqkv), "s"(wg), "s"(wu), "s"(wd), "s"(wo), "s"(ws), "s"(out));
    const int wid = __builtin_amdgcn_readfirstlane(threadIdx.x >> 6);
    STAMP_DECL; STAMP(0); PSTAMP(63);
    unsigned long long tag = 0;
    if (wid < 4) {
        __builtin_amdgcn_s_barrier();
        if (CODEWARM && wid == 3) {
            unsigned off = ((blockIdx.x >> 3) * 12 + min(threadIdx.x & 63, 11)) * 128;
            asm volatile("s_getpc_b64 vcc\n.Lfb_begin:\n\tv_min_u32 %0, .Lfb_end-.Lfb_begin, %0\n\ts_mov_b32 m0, %1\n\ts_nop 0\n\tglobal_load_lds_dword %0, vcc"
                         : "+v"(off) : "s"((unsigned)(uintptr_t)(lds_ptr)smem + LDS_JUNK) : "vcc", "memory");
        }
    }
    const rsrc_t wsr = __builtin_amdgcn_make_buffer_rsrc(ws, 0, (int)WS_END, 0x00020000);
    const int bid = blockIdx.x, head = (bid & 7) * 4 + ((bid >> 3) & 3), par = wid >> 2, qt0 = (bid >> 5) * 8 + (wid & 3) * 2;
    const char* kv = ws + WS_KV + head * KV_HEAD;
    {
        const int bi = bid >> 3, panel = (bid & 1) * 8 + (bi & 7), grp = ((bid & 7) >> 1) * 4 + (bi >> 3), l0 = threadIdx.x & 63;
        const int rb = K1_ROT ? (5 * (bi & 7)) >> 3 : 0, ra = ROTA == 1 ? (rb + (bi >> 3)) % 5 : ROTA == 2 ? ((bi & 1) * 2 + ((bi >> 3) & 1) * 2) % 4 : 0;
        if (wid < 4) {
            p1_compute((lds_ptr)smem, wid, l0, panel, grp, ra, rb, wsr, Pack{wd, wo, wid * 256 + bid} STAMP_PASS);
            const u32x2 e = *(const LDS_AS u32x2*)((lds_ptr)smem + P1_TAG);
            tag = ((unsigned long long)(unsigned)__builtin_amdgcn_readfirstlane(e[1]) << 32) | (unsigned)__builtin_amdgcn_readfirstlane(e[0]);
        } else {
            tag = p1_helper((lds_ptr)smem, wid - 4, l0, x + (size_t)panel * 128 * (D_ * 2), wqkv + min(grp, 14) * 256, wg + grp * 192, wu + grp * 192, ra, rb,
                            ws, kv, ws + WS_Q + head * Q_HEAD + (bid >> 5) * 8 * Q_TILE);
        }
    }
    int lane = __builtin_amdgcn_mbcnt_hi(~0u, __builtin_amdgcn_mbcnt_lo(~0u, 0u));
    PIN(lane);
    if (wid < 4) __builtin_amdgcn_s_setprio(3); else __builtin_amdgcn_s_setprio(0);
    STAMP(4);
    lds_ptr lds = (lds_ptr)smem + K2_KV0;
    const unsigned lds0 = (unsigned)(uintptr_t)lds;
    const int tp = ((blockIdx.x & 7) >> 1) * 8 + ((blockIdx.x >> 3) & 7), cg = (blockIdx.x & 1) * 4 + (blockIdx.x >> 6);
    K2B kb;
    kb.wsrc = ws + WS_WP + cg * 68 * 5120 + wid * 1024; kb.wdst = (unsigned)(uintptr_t)(lds_ptr)smem + wid * 1024; kb.lane = lane;
    const int hoff = WS_H16 + ((tp * 4 + (wid & 1) * 2) * 48 + (wid >> 1)) * 1024;
    s16x8 hf[24];
    const int h = lane >> 5, l31 = lane & 31, l16 = lane * 16;
    const char* src = kv + wid * 1024 + 3 * K2_CHUNK;
    const int k1off = h * 512 + l31 * 8, k2off = h ? KV_V + 5 * 256 : 1024 + l31 * 8;
    const int vq = min(l31 >> 2, 5), voff = KV_V + vq * 256 + ((h ^ (vq & 3)) << 6) + (l31 < 24 ? l31 & 3 : 1) * 16;

    f32x16 oa[2] = {f32x16{}, f32x16{}}, sc[2];
    s16x8 k1[2], v0[2], v1[2]; s16x4 k2[2];
    const lds_ptr c0 = (lds_ptr)smem + K2_C0;
    lds_ptr pk1 = c0 + k1off + par * KV_TILE, pk2 = c0 + k2off + par * KV_TILE, pv = c0 + voff + par * KV_TILE, pw = c0 + (voff ^ 128) + par * KV_TILE;
    lds_ptr pk3 = pk1 + 256;
    asm volatile("" : "+v"(pk3));
#define LDS8(p) (*(const LDS_AS s16x4*)(p))
#define RD_K(t, j) do { k1[(t) & 1] = CAT8(LDS8(pk1 + (j) * 2 * KV_TILE), LDS8(pk3 + (j) * 2 * KV_TILE)); k2[(t) & 1] = LDS8(pk2 + (j) * 2 * KV_TILE); } while (0)
#define RD_V(t, j) do { v0[(t) & 1] = *(const LDS_AS s16x8*)(pv + (j) * 2 * KV_TILE); v1[(t) & 1] = *(const LDS_AS s16x8*)(pw + (j) * 2 * KV_TILE); } while (0)
    s16x8 q1[2]; s16x4 q2[2];
#pragma unroll
    for (int b = 0; b < 2; ++b) {
        const lds_ptr qp = (lds_ptr)smem + K2_Q0 + ((wid & 3) * 2 + b) * Q_TILE;
        q1[b] = CAT8(LDS8(qp + k1off), LDS8(qp + k1off + 256)); q2[b] = LDS8(qp + 1024 + l31 * 8);
    }
    {
        const s16x8 ka = CAT8(LDS8(c0 + k1off), LDS8(c0 + k1off + 256));
        const s16x4 kb = LDS8(c0 + k2off);
        RD_K(0, 0); RD_V(0, 0);
#pragma unroll
        for (int b = 0; b < 2; ++b) {
            if (h) q2[b] = s16x4{0, 0, 0, 0};
            f32x16 s = MFMA32(ka, q1[b], f32x16{});
            s = MFMA8(kb, q2[b], s);
            const float m = rbf(-max16(s));
            if (h) q2[b][0] = (short)(__float_as_uint(m) >> 16);
        }
        sc[0] = MFMA32(k1[0], q1[0], f32x16{});
        sc[0] = MFMA8(k2[0], q2[0], sc[0]);
        sc[1] = f32x16{}; v1[1] = s16x8{};
    }
    STAMP(5); PSTAMP(42); PSTAMP(43);
#define EXP4(S, i) do { S[i] = __builtin_amdgcn_exp2f(S[i]); S[i + 1] = __builtin_amdgcn_exp2f(S[i + 1]); S[i + 2] = __builtin_amdgcn_exp2f(S[i + 2]); S[i + 3] = __builtin_amdgcn_exp2f(S[i + 3]); PIN(S); SBAR(); } while (0)
    Poll pl = {};
    auto request = [&](int hs) {
        const int p = (hs - 6) >> 3, r = (hs - 6) & 7;
        const int w = (hs >= 9 && hs < 14) ? hs - 9 : (hs >= 17 && hs < 22) ? hs - 12 : (hs >= 54 && hs < 57) ? hs - 44 : hs >= 62 ? hs - 49 : -1;
        const int e = (p >= 2 && p <= 4 && r >= 3) ? (p - 2) * 5 + r - 3 : p == 5 ? 15 + r : hs == 57 ? 23 : -1;
        if (hs < 3) glds16c(src - K2_CHUNK + hs * 8192, l16, lds0 + 2 * K2_SLOTB + wid * 1024 + hs * 8192);
        else if (hs >= 6 && p < 5 && r < 3) glds16c(src + p * K2_CHUNK + r * 8192, l16, lds0 + (p % 3) * K2_SLOTB + wid * 1024 + r * 8192);
        else if (w >= 0) glds16c(kb.wsrc + w * 8192, l16, kb.wdst + w * 8192);
        else if (e >= 0) { hf[e] = LD16(wsr, l16, hoff + e * 4096); MEMFENCE(); }
        if (hs == 6 && wid == 4 && lane == 0) raise_flag(ws, WS_FB, tag);
        if (hs == 16 && wid == 4) poll_request(pl, ws + WS_FB, l16);
        SBAR();
    };
#pragma unroll
    for (int c = 0; c < 8; ++c) {
#pragma unroll
        for (int j = 0; j < 4; ++j) {
            if (j == 3) {
                PSTAMP(44 + 2 * c);
                if (c == 2 && wid == 4) poll_wait(pl, ws + WS_FB, l16, tag);
                if (c == 0) { asm volatile("s_waitcnt vmcnt(0)" ::: "memory"); LGKM_BAR(); } else if (c == 7) WAIT_BAR(0); else WAIT_BAR(7);
                PSTAMP(45 + 2 * c);
                __builtin_amdgcn_s_setprio(2);
                const int dsl = c == 0 ? K2_KV0 + K2_SLOTB - K2_C0 : (c % 3 == 2) ? -2 * K2_SLOTB : K2_SLOTB;
                pk1 += dsl; pk2 += dsl; pk3 += dsl; pv += dsl; pw += dsl;
                asm volatile("" : "+v"(pk1), "+v"(pk2), "+v"(pk3), "+v"(pv), "+v"(pw));
                SBAR();
            }
            RD_K(j + 1, (j + 1) & 3); SBAR();
            oa[1] = SMF32(v1[(j + 1) & 1], hi8(sc[1]), oa[1]); PIN(oa[1]); SBAR();
            EXP4(sc[0], 0);
            sc[1] = MFMA32(k1[j & 1], q1[1], f32x16{}); PIN(sc[1]); SBAR();
            request(8 * c + 2 * j);
            EXP4(sc[0], 4);
            sc[1] = MFMA8(k2[j & 1], q2[1], sc[1]); PIN(sc[1]); SBAR();
            EXP4(sc[0], 8);
            oa[0] = SMF32(v0[j & 1], lo8(sc[0]), oa[0]); PIN(oa[0]); SBAR();
            EXP4(sc[0], 12);
            RD_V(j + 1, (j + 1) & 3);
            if (j == 1 && c > 0) __builtin_amdgcn_s_setprio(1);
            if (j == 2 && c > 0) __builtin_amdgcn_s_setprio(0);
            SBAR();
            oa[0] = SMF32(v1[j & 1], hi8(sc[0]), oa[0]); PIN(oa[0]); SBAR();
            EXP4(sc[1], 0);
            sc[0] = MFMA32(k1[(j + 1) & 1], q1[0], f32x16{}); PIN(sc[0]); SBAR();
            request(8 * c + 2 * j + 1);
            EXP4(sc[1], 4);
            sc[0] = MFMA8(k2[(j + 1) & 1], q2[0], sc[0]); PIN(sc[0]); SBAR();
            EXP4(sc[1], 8);
            oa[1] = SMF32(v0[j & 1], lo8(sc[1]), oa[1]); PIN(oa[1]); SBAR();
            EXP4(sc[1], 12);
        }
    }
    oa[1] = SMF32(v1[1], hi8(sc[1]), oa[1]);
    unsigned qs_ = 0; (void)qs_;
    STAMP(2);
    PSTAMP(60); QSTAMP(60);
    int ln = __builtin_amdgcn_mbcnt_hi(~0u, __builtin_amdgcn_mbcnt_lo(~0u, 0u));
    PIN(ln);
    const int h2 = ln >> 5, l2 = ln & 31;
    const int mt = wid & 1, kq = wid >> 1;
    kb.lane = ln; kb.r0 = (lds_ptr)smem + kq * 5120 + ln * 16; kb.r1 = kb.r0 + 61440; kb.r2 = kb.r0 + 122880;
    asm volatile("" : "+v"(kb.r1), "+v"(kb.r2));
    kb.wsr = wsr; kb.osoff = WS_O16 + ((tp * 4 + mt * 2) * 20 + kq) * 1024;
    s16x8 wa[5], wb[5];
#pragma unroll
    for (int i = 0; i < 5; ++i) { wa[i] = *(const LDS_AS s16x8*)(kb.r0 + i * 1024); wb[i] = *(const LDS_AS s16x8*)(kb.r0 + 20480 + i * 1024); }
    {
        const lds_ptr xp_ = lds + K2_XCH + (wid & 3) * 6144 + ln * 16;
        if (par) {
#pragma unroll
            for (int i = 0; i < 6; ++i) { const f32x16& g = oa[i / 3]; const int r = 4 * (i % 3); *(LDS_AS f32x4*)(xp_ + i * 1024) = f32x4{g[r], g[r + 1], g[r + 2], g[r + 3]}; }
        }
        LGKM_BAR();
        QSTAMP(21);
        if (!par) {
            __builtin_amdgcn_s_setprio(3);
            f32x4 tt[6];
#pragma unroll
            for (int i = 0; i < 6; ++i) tt[i] = *(const LDS_AS f32x4*)(xp_ + i * 1024);
            SBAR();
#pragma unroll
            for (int i = 0; i < 6; ++i) { const f32x4 t = tt[i]; f32x16& g = oa[i / 3]; const int r = 4 * (i % 3); g[r] = add1(g[r], t[0]); g[r + 1] = add1(g[r + 1], t[1]); g[r + 2] = add1(g[r + 2], t[2]); g[r + 3] = add1(g[r + 3], t[3]); }
            const int ob = (l2 >> 4) * 20 * 1024 + (l2 & 15) * 16, os = WS_O16 + qt0 * 2 * 20 * 1024;
            const int o1 = ob + h2 * 256 + os + (head >> 1) * 1024 + (head & 1) * 512, o2 = os + (16 + (head >> 3)) * 1024 + ((head >> 1) & 3) * 256 + (head & 1) * 8;
            auto put = [&](const f32x16& o, int b, u32x2& e) {
                const float l = __uint_as_float(__builtin_amdgcn_permlane32_swap(__float_as_uint(o[8]), __float_as_uint(o[8]), false, false)[1]);
                const float rl = __builtin_amdgcn_rcpf(l);
                unsigned w[6];
#pragma unroll
                for (int i = 0; i < 6; ++i) w[i] = cvtpk(mul1(o[2 * i], rl), mul1(o[2 * i + 1], rl));
                ST16(wsr, (u32x4{w[0], w[1], w[2], w[3]}), o1, b * 40 * 1024);
                e = u32x2{w[4], w[5]};
                return l;
            };
            u32x2 e0, e1;
            QSTAMP(22);
            const float l0 = put(oa[0], 0, e0), l1 = put(oa[1], 1, e1);
            ST8(wsr, (u32x2{__builtin_amdgcn_permlane32_swap(e0[0], e1[0], false, false)[0], __builtin_amdgcn_permlane32_swap(e0[1], e1[1], false, false)[0]}), ob + h2 * 40 * 1024, o2);
            MEMFENCE(); SBAR();
            QSTAMP(23);
            float big = 0.f;
#pragma unroll
            for (int r = 0; r < 12; ++r) big = add1(big, add1(fabsf(oa[0][r]), fabsf(oa[1][r])));
            if (__builtin_expect(__any(!(big < 1e30f) | !(fminf(l0, l1) > 1e-30f)), 0)) {
                const int g1 = h2 * 512 + l2 * 8, g2 = 1024 + l2 * 8, gq = min(l2 >> 2, 5), gv = KV_V + gq * 256 + ((h2 ^ (gq & 3)) << 6) + (l2 & 3) * 16;
                asm volatile("s_waitcnt vmcnt(0)" ::: "memory");
#pragma unroll 1
                for (int b = 0; b < 2; ++b) {
                    const s16x8 qf1 = b ? q1[1] : q1[0];
                    s16x4 qf2 = b ? q2[1] : q2[0];
                    if (h2) qf2[0] = 0;
                    float m = -INFINITY;
                    f32x16 negm, o = f32x16{};
#pragma unroll 1
                    for (int t = 0; t < 64; ++t) {
                        const int g = WS_KV + head * KV_HEAD + t * KV_TILE;
                        f32x16 s = MFMA32(CAT8(LD8(wsr, g1, g), LD8(wsr, g1 + 256, g)), qf1, f32x16{});
                        s = MFMA8(LD8(wsr, g2, g), qf2, s);
                        m = fmaxf(m, max16(s));
                    }
#pragma unroll
                    for (int r = 0; r < 16; ++r) negm[r] = -m;
#pragma unroll 1
                    for (int t = 0; t < 64; ++t) {
                        const int g = WS_KV + head * KV_HEAD + t * KV_TILE;
                        f32x16 s = MFMA32(CAT8(LD8(wsr, g1, g), LD8(wsr, g1 + 256, g)), qf1, negm);
                        s = MFMA8(LD8(wsr, g2, g), qf2, s);
#pragma unroll
                        for (int r = 0; r < 16; ++r) s[r] = __builtin_amdgcn_exp2f(s[r]);
                        o = SMF32(LD16(wsr, gv, g), lo8(s), o);
                        o = SMF32(LD16(wsr, gv ^ 128, g), hi8(s), o);
                    }
                    u32x2 e;
                    put(o, b, e);
                    if (!h2) ST8(wsr, e, ob + b * 40 * 1024, o2);
                }
            }
        }
    }
    PSTAMP(61); QSTAMP(61);
    if (DIAG_PF2) { PFLUSH(); }
    __builtin_amdgcn_s_setprio(0);
    const int ch0 = wid * 128 + ln, ch1 = ch0 + 64, er0 = ch0 / 10, er1 = ch1 / 10, ec0 = ch0 - er0 * 10, ec1 = ch1 - er1 * 10;
    const size_t eb = (size_t)tp * 64 * (D_ * 2) + cg * 160;
    const unsigned eo0 = er0 * (D_ * 2) + ec0 * 16, eo1 = er1 * (D_ * 2) + ec1 * 16;
    f32x4 acc[10];
#pragma unroll
    for (int i = 0; i < 10; ++i) acc[i] = f32x4{};
    s16x8 of[10];
    QSTAMP(0); k2_bstep<0, 0, 0, -1>(kb, acc, wa, wb, hf[0], hf[12], of);
    QSTAMP(1); k2_bstep<1, 0, 0, -1>(kb, acc, wb, wa, hf[1], hf[13], of);
    QSTAMP(2); k2_bstep<2, 0, 0, -1>(kb, acc, wa, wb, hf[2], hf[14], of);
    if (!par) {
        asm volatile("s_waitcnt vmcnt(0)" ::: "memory");
        if (ln == 0) __hip_atomic_store((gu64*)(ws + WS_FC) + (qt0 >> 1) * 32 + head, tag, __ATOMIC_RELAXED, __HIP_MEMORY_SCOPE_AGENT);
    } else {
#pragma unroll
        for (int i = 0; i < 10; ++i) glds16c(kb.wsrc + 3 * 40960 - 4096 + i * 4096, ln * 16, kb.wdst - 4096 + i * 4096);
    }
    WAIT_BAR(10);
    QSTAMP(3); k2_bstep<3, 4, 5, -1>(kb, acc, wb, wa, hf[3], hf[15], of);
    QSTAMP(4); k2_bstep<4, 5, 5, -1>(kb, acc, wa, wb, hf[4], hf[16], of);
    WAIT_BAR(10);
    QSTAMP(5); k2_bstep<5, 6, 5, -1>(kb, acc, wb, wa, hf[5], hf[17], of);
    unsigned long long seen = 0;
    const char* cw = ws + WS_FC + tp * 256;
    const unsigned cvo = l2 * 8;
    if (wid == 4) asm volatile("global_load_dwordx2 %0, %1, %2 sc1" : "+v"(seen) : "v"(cvo), "s"(cw) : "memory");
    SBAR();
    QSTAMP(6); k2_bstep<6, 0, 0, -1>(kb, acc, wa, wb, hf[6], hf[18], of);
    if (wid == 4) {
        asm volatile("s_waitcnt vmcnt(0)" : "+v"(seen) :: "memory");
#pragma unroll 1
        for (unsigned spin = 0; spin < (1u << 18) && !__all(seen == tag); ++spin) {
            __builtin_amdgcn_s_sleep(1);
            asm volatile("global_load_dwordx2 %0, %1, %2 sc1\n\ts_waitcnt vmcnt(0)" : "=&v"(seen) : "v"(cvo), "s"(cw) : "memory");
        }
    }
    WAIT_BAR(10);
    QSTAMP(7); k2_bstep<7, 0, 0, 0>(kb, acc, wb, wa, hf[7], hf[19], of);
    QSTAMP(8); k2_bstep<8, 0, 0, 5>(kb, acc, wa, wb, hf[8], hf[20], of);
    WAIT_BAR(15);
    QSTAMP(9); k2_bstep<9, 7, 5, -1>(kb, acc, wb, wa, hf[9], hf[21], of);
    QSTAMP(10); k2_bstep<10, 8, 3, -1>(kb, acc, wa, wb, hf[10], hf[22], of);
    WAIT_BAR(8);
    QSTAMP(11); k2_bstep<11, 0, 0, -1>(kb, acc, wb, wa, hf[11], hf[23], of);
    QSTAMP(12); k2_bstep<12, 0, 0, -1>(kb, acc, wa, wb, of[0], of[1], of);
    WAIT_BAR(3);
    QSTAMP(13); k2_bstep<13, 0, 0, -1>(kb, acc, wb, wa, of[2], of[3], of);
    QSTAMP(14); k2_bstep<14, 0, 0, -1>(kb, acc, wa, wb, of[4], of[5], of);
    WAIT_BAR(0);
    u32x4 xv0 = u32x4{}, xv1 = u32x4{};
    if (wid < 5) { xv0 = *(const u32x4*)(x + eb + eo0); xv1 = *(const u32x4*)(x + eb + eo1); }
    MEMFENCE(); SBAR();
    QSTAMP(15); k2_bstep<15, 0, 0, -1>(kb, acc, wb, wa, of[6], of[7], of);
    QSTAMP(16); k2_bstep<16, 0, 0, -1>(kb, acc, wa, wb, of[8], of[9], of);
    QSTAMP(17);
    {
        const int tk = ln & 15;
        const lds_ptr pp = (lds_ptr)smem + kq * 20480 + (mt * 32 + tk) * 320 + (((ln >> 4) ^ ((tk >> 1) & 3)) << 4);
#pragma unroll
        for (int i = 0; i < 10; ++i) *(LDS_AS f32x4*)(pp + (i & 1) * 16 * 320 + (i >> 1) * 64) = acc[i];
        QSTAMP(18);
        LGKM_BAR();
        QSTAMP(19);
        if (wid < 5) {
#pragma unroll
            for (int e = 0; e < 2; ++e) {
                const int er = e ? er1 : er0, ec = e ? ec1 : ec0, sw = (er >> 1) & 3;
                const lds_ptr p0 = (lds_ptr)smem + er * 320 + (((2 * ec) ^ sw) << 4), p1 = (lds_ptr)smem + er * 320 + (((2 * ec + 1) ^ sw) << 4);
                f32x4 a = *(const LDS_AS f32x4*)p0, b = *(const LDS_AS f32x4*)p1;
#pragma unroll
                for (int k = 1; k < 4; ++k) { a += *(const LDS_AS f32x4*)(p0 + k * 20480); b += *(const LDS_AS f32x4*)(p1 + k * 20480); }
                const u32x4 xv = e ? xv1 : xv0;
                *(u32x4*)(out + eb + (e ? eo1 : eo0)) = u32x4{cvtpk(a[0] + bf_lo(xv[0]), a[1] + bf_hi(xv[0])), cvtpk(a[2] + bf_lo(xv[1]), a[3] + bf_hi(xv[1])),
                                                              cvtpk(b[0] + bf_lo(xv[2]), b[1] + bf_hi(xv[2])), cvtpk(b[2] + bf_lo(xv[3]), b[3] + bf_hi(xv[3]))};
            }
        }
    }
    QSTAMP(20);
    if (CODEWARM) asm volatile(".Lfb_end:");
    STAMP(7); STAMP_FLUSH(0); if (!DIAG_PF2) { PFLUSH(); }
}

extern "C" void kernel_launch(void* const* d_in, const int* in_sizes, int n_in, void* d_out, int out_size, void* d_ws, size_t ws_size,
                              hipStream_t stream) {
    const char* x = (const char*)d_in[0];
    const char* wqkv = (const char*)d_in[1];
    const char* wo = (const char*)d_in[2];
    const char* wg = (const char*)d_in[4];
    const char* wu = (const char*)d_in[5];
    const char* wd = (const char*)d_in[6];
    char* ws = (char*)d_ws;
    (void)hipFuncSetAttribute((const void*)fused_block, hipFuncAttributeMaxDynamicSharedMemorySize, LDS_BYTES);
    fused_block<<<256, 512, LDS_BYTES, stream>>>(x, wqkv, wg, wu, wd, wo, ws, (char*)d_out);
}
```

```cpp
#include <hip/hip_runtime.h>
#include <hip/hip_bf16.h>
#include <stdint.h>

typedef __bf16 bfx4 __attribute__((ext_vector_type(4)));
typedef __bf16 bfx8 __attribute__((ext_vector_type(8)));
typedef __bf16 bfx16 __attribute__((ext_vector_type(16)));
typedef short s16x4 __attribute__((ext_vector_type(4)));
typedef short s16x8 __attribute__((ext_vector_type(8)));
typedef short s16x16 __attribute__((ext_vector_type(16)));
typedef float f32x2 __attribute__((ext_vector_type(2)));
typedef float f32x4 __attribute__((ext_vector_type(4)));
typedef float f32x8 __attribute__((ext_vector_type(8)));
typedef float f32x16 __attribute__((ext_vector_type(16)));
typedef unsigned u32x2 __attribute__((ext_vector_type(2)));
typedef unsigned u32x4 __attribute__((ext_vector_type(4)));
typedef unsigned u32x8 __attribute__((ext_vector_type(8)));
#define LDS_AS __attribute__((address_space(3)))
typedef LDS_AS char* lds_ptr;

#define ABL 0
#define DPRIO 1
#define CODEWARM 1
#define EVEN 1
#define HPRIO 3
#define CPRIO 0
#define WPWARM 0
#define UNR2 2
#define HUNR 1
#define FA_POS 14
#define ROTA 2
#define OUTAUX 17
#define K1_ROT 1
constexpr int S_ = 2048, D_ = 640, H_ = 32, F_ = 1536;

constexpr int Q_TILE = 1280, Q_HEAD = 64 * Q_TILE, KV_TILE = 2816, KV_V = 1280, KV_HEAD = 64 * KV_TILE;
constexpr int WS_Q = 0;
constexpr int WS_KV = WS_Q + H_ * Q_HEAD;
constexpr int WS_O16 = WS_KV + H_ * KV_HEAD + 8192;
constexpr int WS_H16 = WS_O16 + 128 * 20 * 1024;
constexpr int WS_WP = WS_H16 + 128 * 48 * 1024;
constexpr int WS_EPOCH = WS_WP + 8 * 68 * 5120 + 8192;
constexpr int WS_FA = WS_EPOCH + 256, WS_FB = WS_FA + 2048, WS_FC = WS_FB + 2048, WS_END = WS_FC + 8192;

#define PSTAMP(i) do {} while (0)
#define PFLUSH() do {} while (0)
#define DIAG_PF2 0
#define QSTAMP(i) do {} while (0)
#define STAMP_ARG
#define STAMP_PASS
#define STAMP_DECL do {} while (0)
#define STAMP(i) do {} while (0)
#define STAMP_FLUSH(kern) do {} while (0)
#define SBAR() __builtin_amdgcn_sched_barrier(0)
#define MFMA32(a, b, c) __builtin_amdgcn_mfma_f32_32x32x16_bf16((bfx8)(a), (bfx8)(b), (c), 0, 0, 0)

__device__ __forceinline__ unsigned cvtpk(float lo, float hi) {
    typedef __bf16 bfx2 __attribute__((ext_vector_type(2)));
    bfx2 r = {(__bf16)lo, (__bf16)hi};
    return __builtin_bit_cast(unsigned, r);
}
__device__ __forceinline__ void glds16(const void* g, unsigned lds_base) {
    unsigned sv;
    asm volatile("s_mov_b32 %0, m0\n\ts_mov_b32 m0, %2\n\ts_nop 0\n\tglobal_load_lds_dwordx4 %1, off\n\ts_mov_b32 m0, %0"
                 : "=&s"(sv) : "v"(g), "s"(lds_base) : "memory");
}
__device__ __forceinline__ s16x4 trread(const LDS_AS char* p) {
    return __builtin_amdgcn_ds_read_tr16_b64_v4i16((LDS_AS s16x4*)p);
}
__device__ __forceinline__ float rbf(float v) { return (float)(__bf16)v; }
__device__ __forceinline__ float bf_lo(unsigned u) { return __uint_as_float(u << 16); }
__device__ __forceinline__ float bf_hi(unsigned u) { return __uint_as_float(u & 0xffff0000u); }
__device__ __forceinline__ float swap32_add(float v) {
    auto r = __builtin_amdgcn_permlane32_swap(__float_as_uint(v), __float_as_uint(v), false, false);
    return __uint_as_float(r[0]) + __uint_as_float(r[1]);
}
__device__ __forceinline__ float swap32_max(float v) {
    auto r = __builtin_amdgcn_permlane32_swap(__float_as_uint(v), __float_as_uint(v), false, false);
    return fmaxf(__uint_as_float(r[0]), __uint_as_float(r[1]));
}

constexpr int P1_WSLOT = 24576, P1_X0 = 3 * P1_WSLOT, P1_XSLOT = 16384, P1_STAT = P1_X0 + 5 * P1_XSLOT;
constexpr int P1_COLA = P1_STAT + 1024, P1_COLB = P1_COLA + 512, P1_TAG = P1_COLB + 1024, LDS_JUNK = 161792, LDS_BYTES = 163840;
constexpr int K2_CHUNK = 8 * KV_TILE, K2_SLOTB = 24576, K2_SLOTS = 3, K2_KV0 = 81920, K2_C0 = 2 * P1_WSLOT, K2_Q0 = 0;
typedef __attribute__((address_space(1))) unsigned long long gu64;

#define PIN(x) asm volatile("" : "+v"(x))
#define MEMFENCE() asm volatile("" ::: "memory")
#define WAIT_BAR(N) do { MEMFENCE(); __builtin_amdgcn_s_waitcnt(0x70 | ((N) & 15) | (((N) >> 4) << 14)); __builtin_amdgcn_s_barrier(); MEMFENCE(); SBAR(); } while (0)
#define LGKM_BAR() do { MEMFENCE(); __builtin_amdgcn_s_waitcnt(0xC07F); __builtin_amdgcn_s_barrier(); MEMFENCE(); SBAR(); } while (0)
#define MFMA16(a, b, c) __builtin_amdgcn_mfma_f32_16x16x32_bf16((bfx8)(a), (bfx8)(b), (c), 0, 0, 0)
#define SMF16(a, b, c, i) __builtin_amdgcn_smfmac_f32_16x16x64_bf16((bfx8)(a), (bfx16)(b), (c), (i), 0, 0)
#define CAT8(a, b) __builtin_shufflevector(a, b, 0, 1, 2, 3, 4, 5, 6, 7)
#define CAT16(a, b) __builtin_shufflevector(a, b, 0, 1, 2, 3, 4, 5, 6, 7, 8, 9, 10, 11, 12, 13, 14, 15)

__device__ __forceinline__ void glds16s(const char* sbase, unsigned voff, unsigned lds_base) {
    unsigned sv;
    asm volatile("s_mov_b32 %0, m0\n\ts_mov_b32 m0, %3\n\ts_nop 0\n\tglobal_load_lds_dwordx4 %1, %2\n\ts_mov_b32 m0, %0"
                 : "=&s"(sv) : "v"(voff), "s"(sbase), "s"(lds_base) : "memory");
}

__device__ __forceinline__ void glds16c(const char* sbase, unsigned voff, unsigned lds_base) {
    asm volatile("s_mov_b32 m0, %2\n\ts_nop 0\n\tglobal_load_lds_dwordx4 %0, %1 sc1" :: "v"(voff), "s"(sbase), "s"(lds_base) : "memory");
}
typedef __amdgpu_buffer_rsrc_t rsrc_t;
#define ST16(rs, v, voff, soff) __builtin_amdgcn_raw_buffer_store_b128((v), (rs), (voff) + (soff), 0, (ABL & 32) ? 0 : 16)
#define ST8(rs, v, voff, soff) __builtin_amdgcn_raw_buffer_store_b64((v), (rs), (voff), (soff), 16)
#define LD16(rs, voff, soff) __builtin_bit_cast(s16x8, __builtin_amdgcn_raw_buffer_load_b128((rs), (voff), (soff), 16))
#define LD8(rs, voff, soff) __builtin_bit_cast(s16x4, __builtin_amdgcn_raw_buffer_load_b64((rs), (voff), (soff), 16))
__device__ __forceinline__ f32x2 pk(const f32x16& a, int i) { return f32x2{a[i], a[i + 1]}; }
__device__ __forceinline__ f32x2 pk4(const f32x4& a, int i) { return f32x2{a[i], a[i + 1]}; }
__device__ __forceinline__ f32x2 pfma(f32x2 a, f32x2 b, f32x2 c) { return __builtin_elementwise_fma(a, b, c); }
__device__ __forceinline__ unsigned cvt2(f32x2 v) { return cvtpk(v[0], v[1]); }
__device__ __forceinline__ float add1(float a, float b) { float r = a + b; asm volatile("" : "+v"(r)); return r; }
__device__ __forceinline__ float mul1(float a, float b) { float r = a * b; asm volatile("" : "+v"(r)); return r; }

__device__ __forceinline__ void raise_flag(char* ws, int arr, unsigned long long tag) {
    __hip_atomic_store((gu64*)(ws + arr) + blockIdx.x, tag, __ATOMIC_RELAXED, __HIP_MEMORY_SCOPE_AGENT);
}
struct Poll { u32x4 a, b; };
__device__ __forceinline__ void poll_request(Poll& p, const char* arr, unsigned voff) {
    asm volatile("global_load_dwordx4 %0, %2, %3 sc1\n\tglobal_load_dwordx4 %1, %2, %3 offset:1024 sc1" : "+v"(p.a), "+v"(p.b) : "v"(voff), "s"(arr) : "memory");
}
__device__ __forceinline__ void poll_wait(Poll& p, const char* arr, unsigned voff, unsigned long long tag) {
    const unsigned tl = (unsigned)tag, th = (unsigned)(tag >> 32);
#pragma unroll 1
    for (unsigned spin = 0; spin < (1u << 18); ++spin) {
        asm volatile("s_waitcnt vmcnt(0)" : "+v"(p.a), "+v"(p.b) :: "memory");
        if (__all(p.a[0] == tl && p.a[1] == th && p.a[2] == tl && p.a[3] == th && p.b[0] == tl && p.b[1] == th && p.b[2] == tl && p.b[3] == th)) break;
        __builtin_amdgcn_s_sleep(1);
        poll_request(p, arr, voff);
    }
    asm volatile("s_waitcnt vmcnt(0)" : "+v"(p.a), "+v"(p.b) :: "memory");
}

template <int MODE> struct P1F { s16x8 x[2]; s16x4 w[MODE == 2 ? 3 : 2][2]; };

template <int MODE>
__device__ __forceinline__ void p1_read(P1F<MODE>& n, lds_ptr xa, lds_ptr w0, lds_ptr w1, int wimm, int i) {
    constexpr int TS = MODE == 2 ? 256 : 512;
    if (i == 0) { n.x[0] = *(const LDS_AS s16x8*)xa; n.w[0][0] = trread(w0 + wimm); n.w[0][1] = trread(w0 + wimm + TS); }
    if (i == 1) { n.x[1] = *(const LDS_AS s16x8*)(xa + 4096); n.w[1][0] = trread(w1 + wimm); n.w[1][1] = trread(w1 + wimm + TS); }
    if (i == 2 && MODE == 2) { n.w[MODE == 2 ? 2 : 0][0] = trread(w0 + wimm + 1024); n.w[MODE == 2 ? 2 : 0][1] = trread(w0 + wimm + 1024 + TS); }
}
template <int MODE, int NR>
__device__ __forceinline__ void p1_step(const P1F<MODE>& f, f32x16 (&acc)[MODE == 2 ? 6 : 4], P1F<MODE>& n0, lds_ptr xa0, int wi0, P1F<MODE>& n1, lds_ptr xa1, int wi1,
                                        lds_ptr w0, lds_ptr w1) {
    constexpr int NM = MODE == 2 ? 6 : 4;
#pragma unroll
    for (int g = 0; g < NM; ++g) {
        const s16x8 w = CAT8(f.w[g >> 1][0], f.w[g >> 1][1]);
#if !(ABL & 1)
        acc[g] = MODE == 1 ? MFMA32(f.x[g & 1], w, acc[g]) : MFMA32(w, f.x[g & 1], acc[g]);
#else
        { s16x8 x_ = f.x[g & 1]; asm volatile("" :: "v"(w), "v"(x_)); }
#endif
        PIN(acc[g]); SBAR();
        if (NR >= 1 && g < NM / 2) p1_read<MODE>(n0, xa0, w0, w1, wi0, g);
        if (NR >= 2 && g >= NM / 2) p1_read<MODE>(n1, xa1, w0, w1, wi1, g - NM / 2);
        SBAR();
    }
}
struct P1X { lds_ptr a0, a1, a2, a3; };
template <int MODE>
__device__ __forceinline__ void p1_first(P1F<MODE> (&f)[4], const P1X& xa, lds_ptr w0, lds_ptr w1) {
    constexpr int KS = MODE == 2 ? 6144 : 4096;
#pragma unroll
    for (int i = 0; i < 3; ++i) p1_read<MODE>(f[0], xa.a0, w0, w1, 0, i);
#pragma unroll
    for (int i = 0; i < 3; ++i) p1_read<MODE>(f[1], xa.a1, w0, w1, KS, i);
    SBAR();
}
template <int MODE>
__device__ __forceinline__ void p1_lo(P1F<MODE> (&f)[4], f32x16 (&acc)[MODE == 2 ? 6 : 4], const P1X& xa, lds_ptr w0, lds_ptr w1) {
    constexpr int KS = MODE == 2 ? 6144 : 4096;
    p1_step<MODE, 1>(f[0], acc, f[2], xa.a2, 2 * KS, f[2], xa.a2, 0, w0, w1);
    p1_step<MODE, 1>(f[1], acc, f[3], xa.a3, 3 * KS, f[3], xa.a3, 0, w0, w1);
}
template <int MODE>
__device__ __forceinline__ void p1_hi(P1F<MODE> (&f)[4], f32x16 (&acc)[MODE == 2 ? 6 : 4], const P1X& xa, lds_ptr w0, lds_ptr w1) {
    constexpr int KS = MODE == 2 ? 6144 : 4096;
    p1_step<MODE, 1>(f[2], acc, f[0], xa.a0, 0, f[0], xa.a0, 0, w0, w1);
    p1_step<MODE, 1>(f[3], acc, f[1], xa.a1, KS, f[1], xa.a1, 0, w0, w1);
}
__device__ __forceinline__ P1X p1_xaddr(lds_ptr lds, int wid, int lane) {
    const int xrow = (wid & 1) * 64 + (lane & 31), xo = P1_X0 + xrow * 128 + (((lane >> 5) ^ ((xrow >> 1) & 7)) << 4);
    return P1X{lds + xo, lds + (xo ^ 32), lds + (xo ^ 64), lds + (xo ^ 96)};
}
template <int MODE>
__device__ __forceinline__ void p1_five(P1F<MODE> (&f)[4], f32x16 (&acc)[MODE == 2 ? 6 : 4], P1X& xa, lds_ptr& w0, lds_ptr& w1, int& ws3, int wrap, int drain, int ps) {
#pragma unroll
    for (int n = 0; n < 5; ++n) {
        const int dw = ws3 == 2 ? -2 * P1_WSLOT : P1_WSLOT, dx = n == wrap ? -4 * P1_XSLOT : P1_XSLOT;
        ws3 = ws3 == 2 ? 0 : ws3 + 1;
        p1_lo<MODE>(f, acc, xa, w0, w1);
        w0 += dw; w1 += dw; xa.a0 += dx; xa.a1 += dx; xa.a2 += dx; xa.a3 += dx;
        asm volatile("" : "+v"(w0), "+v"(w1), "+v"(xa.a0), "+v"(xa.a1), "+v"(xa.a2), "+v"(xa.a3));
        if (n == drain) asm volatile("s_waitcnt vmcnt(0)" ::: "memory");
        PSTAMP(ps + 2 * n);
        LGKM_BAR();
        PSTAMP(ps + 2 * n + 1);
        p1_hi<MODE>(f, acc, xa, w0, w1);
    }
}

struct Pack { const char *wd, *wo; int job; };
__device__ __forceinline__ void pack_fetch(const Pack& pk_, lds_ptr lds, int wid, int lane) {
    const int step = pk_.job / 10, cb = pk_.job - step * 10, r = lane >> 3;
    const unsigned va = ((r & 3) + 8 * (r >> 2)) * (D_ * 2) + (lane & 7) * 16, vb = ((r >> 2) * 20 + 16 + (r & 3)) * (D_ * 2) + (lane & 7) * 16;
#pragma unroll
    for (int g = 0; g < 4; ++g) {
        const int ks = (step - 48) * 2 + (g >> 1), hh = g & 1;
        const char* a = step < 48 ? pk_.wd + (step * 32 + (g >> 1) * 16 + hh * 4) * (D_ * 2) : ks < 32 ? pk_.wo + (ks * 20 + hh * 4) * (D_ * 2) : pk_.wo + ((ks - 32) * 4 + hh * 2) * 20 * (D_ * 2);
        glds16s(a + cb * 128, step < 64 ? va : vb, (unsigned)(uintptr_t)lds + wid * P1_WSLOT + 16384 + g * 1024);
    }
}
__device__ __forceinline__ void pack_read(s16x8 (&pf)[4], lds_ptr lds, int wid, int lane) {
    asm volatile("s_waitcnt vmcnt(0)" ::: "memory");
    const lds_ptr pr = lds + wid * P1_WSLOT + 16384 + (lane >> 4) * 1024 + ((lane >> 2) & 3) * 128 + (lane & 3) * 8;
#pragma unroll
    for (int nt = 0; nt < 4; ++nt) pf[nt] = CAT8(trread(pr + nt * 32), trread(pr + nt * 32 + 512));
}
__device__ __forceinline__ void pack_store(const Pack& pk_, const s16x8 (&pf)[4], int lane, rsrc_t wsr) {
    const int step = pk_.job / 10, cb = pk_.job - step * 10;
#pragma unroll
    for (int nt = 0; nt < 4; ++nt) {
        const int N = cb * 4 + nt, cg = N / 5;
        ST16(wsr, __builtin_bit_cast(u32x4, pf[nt]), lane * 16, WS_WP + ((cg * 68 + step) * 5 + N - cg * 5) * 1024);
    }
}

template <int MODE>
__device__ __forceinline__ void p1a_compute(f32x16 (&acc)[4], lds_ptr lds, int wid, int lane, int ra, const Pack& pk_, s16x8 (&pf)[4]) {
    const int h = lane >> 5, blk = (lane >> 4) & 1, q = (lane >> 2) & 3, fsw = 3 * (q >> 1);
    P1X xa = p1_xaddr(lds + ra * P1_XSLOT, wid, lane);
    const int wb = h * 2048 + (wid >> 1) * 1024 + q * 128 + (lane & 3) * 8;
    lds_ptr w0 = lds + wb + ((blk ^ fsw) << 5), w1 = lds + wb + (((2 + blk) ^ fsw) << 5);
    P1F<MODE> f[4];
    int ws3 = 0;
    PSTAMP(0);
    LGKM_BAR();
    PSTAMP(1);
    p1_first<MODE>(f, xa, w0, w1);
#pragma unroll UNR2
    for (int hf_ = 0; hf_ < 2; ++hf_) {
        p1_five<MODE>(f, acc, xa, w0, w1, ws3, 4 - ra, -1, 2 + 10 * hf_);
        if (hf_ == 0) {
            if (pk_.job < 680) pack_read(pf, lds, wid, lane);
        }
    }
}

__device__ __forceinline__ void p1_compute(lds_ptr lds, int wid, int lane, int panel, int grp, int ra, int rb, rsrc_t wsr, const Pack& pk_ STAMP_ARG) {
    const int h = lane >> 5, l31 = lane & 31, wm = wid & 1, wn = wid >> 1, tt = panel * 4 + wm * 2;
    const LDS_AS f32x2* rowf = (const LDS_AS f32x2*)(lds + P1_STAT);
    __builtin_amdgcn_s_setprio(CPRIO);
    if (pk_.job < 680) pack_fetch(pk_, lds, wid, lane);
    {
        f32x16 acc[4] = {f32x16{}, f32x16{}, f32x16{}, f32x16{}};
        s16x8 pf[4] = {s16x8{}, s16x8{}, s16x8{}, s16x8{}};
        const LDS_AS float* colf = (const LDS_AS float*)(lds + P1_COLA) + wn * 64;
        const int sec = grp / 5, q0 = (grp - sec * 5) * 32 + wn * 16;
        if (sec < 2) {
            p1a_compute<0>(acc, lds, wid, lane, ra, pk_, pf);
            STAMP(1);
            const int pos = sec ? ((l31 & 19) | ((l31 & 4) << 1) | ((l31 & 8) >> 1)) : l31;
            const int hs = sec ? KV_HEAD : Q_HEAD, ts = sec ? KV_TILE : Q_TILE, so = (sec ? WS_KV : WS_Q) + tt * ts;
            const float qs = sec ? 1.0f : 0.22360679774997896f * 1.4426950408889634f;
            f32x2 nm[2], rr[2];
#pragma unroll
            for (int m = 0; m < 2; ++m) { const f32x2 rs = rowf[wm * 64 + m * 32 + l31]; nm[m] = f32x2{rs[1], rs[1]}; rr[m] = f32x2{rs[0] * qs, rs[0] * qs}; }
            const bool odd = lane & 1;
#pragma unroll
            for (int gam = 0; gam < 8; gam += 2) {
                const int qd = q0 + 2 * (gam + (lane & 1)) + h, hd = (qd * 205) >> 10, vo = hd * hs + (qd - hd * 5) * 256 + (pos & ~1) * 8;
                const f32x4 ca = *(const LDS_AS f32x4*)(colf + gam * 8 + h * 4), cb = *(const LDS_AS f32x4*)(colf + gam * 8 + 8 + h * 4);
#pragma unroll
                for (int m = 0; m < 2; ++m) {
                    const f32x16& a = acc[(gam >> 2) * 2 + m];
                    const int r0 = (gam & 3) * 4;
                    const unsigned a0 = cvt2(pfma(pk4(ca, 0), nm[m], pk(a, r0)) * rr[m]), a1 = cvt2(pfma(pk4(ca, 2), nm[m], pk(a, r0 + 2)) * rr[m]);
                    const unsigned b0 = cvt2(pfma(pk4(cb, 0), nm[m], pk(a, r0 + 4)) * rr[m]), b1 = cvt2(pfma(pk4(cb, 2), nm[m], pk(a, r0 + 6)) * rr[m]);
                    const unsigned g0 = __builtin_amdgcn_update_dpp(0, odd ? a0 : b0, 0xB1, 0xF, 0xF, true), g1 = __builtin_amdgcn_update_dpp(0, odd ? a1 : b1, 0xB1, 0xF, 0xF, true);
                    ST16(wsr, (odd ? u32x4{g0, g1, b0, b1} : u32x4{a0, a1, g0, g1}), vo, so + m * ts);
                }
            }
        } else {
            p1a_compute<1>(acc, lds, wid, lane, ra, pk_, pf);
            STAMP(1);
            if (sec == 2) {
                const int so = WS_KV + tt * KV_TILE + KV_V;
#pragma unroll
                for (int m = 0; m < 2; ++m) {
                    f32x2 rr[8], nm[8];
#pragma unroll
                    for (int g = 0; g < 4; ++g) {
                        const LDS_AS f32x2* rp = rowf + wm * 64 + m * 32 + g * 8 + h * 4;
                        const f32x4 a = *(const LDS_AS f32x4*)rp, b = *(const LDS_AS f32x4*)(rp + 2);
                        rr[2 * g] = f32x2{a[0], a[2]}; nm[2 * g] = f32x2{a[1], a[3]}; rr[2 * g + 1] = f32x2{b[0], b[2]}; nm[2 * g + 1] = f32x2{b[1], b[3]};
                    }
#pragma unroll
                    for (int t = 0; t < 2; ++t) {
                        const int qd = q0 + t * 8 + (l31 >> 2), hd = (qd * 205) >> 10, pl = qd - hd * 5;
                        const int vo = hd * KV_HEAD + pl * 256 + ((h ^ (pl & 3)) << 6) + (l31 & 3) * 16;
                        const float c1 = colf[t * 32 + l31];
                        const f32x2 c2 = {c1, c1};
                        unsigned w[8];
#pragma unroll
                        for (int i = 0; i < 8; ++i) w[i] = cvt2(pfma(nm[i], c2, pk(acc[t * 2 + m], 2 * i)) * rr[i]);
                        ST16(wsr, (u32x4{w[0], w[1], w[2], w[3]}), vo, so + m * KV_TILE);
                        ST16(wsr, (u32x4{w[4], w[5], w[6], w[7]}), vo ^ 128, so + m * KV_TILE);
                    }
                }
                const int hd = q0 / 5 + (lane >> 4);
                const unsigned ov = (lane & 3) ? 0u : 0x3f803f80u;
                if (hd * 5 + 4 < q0 + 16) {
                    ST16(wsr, (u32x4{ov, ov, ov, ov}), hd * KV_HEAD + 5 * 256 + (lane & 15) * 16, so);
                    ST16(wsr, (u32x4{ov, ov, ov, ov}), hd * KV_HEAD + 5 * 256 + (lane & 15) * 16, so + KV_TILE);
                }
            }
        }
        if (pk_.job < 680) pack_store(pk_, pf, lane, wsr);
    }
    STAMP(6);
    f32x16 acc[6];
#pragma unroll
    for (int t = 0; t < 6; ++t) acc[t] = f32x16{};
    {
        const int blk = (lane >> 4) & 1, T0 = wn * 3, T1 = T0 + 1;
        P1X xa = p1_xaddr(lds + rb * P1_XSLOT, wid, lane);
        const int wb = P1_WSLOT + 3 * blk * 1024 + h * 512 + ((lane >> 2) & 3) * 64 + (lane & 3) * 8;
        lds_ptr w0 = lds + wb + (T0 >> 1) * 1024 + (((T0 & 1) ^ blk) << 5), w1 = lds + wb + (T1 >> 1) * 1024 + (((T1 & 1) ^ blk) << 5);
        P1F<2> f[4];
        p1_first<2>(f, xa, w0, w1);
        int ws3 = 1;
#pragma unroll UNR2
        for (int hf_ = 0; hf_ < 2; ++hf_) {
            p1_five<2>(f, acc, xa, w0, w1, ws3, 4 - rb, hf_ == 0 ? FA_POS - 11 : -1, 22 + 10 * hf_);
        }
    }
    STAMP(3);
    {
        const LDS_AS float* colf = (const LDS_AS float*)(lds + P1_COLB) + wn * 96;
        const int vo = (l31 >> 4) * 48 * 1024 + (h * 16 + (l31 & 15)) * 16;
        f32x2 nm[2], r2[2];
#pragma unroll
        for (int m = 0; m < 2; ++m) { const f32x2 rs = rowf[wm * 64 + m * 32 + l31]; nm[m] = f32x2{rs[1], rs[1]}; r2[m] = f32x2{rs[0] * rs[0], rs[0] * rs[0]}; }
#pragma unroll
        for (int c = 0; c < 3; ++c) {
            f32x4 cv[4];
#pragma unroll
            for (int g = 0; g < 4; ++g) cv[g] = *(const LDS_AS f32x4*)(colf + c * 32 + g * 8 + h * 4);
            if (c >= 1 && wn) {
#pragma unroll
                for (int g = 0; g < 4; ++g) cv[g] += *(const LDS_AS f32x4*)(colf + 64 + c * 32 + g * 8 + h * 4);
            }
            const int so = WS_H16 + tt * 2 * 48 * 1024 + (grp * 6 + wn * 3 + c) * 512;
#pragma unroll
            for (int m = 0; m < 2; ++m) {
                const f32x16& a = acc[c * 2 + m];
                f32x2 g[4], u[4];
#pragma unroll
                for (int i = 0; i < 4; ++i) { g[i] = pfma(pk4(cv[i >> 1], (i & 1) * 2), nm[m], pk(a, 2 * i)); u[i] = pfma(pk4(cv[2 + (i >> 1)], (i & 1) * 2), nm[m], pk(a, 8 + 2 * i)); }
#pragma unroll
                for (int i = 0; i < 4; ++i) { g[i] = f32x2{fmaxf(g[i][0], 0.f), fmaxf(g[i][1], 0.f)}; u[i] *= r2[m]; }
                ST16(wsr, (u32x4{cvt2(g[0] * u[0]), cvt2(g[1] * u[1]), cvt2(g[2] * u[2]), cvt2(g[3] * u[3])}), vo, so + m * 2 * 48 * 1024);
            }
        }
    }
}

__device__ __forceinline__ void glds_x4(const char* sbase, const unsigned (&v)[4], unsigned lds_base) {
    asm volatile("s_mov_b32 m0, %5\n\ts_nop 0\n\tglobal_load_lds_dwordx4 %0, %4\n\tglobal_load_lds_dwordx4 %1, %4 offset:1024\n\t"
                 "global_load_lds_dwordx4 %2, %4 offset:2048\n\tglobal_load_lds_dwordx4 %3, %4 offset:3072"
                 :: "v"(v[0]), "v"(v[1]), "v"(v[2]), "v"(v[3]), "s"(sbase), "s"(lds_base) : "memory");
}
__device__ __forceinline__ void glds_x3(const char* sbase, const unsigned* v, unsigned lds_base) {
    asm volatile("s_mov_b32 m0, %4\n\ts_nop 0\n\tglobal_load_lds_dwordx4 %0, %3\n\tglobal_load_lds_dwordx4 %1, %3 offset:1024\n\tglobal_load_lds_dwordx4 %2, %3 offset:2048"
                 :: "v"(v[0]), "v"(v[1]), "v"(v[2]), "s"(sbase), "s"(lds_base) : "memory");
}

__device__ __forceinline__ unsigned long long p1_helper(lds_ptr lds, int hw, int lane, const char* xp, const char* wq, const char* wgp, const char* wup, int ra, int rb,
                                          char* ws, const char* kv, const char* qsrc) {
    const unsigned lds0 = (unsigned)(uintptr_t)lds;
    const int r = lane >> 3, pos = lane & 7;
    unsigned xv[4], wa[4], wb[6];
#pragma unroll
    for (int j = 0; j < 4; ++j) {
        xv[j] = 4096 + (hw * 32 + j * 8 + r) * (D_ * 2) + ((pos ^ ((j & 1) * 4 + (r >> 1))) << 4) - j * 1024;
        wa[j] = 4096 + (hw * 16 + (j >> 1) * 8 + r) * (3 * D_ * 2) + (j & 1) * 128 + ((pos ^ (6 * ((r >> 1) & 1))) << 4) - j * 1024;
    }
#pragma unroll
    for (int j = 0; j < 6; ++j) wb[j] = 4096 + (hw * 16 + (lane >> 2)) * (F_ * 2) + (j % 3) * 64 + (((lane & 3) ^ (j < 3 ? 0 : 2)) << 4) - (j % 3) * 1024;
    xp -= 4096; wq -= 4096; wgp -= 4096; wup -= 4096;
    int kb_ = 0, xs = ra, dws = 0, left = 5;
    auto issue1 = [&](bool pa) {
        if (ABL & 4) return;
        if (pa) glds_x4(wq + (unsigned)((kb_ + xs) * 64 * 3 * D_ * 2), wa, lds0 + dws * P1_WSLOT + hw * 4096);
        else glds_x3(wgp + (unsigned)((kb_ + xs) * 64 * F_ * 2), wb, lds0 + dws * P1_WSLOT + hw * 6144);
        SBAR();
    };
    auto issue2 = [&](bool pa, bool xx) {
        SBAR();
        if (ABL & 4) return;
        if (!pa) glds_x3(wup + (unsigned)((kb_ + xs) * 64 * F_ * 2), wb + 3, lds0 + dws * P1_WSLOT + hw * 6144 + 3072);
        xs = xs == 4 ? 0 : xs + 1; dws = dws == 2 ? 0 : dws + 1; --left; kb_ = left == 0 ? 5 - kb_ : kb_; left = left == 0 ? 5 : left;
        if (xx) glds_x4(xp + (kb_ + xs) * 128, xv, lds0 + P1_X0 + xs * P1_XSLOT + hw * 4096);
    };
    glds_x4(xp + ra * 128, xv, lds0 + P1_X0 + ra * P1_XSLOT + hw * 4096);
    issue1(true); issue2(true, true);
    __builtin_amdgcn_s_barrier();
    unsigned long long ep = 0;
    asm volatile("global_load_dwordx2 %0, %1, %2 sc1" : "+v"(ep) : "v"(0u), "s"(ws + WS_EPOCH) : "memory");
    __builtin_amdgcn_s_setprio(HPRIO);
    const int r16 = lane & 15, h = lane >> 5, blk = (lane >> 4) & 1, q = (lane >> 2) & 3, fsw = 3 * (q >> 1);
    const unsigned olo = r16 < 2 ? 0x3f803f80u : 0u, ohi = (r16 >> 1) == 1 ? 0x3f803f80u : 0u;
    const s16x8 ones = __builtin_bit_cast(s16x8, (u32x4{olo, olo, ohi, ohi}));
    const int idx = (r16 & 1) ? 0xEEEE : 0x4444;
    int ws3 = 0;
    {
        lds_ptr b = lds + h * 2048 + (hw >> 1) * 1024 + q * 128 + (lane & 3) * 8 + ((((hw & 1) * 2 + blk) ^ fsw) << 5);
        const int so = P1_X0 + ra * P1_XSLOT + (hw * 32 + r16) * 128 + (((lane >> 4) ^ (r16 >> 1)) << 4);
        lds_ptr sp0 = lds + so, sp1 = lds + (so ^ 64);
        const s16x8 ones2 = __builtin_bit_cast(s16x8, (u32x4{olo, olo, olo, olo}));
        f32x4 cs = f32x4{}, s1[2] = {f32x4{}, f32x4{}}, s2[2] = {f32x4{}, f32x4{}};
        s16x16 wv0, wv1; s16x8 xa[2], xb[2];
        auto col_rd = [&]() {
            wv0 = CAT16(CAT8(trread(b), trread(b + 512)), CAT8(trread(b + 4096), trread(b + 4096 + 512)));
            wv1 = CAT16(CAT8(trread(b + 8192), trread(b + 8192 + 512)), CAT8(trread(b + 12288), trread(b + 12288 + 512)));
            b += ws3 == 2 ? -2 * P1_WSLOT : P1_WSLOT; ws3 = ws3 == 2 ? 0 : ws3 + 1;
        };
        auto row_rd = [&](int dx) {
#pragma unroll
            for (int t = 0; t < 2; ++t) { xa[t] = *(const LDS_AS s16x8*)(sp0 + t * 2048); xb[t] = *(const LDS_AS s16x8*)(sp1 + t * 2048); }
            sp0 += dx; sp1 += dx;
        };
        auto row_mm = [&]() {
#pragma unroll
            for (int t = 0; t < 2; ++t) { s2[t] = MFMA16(xa[t], xa[t], s2[t]); s1[t] = SMF16(ones2, CAT16(xa[t], xb[t]), s1[t], idx); }
#pragma unroll
            for (int t = 0; t < 2; ++t) { s2[t] = MFMA16(xb[t], xb[t], s2[t]); PIN(s1[t]); PIN(s2[t]); }
        };
#pragma unroll
        for (int nn = 0; nn < 9; ++nn) {
            int n = nn;
            if (!HUNR) asm volatile("" : "+s"(n));
            PSTAMP(2 * n);
            if (n == 0) WAIT_BAR(5); else if (n < 8) WAIT_BAR(12); else WAIT_BAR(8);
            PSTAMP(2 * n + 1);
            if (!(ABL & 8) || n == 0) { col_rd(); row_rd(n % 5 == 4 - ra ? -4 * P1_XSLOT : P1_XSLOT); }
            if (n == 0) { issue1(true); issue2(true, true); }
            if (n == 8) { kb_ = 5; xs = rb; }
            issue1(n < 8);
            if (!(ABL & 8)) {
            cs = SMF16(ones, wv0, cs, idx);
            row_mm();
            cs = SMF16(ones, wv1, cs, idx); PIN(cs); }
            issue2(n < 8, n < 7);
        }
        MEMFENCE(); __builtin_amdgcn_s_waitcnt(0xF70 | 10); MEMFENCE();
        row_rd(0); row_mm();
        {
            const int t = blk;
            const float d0 = (lane & 2) ? ((lane & 1) ? s2[0][3] : s2[0][2]) : ((lane & 1) ? s2[0][1] : s2[0][0]);
            const float d1 = (lane & 2) ? ((lane & 1) ? s2[1][3] : s2[1][2]) : ((lane & 1) ? s2[1][1] : s2[1][0]);
            const float a0 = __shfl(s1[0][0] + s1[0][1], r16), a1 = __shfl(s1[1][0] + s1[1][1], r16), c0 = __shfl(d0, r16 + 16 * q), c1 = __shfl(d1, r16 + 16 * q);
            const float a = t ? a1 : a0, c = t ? c1 : c0;
            const float mean = a * (1.0f / D_), var = fmaxf(c * (1.0f / D_) - mean * mean, 0.f);
            const float rstd = __builtin_amdgcn_rcpf(rbf(__builtin_amdgcn_sqrtf(rbf(rbf(var) + 1e-5f))));
            if (lane < 32) ((LDS_AS f32x2*)(lds + P1_STAT))[hw * 32 + lane] = f32x2{rstd, -rbf(mean)};
        }
        PSTAMP(18);
        WAIT_BAR(6);
        PSTAMP(19);
        col_rd();
        issue1(false);
        cs = SMF16(ones, wv0, cs, idx);
        cs = SMF16(ones, wv1, cs, idx); PIN(cs);
        issue2(false, false);
        LDS_AS float* colf = (LDS_AS float*)(lds + P1_COLA) + hw * 32;
        if (lane < 16) { colf[lane] = cs[0] + cs[1]; colf[16 + lane] = cs[2] + cs[3]; }
    }
    asm volatile("" : "+v"(ep));
    const unsigned long long tag = (((unsigned long long)(unsigned)__builtin_amdgcn_readfirstlane((unsigned)(ep >> 32)) << 32) | (unsigned)__builtin_amdgcn_readfirstlane((unsigned)ep)) + 1ull;
    if (hw == 0 && lane == 0) *(LDS_AS unsigned long long*)(lds + P1_TAG) = tag;
    {
        const int ro = P1_WSLOT + 3 * blk * 1024 + h * 512 + q * 64 + (lane & 3) * 8 + (((hw & 1) ^ blk) << 5);
        lds_ptr b = lds + ro + (hw >> 1) * 1024, c = lds + ro + 2048 + (hw >> 1) * 12288;
        f32x4 cs = f32x4{}, ct = f32x4{};
        Poll pl = {};
#pragma unroll
        for (int nn = 10; nn < 20; ++nn) {
            int n = nn;
            if (!HUNR) asm volatile("" : "+s"(n));
            PSTAMP(2 * n);
            if (n < 13 || n == 19) WAIT_BAR(6); else if (n == 13) WAIT_BAR(10); else if (n == 18) WAIT_BAR(12); else WAIT_BAR(14);
            PSTAMP(2 * n + 1);
            const s16x16 wv0 = CAT16(CAT8(trread(b), trread(b + 256)), CAT8(trread(b + 6144), trread(b + 6144 + 256)));
            const s16x16 wv2 = CAT16(CAT8(trread(c), trread(c + 256)), CAT8(trread(c + 6144), trread(c + 6144 + 256)));
            const s16x16 wv1 = CAT16(CAT8(trread(b + 12288), trread(b + 12288 + 256)), CAT8(trread(b + 18432), trread(b + 18432 + 256)));
            if (n == FA_POS && hw == 0 && lane == 0) raise_flag(ws, WS_FA, tag);
            if (n < 18) issue1(false);
            cs = SMF16(ones, wv0, cs, idx);
            ct = SMF16(ones, wv2, ct, idx);
            cs = SMF16(ones, wv1, cs, idx); PIN(cs); PIN(ct);
            if (n < 18) issue2(false, n >= 12 && n < 17);
            if (n == 17) poll_request(pl, ws + WS_FA, lane * 16);
            if (n == 18) {
                poll_wait(pl, ws + WS_FA, lane * 16, tag);
#pragma unroll
                for (int j = 0; j < 6; ++j) glds16c(kv + (hw + 4 * j) * 1024, lane * 16, lds0 + K2_C0 + (hw + 4 * j) * 1024);
            }
            if (n == 19) {
#pragma unroll
                for (int j = 0; j < 3; ++j) glds16c(qsrc + (hw + 4 * j) * 1024, lane * 16, lds0 + K2_Q0 + (hw + 4 * j) * 1024);
            }
            const int dw = ws3 == 2 ? -2 * P1_WSLOT : P1_WSLOT;
            b += dw; c += dw; ws3 = ws3 == 2 ? 0 : ws3 + 1;
        }
        LDS_AS float* colf = (LDS_AS float*)(lds + P1_COLB);
        if (lane < 16) { colf[hw * 32 + lane] = cs[0] + cs[1]; colf[hw * 32 + 16 + lane] = cs[2] + cs[3]; }
        if (lane < 16) { colf[128 + hw * 32 + lane] = ct[0] + ct[1]; colf[128 + hw * 32 + 16 + lane] = ct[2] + ct[3]; }
        WAIT_BAR(0);
        if (blockIdx.x == 0 && hw == 0 && lane == 0) __hip_atomic_store((gu64*)(ws + WS_EPOCH), tag, __ATOMIC_RELAXED, __HIP_MEMORY_SCOPE_AGENT);
#pragma unroll
        for (int j = 0; j < 6; ++j) glds16c(kv + K2_CHUNK + (hw + 4 * j) * 1024, lane * 16, lds0 + K2_KV0 + K2_SLOTB + (hw + 4 * j) * 1024);
    }
    return tag;
}

constexpr int K2_XCH = 2 * K2_SLOTB;

#define SMF32(a, b, c) __builtin_amdgcn_smfmac_f32_32x32x32_bf16((bfx8)(a), __builtin_bit_cast(bfx16, (b)), (c), 0xDDDD, 0, 0)
#define MFMA8(a, b, c) __builtin_amdgcn_mfma_f32_32x32x8bf16_1k((a), (b), (c), 0, 0, 0)
__device__ __forceinline__ f32x8 lo8(const f32x16& v) { return __builtin_shufflevector(v, v, 0, 1, 2, 3, 4, 5, 6, 7); }
__device__ __forceinline__ f32x8 hi8(const f32x16& v) { return __builtin_shufflevector(v, v, 8, 9, 10, 11, 12, 13, 14, 15); }
__device__ __forceinline__ float max16(const f32x16& s) {
    float m = fmaxf(fmaxf(s[0], s[1]), s[2]);
#pragma unroll
    for (int r = 3; r < 15; r += 2) m = fmaxf(fmaxf(m, s[r]), s[r + 1]);
    return swap32_max(fmaxf(m, s[15]));
}

struct K2B { lds_ptr r0, r1, r2; const char* wsrc; unsigned wdst; int lane, osoff; rsrc_t wsr; };
constexpr int k2_slot(int st) { return st < 3 ? st : st == 3 ? 0 : st == 4 ? 3 : st == 5 ? 1 : st == 6 ? 2 : st == 7 ? 0 : 3; }
__device__ __forceinline__ void k2_wpiece(const K2B& c, int st, int i) {
    glds16c(c.wsrc + st * 40960 + i * 8192, c.lane * 16, c.wdst + k2_slot(st) * 40960 + i * 8192);
}
template <int I, int D, int NP, int OF>
__device__ __forceinline__ void k2_bstep(const K2B& c, f32x4 (&acc)[10], const s16x8 (&w)[5], s16x8 (&n)[5], const s16x8& a0, const s16x8& a1, s16x8 (&of)[10]) {
    constexpr int O = k2_slot((I + 1) >> 1) * 40960 + ((I + 1) & 1) * 20480;
#pragma unroll
    for (int i = 0; i < 10; ++i) {
        if (I > 0 && I < 16 && i < 5) n[i] = *(const LDS_AS s16x8*)((O < 61440 ? c.r0 : O < 122880 ? c.r1 : c.r2) + O % 61440 + i * 1024);
        acc[i] = MFMA16(w[i >> 1], (i & 1) ? a1 : a0, acc[i]);
        PIN(acc[i]);
        if (OF >= 0 && !(i & 1)) { const int k = (OF < 0 ? 0 : OF) + (i >> 1); of[k] = LD16(c.wsr, c.lane * 16, c.osoff + ((k & 1) * 20 + (k >> 1) * 4) * 1024); MEMFENCE(); }
        if ((i & 1) && (i >> 1) < NP) k2_wpiece(c, D, i >> 1);
        SBAR();
    }
}

__global__ __launch_bounds__(512, 2) void fused_block(const char* __restrict__ x, const char* __restrict__ wqkv, const char* __restrict__ wg, const char* __restrict__ wu,
                                                      const char* __restrict__ wd, const char* __restrict__ wo, char* __restrict__ ws, char* __restrict__ out) {
    extern __shared__ __attribute__((aligned(16))) char smem[];
    asm volatile("" :: "s"(x), "s"(wqkv), "s"(wg), "s"(wu), "s"(wd), "s"(wo), "s"(ws), "s"(out));
    const int wid = __builtin_amdgcn_readfirstlane(threadIdx.x >> 6);
    STAMP_DECL; STAMP(0); PSTAMP(63);
    unsigned long long tag = 0;
    if (wid < 4) {
        __builtin_amdgcn_s_barrier();
        if (CODEWARM && wid == 3) {
            unsigned off = ((blockIdx.x >> 3) * 12 + min(threadIdx.x & 63, 11)) * 128;
            asm volatile("s_getpc_b64 vcc\n.Lfb_begin:\n\tv_min_u32 %0, .Lfb_end-.Lfb_begin, %0\n\ts_mov_b32 m0, %1\n\ts_nop 0\n\tglobal_load_lds_dword %0, vcc"
                         : "+v"(off) : "s"((unsigned)(uintptr_t)(lds_ptr)smem + LDS_JUNK) : "vcc", "memory");
        }
    }
    const rsrc_t wsr = __builtin_amdgcn_make_buffer_rsrc(ws, 0, (int)WS_END, 0x00020000);
    const int bid = blockIdx.x, head = (bid & 7) * 4 + ((bid >> 3) & 3), par = wid >> 2, qt0 = (bid >> 5) * 8 + (wid & 3) * 2;
    const char* kv = ws + WS_KV + head * KV_HEAD;
    {
        const int bi = bid >> 3, panel = (bid & 1) * 8 + (bi & 7), grp = ((bid & 7) >> 1) * 4 + (bi >> 3), l0 = threadIdx.x & 63;
        const int rb = K1_ROT ? (5 * (bi & 7)) >> 3 : 0, ra = ROTA == 1 ? (rb + (bi >> 3)) % 5 : ROTA == 2 ? ((bi & 1) * 2 + ((bi >> 3) & 1) * 2) % 4 : 0;
        if (wid < 4) {
            p1_compute((lds_ptr)smem, wid, l0, panel, grp, ra, rb, wsr, Pack{wd, wo, wid * 256 + bid} STAMP_PASS);
            const u32x2 e = *(const LDS_AS u32x2*)((lds_ptr)smem + P1_TAG);
            tag = ((unsigned long long)(unsigned)__builtin_amdgcn_readfirstlane(e[1]) << 32) | (unsigned)__builtin_amdgcn_readfirstlane(e[0]);
        } else {
            tag = p1_helper((lds_ptr)smem, wid - 4, l0, x + (size_t)panel * 128 * (D_ * 2), wqkv + min(grp, 14) * 256, wg + grp * 192, wu + grp * 192, ra, rb,
                            ws, kv, ws + WS_Q + head * Q_HEAD + (bid >> 5) * 8 * Q_TILE);
        }
    }
    int lane = __builtin_amdgcn_mbcnt_hi(~0u, __builtin_amdgcn_mbcnt_lo(~0u, 0u));
    PIN(lane);
    if (wid < 4) __builtin_amdgcn_s_setprio(3); else __builtin_amdgcn_s_setprio(0);
    STAMP(4);
    lds_ptr lds = (lds_ptr)smem + K2_KV0;
    const unsigned lds0 = (unsigned)(uintptr_t)lds;
    const int tp = ((blockIdx.x & 7) >> 1) * 8 + ((blockIdx.x >> 3) & 7), cg = (blockIdx.x & 1) * 4 + (blockIdx.x >> 6);
    K2B kb;
    kb.wsrc = ws + WS_WP + cg * 68 * 5120 + wid * 1024; kb.wdst = (unsigned)(uintptr_t)(lds_ptr)smem + wid * 1024; kb.lane = lane;
    const int hoff = WS_H16 + ((tp * 4 + (wid & 1) * 2) * 48 + (wid >> 1)) * 1024;
    s16x8 hf[24];
    const int h = lane >> 5, l31 = lane & 31, l16 = lane * 16;
    const char* src = kv + wid * 1024 + 3 * K2_CHUNK;
    const int k1off = h * 512 + l31 * 8, k2off = h ? KV_V + 5 * 256 : 1024 + l31 * 8;
    const int vq = min(l31 >> 2, 5), voff = KV_V + vq * 256 + ((h ^ (vq & 3)) << 6) + (l31 < 24 ? l31 & 3 : 1) * 16;

    f32x16 oa[2] = {f32x16{}, f32x16{}}, sc[2];
    s16x8 k1[2], v0[2], v1[2]; s16x4 k2[2];
    const lds_ptr c0 = (lds_ptr)smem + K2_C0;
    lds_ptr pk1 = c0 + k1off + par * KV_TILE, pk2 = c0 + k2off + par * KV_TILE, pv = c0 + voff + par * KV_TILE, pw = c0 + (voff ^ 128) + par * KV_TILE;
    lds_ptr pk3 = pk1 + 256;
    asm volatile("" : "+v"(pk3));
#define LDS8(p) (*(const LDS_AS s16x4*)(p))
#define RD_K(t, j) do { k1[(t) & 1] = CAT8(LDS8(pk1 + (j) * 2 * KV_TILE), LDS8(pk3 + (j) * 2 * KV_TILE)); k2[(t) & 1] = LDS8(pk2 + (j) * 2 * KV_TILE); } while (0)
#define RD_V(t, j) do { v0[(t) & 1] = *(const LDS_AS s16x8*)(pv + (j) * 2 * KV_TILE); v1[(t) & 1] = *(const LDS_AS s16x8*)(pw + (j) * 2 * KV_TILE); } while (0)
    s16x8 q1[2]; s16x4 q2[2];
#pragma unroll
    for (int b = 0; b < 2; ++b) {
        const lds_ptr qp = (lds_ptr)smem + K2_Q0 + ((wid & 3) * 2 + b) * Q_TILE;
        q1[b] = CAT8(LDS8(qp + k1off), LDS8(qp + k1off + 256)); q2[b] = LDS8(qp + 1024 + l31 * 8);
    }
    {
        const s16x8 ka = CAT8(LDS8(c0 + k1off), LDS8(c0 + k1off + 256));
        const s16x4 kb = LDS8(c0 + k2off);
        RD_K(0, 0); RD_V(0, 0);
#pragma unroll
        for (int b = 0; b < 2; ++b) {
            if (h) q2[b] = s16x4{0, 0, 0, 0};
            f32x16 s = MFMA32(ka, q1[b], f32x16{});
            s = MFMA8(kb, q2[b], s);
            const float m = rbf(-max16(s));
            if (h) q2[b][0] = (short)(__float_as_uint(m) >> 16);
        }
        sc[0] = MFMA32(k1[0], q1[0], f32x16{});
        sc[0] = MFMA8(k2[0], q2[0], sc[0]);
        sc[1] = f32x16{}; v1[1] = s16x8{};
    }
    STAMP(5); PSTAMP(42); PSTAMP(43);
#define EXP4(S, i) do { S[i] = __builtin_amdgcn_exp2f(S[i]); S[i + 1] = __builtin_amdgcn_exp2f(S[i + 1]); S[i + 2] = __builtin_amdgcn_exp2f(S[i + 2]); S[i + 3] = __builtin_amdgcn_exp2f(S[i + 3]); PIN(S); SBAR(); } while (0)
    Poll pl = {};
    auto request = [&](int hs) {
        const int p = (hs - 6) >> 3, r = (hs - 6) & 7;
        const int w = (hs >= 9 && hs < 14) ? hs - 9 : (hs >= 17 && hs < 22) ? hs - 12 : (hs >= 54 && hs < 57) ? hs - 44 : hs >= 62 ? hs - 49 : -1;
        const int e = (p >= 2 && p <= 4 && r >= 3) ? (p - 2) * 5 + r - 3 : p == 5 ? 15 + r : hs == 57 ? 23 : -1;
        if (hs < 3) glds16c(src - K2_CHUNK + hs * 8192, l16, lds0 + 2 * K2_SLOTB + wid * 1024 + hs * 8192);
        else if (hs >= 6 && p < 5 && r < 3) glds16c(src + p * K2_CHUNK + r * 8192, l16, lds0 + (p % 3) * K2_SLOTB + wid * 1024 + r * 8192);
        else if (w >= 0) glds16c(kb.wsrc + w * 8192, l16, kb.wdst + w * 8192);
        else if (e >= 0) { hf[e] = LD16(wsr, l16, hoff + e * 4096); MEMFENCE(); }
        if (hs == 6 && wid == 4 && lane == 0) raise_flag(ws, WS_FB, tag);
        if (hs == 16 && wid == 4) poll_request(pl, ws + WS_FB, l16);
        SBAR();
    };
#pragma unroll
    for (int c = 0; c < 8; ++c) {
#pragma unroll
        for (int j = 0; j < 4; ++j) {
            if (j == 3) {
                PSTAMP(44 + 2 * c);
                if (c == 2 && wid == 4) poll_wait(pl, ws + WS_FB, l16, tag);
                if (c == 0) { asm volatile("s_waitcnt vmcnt(0)" ::: "memory"); LGKM_BAR(); } else if (c == 7) WAIT_BAR(0); else WAIT_BAR(7);
                PSTAMP(45 + 2 * c);
                __builtin_amdgcn_s_setprio(2);
                const int dsl = c == 0 ? K2_KV0 + K2_SLOTB - K2_C0 : (c % 3 == 2) ? -2 * K2_SLOTB : K2_SLOTB;
                pk1 += dsl; pk2 += dsl; pk3 += dsl; pv += dsl; pw += dsl;
                asm volatile("" : "+v"(pk1), "+v"(pk2), "+v"(pk3), "+v"(pv), "+v"(pw));
                SBAR();
            }
            RD_K(j + 1, (j + 1) & 3); SBAR();
            oa[1] = SMF32(v1[(j + 1) & 1], hi8(sc[1]), oa[1]); PIN(oa[1]); SBAR();
            EXP4(sc[0], 0);
            sc[1] = MFMA32(k1[j & 1], q1[1], f32x16{}); PIN(sc[1]); SBAR();
            request(8 * c + 2 * j);
            EXP4(sc[0], 4);
            sc[1] = MFMA8(k2[j & 1], q2[1], sc[1]); PIN(sc[1]); SBAR();
            EXP4(sc[0], 8);
            oa[0] = SMF32(v0[j & 1], lo8(sc[0]), oa[0]); PIN(oa[0]); SBAR();
            EXP4(sc[0], 12);
            RD_V(j + 1, (j + 1) & 3);
            if (j == 1 && c > 0) __builtin_amdgcn_s_setprio(1);
            if (j == 2 && c > 0) __builtin_amdgcn_s_setprio(0);
            SBAR();
            oa[0] = SMF32(v1[j & 1], hi8(sc[0]), oa[0]); PIN(oa[0]); SBAR();
            EXP4(sc[1], 0);
            sc[0] = MFMA32(k1[(j + 1) & 1], q1[0], f32x16{}); PIN(sc[0]); SBAR();
            request(8 * c + 2 * j + 1);
            EXP4(sc[1], 4);
            sc[0] = MFMA8(k2[(j + 1) & 1], q2[0], sc[0]); PIN(sc[0]); SBAR();
            EXP4(sc[1], 8);
            oa[1] = SMF32(v0[j & 1], lo8(sc[1]), oa[1]); PIN(oa[1]); SBAR();
            EXP4(sc[1], 12);
        }
    }
    oa[1] = SMF32(v1[1], hi8(sc[1]), oa[1]);
    unsigned qs_ = 0; (void)qs_;
    STAMP(2);
    PSTAMP(60); QSTAMP(60);
    int ln = __builtin_amdgcn_mbcnt_hi(~0u, __builtin_amdgcn_mbcnt_lo(~0u, 0u));
    PIN(ln);
    const int h2 = ln >> 5, l2 = ln & 31;
    const int mt = wid & 1, kq = wid >> 1;
    kb.lane = ln; kb.r0 = (lds_ptr)smem + kq * 5120 + ln * 16; kb.r1 = kb.r0 + 61440; kb.r2 = kb.r0 + 122880;
    asm volatile("" : "+v"(kb.r1), "+v"(kb.r2));
    kb.wsr = wsr; kb.osoff = WS_O16 + ((tp * 4 + mt * 2) * 20 + kq) * 1024;
    s16x8 wa[5], wb[5];
#pragma unroll
    for (int i = 0; i < 5; ++i) { wa[i] = *(const LDS_AS s16x8*)(kb.r0 + i * 1024); wb[i] = *(const LDS_AS s16x8*)(kb.r0 + 20480 + i * 1024); }
    {
        const lds_ptr xp_ = lds + K2_XCH + (wid & 3) * 6144 + ln * 16;
        if (par) {
#pragma unroll
            for (int i = 0; i < 6; ++i) { const f32x16& g = oa[i / 3]; const int r = 4 * (i % 3); *(LDS_AS f32x4*)(xp_ + i * 1024) = f32x4{g[r], g[r + 1], g[r + 2], g[r + 3]}; }
        }
        LGKM_BAR();
        QSTAMP(21);
        if (!par) {
            __builtin_amdgcn_s_setprio(3);
            f32x4 tt[6];
#pragma unroll
            for (int i = 0; i < 6; ++i) tt[i] = *(const LDS_AS f32x4*)(xp_ + i * 1024);
            SBAR();
#pragma unroll
            for (int i = 0; i < 6; ++i) { const f32x4 t = tt[i]; f32x16& g = oa[i / 3]; const int r = 4 * (i % 3); g[r] = add1(g[r], t[0]); g[r + 1] = add1(g[r + 1], t[1]); g[r + 2] = add1(g[r + 2], t[2]); g[r + 3] = add1(g[r + 3], t[3]); }
            const int ob = (l2 >> 4) * 20 * 1024 + (l2 & 15) * 16, os = WS_O16 + qt0 * 2 * 20 * 1024;
            const int o1 = ob + h2 * 256 + os + (head >> 1) * 1024 + (head & 1) * 512, o2 = os + (16 + (head >> 3)) * 1024 + ((head >> 1) & 3) * 256 + (head & 1) * 8;
            auto put = [&](const f32x16& o, int b, u32x2& e) {
                const float l = __uint_as_float(__builtin_amdgcn_permlane32_swap(__float_as_uint(o[8]), __float_as_uint(o[8]), false, false)[1]);
                const float rl = __builtin_amdgcn_rcpf(l);
                unsigned w[6];
#pragma unroll
                for (int i = 0; i < 6; ++i) w[i] = cvtpk(mul1(o[2 * i], rl), mul1(o[2 * i + 1], rl));
                ST16(wsr, (u32x4{w[0], w[1], w[2], w[3]}), o1, b * 40 * 1024);
                e = u32x2{w[4], w[5]};
                return l;
            };
            u32x2 e0, e1;
            QSTAMP(22);
            const float l0 = put(oa[0], 0, e0), l1 = put(oa[1], 1, e1);
            ST8(wsr, (u32x2{__builtin_amdgcn_permlane32_swap(e0[0], e1[0], false, false)[0], __builtin_amdgcn_permlane32_swap(e0[1], e1[1], false, false)[0]}), ob + h2 * 40 * 1024, o2);
            MEMFENCE(); SBAR();
            QSTAMP(23);
            float big = 0.f;
#pragma unroll
            for (int r = 0; r < 12; ++r) big = add1(big, add1(fabsf(oa[0][r]), fabsf(oa[1][r])));
            if (__builtin_expect(__any(!(big < 1e30f) | !(fminf(l0, l1) > 1e-30f)), 0)) {
                const int g1 = h2 * 512 + l2 * 8, g2 = 1024 + l2 * 8, gq = min(l2 >> 2, 5), gv = KV_V + gq * 256 + ((h2 ^ (gq & 3)) << 6) + (l2 & 3) * 16;
                asm volatile("s_waitcnt vmcnt(0)" ::: "memory");
#pragma unroll 1
                for (int b = 0; b < 2; ++b) {
                    const s16x8 qf1 = b ? q1[1] : q1[0];
                    s16x4 qf2 = b ? q2[1] : q2[0];
                    if (h2) qf2[0] = 0;
                    float m = -INFINITY;
                    f32x16 negm, o = f32x16{};
#pragma unroll 1
                    for (int t = 0; t < 64; ++t) {
                        const int g = WS_KV + head * KV_HEAD + t * KV_TILE;
                        f32x16 s = MFMA32(CAT8(LD8(wsr, g1, g), LD8(wsr, g1 + 256, g)), qf1, f32x16{});
                        s = MFMA8(LD8(wsr, g2, g), qf2, s);
                        m = fmaxf(m, max16(s));
                    }
#pragma unroll
                    for (int r = 0; r < 16; ++r) negm[r] = -m;
#pragma unroll 1
                    for (int t = 0; t < 64; ++t) {
                        const int g = WS_KV + head * KV_HEAD + t * KV_TILE;
                        f32x16 s = MFMA32(CAT8(LD8(wsr, g1, g), LD8(wsr, g1 + 256, g)), qf1, negm);
                        s = MFMA8(LD8(wsr, g2, g), qf2, s);
#pragma unroll
                        for (int r = 0; r < 16; ++r) s[r] = __builtin_amdgcn_exp2f(s[r]);
                        o = SMF32(LD16(wsr, gv, g), lo8(s), o);
                        o = SMF32(LD16(wsr, gv ^ 128, g), hi8(s), o);
                    }
                    u32x2 e;
                    put(o, b, e);
                    if (!h2) ST8(wsr, e, ob + b * 40 * 1024, o2);
                }
            }
        }
    }
    PSTAMP(61); QSTAMP(61);
    if (DIAG_PF2) { PFLUSH(); }
    __builtin_amdgcn_s_setprio(0);
    const int ch0 = wid * 128 + ln, ch1 = ch0 + 64, er0 = ch0 / 10, er1 = ch1 / 10, ec0 = ch0 - er0 * 10, ec1 = ch1 - er1 * 10;
    const size_t eb = (size_t)tp * 64 * (D_ * 2) + cg * 160;
    const unsigned eo0 = er0 * (D_ * 2) + ec0 * 16, eo1 = er1 * (D_ * 2) + ec1 * 16;
    f32x4 acc[10];
#pragma unroll
    for (int i = 0; i < 10; ++i) acc[i] = f32x4{};
    s16x8 of[10];
    QSTAMP(0); k2_bstep<0, 0, 0, -1>(kb, acc, wa, wb, hf[0], hf[12], of);
    QSTAMP(1); k2_bstep<1, 0, 0, -1>(kb, acc, wb, wa, hf[1], hf[13], of);
    QSTAMP(2); k2_bstep<2, 0, 0, -1>(kb, acc, wa, wb, hf[2], hf[14], of);
    if (!par) {
        asm volatile("s_waitcnt vmcnt(0)" ::: "memory");
        if (ln == 0) __hip_atomic_store((gu64*)(ws + WS_FC) + (qt0 >> 1) * 32 + head, tag, __ATOMIC_RELAXED, __HIP_MEMORY_SCOPE_AGENT);
    } else {
#pragma unroll
        for (int i = 0; i < 10; ++i) glds16c(kb.wsrc + 3 * 40960 - 4096 + i * 4096, ln * 16, kb.wdst - 4096 + i * 4096);
    }
    WAIT_BAR(10);
    QSTAMP(3); k2_bstep<3, 4, 5, -1>(kb, acc, wb, wa, hf[3], hf[15], of);
    QSTAMP(4); k2_bstep<4, 5, 5, -1>(kb, acc, wa, wb, hf[4], hf[16], of);
    WAIT_BAR(10);
    QSTAMP(5); k2_bstep<5, 6, 5, -1>(kb, acc, wb, wa, hf[5], hf[17], of);
    unsigned long long seen = 0;
    const char* cw = ws + WS_FC + tp * 256;
    const unsigned cvo = l2 * 8;
    if (wid == 4) asm volatile("global_load_dwordx2 %0, %1, %2 sc1" : "+v"(seen) : "v"(cvo), "s"(cw) : "memory");
    SBAR();
    QSTAMP(6); k2_bstep<6, 0, 0, -1>(kb, acc, wa, wb, hf[6], hf[18], of);
    if (wid == 4) {
        asm volatile("s_waitcnt vmcnt(0)" : "+v"(seen) :: "memory");
#pragma unroll 1
        for (unsigned spin = 0; spin < (1u << 18) && !__all(seen == tag); ++spin) {
            __builtin_amdgcn_s_sleep(1);
            asm volatile("global_load_dwordx2 %0, %1, %2 sc1\n\ts_waitcnt vmcnt(0)" : "=&v"(seen) : "v"(cvo), "s"(cw) : "memory");
        }
    }
    WAIT_BAR(10);
    QSTAMP(7); k2_bstep<7, 0, 0, 0>(kb, acc, wb, wa, hf[7], hf[19], of);
    QSTAMP(8); k2_bstep<8, 0, 0, 5>(kb, acc, wa, wb, hf[8], hf[20], of);
    WAIT_BAR(15);
    QSTAMP(9); k2_bstep<9, 7, 5, -1>(kb, acc, wb, wa, hf[9], hf[21], of);
    QSTAMP(10); k2_bstep<10, 8, 3, -1>(kb, acc, wa, wb, hf[10], hf[22], of);
    WAIT_BAR(8);
    QSTAMP(11); k2_bstep<11, 0, 0, -1>(kb, acc, wb, wa, hf[11], hf[23], of);
    QSTAMP(12); k2_bstep<12, 0, 0, -1>(kb, acc, wa, wb, of[0], of[1], of);
    WAIT_BAR(3);
    QSTAMP(13); k2_bstep<13, 0, 0, -1>(kb, acc, wb, wa, of[2], of[3], of);
    QSTAMP(14); k2_bstep<14, 0, 0, -1>(kb, acc, wa, wb, of[4], of[5], of);
    WAIT_BAR(0);
    u32x4 xv0 = u32x4{}, xv1 = u32x4{};
    if (wid < 5) { xv0 = *(const u32x4*)(x + eb + eo0); xv1 = *(const u32x4*)(x + eb + eo1); }
    MEMFENCE(); SBAR();
    QSTAMP(15); k2_bstep<15, 0, 0, -1>(kb, acc, wb, wa, of[6], of[7], of);
    QSTAMP(16); k2_bstep<16, 0, 0, -1>(kb, acc, wa, wb, of[8], of[9], of);
    QSTAMP(17);
    {
        const int tk = ln & 15;
        const lds_ptr pp = (lds_ptr)smem + kq * 20480 + (mt * 32 + tk) * 320 + (((ln >> 4) ^ ((tk >> 1) & 3)) << 4);
#pragma unroll
        for (int i = 0; i < 10; ++i) *(LDS_AS f32x4*)(pp + (i & 1) * 16 * 320 + (i >> 1) * 64) = acc[i];
        QSTAMP(18);
        LGKM_BAR();
        QSTAMP(19);
        if (wid < 5) {
#pragma unroll
            for (int e = 0; e < 2; ++e) {
                const int er = e ? er1 : er0, ec = e ? ec1 : ec0, sw = (er >> 1) & 3;
                const lds_ptr p0 = (lds_ptr)smem + er * 320 + (((2 * ec) ^ sw) << 4), p1 = (lds_ptr)smem + er * 320 + (((2 * ec + 1) ^ sw) << 4);
                f32x4 a = *(const LDS_AS f32x4*)p0, b = *(const LDS_AS f32x4*)p1;
#pragma unroll
                for (int k = 1; k < 4; ++k) { a += *(const LDS_AS f32x4*)(p0 + k * 20480); b += *(const LDS_AS f32x4*)(p1 + k * 20480); }
                const u32x4 xv = e ? xv1 : xv0;
                const u32x4 ov = u32x4{cvtpk(a[0] + bf_lo(xv[0]), a[1] + bf_hi(xv[0])), cvtpk(a[2] + bf_lo(xv[1]), a[3] + bf_hi(xv[1])),
                                       cvtpk(b[0] + bf_lo(xv[2]), b[1] + bf_hi(xv[2])), cvtpk(b[2] + bf_lo(xv[3]), b[3] + bf_hi(xv[3]))};
                __builtin_amdgcn_raw_buffer_store_b128(ov, __builtin_amdgcn_make_buffer_rsrc(out, 0, S_ * D_ * 2, 0x00020000), (int)eb + (e ? eo1 : eo0), 0, OUTAUX);
            }
        }
    }
    QSTAMP(20);
    if (CODEWARM) asm volatile(".Lfb_end:");
    STAMP(7); STAMP_FLUSH(0); if (!DIAG_PF2) { PFLUSH(); }
}

extern "C" void kernel_launch(void* const* d_in, const int* in_sizes, int n_in, void* d_out, int out_size, void* d_ws, size_t ws_size,
                              hipStream_t stream) {
    const char* x = (const char*)d_in[0];
    const char* wqkv = (const char*)d_in[1];
    const char* wo = (const char*)d_in[2];
    const char* wg = (const char*)d_in[4];
    const char* wu = (const char*)d_in[5];
    const char* wd = (const char*)d_in[6];
    char* ws = (char*)d_ws;
    (void)hipFuncSetAttribute((const void*)fused_block, hipFuncAttributeMaxDynamicSharedMemorySize, LDS_BYTES);
    fused_block<<<256, 512, LDS_BYTES, stream>>>(x, wqkv, wg, wu, wd, wo, ws, (char*)d_out);
}
```

```cpp
#include <hip/hip_runtime.h>
#include <hip/hip_bf16.h>
#include <stdint.h>

typedef __bf16 bfx4 __attribute__((ext_vector_type(4)));
typedef __bf16 bfx8 __attribute__((ext_vector_type(8)));
typedef __bf16 bfx16 __attribute__((ext_vector_type(16)));
typedef short s16x4 __attribute__((ext_vector_type(4)));
typedef short s16x8 __attribute__((ext_vector_type(8)));
typedef short s16x16 __attribute__((ext_vector_type(16)));
typedef float f32x2 __attribute__((ext_vector_type(2)));
typedef float f32x4 __attribute__((ext_vector_type(4)));
typedef float f32x8 __attribute__((ext_vector_type(8)));
typedef float f32x16 __attribute__((ext_vector_type(16)));
typedef unsigned u32x2 __attribute__((ext_vector_type(2)));
typedef unsigned u32x4 __attribute__((ext_vector_type(4)));
typedef unsigned u32x8 __attribute__((ext_vector_type(8)));
#define LDS_AS __attribute__((address_space(3)))
typedef LDS_AS char* lds_ptr;

#define ABL 0
#define DPRIO 1
#define CODEWARM 0
#define EVEN 1
#define HPRIO 3
#define CPRIO 0
#define WPWARM 0
#define UNR2 2
#define HUNR 1
#define FA_POS 14
#define ROTA 2
#define OUTAUX 16
#define K1_ROT 1
constexpr int S_ = 2048, D_ = 640, H_ = 32, F_ = 1536;

constexpr int Q_TILE = 1280, Q_HEAD = 64 * Q_TILE, KV_TILE = 2816, KV_V = 1280, KV_HEAD = 64 * KV_TILE;
constexpr int WS_Q = 0;
constexpr int WS_KV = WS_Q + H_ * Q_HEAD;
constexpr int WS_O16 = WS_KV + H_ * KV_HEAD + 8192;
constexpr int WS_H16 = WS_O16 + 128 * 20 * 1024;
constexpr int WS_WP = WS_H16 + 128 * 48 * 1024;
constexpr int WS_EPOCH = WS_WP + 8 * 68 * 5120 + 8192;
constexpr int WS_FA = WS_EPOCH + 256, WS_FB = WS_FA + 2048, WS_FC = WS_FB + 2048, WS_END = WS_FC + 8192;

#define PSTAMP(i) do {} while (0)
#define PFLUSH() do {} while (0)
#define DIAG_PF2 0
#define QSTAMP(i) do {} while (0)
#define STAMP_ARG , unsigned long long (&str_)[2]
#define STAMP_PASS , str_
#define STAMP_DECL unsigned long long str_[2] = {0, 0}
#define STAMP(i) do { if ((i) == 0) { str_[0] = __builtin_amdgcn_s_memrealtime(); str_[1] = str_[0]; } else if ((i) == 0) str_[1] = __builtin_amdgcn_s_memrealtime(); } while (0)
#define STAMP_FLUSH(kern) do { for (int i_ = 0; i_ < 200000 && __builtin_amdgcn_s_memrealtime() < str_[1] + 6000; ++i_) __builtin_amdgcn_s_sleep(1); } while (0)
#define SBAR() __builtin_amdgcn_sched_barrier(0)
#define MFMA32(a, b, c) __builtin_amdgcn_mfma_f32_32x32x16_bf16((bfx8)(a), (bfx8)(b), (c), 0, 0, 0)

__device__ __forceinline__ unsigned cvtpk(float lo, float hi) {
    typedef __bf16 bfx2 __attribute__((ext_vector_type(2)));
    bfx2 r = {(__bf16)lo, (__bf16)hi};
    return __builtin_bit_cast(unsigned, r);
}
__device__ __forceinline__ void glds16(const void* g, unsigned lds_base) {
    unsigned sv;
    asm volatile("s_mov_b32 %0, m0\n\ts_mov_b32 m0, %2\n\ts_nop 0\n\tglobal_load_lds_dwordx4 %1, off\n\ts_mov_b32 m0, %0"
                 : "=&s"(sv) : "v"(g), "s"(lds_base) : "memory");
}
__device__ __forceinline__ s16x4 trread(const LDS_AS char* p) {
    return __builtin_amdgcn_ds_read_tr16_b64_v4i16((LDS_AS s16x4*)p);
}
__device__ __forceinline__ float rbf(float v) { return (float)(__bf16)v; }
__device__ __forceinline__ float bf_lo(unsigned u) { return __uint_as_float(u << 16); }
__device__ __forceinline__ float bf_hi(unsigned u) { return __uint_as_float(u & 0xffff0000u); }
__device__ __forceinline__ float swap32_add(float v) {
    auto r = __builtin_amdgcn_permlane32_swap(__float_as_uint(v), __float_as_uint(v), false, false);
    return __uint_as_float(r[0]) + __uint_as_float(r[1]);
}
__device__ __forceinline__ float swap32_max(float v) {
    auto r = __builtin_amdgcn_permlane32_swap(__float_as_uint(v), __float_as_uint(v), false, false);
    return fmaxf(__uint_as_float(r[0]), __uint_as_float(r[1]));
}

constexpr int P1_WSLOT = 24576, P1_X0 = 3 * P1_WSLOT, P1_XSLOT = 16384, P1_STAT = P1_X0 + 5 * P1_XSLOT;
constexpr int P1_COLA = P1_STAT + 1024, P1_COLB = P1_COLA + 512, P1_TAG = P1_COLB + 1024, LDS_JUNK = 161792, LDS_BYTES = 163840;
constexpr int K2_CHUNK = 8 * KV_TILE, K2_SLOTB = 24576, K2_SLOTS = 3, K2_KV0 = 81920, K2_C0 = 2 * P1_WSLOT, K2_Q0 = 0;
typedef __attribute__((address_space(1))) unsigned long long gu64;

#define PIN(x) asm volatile("" : "+v"(x))
#define MEMFENCE() asm volatile("" ::: "memory")
#define WAIT_BAR(N) do { MEMFENCE(); __builtin_amdgcn_s_waitcnt(0x70 | ((N) & 15) | (((N) >> 4) << 14)); __builtin_amdgcn_s_barrier(); MEMFENCE(); SBAR(); } while (0)
#define LGKM_BAR() do { MEMFENCE(); __builtin_amdgcn_s_waitcnt(0xC07F); __builtin_amdgcn_s_barrier(); MEMFENCE(); SBAR(); } while (0)
#define MFMA16(a, b, c) __builtin_amdgcn_mfma_f32_16x16x32_bf16((bfx8)(a), (bfx8)(b), (c), 0, 0, 0)
#define SMF16(a, b, c, i) __builtin_amdgcn_smfmac_f32_16x16x64_bf16((bfx8)(a), (bfx16)(b), (c), (i), 0, 0)
#define CAT8(a, b) __builtin_shufflevector(a, b, 0, 1, 2, 3, 4, 5, 6, 7)
#define CAT16(a, b) __builtin_shufflevector(a, b, 0, 1, 2, 3, 4, 5, 6, 7, 8, 9, 10, 11, 12, 13, 14, 15)

__device__ __forceinline__ void glds16s(const char* sbase, unsigned voff, unsigned lds_base) {
    unsigned sv;
    asm volatile("s_mov_b32 %0, m0\n\ts_mov_b32 m0, %3\n\ts_nop 0\n\tglobal_load_lds_dwordx4 %1, %2\n\ts_mov_b32 m0, %0"
                 : "=&s"(sv) : "v"(voff), "s"(sbase), "s"(lds_base) : "memory");
}

__device__ __forceinline__ void glds16c(const char* sbase, unsigned voff, unsigned lds_base) {
    asm volatile("s_mov_b32 m0, %2\n\ts_nop 0\n\tglobal_load_lds_dwordx4 %0, %1 sc1" :: "v"(voff), "s"(sbase), "s"(lds_base) : "memory");
}
typedef __amdgpu_buffer_rsrc_t rsrc_t;
#define ST16(rs, v, voff, soff) __builtin_amdgcn_raw_buffer_store_b128((v), (rs), (voff) + (soff), 0, (ABL & 32) ? 0 : 16)
#define ST8(rs, v, voff, soff) __builtin_amdgcn_raw_buffer_store_b64((v), (rs), (voff), (soff), 16)
#define LD16(rs, voff, soff) __builtin_bit_cast(s16x8, __builtin_amdgcn_raw_buffer_load_b128((rs), (voff), (soff), 16))
#define LD8(rs, voff, soff) __builtin_bit_cast(s16x4, __builtin_amdgcn_raw_buffer_load_b64((rs), (voff), (soff), 16))
__device__ __forceinline__ f32x2 pk(const f32x16& a, int i) { return f32x2{a[i], a[i + 1]}; }
__device__ __forceinline__ f32x2 pk4(const f32x4& a, int i) { return f32x2{a[i], a[i + 1]}; }
__device__ __forceinline__ f32x2 pfma(f32x2 a, f32x2 b, f32x2 c) { return __builtin_elementwise_fma(a, b, c); }
__device__ __forceinline__ unsigned cvt2(f32x2 v) { return cvtpk(v[0], v[1]); }
__device__ __forceinline__ float add1(float a, float b) { float r = a + b; asm volatile("" : "+v"(r)); return r; }
__device__ __forceinline__ float mul1(float a, float b) { float r = a * b; asm volatile("" : "+v"(r)); return r; }

__device__ __forceinline__ void raise_flag(char* ws, int arr, unsigned long long tag) {
    __hip_atomic_store((gu64*)(ws + arr) + blockIdx.x, tag, __ATOMIC_RELAXED, __HIP_MEMORY_SCOPE_AGENT);
}
struct Poll { u32x4 a, b; };
__device__ __forceinline__ void poll_request(Poll& p, const char* arr, unsigned voff) {
    asm volatile("global_load_dwordx4 %0, %2, %3 sc1\n\tglobal_load_dwordx4 %1, %2, %3 offset:1024 sc1" : "+v"(p.a), "+v"(p.b) : "v"(voff), "s"(arr) : "memory");
}
__device__ __forceinline__ void poll_wait(Poll& p, const char* arr, unsigned voff, unsigned long long tag) {
    const unsigned tl = (unsigned)tag, th = (unsigned)(tag >> 32);
#pragma unroll 1
    for (unsigned spin = 0; spin < (1u << 18); ++spin) {
        asm volatile("s_waitcnt vmcnt(0)" : "+v"(p.a), "+v"(p.b) :: "memory");
        if (__all(p.a[0] == tl && p.a[1] == th && p.a[2] == tl && p.a[3] == th && p.b[0] == tl && p.b[1] == th && p.b[2] == tl && p.b[3] == th)) break;
        __builtin_amdgcn_s_sleep(1);
        poll_request(p, arr, voff);
    }
    asm volatile("s_waitcnt vmcnt(0)" : "+v"(p.a), "+v"(p.b) :: "memory");
}

template <int MODE> struct P1F { s16x8 x[2]; s16x4 w[MODE == 2 ? 3 : 2][2]; };

template <int MODE>
__device__ __forceinline__ void p1_read(P1F<MODE>& n, lds_ptr xa, lds_ptr w0, lds_ptr w1, int wimm, int i) {
    constexpr int TS = MODE == 2 ? 256 : 512;
    if (i == 0) { n.x[0] = *(const LDS_AS s16x8*)xa; n.w[0][0] = trread(w0 + wimm); n.w[0][1] = trread(w0 + wimm + TS); }
    if (i == 1) { n.x[1] = *(const LDS_AS s16x8*)(xa + 4096); n.w[1][0] = trread(w1 + wimm); n.w[1][1] = trread(w1 + wimm + TS); }
    if (i == 2 && MODE == 2) { n.w[MODE == 2 ? 2 : 0][0] = trread(w0 + wimm + 1024); n.w[MODE == 2 ? 2 : 0][1] = trread(w0 + wimm + 1024 + TS); }
}
template <int MODE, int NR>
__device__ __forceinline__ void p1_step(const P1F<MODE>& f, f32x16 (&acc)[MODE == 2 ? 6 : 4], P1F<MODE>& n0, lds_ptr xa0, int wi0, P1F<MODE>& n1, lds_ptr xa1, int wi1,
                                        lds_ptr w0, lds_ptr w1) {
    constexpr int NM = MODE == 2 ? 6 : 4;
#pragma unroll
    for (int g = 0; g < NM; ++g) {
        const s16x8 w = CAT8(f.w[g >> 1][0], f.w[g >> 1][1]);
#if !(ABL & 1)
        acc[g] = MODE == 1 ? MFMA32(f.x[g & 1], w, acc[g]) : MFMA32(w, f.x[g & 1], acc[g]);
#else
        { s16x8 x_ = f.x[g & 1]; asm volatile("" :: "v"(w), "v"(x_)); }
#endif
        PIN(acc[g]); SBAR();
        if (NR >= 1 && g < NM / 2) p1_read<MODE>(n0, xa0, w0, w1, wi0, g);
        if (NR >= 2 && g >= NM / 2) p1_read<MODE>(n1, xa1, w0, w1, wi1, g - NM / 2);
        SBAR();
    }
}
struct P1X { lds_ptr a0, a1, a2, a3; };
template <int MODE>
__device__ __forceinline__ void p1_first(P1F<MODE> (&f)[4], const P1X& xa, lds_ptr w0, lds_ptr w1) {
    constexpr int KS = MODE == 2 ? 6144 : 4096;
#pragma unroll
    for (int i = 0; i < 3; ++i) p1_read<MODE>(f[0], xa.a0, w0, w1, 0, i);
#pragma unroll
    for (int i = 0; i < 3; ++i) p1_read<MODE>(f[1], xa.a1, w0, w1, KS, i);
    SBAR();
}
template <int MODE>
__device__ __forceinline__ void p1_lo(P1F<MODE> (&f)[4], f32x16 (&acc)[MODE == 2 ? 6 : 4], const P1X& xa, lds_ptr w0, lds_ptr w1) {
    constexpr int KS = MODE == 2 ? 6144 : 4096;
    p1_step<MODE, 1>(f[0], acc, f[2], xa.a2, 2 * KS, f[2], xa.a2, 0, w0, w1);
    p1_step<MODE, 1>(f[1], acc, f[3], xa.a3, 3 * KS, f[3], xa.a3, 0, w0, w1);
}
template <int MODE>
__device__ __forceinline__ void p1_hi(P1F<MODE> (&f)[4], f32x16 (&acc)[MODE == 2 ? 6 : 4], const P1X& xa, lds_ptr w0, lds_ptr w1) {
    constexpr int KS = MODE == 2 ? 6144 : 4096;
    p1_step<MODE, 1>(f[2], acc, f[0], xa.a0, 0, f[0], xa.a0, 0, w0, w1);
    p1_step<MODE, 1>(f[3], acc, f[1], xa.a1, KS, f[1], xa.a1, 0, w0, w1);
}
__device__ __forceinline__ P1X p1_xaddr(lds_ptr lds, int wid, int lane) {
    const int xrow = (wid & 1) * 64 + (lane & 31), xo = P1_X0 + xrow * 128 + (((lane >> 5) ^ ((xrow >> 1) & 7)) << 4);
    return P1X{lds + xo, lds + (xo ^ 32), lds + (xo ^ 64), lds + (xo ^ 96)};
}
template <int MODE>
__device__ __forceinline__ void p1_five(P1F<MODE> (&f)[4], f32x16 (&acc)[MODE == 2 ? 6 : 4], P1X& xa, lds_ptr& w0, lds_ptr& w1, int& ws3, int wrap, int drain, int ps) {
#pragma unroll
    for (int n = 0; n < 5; ++n) {
        const int dw = ws3 == 2 ? -2 * P1_WSLOT : P1_WSLOT, dx = n == wrap ? -4 * P1_XSLOT : P1_XSLOT;
        ws3 = ws3 == 2 ? 0 : ws3 + 1;
        p1_lo<MODE>(f, acc, xa, w0, w1);
        w0 += dw; w1 += dw; xa.a0 += dx; xa.a1 += dx; xa.a2 += dx; xa.a3 += dx;
        asm volatile("" : "+v"(w0), "+v"(w1), "+v"(xa.a0), "+v"(xa.a1), "+v"(xa.a2), "+v"(xa.a3));
        if (n == drain) asm volatile("s_waitcnt vmcnt(0)" ::: "memory");
        PSTAMP(ps + 2 * n);
        LGKM_BAR();
        PSTAMP(ps + 2 * n + 1);
        p1_hi<MODE>(f, acc, xa, w0, w1);
    }
}

struct Pack { const char *wd, *wo; int job; };
__device__ __forceinline__ void pack_fetch(const Pack& pk_, lds_ptr lds, int wid, int lane) {
    const int step = pk_.job / 10, cb = pk_.job - step * 10, r = lane >> 3;
    const unsigned va = ((r & 3) + 8 * (r >> 2)) * (D_ * 2) + (lane & 7) * 16, vb = ((r >> 2) * 20 + 16 + (r & 3)) * (D_ * 2) + (lane & 7) * 16;
#pragma unroll
    for (int g = 0; g < 4; ++g) {
        const int ks = (step - 48) * 2 + (g >> 1), hh = g & 1;
        const char* a = step < 48 ? pk_.wd + (step * 32 + (g >> 1) * 16 + hh * 4) * (D_ * 2) : ks < 32 ? pk_.wo + (ks * 20 + hh * 4) * (D_ * 2) : pk_.wo + ((ks - 32) * 4 + hh * 2) * 20 * (D_ * 2);
        glds16s(a + cb * 128, step < 64 ? va : vb, (unsigned)(uintptr_t)lds + wid * P1_WSLOT + 16384 + g * 1024);
    }
}
__device__ __forceinline__ void pack_read(s16x8 (&pf)[4], lds_ptr lds, int wid, int lane) {
    asm volatile("s_waitcnt vmcnt(0)" ::: "memory");
    const lds_ptr pr = lds + wid * P1_WSLOT + 16384 + (lane >> 4) * 1024 + ((lane >> 2) & 3) * 128 + (lane & 3) * 8;
#pragma unroll
    for (int nt = 0; nt < 4; ++nt) pf[nt] = CAT8(trread(pr + nt * 32), trread(pr + nt * 32 + 512));
}
__device__ __forceinline__ void pack_store(const Pack& pk_, const s16x8 (&pf)[4], int lane, rsrc_t wsr) {
    const int step = pk_.job / 10, cb = pk_.job - step * 10;
#pragma unroll
    for (int nt = 0; nt < 4; ++nt) {
        const int N = cb * 4 + nt, cg = N / 5;
        ST16(wsr, __builtin_bit_cast(u32x4, pf[nt]), lane * 16, WS_WP + ((cg * 68 + step) * 5 + N - cg * 5) * 1024);
    }
}

template <int MODE>
__device__ __forceinline__ void p1a_compute(f32x16 (&acc)[4], lds_ptr lds, int wid, int lane, int ra, const Pack& pk_, s16x8 (&pf)[4]) {
    const int h = lane >> 5, blk = (lane >> 4) & 1, q = (lane >> 2) & 3, fsw = 3 * (q >> 1);
    P1X xa = p1_xaddr(lds + ra * P1_XSLOT, wid, lane);
    const int wb = h * 2048 + (wid >> 1) * 1024 + q * 128 + (lane & 3) * 8;
    lds_ptr w0 = lds + wb + ((blk ^ fsw) << 5), w1 = lds + wb + (((2 + blk) ^ fsw) << 5);
    P1F<MODE> f[4];
    int ws3 = 0;
    PSTAMP(0);
    LGKM_BAR();
    PSTAMP(1);
    p1_first<MODE>(f, xa, w0, w1);
#pragma unroll UNR2
    for (int hf_ = 0; hf_ < 2; ++hf_) {
        p1_five<MODE>(f, acc, xa, w0, w1, ws3, 4 - ra, -1, 2 + 10 * hf_);
        if (hf_ == 0) {
            if (pk_.job < 680) pack_read(pf, lds, wid, lane);
        }
    }
}

__device__ __forceinline__ void p1_compute(lds_ptr lds, int wid, int lane, int panel, int grp, int ra, int rb, rsrc_t wsr, const Pack& pk_ STAMP_ARG) {
    const int h = lane >> 5, l31 = lane & 31, wm = wid & 1, wn = wid >> 1, tt = panel * 4 + wm * 2;
    const LDS_AS f32x2* rowf = (const LDS_AS f32x2*)(lds + P1_STAT);
    __builtin_amdgcn_s_setprio(CPRIO);
    if (pk_.job < 680) pack_fetch(pk_, lds, wid, lane);
    {
        f32x16 acc[4] = {f32x16{}, f32x16{}, f32x16{}, f32x16{}};
        s16x8 pf[4] = {s16x8{}, s16x8{}, s16x8{}, s16x8{}};
        const LDS_AS float* colf = (const LDS_AS float*)(lds + P1_COLA) + wn * 64;
        const int sec = grp / 5, q0 = (grp - sec * 5) * 32 + wn * 16;
        if (sec < 2) {
            p1a_compute<0>(acc, lds, wid, lane, ra, pk_, pf);
            STAMP(1);
            const int pos = sec ? ((l31 & 19) | ((l31 & 4) << 1) | ((l31 & 8) >> 1)) : l31;
            const int hs = sec ? KV_HEAD : Q_HEAD, ts = sec ? KV_TILE : Q_TILE, so = (sec ? WS_KV : WS_Q) + tt * ts;
            const float qs = sec ? 1.0f : 0.22360679774997896f * 1.4426950408889634f;
            f32x2 nm[2], rr[2];
#pragma unroll
            for (int m = 0; m < 2; ++m) { const f32x2 rs = rowf[wm * 64 + m * 32 + l31]; nm[m] = f32x2{rs[1], rs[1]}; rr[m] = f32x2{rs[0] * qs, rs[0] * qs}; }
            const bool odd = lane & 1;
#pragma unroll
            for (int gam = 0; gam < 8; gam += 2) {
                const int qd = q0 + 2 * (gam + (lane & 1)) + h, hd = (qd * 205) >> 10, vo = hd * hs + (qd - hd * 5) * 256 + (pos & ~1) * 8;
                const f32x4 ca = *(const LDS_AS f32x4*)(colf + gam * 8 + h * 4), cb = *(const LDS_AS f32x4*)(colf + gam * 8 + 8 + h * 4);
#pragma unroll
                for (int m = 0; m < 2; ++m) {
                    const f32x16& a = acc[(gam >> 2) * 2 + m];
                    const int r0 = (gam & 3) * 4;
                    const unsigned a0 = cvt2(pfma(pk4(ca, 0), nm[m], pk(a, r0)) * rr[m]), a1 = cvt2(pfma(pk4(ca, 2), nm[m], pk(a, r0 + 2)) * rr[m]);
                    const unsigned b0 = cvt2(pfma(pk4(cb, 0), nm[m], pk(a, r0 + 4)) * rr[m]), b1 = cvt2(pfma(pk4(cb, 2), nm[m], pk(a, r0 + 6)) * rr[m]);
                    const unsigned g0 = __builtin_amdgcn_update_dpp(0, odd ? a0 : b0, 0xB1, 0xF, 0xF, true), g1 = __builtin_amdgcn_update_dpp(0, odd ? a1 : b1, 0xB1, 0xF, 0xF, true);
                    ST16(wsr, (odd ? u32x4{g0, g1, b0, b1} : u32x4{a0, a1, g0, g1}), vo, so + m * ts);
                }
            }
        } else {
            p1a_compute<1>(acc, lds, wid, lane, ra, pk_, pf);
            STAMP(1);
            if (sec == 2) {
                const int so = WS_KV + tt * KV_TILE + KV_V;
#pragma unroll
                for (int m = 0; m < 2; ++m) {
                    f32x2 rr[8], nm[8];
#pragma unroll
                    for (int g = 0; g < 4; ++g) {
                        const LDS_AS f32x2* rp = rowf + wm * 64 + m * 32 + g * 8 + h * 4;
                        const f32x4 a = *(const LDS_AS f32x4*)rp, b = *(const LDS_AS f32x4*)(rp + 2);
                        rr[2 * g] = f32x2{a[0], a[2]}; nm[2 * g] = f32x2{a[1], a[3]}; rr[2 * g + 1] = f32x2{b[0], b[2]}; nm[2 * g + 1] = f32x2{b[1], b[3]};
                    }
#pragma unroll
                    for (int t = 0; t < 2; ++t) {
                        const int qd = q0 + t * 8 + (l31 >> 2), hd = (qd * 205) >> 10, pl = qd - hd * 5;
                        const int vo = hd * KV_HEAD + pl * 256 + ((h ^ (pl & 3)) << 6) + (l31 & 3) * 16;
                        const float c1 = colf[t * 32 + l31];
                        const f32x2 c2 = {c1, c1};
                        unsigned w[8];
#pragma unroll
                        for (int i = 0; i < 8; ++i) w[i] = cvt2(pfma(nm[i], c2, pk(acc[t * 2 + m], 2 * i)) * rr[i]);
                        ST16(wsr, (u32x4{w[0], w[1], w[2], w[3]}), vo, so + m * KV_TILE);
                        ST16(wsr, (u32x4{w[4], w[5], w[6], w[7]}), vo ^ 128, so + m * KV_TILE);
                    }
                }
                const int hd = q0 / 5 + (lane >> 4);
                const unsigned ov = (lane & 3) ? 0u : 0x3f803f80u;
                if (hd * 5 + 4 < q0 + 16) {
                    ST16(wsr, (u32x4{ov, ov, ov, ov}), hd * KV_HEAD + 5 * 256 + (lane & 15) * 16, so);
                    ST16(wsr, (u32x4{ov, ov, ov, ov}), hd * KV_HEAD + 5 * 256 + (lane & 15) * 16, so + KV_TILE);
                }
            }
        }
        if (pk_.job < 680) pack_store(pk_, pf, lane, wsr);
    }
    STAMP(6);
    f32x16 acc[6];
#pragma unroll
    for (int t = 0; t < 6; ++t) acc[t] = f32x16{};
    {
        const int blk = (lane >> 4) & 1, T0 = wn * 3, T1 = T0 + 1;
        P1X xa = p1_xaddr(lds + rb * P1_XSLOT, wid, lane);
        const int wb = P1_WSLOT + 3 * blk * 1024 + h * 512 + ((lane >> 2) & 3) * 64 + (lane & 3) * 8;
        lds_ptr w0 = lds + wb + (T0 >> 1) * 1024 + (((T0 & 1) ^ blk) << 5), w1 = lds + wb + (T1 >> 1) * 1024 + (((T1 & 1) ^ blk) << 5);
        P1F<2> f[4];
        p1_first<2>(f, xa, w0, w1);
        int ws3 = 1;
#pragma unroll UNR2
        for (int hf_ = 0; hf_ < 2; ++hf_) {
            p1_five<2>(f, acc, xa, w0, w1, ws3, 4 - rb, hf_ == 0 ? FA_POS - 11 : -1, 22 + 10 * hf_);
        }
    }
    STAMP(3);
    {
        const LDS_AS float* colf = (const LDS_AS float*)(lds + P1_COLB) + wn * 96;
        const int vo = (l31 >> 4) * 48 * 1024 + (h * 16 + (l31 & 15)) * 16;
        f32x2 nm[2], r2[2];
#pragma unroll
        for (int m = 0; m < 2; ++m) { const f32x2 rs = rowf[wm * 64 + m * 32 + l31]; nm[m] = f32x2{rs[1], rs[1]}; r2[m] = f32x2{rs[0] * rs[0], rs[0] * rs[0]}; }
#pragma unroll
        for (int c = 0; c < 3; ++c) {
            f32x4 cv[4];
#pragma unroll
            for (int g = 0; g < 4; ++g) cv[g] = *(const LDS_AS f32x4*)(colf + c * 32 + g * 8 + h * 4);
            if (c >= 1 && wn) {
#pragma unroll
                for (int g = 0; g < 4; ++g) cv[g] += *(const LDS_AS f32x4*)(colf + 64 + c * 32 + g * 8 + h * 4);
            }
            const int so = WS_H16 + tt * 2 * 48 * 1024 + (grp * 6 + wn * 3 + c) * 512;
#pragma unroll
            for (int m = 0; m < 2; ++m) {
                const f32x16& a = acc[c * 2 + m];
                f32x2 g[4], u[4];
#pragma unroll
                for (int i = 0; i < 4; ++i) { g[i] = pfma(pk4(cv[i >> 1], (i & 1) * 2), nm[m], pk(a, 2 * i)); u[i] = pfma(pk4(cv[2 + (i >> 1)], (i & 1) * 2), nm[m], pk(a, 8 + 2 * i)); }
#pragma unroll
                for (int i = 0; i < 4; ++i) { g[i] = f32x2{fmaxf(g[i][0], 0.f), fmaxf(g[i][1], 0.f)}; u[i] *= r2[m]; }
                ST16(wsr, (u32x4{cvt2(g[0] * u[0]), cvt2(g[1] * u[1]), cvt2(g[2] * u[2]), cvt2(g[3] * u[3])}), vo, so + m * 2 * 48 * 1024);
            }
        }
    }
}

__device__ __forceinline__ void glds_x4(const char* sbase, const unsigned (&v)[4], unsigned lds_base) {
    asm volatile("s_mov_b32 m0, %5\n\ts_nop 0\n\tglobal_load_lds_dwordx4 %0, %4\n\tglobal_load_lds_dwordx4 %1, %4 offset:1024\n\t"
                 "global_load_lds_dwordx4 %2, %4 offset:2048\n\tglobal_load_lds_dwordx4 %3, %4 offset:3072"
                 :: "v"(v[0]), "v"(v[1]), "v"(v[2]), "v"(v[3]), "s"(sbase), "s"(lds_base) : "memory");
}
__device__ __forceinline__ void glds_x3(const char* sbase, const unsigned* v, unsigned lds_base) {
    asm volatile("s_mov_b32 m0, %4\n\ts_nop 0\n\tglobal_load_lds_dwordx4 %0, %3\n\tglobal_load_lds_dwordx4 %1, %3 offset:1024\n\tglobal_load_lds_dwordx4 %2, %3 offset:2048"
                 :: "v"(v[0]), "v"(v[1]), "v"(v[2]), "s"(sbase), "s"(lds_base) : "memory");
}

__device__ __forceinline__ unsigned long long p1_helper(lds_ptr lds, int hw, int lane, const char* xp, const char* wq, const char* wgp, const char* wup, int ra, int rb,
                                          char* ws, const char* kv, const char* qsrc) {
    const unsigned lds0 = (unsigned)(uintptr_t)lds;
    const int r = lane >> 3, pos = lane & 7;
    unsigned xv[4], wa[4], wb[6];
#pragma unroll
    for (int j = 0; j < 4; ++j) {
        xv[j] = 4096 + (hw * 32 + j * 8 + r) * (D_ * 2) + ((pos ^ ((j & 1) * 4 + (r >> 1))) << 4) - j * 1024;
        wa[j] = 4096 + (hw * 16 + (j >> 1) * 8 + r) * (3 * D_ * 2) + (j & 1) * 128 + ((pos ^ (6 * ((r >> 1) & 1))) << 4) - j * 1024;
    }
#pragma unroll
    for (int j = 0; j < 6; ++j) wb[j] = 4096 + (hw * 16 + (lane >> 2)) * (F_ * 2) + (j % 3) * 64 + (((lane & 3) ^ (j < 3 ? 0 : 2)) << 4) - (j % 3) * 1024;
    xp -= 4096; wq -= 4096; wgp -= 4096; wup -= 4096;
    int kb_ = 0, xs = ra, dws = 0, left = 5;
    auto issue1 = [&](bool pa) {
        if (ABL & 4) return;
        if (pa) glds_x4(wq + (unsigned)((kb_ + xs) * 64 * 3 * D_ * 2), wa, lds0 + dws * P1_WSLOT + hw * 4096);
        else glds_x3(wgp + (unsigned)((kb_ + xs) * 64 * F_ * 2), wb, lds0 + dws * P1_WSLOT + hw * 6144);
        SBAR();
    };
    auto issue2 = [&](bool pa, bool xx) {
        SBAR();
        if (ABL & 4) return;
        if (!pa) glds_x3(wup + (unsigned)((kb_ + xs) * 64 * F_ * 2), wb + 3, lds0 + dws * P1_WSLOT + hw * 6144 + 3072);
        xs = xs == 4 ? 0 : xs + 1; dws = dws == 2 ? 0 : dws + 1; --left; kb_ = left == 0 ? 5 - kb_ : kb_; left = left == 0 ? 5 : left;
        if (xx) glds_x4(xp + (kb_ + xs) * 128, xv, lds0 + P1_X0 + xs * P1_XSLOT + hw * 4096);
    };
    glds_x4(xp + ra * 128, xv, lds0 + P1_X0 + ra * P1_XSLOT + hw * 4096);
    issue1(true); issue2(true, true);
    __builtin_amdgcn_s_barrier();
    unsigned long long ep = 0;
    asm volatile("global_load_dwordx2 %0, %1, %2 sc1" : "+v"(ep) : "v"(0u), "s"(ws + WS_EPOCH) : "memory");
    __builtin_amdgcn_s_setprio(HPRIO);
    const int r16 = lane & 15, h = lane >> 5, blk = (lane >> 4) & 1, q = (lane >> 2) & 3, fsw = 3 * (q >> 1);
    const unsigned olo = r16 < 2 ? 0x3f803f80u : 0u, ohi = (r16 >> 1) == 1 ? 0x3f803f80u : 0u;
    const s16x8 ones = __builtin_bit_cast(s16x8, (u32x4{olo, olo, ohi, ohi}));
    const int idx = (r16 & 1) ? 0xEEEE : 0x4444;
    int ws3 = 0;
    {
        lds_ptr b = lds + h * 2048 + (hw >> 1) * 1024 + q * 128 + (lane & 3) * 8 + ((((hw & 1) * 2 + blk) ^ fsw) << 5);
        const int so = P1_X0 + ra * P1_XSLOT + (hw * 32 + r16) * 128 + (((lane >> 4) ^ (r16 >> 1)) << 4);
        lds_ptr sp0 = lds + so, sp1 = lds + (so ^ 64);
        const s16x8 ones2 = __builtin_bit_cast(s16x8, (u32x4{olo, olo, olo, olo}));
        f32x4 cs = f32x4{}, s1[2] = {f32x4{}, f32x4{}}, s2[2] = {f32x4{}, f32x4{}};
        s16x16 wv0, wv1; s16x8 xa[2], xb[2];
        auto col_rd = [&]() {
            wv0 = CAT16(CAT8(trread(b), trread(b + 512)), CAT8(trread(b + 4096), trread(b + 4096 + 512)));
            wv1 = CAT16(CAT8(trread(b + 8192), trread(b + 8192 + 512)), CAT8(trread(b + 12288), trread(b + 12288 + 512)));
            b += ws3 == 2 ? -2 * P1_WSLOT : P1_WSLOT; ws3 = ws3 == 2 ? 0 : ws3 + 1;
        };
        auto row_rd = [&](int dx) {
#pragma unroll
            for (int t = 0; t < 2; ++t) { xa[t] = *(const LDS_AS s16x8*)(sp0 + t * 2048); xb[t] = *(const LDS_AS s16x8*)(sp1 + t * 2048); }
            sp0 += dx; sp1 += dx;
        };
        auto row_mm = [&]() {
#pragma unroll
            for (int t = 0; t < 2; ++t) { s2[t] = MFMA16(xa[t], xa[t], s2[t]); s1[t] = SMF16(ones2, CAT16(xa[t], xb[t]), s1[t], idx); }
#pragma unroll
            for (int t = 0; t < 2; ++t) { s2[t] = MFMA16(xb[t], xb[t], s2[t]); PIN(s1[t]); PIN(s2[t]); }
        };
#pragma unroll
        for (int nn = 0; nn < 9; ++nn) {
            int n = nn;
            if (!HUNR) asm volatile("" : "+s"(n));
            PSTAMP(2 * n);
            if (n == 0) WAIT_BAR(5); else if (n < 8) WAIT_BAR(12); else WAIT_BAR(8);
            PSTAMP(2 * n + 1);
            if (!(ABL & 8) || n == 0) { col_rd(); row_rd(n % 5 == 4 - ra ? -4 * P1_XSLOT : P1_XSLOT); }
            if (n == 0) { issue1(true); issue2(true, true); }
            if (n == 8) { kb_ = 5; xs = rb; }
            issue1(n < 8);
            if (!(ABL & 8)) {
            cs = SMF16(ones, wv0, cs, idx);
            row_mm();
            cs = SMF16(ones, wv1, cs, idx); PIN(cs); }
            issue2(n < 8, n < 7);
        }
        MEMFENCE(); __builtin_amdgcn_s_waitcnt(0xF70 | 10); MEMFENCE();
        row_rd(0); row_mm();
        {
            const int t = blk;
            const float d0 = (lane & 2) ? ((lane & 1) ? s2[0][3] : s2[0][2]) : ((lane & 1) ? s2[0][1] : s2[0][0]);
            const float d1 = (lane & 2) ? ((lane & 1) ? s2[1][3] : s2[1][2]) : ((lane & 1) ? s2[1][1] : s2[1][0]);
            const float a0 = __shfl(s1[0][0] + s1[0][1], r16), a1 = __shfl(s1[1][0] + s1[1][1], r16), c0 = __shfl(d0, r16 + 16 * q), c1 = __shfl(d1, r16 + 16 * q);
            const float a = t ? a1 : a0, c = t ? c1 : c0;
            const float mean = a * (1.0f / D_), var = fmaxf(c * (1.0f / D_) - mean * mean, 0.f);
            const float rstd = __builtin_amdgcn_rcpf(rbf(__builtin_amdgcn_sqrtf(rbf(rbf(var) + 1e-5f))));
            if (lane < 32) ((LDS_AS f32x2*)(lds + P1_STAT))[hw * 32 + lane] = f32x2{rstd, -rbf(mean)};
        }
        PSTAMP(18);
        WAIT_BAR(6);
        PSTAMP(19);
        col_rd();
        issue1(false);
        cs = SMF16(ones, wv0, cs, idx);
        cs = SMF16(ones, wv1, cs, idx); PIN(cs);
        issue2(false, false);
        LDS_AS float* colf = (LDS_AS float*)(lds + P1_COLA) + hw * 32;
        if (lane < 16) { colf[lane] = cs[0] + cs[1]; colf[16 + lane] = cs[2] + cs[3]; }
    }
    asm volatile("" : "+v"(ep));
    const unsigned long long tag = (((unsigned long long)(unsigned)__builtin_amdgcn_readfirstlane((unsigned)(ep >> 32)) << 32) | (unsigned)__builtin_amdgcn_readfirstlane((unsigned)ep)) + 1ull;
    if (hw == 0 && lane == 0) *(LDS_AS unsigned long long*)(lds + P1_TAG) = tag;
    {
        const int ro = P1_WSLOT + 3 * blk * 1024 + h * 512 + q * 64 + (lane & 3) * 8 + (((hw & 1) ^ blk) << 5);
        lds_ptr b = lds + ro + (hw >> 1) * 1024, c = lds + ro + 2048 + (hw >> 1) * 12288;
        f32x4 cs = f32x4{}, ct = f32x4{};
        Poll pl = {};
#pragma unroll
        for (int nn = 10; nn < 20; ++nn) {
            int n = nn;
            if (!HUNR) asm volatile("" : "+s"(n));
            PSTAMP(2 * n);
            if (n < 13 || n == 19) WAIT_BAR(6); else if (n == 13) WAIT_BAR(10); else if (n == 18) WAIT_BAR(12); else WAIT_BAR(14);
            PSTAMP(2 * n + 1);
            const s16x16 wv0 = CAT16(CAT8(trread(b), trread(b + 256)), CAT8(trread(b + 6144), trread(b + 6144 + 256)));
            const s16x16 wv2 = CAT16(CAT8(trread(c), trread(c + 256)), CAT8(trread(c + 6144), trread(c + 6144 + 256)));
            const s16x16 wv1 = CAT16(CAT8(trread(b + 12288), trread(b + 12288 + 256)), CAT8(trread(b + 18432), trread(b + 18432 + 256)));
            if (n == FA_POS && hw == 0 && lane == 0) raise_flag(ws, WS_FA, tag);
            if (n < 18) issue1(false);
            cs = SMF16(ones, wv0, cs, idx);
            ct = SMF16(ones, wv2, ct, idx);
            cs = SMF16(ones, wv1, cs, idx); PIN(cs); PIN(ct);
            if (n < 18) issue2(false, n >= 12 && n < 17);
            if (n == 17) poll_request(pl, ws + WS_FA, lane * 16);
            if (n == 18) {
                poll_wait(pl, ws + WS_FA, lane * 16, tag);
#pragma unroll
                for (int j = 0; j < 6; ++j) glds16c(kv + (hw + 4 * j) * 1024, lane * 16, lds0 + K2_C0 + (hw + 4 * j) * 1024);
            }
            if (n == 19) {
#pragma unroll
                for (int j = 0; j < 3; ++j) glds16c(qsrc + (hw + 4 * j) * 1024, lane * 16, lds0 + K2_Q0 + (hw + 4 * j) * 1024);
            }
            const int dw = ws3 == 2 ? -2 * P1_WSLOT : P1_WSLOT;
            b += dw; c += dw; ws3 = ws3 == 2 ? 0 : ws3 + 1;
        }
        LDS_AS float* colf = (LDS_AS float*)(lds + P1_COLB);
        if (lane < 16) { colf[hw * 32 + lane] = cs[0] + cs[1]; colf[hw * 32 + 16 + lane] = cs[2] + cs[3]; }
        if (lane < 16) { colf[128 + hw * 32 + lane] = ct[0] + ct[1]; colf[128 + hw * 32 + 16 + lane] = ct[2] + ct[3]; }
        WAIT_BAR(0);
        if (blockIdx.x == 0 && hw == 0 && lane == 0) __hip_atomic_store((gu64*)(ws + WS_EPOCH), tag, __ATOMIC_RELAXED, __HIP_MEMORY_SCOPE_AGENT);
#pragma unroll
        for (int j = 0; j < 6; ++j) glds16c(kv + K2_CHUNK + (hw + 4 * j) * 1024, lane * 16, lds0 + K2_KV0 + K2_SLOTB + (hw + 4 * j) * 1024);
    }
    return tag;
}

constexpr int K2_XCH = 2 * K2_SLOTB;

#define SMF32(a, b, c) __builtin_amdgcn_smfmac_f32_32x32x32_bf16((bfx8)(a), __builtin_bit_cast(bfx16, (b)), (c), 0xDDDD, 0, 0)
#define MFMA8(a, b, c) __builtin_amdgcn_mfma_f32_32x32x8bf16_1k((a), (b), (c), 0, 0, 0)
__device__ __forceinline__ f32x8 lo8(const f32x16& v) { return __builtin_shufflevector(v, v, 0, 1, 2, 3, 4, 5, 6, 7); }
__device__ __forceinline__ f32x8 hi8(const f32x16& v) { return __builtin_shufflevector(v, v, 8, 9, 10, 11, 12, 13, 14, 15); }
__device__ __forceinline__ float max16(const f32x16& s) {
    float m = fmaxf(fmaxf(s[0], s[1]), s[2]);
#pragma unroll
    for (int r = 3; r < 15; r += 2) m = fmaxf(fmaxf(m, s[r]), s[r + 1]);
    return swap32_max(fmaxf(m, s[15]));
}

struct K2B { lds_ptr r0, r1, r2; const char* wsrc; unsigned wdst; int lane, osoff; rsrc_t wsr; };
constexpr int k2_slot(int st) { return st < 3 ? st : st == 3 ? 0 : st == 4 ? 3 : st == 5 ? 1 : st == 6 ? 2 : st == 7 ? 0 : 3; }
__device__ __forceinline__ void k2_wpiece(const K2B& c, int st, int i) {
    glds16c(c.wsrc + st * 40960 + i * 8192, c.lane * 16, c.wdst + k2_slot(st) * 40960 + i * 8192);
}
template <int I, int D, int NP, int OF>
__device__ __forceinline__ void k2_bstep(const K2B& c, f32x4 (&acc)[10], const s16x8 (&w)[5], s16x8 (&n)[5], const s16x8& a0, const s16x8& a1, s16x8 (&of)[10]) {
    constexpr int O = k2_slot((I + 1) >> 1) * 40960 + ((I + 1) & 1) * 20480;
#pragma unroll
    for (int i = 0; i < 10; ++i) {
        if (I > 0 && I < 16 && i < 5) n[i] = *(const LDS_AS s16x8*)((O < 61440 ? c.r0 : O < 122880 ? c.r1 : c.r2) + O % 61440 + i * 1024);
        acc[i] = MFMA16(w[i >> 1], (i & 1) ? a1 : a0, acc[i]);
        PIN(acc[i]);
        if (OF >= 0 && !(i & 1)) { const int k = (OF < 0 ? 0 : OF) + (i >> 1); of[k] = LD16(c.wsr, c.lane * 16, c.osoff + ((k & 1) * 20 + (k >> 1) * 4) * 1024); MEMFENCE(); }
        if ((i & 1) && (i >> 1) < NP) k2_wpiece(c, D, i >> 1);
        SBAR();
    }
}

__global__ __launch_bounds__(512, 2) void fused_block(const char* __restrict__ x, const char* __restrict__ wqkv, const char* __restrict__ wg, const char* __restrict__ wu,
                                                      const char* __restrict__ wd, const char* __restrict__ wo, char* __restrict__ ws, char* __restrict__ out) {
    extern __shared__ __attribute__((aligned(16))) char smem[];
    STAMP_DECL; asm volatile("s_memrealtime %0\n\ts_waitcnt lgkmcnt(0)" : "=s"(str_[1])); __builtin_amdgcn_sched_barrier(0);
    asm volatile("" :: "s"(x), "s"(wqkv), "s"(wg), "s"(wu), "s"(wd), "s"(wo), "s"(ws), "s"(out));
    const int wid = __builtin_amdgcn_readfirstlane(threadIdx.x >> 6);
    unsigned long long tag = 0;
    if (wid < 4) {
        __builtin_amdgcn_s_barrier();
        if (CODEWARM && wid == 3) {
            unsigned off = ((blockIdx.x >> 3) * 12 + min(threadIdx.x & 63, 11)) * 128;
            asm volatile("s_getpc_b64 vcc\n.Lfb_begin:\n\tv_min_u32 %0, .Lfb_end-.Lfb_begin, %0\n\ts_mov_b32 m0, %1\n\ts_nop 0\n\tglobal_load_lds_dword %0, vcc"
                         : "+v"(off) : "s"((unsigned)(uintptr_t)(lds_ptr)smem + LDS_JUNK) : "vcc", "memory");
        }
    }
    const rsrc_t wsr = __builtin_amdgcn_make_buffer_rsrc(ws, 0, (int)WS_END, 0x00020000);
    const int bid = blockIdx.x, head = (bid & 7) * 4 + ((bid >> 3) & 3), par = wid >> 2, qt0 = (bid >> 5) * 8 + (wid & 3) * 2;
    const char* kv = ws + WS_KV + head * KV_HEAD;
    {
        const int bi = bid >> 3, panel = (bid & 1) * 8 + (bi & 7), grp = ((bid & 7) >> 1) * 4 + (bi >> 3), l0 = threadIdx.x & 63;
        const int rb = K1_ROT ? (5 * (bi & 7)) >> 3 : 0, ra = ROTA == 1 ? (rb + (bi >> 3)) % 5 : ROTA == 2 ? ((bi & 1) * 2 + ((bi >> 3) & 1) * 2) % 4 : 0;
        if (wid < 4) {
            p1_compute((lds_ptr)smem, wid, l0, panel, grp, ra, rb, wsr, Pack{wd, wo, wid * 256 + bid} STAMP_PASS);
            const u32x2 e = *(const LDS_AS u32x2*)((lds_ptr)smem + P1_TAG);
            tag = ((unsigned long long)(unsigned)__builtin_amdgcn_readfirstlane(e[1]) << 32) | (unsigned)__builtin_amdgcn_readfirstlane(e[0]);
        } else {
            tag = p1_helper((lds_ptr)smem, wid - 4, l0, x + (size_t)panel * 128 * (D_ * 2), wqkv + min(grp, 14) * 256, wg + grp * 192, wu + grp * 192, ra, rb,
                            ws, kv, ws + WS_Q + head * Q_HEAD + (bid >> 5) * 8 * Q_TILE);
        }
    }
    int lane = __builtin_amdgcn_mbcnt_hi(~0u, __builtin_amdgcn_mbcnt_lo(~0u, 0u));
    PIN(lane);
    if (wid < 4) __builtin_amdgcn_s_setprio(3); else __builtin_amdgcn_s_setprio(0);
    STAMP(4);
    lds_ptr lds = (lds_ptr)smem + K2_KV0;
    const unsigned lds0 = (unsigned)(uintptr_t)lds;
    const int tp = ((blockIdx.x & 7) >> 1) * 8 + ((blockIdx.x >> 3) & 7), cg = (blockIdx.x & 1) * 4 + (blockIdx.x >> 6);
    K2B kb;
    kb.wsrc = ws + WS_WP + cg * 68 * 5120 + wid * 1024; kb.wdst = (unsigned)(uintptr_t)(lds_ptr)smem + wid * 1024; kb.lane = lane;
    const int hoff = WS_H16 + ((tp * 4 + (wid & 1) * 2) * 48 + (wid >> 1)) * 1024;
    s16x8 hf[24];
    const int h = lane >> 5, l31 = lane & 31, l16 = lane * 16;
    const char* src = kv + wid * 1024 + 3 * K2_CHUNK;
    const int k1off = h * 512 + l31 * 8, k2off = h ? KV_V + 5 * 256 : 1024 + l31 * 8;
    const int vq = min(l31 >> 2, 5), voff = KV_V + vq * 256 + ((h ^ (vq & 3)) << 6) + (l31 < 24 ? l31 & 3 : 1) * 16;

    f32x16 oa[2] = {f32x16{}, f32x16{}}, sc[2];
    s16x8 k1[2], v0[2], v1[2]; s16x4 k2[2];
    const lds_ptr c0 = (lds_ptr)smem + K2_C0;
    lds_ptr pk1 = c0 + k1off + par * KV_TILE, pk2 = c0 + k2off + par * KV_TILE, pv = c0 + voff + par * KV_TILE, pw = c0 + (voff ^ 128) + par * KV_TILE;
    lds_ptr pk3 = pk1 + 256;
    asm volatile("" : "+v"(pk3));
#define LDS8(p) (*(const LDS_AS s16x4*)(p))
#define RD_K(t, j) do { k1[(t) & 1] = CAT8(LDS8(pk1 + (j) * 2 * KV_TILE), LDS8(pk3 + (j) * 2 * KV_TILE)); k2[(t) & 1] = LDS8(pk2 + (j) * 2 * KV_TILE); } while (0)
#define RD_V(t, j) do { v0[(t) & 1] = *(const LDS_AS s16x8*)(pv + (j) * 2 * KV_TILE); v1[(t) & 1] = *(const LDS_AS s16x8*)(pw + (j) * 2 * KV_TILE); } while (0)
    s16x8 q1[2]; s16x4 q2[2];
#pragma unroll
    for (int b = 0; b < 2; ++b) {
        const lds_ptr qp = (lds_ptr)smem + K2_Q0 + ((wid & 3) * 2 + b) * Q_TILE;
        q1[b] = CAT8(LDS8(qp + k1off), LDS8(qp + k1off + 256)); q2[b] = LDS8(qp + 1024 + l31 * 8);
    }
    {
        const s16x8 ka = CAT8(LDS8(c0 + k1off), LDS8(c0 + k1off + 256));
        const s16x4 kb = LDS8(c0 + k2off);
        RD_K(0, 0); RD_V(0, 0);
#pragma unroll
        for (int b = 0; b < 2; ++b) {
            if (h) q2[b] = s16x4{0, 0, 0, 0};
            f32x16 s = MFMA32(ka, q1[b], f32x16{});
            s = MFMA8(kb, q2[b], s);
            const float m = rbf(-max16(s));
            if (h) q2[b][0] = (short)(__float_as_uint(m) >> 16);
        }
        sc[0] = MFMA32(k1[0], q1[0], f32x16{});
        sc[0] = MFMA8(k2[0], q2[0], sc[0]);
        sc[1] = f32x16{}; v1[1] = s16x8{};
    }
    STAMP(5); PSTAMP(42); PSTAMP(43);
#define EXP4(S, i) do { S[i] = __builtin_amdgcn_exp2f(S[i]); S[i + 1] = __builtin_amdgcn_exp2f(S[i + 1]); S[i + 2] = __builtin_amdgcn_exp2f(S[i + 2]); S[i + 3] = __builtin_amdgcn_exp2f(S[i + 3]); PIN(S); SBAR(); } while (0)
    Poll pl = {};
    auto request = [&](int hs) {
        const int p = (hs - 6) >> 3, r = (hs - 6) & 7;
        const int w = (hs >= 9 && hs < 14) ? hs - 9 : (hs >= 17 && hs < 22) ? hs - 12 : (hs >= 54 && hs < 57) ? hs - 44 : hs >= 62 ? hs - 49 : -1;
        const int e = (p >= 2 && p <= 4 && r >= 3) ? (p - 2) * 5 + r - 3 : p == 5 ? 15 + r : hs == 57 ? 23 : -1;
        if (hs < 3) glds16c(src - K2_CHUNK + hs * 8192, l16, lds0 + 2 * K2_SLOTB + wid * 1024 + hs * 8192);
        else if (hs >= 6 && p < 5 && r < 3) glds16c(src + p * K2_CHUNK + r * 8192, l16, lds0 + (p % 3) * K2_SLOTB + wid * 1024 + r * 8192);
        else if (w >= 0) glds16c(kb.wsrc + w * 8192, l16, kb.wdst + w * 8192);
        else if (e >= 0) { hf[e] = LD16(wsr, l16, hoff + e * 4096); MEMFENCE(); }
        if (hs == 6 && wid == 4 && lane == 0) raise_flag(ws, WS_FB, tag);
        if (hs == 16 && wid == 4) poll_request(pl, ws + WS_FB, l16);
        SBAR();
    };
#pragma unroll
    for (int c = 0; c < 8; ++c) {
#pragma unroll
        for (int j = 0; j < 4; ++j) {
            if (j == 3) {
                PSTAMP(44 + 2 * c);
                if (c == 2 && wid == 4) poll_wait(pl, ws + WS_FB, l16, tag);
                if (c == 0) { asm volatile("s_waitcnt vmcnt(0)" ::: "memory"); LGKM_BAR(); } else if (c == 7) WAIT_BAR(0); else WAIT_BAR(7);
                PSTAMP(45 + 2 * c);
                __builtin_amdgcn_s_setprio(2);
                const int dsl = c == 0 ? K2_KV0 + K2_SLOTB - K2_C0 : (c % 3 == 2) ? -2 * K2_SLOTB : K2_SLOTB;
                pk1 += dsl; pk2 += dsl; pk3 += dsl; pv += dsl; pw += dsl;
                asm volatile("" : "+v"(pk1), "+v"(pk2), "+v"(pk3), "+v"(pv), "+v"(pw));
                SBAR();
            }
            RD_K(j + 1, (j + 1) & 3); SBAR();
            oa[1] = SMF32(v1[(j + 1) & 1], hi8(sc[1]), oa[1]); PIN(oa[1]); SBAR();
            EXP4(sc[0], 0);
            sc[1] = MFMA32(k1[j & 1], q1[1], f32x16{}); PIN(sc[1]); SBAR();
            request(8 * c + 2 * j);
            EXP4(sc[0], 4);
            sc[1] = MFMA8(k2[j & 1], q2[1], sc[1]); PIN(sc[1]); SBAR();
            EXP4(sc[0], 8);
            oa[0] = SMF32(v0[j & 1], lo8(sc[0]), oa[0]); PIN(oa[0]); SBAR();
            EXP4(sc[0], 12);
            RD_V(j + 1, (j + 1) & 3);
            if (j == 1 && c > 0) __builtin_amdgcn_s_setprio(1);
            if (j == 2 && c > 0) __builtin_amdgcn_s_setprio(0);
            SBAR();
            oa[0] = SMF32(v1[j & 1], hi8(sc[0]), oa[0]); PIN(oa[0]); SBAR();
            EXP4(sc[1], 0);
            sc[0] = MFMA32(k1[(j + 1) & 1], q1[0], f32x16{}); PIN(sc[0]); SBAR();
            request(8 * c + 2 * j + 1);
            EXP4(sc[1], 4);
            sc[0] = MFMA8(k2[(j + 1) & 1], q2[0], sc[0]); PIN(sc[0]); SBAR();
            EXP4(sc[1], 8);
            oa[1] = SMF32(v0[j & 1], lo8(sc[1]), oa[1]); PIN(oa[1]); SBAR();
            EXP4(sc[1], 12);
        }
    }
    oa[1] = SMF32(v1[1], hi8(sc[1]), oa[1]);
    unsigned qs_ = 0; (void)qs_;
    STAMP(2);
    PSTAMP(60); QSTAMP(60);
    int ln = __builtin_amdgcn_mbcnt_hi(~0u, __builtin_amdgcn_mbcnt_lo(~0u, 0u));
    PIN(ln);
    const int h2 = ln >> 5, l2 = ln & 31;
    const int mt = wid & 1, kq = wid >> 1;
    kb.lane = ln; kb.r0 = (lds_ptr)smem + kq * 5120 + ln * 16; kb.r1 = kb.r0 + 61440; kb.r2 = kb.r0 + 122880;
    asm volatile("" : "+v"(kb.r1), "+v"(kb.r2));
    kb.wsr = wsr; kb.osoff = WS_O16 + ((tp * 4 + mt * 2) * 20 + kq) * 1024;
    s16x8 wa[5], wb[5];
#pragma unroll
    for (int i = 0; i < 5; ++i) { wa[i] = *(const LDS_AS s16x8*)(kb.r0 + i * 1024); wb[i] = *(const LDS_AS s16x8*)(kb.r0 + 20480 + i * 1024); }
    {
        const lds_ptr xp_ = lds + K2_XCH + (wid & 3) * 6144 + ln * 16;
        if (par) {
#pragma unroll
            for (int i = 0; i < 6; ++i) { const f32x16& g = oa[i / 3]; const int r = 4 * (i % 3); *(LDS_AS f32x4*)(xp_ + i * 1024) = f32x4{g[r], g[r + 1], g[r + 2], g[r + 3]}; }
        }
        LGKM_BAR();
        QSTAMP(21);
        if (!par) {
            __builtin_amdgcn_s_setprio(3);
            f32x4 tt[6];
#pragma unroll
            for (int i = 0; i < 6; ++i) tt[i] = *(const LDS_AS f32x4*)(xp_ + i * 1024);
            SBAR();
#pragma unroll
            for (int i = 0; i < 6; ++i) { const f32x4 t = tt[i]; f32x16& g = oa[i / 3]; const int r = 4 * (i % 3); g[r] = add1(g[r], t[0]); g[r + 1] = add1(g[r + 1], t[1]); g[r + 2] = add1(g[r + 2], t[2]); g[r + 3] = add1(g[r + 3], t[3]); }
            const int ob = (l2 >> 4) * 20 * 1024 + (l2 & 15) * 16, os = WS_O16 + qt0 * 2 * 20 * 1024;
            const int o1 = ob + h2 * 256 + os + (head >> 1) * 1024 + (head & 1) * 512, o2 = os + (16 + (head >> 3)) * 1024 + ((head >> 1) & 3) * 256 + (head & 1) * 8;
            auto put = [&](const f32x16& o, int b, u32x2& e) {
                const float l = __uint_as_float(__builtin_amdgcn_permlane32_swap(__float_as_uint(o[8]), __float_as_uint(o[8]), false, false)[1]);
                const float rl = __builtin_amdgcn_rcpf(l);
                unsigned w[6];
#pragma unroll
                for (int i = 0; i < 6; ++i) w[i] = cvtpk(mul1(o[2 * i], rl), mul1(o[2 * i + 1], rl));
                ST16(wsr, (u32x4{w[0], w[1], w[2], w[3]}), o1, b * 40 * 1024);
                e = u32x2{w[4], w[5]};
                return l;
            };
            u32x2 e0, e1;
            QSTAMP(22);
            const float l0 = put(oa[0], 0, e0), l1 = put(oa[1], 1, e1);
            ST8(wsr, (u32x2{__builtin_amdgcn_permlane32_swap(e0[0], e1[0], false, false)[0], __builtin_amdgcn_permlane32_swap(e0[1], e1[1], false, false)[0]}), ob + h2 * 40 * 1024, o2);
            MEMFENCE(); SBAR();
            QSTAMP(23);
            float big = 0.f;
#pragma unroll
            for (int r = 0; r < 12; ++r) big = add1(big, add1(fabsf(oa[0][r]), fabsf(oa[1][r])));
            if (__builtin_expect(__any(!(big < 1e30f) | !(fminf(l0, l1) > 1e-30f)), 0)) {
                const int g1 = h2 * 512 + l2 * 8, g2 = 1024 + l2 * 8, gq = min(l2 >> 2, 5), gv = KV_V + gq * 256 + ((h2 ^ (gq & 3)) << 6) + (l2 & 3) * 16;
                asm volatile("s_waitcnt vmcnt(0)" ::: "memory");
#pragma unroll 1
                for (int b = 0; b < 2; ++b) {
                    const s16x8 qf1 = b ? q1[1] : q1[0];
                    s16x4 qf2 = b ? q2[1] : q2[0];
                    if (h2) qf2[0] = 0;
                    float m = -INFINITY;
                    f32x16 negm, o = f32x16{};
#pragma unroll 1
                    for (int t = 0; t < 64; ++t) {
                        const int g = WS_KV + head * KV_HEAD + t * KV_TILE;
                        f32x16 s = MFMA32(CAT8(LD8(wsr, g1, g), LD8(wsr, g1 + 256, g)), qf1, f32x16{});
                        s = MFMA8(LD8(wsr, g2, g), qf2, s);
                        m = fmaxf(m, max16(s));
                    }
#pragma unroll
                    for (int r = 0; r < 16; ++r) negm[r] = -m;
#pragma unroll 1
                    for (int t = 0; t < 64; ++t) {
                        const int g = WS_KV + head * KV_HEAD + t * KV_TILE;
                        f32x16 s = MFMA32(CAT8(LD8(wsr, g1, g), LD8(wsr, g1 + 256, g)), qf1, negm);
                        s = MFMA8(LD8(wsr, g2, g), qf2, s);
#pragma unroll
                        for (int r = 0; r < 16; ++r) s[r] = __builtin_amdgcn_exp2f(s[r]);
                        o = SMF32(LD16(wsr, gv, g), lo8(s), o);
                        o = SMF32(LD16(wsr, gv ^ 128, g), hi8(s), o);
                    }
                    u32x2 e;
                    put(o, b, e);
                    if (!h2) ST8(wsr, e, ob + b * 40 * 1024, o2);
                }
            }
        }
    }
    PSTAMP(61); QSTAMP(61);
    if (DIAG_PF2) { PFLUSH(); }
    __builtin_amdgcn_s_setprio(0);
    const int ch0 = wid * 128 + ln, ch1 = ch0 + 64, er0 = ch0 / 10, er1 = ch1 / 10, ec0 = ch0 - er0 * 10, ec1 = ch1 - er1 * 10;
    const size_t eb = (size_t)tp * 64 * (D_ * 2) + cg * 160;
    const unsigned eo0 = er0 * (D_ * 2) + ec0 * 16, eo1 = er1 * (D_ * 2) + ec1 * 16;
    f32x4 acc[10];
#pragma unroll
    for (int i = 0; i < 10; ++i) acc[i] = f32x4{};
    s16x8 of[10];
    QSTAMP(0); k2_bstep<0, 0, 0, -1>(kb, acc, wa, wb, hf[0], hf[12], of);
    QSTAMP(1); k2_bstep<1, 0, 0, -1>(kb, acc, wb, wa, hf[1], hf[13], of);
    QSTAMP(2); k2_bstep<2, 0, 0, -1>(kb, acc, wa, wb, hf[2], hf[14], of);
    if (!par) {
        asm volatile("s_waitcnt vmcnt(0)" ::: "memory");
        if (ln == 0) __hip_atomic_store((gu64*)(ws + WS_FC) + (qt0 >> 1) * 32 + head, tag, __ATOMIC_RELAXED, __HIP_MEMORY_SCOPE_AGENT);
    } else {
#pragma unroll
        for (int i = 0; i < 10; ++i) glds16c(kb.wsrc + 3 * 40960 - 4096 + i * 4096, ln * 16, kb.wdst - 4096 + i * 4096);
    }
    WAIT_BAR(10);
    QSTAMP(3); k2_bstep<3, 4, 5, -1>(kb, acc, wb, wa, hf[3], hf[15], of);
    QSTAMP(4); k2_bstep<4, 5, 5, -1>(kb, acc, wa, wb, hf[4], hf[16], of);
    WAIT_BAR(10);
    QSTAMP(5); k2_bstep<5, 6, 5, -1>(kb, acc, wb, wa, hf[5], hf[17], of);
    unsigned long long seen = 0;
    const char* cw = ws + WS_FC + tp * 256;
    const unsigned cvo = l2 * 8;
    if (wid == 4) asm volatile("global_load_dwordx2 %0, %1, %2 sc1" : "+v"(seen) : "v"(cvo), "s"(cw) : "memory");
    SBAR();
    QSTAMP(6); k2_bstep<6, 0, 0, -1>(kb, acc, wa, wb, hf[6], hf[18], of);
    if (wid == 4) {
        asm volatile("s_waitcnt vmcnt(0)" : "+v"(seen) :: "memory");
#pragma unroll 1
        for (unsigned spin = 0; spin < (1u << 18) && !__all(seen == tag); ++spin) {
            __builtin_amdgcn_s_sleep(1);
            asm volatile("global_load_dwordx2 %0, %1, %2 sc1\n\ts_waitcnt vmcnt(0)" : "=&v"(seen) : "v"(cvo), "s"(cw) : "memory");
        }
    }
    WAIT_BAR(10);
    QSTAMP(7); k2_bstep<7, 0, 0, 0>(kb, acc, wb, wa, hf[7], hf[19], of);
    QSTAMP(8); k2_bstep<8, 0, 0, 5>(kb, acc, wa, wb, hf[8], hf[20], of);
    WAIT_BAR(15);
    QSTAMP(9); k2_bstep<9, 7, 5, -1>(kb, acc, wb, wa, hf[9], hf[21], of);
    QSTAMP(10); k2_bstep<10, 8, 3, -1>(kb, acc, wa, wb, hf[10], hf[22], of);
    WAIT_BAR(8);
    QSTAMP(11); k2_bstep<11, 0, 0, -1>(kb, acc, wb, wa, hf[11], hf[23], of);
    QSTAMP(12); k2_bstep<12, 0, 0, -1>(kb, acc, wa, wb, of[0], of[1], of);
    WAIT_BAR(3);
    QSTAMP(13); k2_bstep<13, 0, 0, -1>(kb, acc, wb, wa, of[2], of[3], of);
    QSTAMP(14); k2_bstep<14, 0, 0, -1>(kb, acc, wa, wb, of[4], of[5], of);
    WAIT_BAR(0);
    u32x4 xv0 = u32x4{}, xv1 = u32x4{};
    if (wid < 5) { xv0 = *(const u32x4*)(x + eb + eo0); xv1 = *(const u32x4*)(x + eb + eo1); }
    MEMFENCE(); SBAR();
    QSTAMP(15); k2_bstep<15, 0, 0, -1>(kb, acc, wb, wa, of[6], of[7], of);
    QSTAMP(16); k2_bstep<16, 0, 0, -1>(kb, acc, wa, wb, of[8], of[9], of);
    QSTAMP(17);
    {
        const int tk = ln & 15;
        const lds_ptr pp = (lds_ptr)smem + kq * 20480 + (mt * 32 + tk) * 320 + (((ln >> 4) ^ ((tk >> 1) & 3)) << 4);
#pragma unroll
        for (int i = 0; i < 10; ++i) *(LDS_AS f32x4*)(pp + (i & 1) * 16 * 320 + (i >> 1) * 64) = acc[i];
        QSTAMP(18);
        LGKM_BAR();
        QSTAMP(19);
        if (wid < 5) {
#pragma unroll
            for (int e = 0; e < 2; ++e) {
                const int er = e ? er1 : er0, ec = e ? ec1 : ec0, sw = (er >> 1) & 3;
                const lds_ptr p0 = (lds_ptr)smem + er * 320 + (((2 * ec) ^ sw) << 4), p1 = (lds_ptr)smem + er * 320 + (((2 * ec + 1) ^ sw) << 4);
                f32x4 a = *(const LDS_AS f32x4*)p0, b = *(const LDS_AS f32x4*)p1;
#pragma unroll
                for (int k = 1; k < 4; ++k) { a += *(const LDS_AS f32x4*)(p0 + k * 20480); b += *(const LDS_AS f32x4*)(p1 + k * 20480); }
                const u32x4 xv = e ? xv1 : xv0;
                const u32x4 ov = u32x4{cvtpk(a[0] + bf_lo(xv[0]), a[1] + bf_hi(xv[0])), cvtpk(a[2] + bf_lo(xv[1]), a[3] + bf_hi(xv[1])),
                                       cvtpk(b[0] + bf_lo(xv[2]), b[1] + bf_hi(xv[2])), cvtpk(b[2] + bf_lo(xv[3]), b[3] + bf_hi(xv[3]))};
                __builtin_amdgcn_raw_buffer_store_b128(ov, __builtin_amdgcn_make_buffer_rsrc(out, 0, S_ * D_ * 2, 0x00020000), (int)eb + (e ? eo1 : eo0), 0, OUTAUX);
            }
        }
    }
    QSTAMP(20);
    if (CODEWARM) asm volatile(".Lfb_end:");
    STAMP(7); STAMP_FLUSH(0); if (!DIAG_PF2) { PFLUSH(); }
}

extern "C" void kernel_launch(void* const* d_in, const int* in_sizes, int n_in, void* d_out, int out_size, void* d_ws, size_t ws_size,
                              hipStream_t stream) {
    const char* x = (const char*)d_in[0];
    const char* wqkv = (const char*)d_in[1];
    const char* wo = (const char*)d_in[2];
    const char* wg = (const char*)d_in[4];
    const char* wu = (const char*)d_in[5];
    const char* wd = (const char*)d_in[6];
    char* ws = (char*)d_ws;
    (void)hipFuncSetAttribute((const void*)fused_block, hipFuncAttributeMaxDynamicSharedMemorySize, LDS_BYTES);
    fused_block<<<256, 512, LDS_BYTES, stream>>>(x, wqkv, wg, wu, wd, wo, ws, (char*)d_out);
}
```

```cpp
#include <hip/hip_runtime.h>
#include <hip/hip_bf16.h>
#include <stdint.h>

typedef __bf16 bfx4 __attribute__((ext_vector_type(4)));
typedef __bf16 bfx8 __attribute__((ext_vector_type(8)));
typedef __bf16 bfx16 __attribute__((ext_vector_type(16)));
typedef short s16x4 __attribute__((ext_vector_type(4)));
typedef short s16x8 __attribute__((ext_vector_type(8)));
typedef short s16x16 __attribute__((ext_vector_type(16)));
typedef float f32x2 __attribute__((ext_vector_type(2)));
typedef float f32x4 __attribute__((ext_vector_type(4)));
typedef float f32x8 __attribute__((ext_vector_type(8)));
typedef float f32x16 __attribute__((ext_vector_type(16)));
typedef unsigned u32x2 __attribute__((ext_vector_type(2)));
typedef unsigned u32x4 __attribute__((ext_vector_type(4)));
typedef unsigned u32x8 __attribute__((ext_vector_type(8)));
#define LDS_AS __attribute__((address_space(3)))
typedef LDS_AS char* lds_ptr;

#define ABL 0
#define DPRIO 1
#define CODEWARM 1
#define EVEN 1
#define HPRIO 3
#define CPRIO 0
#define WPWARM 0
#define UNR2 2
#define HUNR 1
#define FA_POS 14
#define ROTA 2
#define OUTAUX 16
#define K1_ROT 1
constexpr int S_ = 2048, D_ = 640, H_ = 32, F_ = 1536;

constexpr int Q_TILE = 1280, Q_HEAD = 64 * Q_TILE, KV_TILE = 2816, KV_V = 1280, KV_HEAD = 64 * KV_TILE;
constexpr int WS_Q = 0;
constexpr int WS_KV = WS_Q + H_ * Q_HEAD;
constexpr int WS_O16 = WS_KV + H_ * KV_HEAD + 8192;
constexpr int WS_H16 = WS_O16 + 128 * 20 * 1024;
constexpr int WS_WP = WS_H16 + 128 * 48 * 1024;
constexpr int WS_EPOCH = WS_WP + 8 * 68 * 5120 + 8192;
constexpr int WS_FA = WS_EPOCH + 256, WS_FB = WS_FA + 2048, WS_FC = WS_FB + 2048, WS_END = WS_FC + 8192;

#define PSTAMP(i) do {} while (0)
#define PFLUSH() do {} while (0)
#define DIAG_PF2 0
#define QSTAMP(i) do {} while (0)
#define STAMP_ARG , unsigned long long (&str_)[2]
#define STAMP_PASS , str_
#define STAMP_DECL unsigned long long str_[2] = {0, 0}
#define STAMP(i) do { if ((i) == 0) { str_[0] = __builtin_amdgcn_s_memrealtime(); str_[1] = str_[0]; } else if ((i) == 0) str_[1] = __builtin_amdgcn_s_memrealtime(); } while (0)
#define STAMP_FLUSH(kern) do { unsigned long long m_ = str_[1]; for (int k_ = 0; k_ < 4; ++k_) { unsigned long long v_ = __hip_atomic_load((gu64*)(ws + (32 << 20)) + k_ * 64 + (threadIdx.x & 63), __ATOMIC_RELAXED, __HIP_MEMORY_SCOPE_AGENT); if (v_ + 2000 > str_[1] && v_ < m_) m_ = v_; } \
    unsigned d_ = (unsigned)(str_[1] - m_); for (int o_ = 32; o_; o_ >>= 1) d_ = max(d_, (unsigned)__shfl_xor((int)d_, o_)); \
    for (int i_ = 0; i_ < 200000 && __builtin_amdgcn_s_memrealtime() < str_[1] - d_ + 6000; ++i_) __builtin_amdgcn_s_sleep(1); } while (0)
#define SBAR() __builtin_amdgcn_sched_barrier(0)
#define MFMA32(a, b, c) __builtin_amdgcn_mfma_f32_32x32x16_bf16((bfx8)(a), (bfx8)(b), (c), 0, 0, 0)

__device__ __forceinline__ unsigned cvtpk(float lo, float hi) {
    typedef __bf16 bfx2 __attribute__((ext_vector_type(2)));
    bfx2 r = {(__bf16)lo, (__bf16)hi};
    return __builtin_bit_cast(unsigned, r);
}
__device__ __forceinline__ void glds16(const void* g, unsigned lds_base) {
    unsigned sv;
    asm volatile("s_mov_b32 %0, m0\n\ts_mov_b32 m0, %2\n\ts_nop 0\n\tglobal_load_lds_dwordx4 %1, off\n\ts_mov_b32 m0, %0"
                 : "=&s"(sv) : "v"(g), "s"(lds_base) : "memory");
}
__device__ __forceinline__ s16x4 trread(const LDS_AS char* p) {
    return __builtin_amdgcn_ds_read_tr16_b64_v4i16((LDS_AS s16x4*)p);
}
__device__ __forceinline__ float rbf(float v) { return (float)(__bf16)v; }
__device__ __forceinline__ float bf_lo(unsigned u) { return __uint_as_float(u << 16); }
__device__ __forceinline__ float bf_hi(unsigned u) { return __uint_as_float(u & 0xffff0000u); }
__device__ __forceinline__ float swap32_add(float v) {
    auto r = __builtin_amdgcn_permlane32_swap(__float_as_uint(v), __float_as_uint(v), false, false);
    return __uint_as_float(r[0]) + __uint_as_float(r[1]);
}
__device__ __forceinline__ float swap32_max(float v) {
    auto r = __builtin_amdgcn_permlane32_swap(__float_as_uint(v), __float_as_uint(v), false, false);
    return fmaxf(__uint_as_float(r[0]), __uint_as_float(r[1]));
}

constexpr int P1_WSLOT = 24576, P1_X0 = 3 * P1_WSLOT, P1_XSLOT = 16384, P1_STAT = P1_X0 + 5 * P1_XSLOT;
constexpr int P1_COLA = P1_STAT + 1024, P1_COLB = P1_COLA + 512, P1_TAG = P1_COLB + 1024, LDS_JUNK = 161792, LDS_BYTES = 163840;
constexpr int K2_CHUNK = 8 * KV_TILE, K2_SLOTB = 24576, K2_SLOTS = 3, K2_KV0 = 81920, K2_C0 = 2 * P1_WSLOT, K2_Q0 = 0;
typedef __attribute__((address_space(1))) unsigned long long gu64;

#define PIN(x) asm volatile("" : "+v"(x))
#define MEMFENCE() asm volatile("" ::: "memory")
#define WAIT_BAR(N) do { MEMFENCE(); __builtin_amdgcn_s_waitcnt(0x70 | ((N) & 15) | (((N) >> 4) << 14)); __builtin_amdgcn_s_barrier(); MEMFENCE(); SBAR(); } while (0)
#define LGKM_BAR() do { MEMFENCE(); __builtin_amdgcn_s_waitcnt(0xC07F); __builtin_amdgcn_s_barrier(); MEMFENCE(); SBAR(); } while (0)
#define MFMA16(a, b, c) __builtin_amdgcn_mfma_f32_16x16x32_bf16((bfx8)(a), (bfx8)(b), (c), 0, 0, 0)
#define SMF16(a, b, c, i) __builtin_amdgcn_smfmac_f32_16x16x64_bf16((bfx8)(a), (bfx16)(b), (c), (i), 0, 0)
#define CAT8(a, b) __builtin_shufflevector(a, b, 0, 1, 2, 3, 4, 5, 6, 7)
#define CAT16(a, b) __builtin_shufflevector(a, b, 0, 1, 2, 3, 4, 5, 6, 7, 8, 9, 10, 11, 12, 13, 14, 15)

__device__ __forceinline__ void glds16s(const char* sbase, unsigned voff, unsigned lds_base) {
    unsigned sv;
    asm volatile("s_mov_b32 %0, m0\n\ts_mov_b32 m0, %3\n\ts_nop 0\n\tglobal_load_lds_dwordx4 %1, %2\n\ts_mov_b32 m0, %0"
                 : "=&s"(sv) : "v"(voff), "s"(sbase), "s"(lds_base) : "memory");
}

__device__ __forceinline__ void glds16c(const char* sbase, unsigned voff, unsigned lds_base) {
    asm volatile("s_mov_b32 m0, %2\n\ts_nop 0\n\tglobal_load_lds_dwordx4 %0, %1 sc1" :: "v"(voff), "s"(sbase), "s"(lds_base) : "memory");
}
typedef __amdgpu_buffer_rsrc_t rsrc_t;
#define ST16(rs, v, voff, soff) __builtin_amdgcn_raw_buffer_store_b128((v), (rs), (voff) + (soff), 0, (ABL & 32) ? 0 : 16)
#define ST8(rs, v, voff, soff) __builtin_amdgcn_raw_buffer_store_b64((v), (rs), (voff), (soff), 16)
#define LD16(rs, voff, soff) __builtin_bit_cast(s16x8, __builtin_amdgcn_raw_buffer_load_b128((rs), (voff), (soff), 16))
#define LD8(rs, voff, soff) __builtin_bit_cast(s16x4, __builtin_amdgcn_raw_buffer_load_b64((rs), (voff), (soff), 16))
__device__ __forceinline__ f32x2 pk(const f32x16& a, int i) { return f32x2{a[i], a[i + 1]}; }
__device__ __forceinline__ f32x2 pk4(const f32x4& a, int i) { return f32x2{a[i], a[i + 1]}; }
__device__ __forceinline__ f32x2 pfma(f32x2 a, f32x2 b, f32x2 c) { return __builtin_elementwise_fma(a, b, c); }
__device__ __forceinline__ unsigned cvt2(f32x2 v) { return cvtpk(v[0], v[1]); }
__device__ __forceinline__ float add1(float a, float b) { float r = a + b; asm volatile("" : "+v"(r)); return r; }
__device__ __forceinline__ float mul1(float a, float b) { float r = a * b; asm volatile("" : "+v"(r)); return r; }

__device__ __forceinline__ void raise_flag(char* ws, int arr, unsigned long long tag) {
    __hip_atomic_store((gu64*)(ws + arr) + blockIdx.x, tag, __ATOMIC_RELAXED, __HIP_MEMORY_SCOPE_AGENT);
}
struct Poll { u32x4 a, b; };
__device__ __forceinline__ void poll_request(Poll& p, const char* arr, unsigned voff) {
    asm volatile("global_load_dwordx4 %0, %2, %3 sc1\n\tglobal_load_dwordx4 %1, %2, %3 offset:1024 sc1" : "+v"(p.a), "+v"(p.b) : "v"(voff), "s"(arr) : "memory");
}
__device__ __forceinline__ void poll_wait(Poll& p, const char* arr, unsigned voff, unsigned long long tag) {
    const unsigned tl = (unsigned)tag, th = (unsigned)(tag >> 32);
#pragma unroll 1
    for (unsigned spin = 0; spin < (1u << 18); ++spin) {
        asm volatile("s_waitcnt vmcnt(0)" : "+v"(p.a), "+v"(p.b) :: "memory");
        if (__all(p.a[0] == tl && p.a[1] == th && p.a[2] == tl && p.a[3] == th && p.b[0] == tl && p.b[1] == th && p.b[2] == tl && p.b[3] == th)) break;
        __builtin_amdgcn_s_sleep(1);
        poll_request(p, arr, voff);
    }
    asm volatile("s_waitcnt vmcnt(0)" : "+v"(p.a), "+v"(p.b) :: "memory");
}

template <int MODE> struct P1F { s16x8 x[2]; s16x4 w[MODE == 2 ? 3 : 2][2]; };

template <int MODE>
__device__ __forceinline__ void p1_read(P1F<MODE>& n, lds_ptr xa, lds_ptr w0, lds_ptr w1, int wimm, int i) {
    constexpr int TS = MODE == 2 ? 256 : 512;
    if (i == 0) { n.x[0] = *(const LDS_AS s16x8*)xa; n.w[0][0] = trread(w0 + wimm); n.w[0][1] = trread(w0 + wimm + TS); }
    if (i == 1) { n.x[1] = *(const LDS_AS s16x8*)(xa + 4096); n.w[1][0] = trread(w1 + wimm); n.w[1][1] = trread(w1 + wimm + TS); }
    if (i == 2 && MODE == 2) { n.w[MODE == 2 ? 2 : 0][0] = trread(w0 + wimm + 1024); n.w[MODE == 2 ? 2 : 0][1] = trread(w0 + wimm + 1024 + TS); }
}
template <int MODE, int NR>
__device__ __forceinline__ void p1_step(const P1F<MODE>& f, f32x16 (&acc)[MODE == 2 ? 6 : 4], P1F<MODE>& n0, lds_ptr xa0, int wi0, P1F<MODE>& n1, lds_ptr xa1, int wi1,
                                        lds_ptr w0, lds_ptr w1) {
    constexpr int NM = MODE == 2 ? 6 : 4;
#pragma unroll
    for (int g = 0; g < NM; ++g) {
        const s16x8 w = CAT8(f.w[g >> 1][0], f.w[g >> 1][1]);
#if !(ABL & 1)
        acc[g] = MODE == 1 ? MFMA32(f.x[g & 1], w, acc[g]) : MFMA32(w, f.x[g & 1], acc[g]);
#else
        { s16x8 x_ = f.x[g & 1]; asm volatile("" :: "v"(w), "v"(x_)); }
#endif
        PIN(acc[g]); SBAR();
        if (NR >= 1 && g < NM / 2) p1_read<MODE>(n0, xa0, w0, w1, wi0, g);
        if (NR >= 2 && g >= NM / 2) p1_read<MODE>(n1, xa1, w0, w1, wi1, g - NM / 2);
        SBAR();
    }
}
struct P1X { lds_ptr a0, a1, a2, a3; };
template <int MODE>
__device__ __forceinline__ void p1_first(P1F<MODE> (&f)[4], const P1X& xa, lds_ptr w0, lds_ptr w1) {
    constexpr int KS = MODE == 2 ? 6144 : 4096;
#pragma unroll
    for (int i = 0; i < 3; ++i) p1_read<MODE>(f[0], xa.a0, w0, w1, 0, i);
#pragma unroll
    for (int i = 0; i < 3; ++i) p1_read<MODE>(f[1], xa.a1, w0, w1, KS, i);
    SBAR();
}
template <int MODE>
__device__ __forceinline__ void p1_lo(P1F<MODE> (&f)[4], f32x16 (&acc)[MODE == 2 ? 6 : 4], const P1X& xa, lds_ptr w0, lds_ptr w1) {
    constexpr int KS = MODE == 2 ? 6144 : 4096;
    p1_step<MODE, 1>(f[0], acc, f[2], xa.a2, 2 * KS, f[2], xa.a2, 0, w0, w1);
    p1_step<MODE, 1>(f[1], acc, f[3], xa.a3, 3 * KS, f[3], xa.a3, 0, w0, w1);
}
template <int MODE>
__device__ __forceinline__ void p1_hi(P1F<MODE> (&f)[4], f32x16 (&acc)[MODE == 2 ? 6 : 4], const P1X& xa, lds_ptr w0, lds_ptr w1) {
    constexpr int KS = MODE == 2 ? 6144 : 4096;
    p1_step<MODE, 1>(f[2], acc, f[0], xa.a0, 0, f[0], xa.a0, 0, w0, w1);
    p1_step<MODE, 1>(f[3], acc, f[1], xa.a1, KS, f[1], xa.a1, 0, w0, w1);
}
__device__ __forceinline__ P1X p1_xaddr(lds_ptr lds, int wid, int lane) {
    const int xrow = (wid & 1) * 64 + (lane & 31), xo = P1_X0 + xrow * 128 + (((lane >> 5) ^ ((xrow >> 1) & 7)) << 4);
    return P1X{lds + xo, lds + (xo ^ 32), lds + (xo ^ 64), lds + (xo ^ 96)};
}
template <int MODE>
__device__ __forceinline__ void p1_five(P1F<MODE> (&f)[4], f32x16 (&acc)[MODE == 2 ? 6 : 4], P1X& xa, lds_ptr& w0, lds_ptr& w1, int& ws3, int wrap, int drain, int ps) {
#pragma unroll
    for (int n = 0; n < 5; ++n) {
        const int dw = ws3 == 2 ? -2 * P1_WSLOT : P1_WSLOT, dx = n == wrap ? -4 * P1_XSLOT : P1_XSLOT;
        ws3 = ws3 == 2 ? 0 : ws3 + 1;
        p1_lo<MODE>(f, acc, xa, w0, w1);
        w0 += dw; w1 += dw; xa.a0 += dx; xa.a1 += dx; xa.a2 += dx; xa.a3 += dx;
        asm volatile("" : "+v"(w0), "+v"(w1), "+v"(xa.a0), "+v"(xa.a1), "+v"(xa.a2), "+v"(xa.a3));
        if (n == drain) asm volatile("s_waitcnt vmcnt(0)" ::: "memory");
        PSTAMP(ps + 2 * n);
        LGKM_BAR();
        PSTAMP(ps + 2 * n + 1);
        p1_hi<MODE>(f, acc, xa, w0, w1);
    }
}

struct Pack { const char *wd, *wo; int job; };
__device__ __forceinline__ void pack_fetch(const Pack& pk_, lds_ptr lds, int wid, int lane) {
    const int step = pk_.job / 10, cb = pk_.job - step * 10, r = lane >> 3;
    const unsigned va = ((r & 3) + 8 * (r >> 2)) * (D_ * 2) + (lane & 7) * 16, vb = ((r >> 2) * 20 + 16 + (r & 3)) * (D_ * 2) + (lane & 7) * 16;
#pragma unroll
    for (int g = 0; g < 4; ++g) {
        const int ks = (step - 48) * 2 + (g >> 1), hh = g & 1;
        const char* a = step < 48 ? pk_.wd + (step * 32 + (g >> 1) * 16 + hh * 4) * (D_ * 2) : ks < 32 ? pk_.wo + (ks * 20 + hh * 4) * (D_ * 2) : pk_.wo + ((ks - 32) * 4 + hh * 2) * 20 * (D_ * 2);
        glds16s(a + cb * 128, step < 64 ? va : vb, (unsigned)(uintptr_t)lds + wid * P1_WSLOT + 16384 + g * 1024);
    }
}
__device__ __forceinline__ void pack_read(s16x8 (&pf)[4], lds_ptr lds, int wid, int lane) {
    asm volatile("s_waitcnt vmcnt(0)" ::: "memory");
    const lds_ptr pr = lds + wid * P1_WSLOT + 16384 + (lane >> 4) * 1024 + ((lane >> 2) & 3) * 128 + (lane & 3) * 8;
#pragma unroll
    for (int nt = 0; nt < 4; ++nt) pf[nt] = CAT8(trread(pr + nt * 32), trread(pr + nt * 32 + 512));
}
__device__ __forceinline__ void pack_store(const Pack& pk_, const s16x8 (&pf)[4], int lane, rsrc_t wsr) {
    const int step = pk_.job / 10, cb = pk_.job - step * 10;
#pragma unroll
    for (int nt = 0; nt < 4; ++nt) {
        const int N = cb * 4 + nt, cg = N / 5;
        ST16(wsr, __builtin_bit_cast(u32x4, pf[nt]), lane * 16, WS_WP + ((cg * 68 + step) * 5 + N - cg * 5) * 1024);
    }
}

template <int MODE>
__device__ __forceinline__ void p1a_compute(f32x16 (&acc)[4], lds_ptr lds, int wid, int lane, int ra, const Pack& pk_, s16x8 (&pf)[4]) {
    const int h = lane >> 5, blk = (lane >> 4) & 1, q = (lane >> 2) & 3, fsw = 3 * (q >> 1);
    P1X xa = p1_xaddr(lds + ra * P1_XSLOT, wid, lane);
    const int wb = h * 2048 + (wid >> 1) * 1024 + q * 128 + (lane & 3) * 8;
    lds_ptr w0 = lds + wb + ((blk ^ fsw) << 5), w1 = lds + wb + (((2 + blk) ^ fsw) << 5);
    P1F<MODE> f[4];
    int ws3 = 0;
    PSTAMP(0);
    LGKM_BAR();
    PSTAMP(1);
    p1_first<MODE>(f, xa, w0, w1);
#pragma unroll UNR2
    for (int hf_ = 0; hf_ < 2; ++hf_) {
        p1_five<MODE>(f, acc, xa, w0, w1, ws3, 4 - ra, -1, 2 + 10 * hf_);
        if (hf_ == 0) {
            if (pk_.job < 680) pack_read(pf, lds, wid, lane);
        }
    }
}

__device__ __forceinline__ void p1_compute(lds_ptr lds, int wid, int lane, int panel, int grp, int ra, int rb, rsrc_t wsr, const Pack& pk_ STAMP_ARG) {
    const int h = lane >> 5, l31 = lane & 31, wm = wid & 1, wn = wid >> 1, tt = panel * 4 + wm * 2;
    const LDS_AS f32x2* rowf = (const LDS_AS f32x2*)(lds + P1_STAT);
    __builtin_amdgcn_s_setprio(CPRIO);
    if (pk_.job < 680) pack_fetch(pk_, lds, wid, lane);
    {
        f32x16 acc[4] = {f32x16{}, f32x16{}, f32x16{}, f32x16{}};
        s16x8 pf[4] = {s16x8{}, s16x8{}, s16x8{}, s16x8{}};
        const LDS_AS float* colf = (const LDS_AS float*)(lds + P1_COLA) + wn * 64;
        const int sec = grp / 5, q0 = (grp - sec * 5) * 32 + wn * 16;
        if (sec < 2) {
            p1a_compute<0>(acc, lds, wid, lane, ra, pk_, pf);
            STAMP(1);
            const int pos = sec ? ((l31 & 19) | ((l31 & 4) << 1) | ((l31 & 8) >> 1)) : l31;
            const int hs = sec ? KV_HEAD : Q_HEAD, ts = sec ? KV_TILE : Q_TILE, so = (sec ? WS_KV : WS_Q) + tt * ts;
            const float qs = sec ? 1.0f : 0.22360679774997896f * 1.4426950408889634f;
            f32x2 nm[2], rr[2];
#pragma unroll
            for (int m = 0; m < 2; ++m) { const f32x2 rs = rowf[wm * 64 + m * 32 + l31]; nm[m] = f32x2{rs[1], rs[1]}; rr[m] = f32x2{rs[0] * qs, rs[0] * qs}; }
            const bool odd = lane & 1;
#pragma unroll
            for (int gam = 0; gam < 8; gam += 2) {
                const int qd = q0 + 2 * (gam + (lane & 1)) + h, hd = (qd * 205) >> 10, vo = hd * hs + (qd - hd * 5) * 256 + (pos & ~1) * 8;
                const f32x4 ca = *(const LDS_AS f32x4*)(colf + gam * 8 + h * 4), cb = *(const LDS_AS f32x4*)(colf + gam * 8 + 8 + h * 4);
#pragma unroll
                for (int m = 0; m < 2; ++m) {
                    const f32x16& a = acc[(gam >> 2) * 2 + m];
                    const int r0 = (gam & 3) * 4;
                    const unsigned a0 = cvt2(pfma(pk4(ca, 0), nm[m], pk(a, r0)) * rr[m]), a1 = cvt2(pfma(pk4(ca, 2), nm[m], pk(a, r0 + 2)) * rr[m]);
                    const unsigned b0 = cvt2(pfma(pk4(cb, 0), nm[m], pk(a, r0 + 4)) * rr[m]), b1 = cvt2(pfma(pk4(cb, 2), nm[m], pk(a, r0 + 6)) * rr[m]);
                    const unsigned g0 = __builtin_amdgcn_update_dpp(0, odd ? a0 : b0, 0xB1, 0xF, 0xF, true), g1 = __builtin_amdgcn_update_dpp(0, odd ? a1 : b1, 0xB1, 0xF, 0xF, true);
                    ST16(wsr, (odd ? u32x4{g0, g1, b0, b1} : u32x4{a0, a1, g0, g1}), vo, so + m * ts);
                }
            }
        } else {
            p1a_compute<1>(acc, lds, wid, lane, ra, pk_, pf);
            STAMP(1);
            if (sec == 2) {
                const int so = WS_KV + tt * KV_TILE + KV_V;
#pragma unroll
                for (int m = 0; m < 2; ++m) {
                    f32x2 rr[8], nm[8];
#pragma unroll
                    for (int g = 0; g < 4; ++g) {
                        const LDS_AS f32x2* rp = rowf + wm * 64 + m * 32 + g * 8 + h * 4;
                        const f32x4 a = *(const LDS_AS f32x4*)rp, b = *(const LDS_AS f32x4*)(rp + 2);
                        rr[2 * g] = f32x2{a[0], a[2]}; nm[2 * g] = f32x2{a[1], a[3]}; rr[2 * g + 1] = f32x2{b[0], b[2]}; nm[2 * g + 1] = f32x2{b[1], b[3]};
                    }
#pragma unroll
                    for (int t = 0; t < 2; ++t) {
                        const int qd = q0 + t * 8 + (l31 >> 2), hd = (qd * 205) >> 10, pl = qd - hd * 5;
                        const int vo = hd * KV_HEAD + pl * 256 + ((h ^ (pl & 3)) << 6) + (l31 & 3) * 16;
                        const float c1 = colf[t * 32 + l31];
                        const f32x2 c2 = {c1, c1};
                        unsigned w[8];
#pragma unroll
                        for (int i = 0; i < 8; ++i) w[i] = cvt2(pfma(nm[i], c2, pk(acc[t * 2 + m], 2 * i)) * rr[i]);
                        ST16(wsr, (u32x4{w[0], w[1], w[2], w[3]}), vo, so + m * KV_TILE);
                        ST16(wsr, (u32x4{w[4], w[5], w[6], w[7]}), vo ^ 128, so + m * KV_TILE);
                    }
                }
                const int hd = q0 / 5 + (lane >> 4);
                const unsigned ov = (lane & 3) ? 0u : 0x3f803f80u;
                if (hd * 5 + 4 < q0 + 16) {
                    ST16(wsr, (u32x4{ov, ov, ov, ov}), hd * KV_HEAD + 5 * 256 + (lane & 15) * 16, so);
                    ST16(wsr, (u32x4{ov, ov, ov, ov}), hd * KV_HEAD + 5 * 256 + (lane & 15) * 16, so + KV_TILE);
                }
            }
        }
        if (pk_.job < 680) pack_store(pk_, pf, lane, wsr);
    }
    STAMP(6);
    f32x16 acc[6];
#pragma unroll
    for (int t = 0; t < 6; ++t) acc[t] = f32x16{};
    {
        const int blk = (lane >> 4) & 1, T0 = wn * 3, T1 = T0 + 1;
        P1X xa = p1_xaddr(lds + rb * P1_XSLOT, wid, lane);
        const int wb = P1_WSLOT + 3 * blk * 1024 + h * 512 + ((lane >> 2) & 3) * 64 + (lane & 3) * 8;
        lds_ptr w0 = lds + wb + (T0 >> 1) * 1024 + (((T0 & 1) ^ blk) << 5), w1 = lds + wb + (T1 >> 1) * 1024 + (((T1 & 1) ^ blk) << 5);
        P1F<2> f[4];
        p1_first<2>(f, xa, w0, w1);
        int ws3 = 1;
#pragma unroll UNR2
        for (int hf_ = 0; hf_ < 2; ++hf_) {
            p1_five<2>(f, acc, xa, w0, w1, ws3, 4 - rb, hf_ == 0 ? FA_POS - 11 : -1, 22 + 10 * hf_);
        }
    }
    STAMP(3);
    {
        const LDS_AS float* colf = (const LDS_AS float*)(lds + P1_COLB) + wn * 96;
        const int vo = (l31 >> 4) * 48 * 1024 + (h * 16 + (l31 & 15)) * 16;
        f32x2 nm[2], r2[2];
#pragma unroll
        for (int m = 0; m < 2; ++m) { const f32x2 rs = rowf[wm * 64 + m * 32 + l31]; nm[m] = f32x2{rs[1], rs[1]}; r2[m] = f32x2{rs[0] * rs[0], rs[0] * rs[0]}; }
#pragma unroll
        for (int c = 0; c < 3; ++c) {
            f32x4 cv[4];
#pragma unroll
            for (int g = 0; g < 4; ++g) cv[g] = *(const LDS_AS f32x4*)(colf + c * 32 + g * 8 + h * 4);
            if (c >= 1 && wn) {
#pragma unroll
                for (int g = 0; g < 4; ++g) cv[g] += *(const LDS_AS f32x4*)(colf + 64 + c * 32 + g * 8 + h * 4);
            }
            const int so = WS_H16 + tt * 2 * 48 * 1024 + (grp * 6 + wn * 3 + c) * 512;
#pragma unroll
            for (int m = 0; m < 2; ++m) {
                const f32x16& a = acc[c * 2 + m];
                f32x2 g[4], u[4];
#pragma unroll
                for (int i = 0; i < 4; ++i) { g[i] = pfma(pk4(cv[i >> 1], (i & 1) * 2), nm[m], pk(a, 2 * i)); u[i] = pfma(pk4(cv[2 + (i >> 1)], (i & 1) * 2), nm[m], pk(a, 8 + 2 * i)); }
#pragma unroll
                for (int i = 0; i < 4; ++i) { g[i] = f32x2{fmaxf(g[i][0], 0.f), fmaxf(g[i][1], 0.f)}; u[i] *= r2[m]; }
                ST16(wsr, (u32x4{cvt2(g[0] * u[0]), cvt2(g[1] * u[1]), cvt2(g[2] * u[2]), cvt2(g[3] * u[3])}), vo, so + m * 2 * 48 * 1024);
            }
        }
    }
}

__device__ __forceinline__ void glds_x4(const char* sbase, const unsigned (&v)[4], unsigned lds_base) {
    asm volatile("s_mov_b32 m0, %5\n\ts_nop 0\n\tglobal_load_lds_dwordx4 %0, %4\n\tglobal_load_lds_dwordx4 %1, %4 offset:1024\n\t"
                 "global_load_lds_dwordx4 %2, %4 offset:2048\n\tglobal_load_lds_dwordx4 %3, %4 offset:3072"
                 :: "v"(v[0]), "v"(v[1]), "v"(v[2]), "v"(v[3]), "s"(sbase), "s"(lds_base) : "memory");
}
__device__ __forceinline__ void glds_x3(const char* sbase, const unsigned* v, unsigned lds_base) {
    asm volatile("s_mov_b32 m0, %4\n\ts_nop 0\n\tglobal_load_lds_dwordx4 %0, %3\n\tglobal_load_lds_dwordx4 %1, %3 offset:1024\n\tglobal_load_lds_dwordx4 %2, %3 offset:2048"
                 :: "v"(v[0]), "v"(v[1]), "v"(v[2]), "s"(sbase), "s"(lds_base) : "memory");
}

__device__ __forceinline__ unsigned long long p1_helper(lds_ptr lds, int hw, int lane, const char* xp, const char* wq, const char* wgp, const char* wup, int ra, int rb,
                                          char* ws, const char* kv, const char* qsrc) {
    const unsigned lds0 = (unsigned)(uintptr_t)lds;
    const int r = lane >> 3, pos = lane & 7;
    unsigned xv[4], wa[4], wb[6];
#pragma unroll
    for (int j = 0; j < 4; ++j) {
        xv[j] = 4096 + (hw * 32 + j * 8 + r) * (D_ * 2) + ((pos ^ ((j & 1) * 4 + (r >> 1))) << 4) - j * 1024;
        wa[j] = 4096 + (hw * 16 + (j >> 1) * 8 + r) * (3 * D_ * 2) + (j & 1) * 128 + ((pos ^ (6 * ((r >> 1) & 1))) << 4) - j * 1024;
    }
#pragma unroll
    for (int j = 0; j < 6; ++j) wb[j] = 4096 + (hw * 16 + (lane >> 2)) * (F_ * 2) + (j % 3) * 64 + (((lane & 3) ^ (j < 3 ? 0 : 2)) << 4) - (j % 3) * 1024;
    xp -= 4096; wq -= 4096; wgp -= 4096; wup -= 4096;
    int kb_ = 0, xs = ra, dws = 0, left = 5;
    auto issue1 = [&](bool pa) {
        if (ABL & 4) return;
        if (pa) glds_x4(wq + (unsigned)((kb_ + xs) * 64 * 3 * D_ * 2), wa, lds0 + dws * P1_WSLOT + hw * 4096);
        else glds_x3(wgp + (unsigned)((kb_ + xs) * 64 * F_ * 2), wb, lds0 + dws * P1_WSLOT + hw * 6144);
        SBAR();
    };
    auto issue2 = [&](bool pa, bool xx) {
        SBAR();
        if (ABL & 4) return;
        if (!pa) glds_x3(wup + (unsigned)((kb_ + xs) * 64 * F_ * 2), wb + 3, lds0 + dws * P1_WSLOT + hw * 6144 + 3072);
        xs = xs == 4 ? 0 : xs + 1; dws = dws == 2 ? 0 : dws + 1; --left; kb_ = left == 0 ? 5 - kb_ : kb_; left = left == 0 ? 5 : left;
        if (xx) glds_x4(xp + (kb_ + xs) * 128, xv, lds0 + P1_X0 + xs * P1_XSLOT + hw * 4096);
    };
    glds_x4(xp + ra * 128, xv, lds0 + P1_X0 + ra * P1_XSLOT + hw * 4096);
    issue1(true); issue2(true, true);
    __builtin_amdgcn_s_barrier();
    unsigned long long ep = 0;
    asm volatile("global_load_dwordx2 %0, %1, %2 sc1" : "+v"(ep) : "v"(0u), "s"(ws + WS_EPOCH) : "memory");
    __builtin_amdgcn_s_setprio(HPRIO);
    const int r16 = lane & 15, h = lane >> 5, blk = (lane >> 4) & 1, q = (lane >> 2) & 3, fsw = 3 * (q >> 1);
    const unsigned olo = r16 < 2 ? 0x3f803f80u : 0u, ohi = (r16 >> 1) == 1 ? 0x3f803f80u : 0u;
    const s16x8 ones = __builtin_bit_cast(s16x8, (u32x4{olo, olo, ohi, ohi}));
    const int idx = (r16 & 1) ? 0xEEEE : 0x4444;
    int ws3 = 0;
    {
        lds_ptr b = lds + h * 2048 + (hw >> 1) * 1024 + q * 128 + (lane & 3) * 8 + ((((hw & 1) * 2 + blk) ^ fsw) << 5);
        const int so = P1_X0 + ra * P1_XSLOT + (hw * 32 + r16) * 128 + (((lane >> 4) ^ (r16 >> 1)) << 4);
        lds_ptr sp0 = lds + so, sp1 = lds + (so ^ 64);
        const s16x8 ones2 = __builtin_bit_cast(s16x8, (u32x4{olo, olo, olo, olo}));
        f32x4 cs = f32x4{}, s1[2] = {f32x4{}, f32x4{}}, s2[2] = {f32x4{}, f32x4{}};
        s16x16 wv0, wv1; s16x8 xa[2], xb[2];
        auto col_rd = [&]() {
            wv0 = CAT16(CAT8(trread(b), trread(b + 512)), CAT8(trread(b + 4096), trread(b + 4096 + 512)));
            wv1 = CAT16(CAT8(trread(b + 8192), trread(b + 8192 + 512)), CAT8(trread(b + 12288), trread(b + 12288 + 512)));
            b += ws3 == 2 ? -2 * P1_WSLOT : P1_WSLOT; ws3 = ws3 == 2 ? 0 : ws3 + 1;
        };
        auto row_rd = [&](int dx) {
#pragma unroll
            for (int t = 0; t < 2; ++t) { xa[t] = *(const LDS_AS s16x8*)(sp0 + t * 2048); xb[t] = *(const LDS_AS s16x8*)(sp1 + t * 2048); }
            sp0 += dx; sp1 += dx;
        };
        auto row_mm = [&]() {
#pragma unroll
            for (int t = 0; t < 2; ++t) { s2[t] = MFMA16(xa[t], xa[t], s2[t]); s1[t] = SMF16(ones2, CAT16(xa[t], xb[t]), s1[t], idx); }
#pragma unroll
            for (int t = 0; t < 2; ++t) { s2[t] = MFMA16(xb[t], xb[t], s2[t]); PIN(s1[t]); PIN(s2[t]); }
        };
#pragma unroll
        for (int nn = 0; nn < 9; ++nn) {
            int n = nn;
            if (!HUNR) asm volatile("" : "+s"(n));
            PSTAMP(2 * n);
            if (n == 0) WAIT_BAR(5); else if (n < 8) WAIT_BAR(12); else WAIT_BAR(8);
            PSTAMP(2 * n + 1);
            if (!(ABL & 8) || n == 0) { col_rd(); row_rd(n % 5 == 4 - ra ? -4 * P1_XSLOT : P1_XSLOT); }
            if (n == 0) { issue1(true); issue2(true, true); }
            if (n == 8) { kb_ = 5; xs = rb; }
            issue1(n < 8);
            if (!(ABL & 8)) {
            cs = SMF16(ones, wv0, cs, idx);
            row_mm();
            cs = SMF16(ones, wv1, cs, idx); PIN(cs); }
            issue2(n < 8, n < 7);
        }
        MEMFENCE(); __builtin_amdgcn_s_waitcnt(0xF70 | 10); MEMFENCE();
        row_rd(0); row_mm();
        {
            const int t = blk;
            const float d0 = (lane & 2) ? ((lane & 1) ? s2[0][3] : s2[0][2]) : ((lane & 1) ? s2[0][1] : s2[0][0]);
            const float d1 = (lane & 2) ? ((lane & 1) ? s2[1][3] : s2[1][2]) : ((lane & 1) ? s2[1][1] : s2[1][0]);
            const float a0 = __shfl(s1[0][0] + s1[0][1], r16), a1 = __shfl(s1[1][0] + s1[1][1], r16), c0 = __shfl(d0, r16 + 16 * q), c1 = __shfl(d1, r16 + 16 * q);
            const float a = t ? a1 : a0, c = t ? c1 : c0;
            const float mean = a * (1.0f / D_), var = fmaxf(c * (1.0f / D_) - mean * mean, 0.f);
            const float rstd = __builtin_amdgcn_rcpf(rbf(__builtin_amdgcn_sqrtf(rbf(rbf(var) + 1e-5f))));
            if (lane < 32) ((LDS_AS f32x2*)(lds + P1_STAT))[hw * 32 + lane] = f32x2{rstd, -rbf(mean)};
        }
        PSTAMP(18);
        WAIT_BAR(6);
        PSTAMP(19);
        col_rd();
        issue1(false);
        cs = SMF16(ones, wv0, cs, idx);
        cs = SMF16(ones, wv1, cs, idx); PIN(cs);
        issue2(false, false);
        LDS_AS float* colf = (LDS_AS float*)(lds + P1_COLA) + hw * 32;
        if (lane < 16) { colf[lane] = cs[0] + cs[1]; colf[16 + lane] = cs[2] + cs[3]; }
    }
    asm volatile("" : "+v"(ep));
    const unsigned long long tag = (((unsigned long long)(unsigned)__builtin_amdgcn_readfirstlane((unsigned)(ep >> 32)) << 32) | (unsigned)__builtin_amdgcn_readfirstlane((unsigned)ep)) + 1ull;
    if (hw == 0 && lane == 0) *(LDS_AS unsigned long long*)(lds + P1_TAG) = tag;
    {
        const int ro = P1_WSLOT + 3 * blk * 1024 + h * 512 + q * 64 + (lane & 3) * 8 + (((hw & 1) ^ blk) << 5);
        lds_ptr b = lds + ro + (hw >> 1) * 1024, c = lds + ro + 2048 + (hw >> 1) * 12288;
        f32x4 cs = f32x4{}, ct = f32x4{};
        Poll pl = {};
#pragma unroll
        for (int nn = 10; nn < 20; ++nn) {
            int n = nn;
            if (!HUNR) asm volatile("" : "+s"(n));
            PSTAMP(2 * n);
            if (n < 13 || n == 19) WAIT_BAR(6); else if (n == 13) WAIT_BAR(10); else if (n == 18) WAIT_BAR(12); else WAIT_BAR(14);
            PSTAMP(2 * n + 1);
            const s16x16 wv0 = CAT16(CAT8(trread(b), trread(b + 256)), CAT8(trread(b + 6144), trread(b + 6144 + 256)));
            const s16x16 wv2 = CAT16(CAT8(trread(c), trread(c + 256)), CAT8(trread(c + 6144), trread(c + 6144 + 256)));
            const s16x16 wv1 = CAT16(CAT8(trread(b + 12288), trread(b + 12288 + 256)), CAT8(trread(b + 18432), trread(b + 18432 + 256)));
            if (n == FA_POS && hw == 0 && lane == 0) raise_flag(ws, WS_FA, tag);
            if (n < 18) issue1(false);
            cs = SMF16(ones, wv0, cs, idx);
            ct = SMF16(ones, wv2, ct, idx);
            cs = SMF16(ones, wv1, cs, idx); PIN(cs); PIN(ct);
            if (n < 18) issue2(false, n >= 12 && n < 17);
            if (n == 17) poll_request(pl, ws + WS_FA, lane * 16);
            if (n == 18) {
                poll_wait(pl, ws + WS_FA, lane * 16, tag);
#pragma unroll
                for (int j = 0; j < 6; ++j) glds16c(kv + (hw + 4 * j) * 1024, lane * 16, lds0 + K2_C0 + (hw + 4 * j) * 1024);
            }
            if (n == 19) {
#pragma unroll
                for (int j = 0; j < 3; ++j) glds16c(qsrc + (hw + 4 * j) * 1024, lane * 16, lds0 + K2_Q0 + (hw + 4 * j) * 1024);
            }
            const int dw = ws3 == 2 ? -2 * P1_WSLOT : P1_WSLOT;
            b += dw; c += dw; ws3 = ws3 == 2 ? 0 : ws3 + 1;
        }
        LDS_AS float* colf = (LDS_AS float*)(lds + P1_COLB);
        if (lane < 16) { colf[hw * 32 + lane] = cs[0] + cs[1]; colf[hw * 32 + 16 + lane] = cs[2] + cs[3]; }
        if (lane < 16) { colf[128 + hw * 32 + lane] = ct[0] + ct[1]; colf[128 + hw * 32 + 16 + lane] = ct[2] + ct[3]; }
        WAIT_BAR(0);
        if (blockIdx.x == 0 && hw == 0 && lane == 0) __hip_atomic_store((gu64*)(ws + WS_EPOCH), tag, __ATOMIC_RELAXED, __HIP_MEMORY_SCOPE_AGENT);
#pragma unroll
        for (int j = 0; j < 6; ++j) glds16c(kv + K2_CHUNK + (hw + 4 * j) * 1024, lane * 16, lds0 + K2_KV0 + K2_SLOTB + (hw + 4 * j) * 1024);
    }
    return tag;
}

constexpr int K2_XCH = 2 * K2_SLOTB;

#define SMF32(a, b, c) __builtin_amdgcn_smfmac_f32_32x32x32_bf16((bfx8)(a), __builtin_bit_cast(bfx16, (b)), (c), 0xDDDD, 0, 0)
#define MFMA8(a, b, c) __builtin_amdgcn_mfma_f32_32x32x8bf16_1k((a), (b), (c), 0, 0, 0)
__device__ __forceinline__ f32x8 lo8(const f32x16& v) { return __builtin_shufflevector(v, v, 0, 1, 2, 3, 4, 5, 6, 7); }
__device__ __forceinline__ f32x8 hi8(const f32x16& v) { return __builtin_shufflevector(v, v, 8, 9, 10, 11, 12, 13, 14, 15); }
__device__ __forceinline__ float max16(const f32x16& s) {
    float m = fmaxf(fmaxf(s[0], s[1]), s[2]);
#pragma unroll
    for (int r = 3; r < 15; r += 2) m = fmaxf(fmaxf(m, s[r]), s[r + 1]);
    return swap32_max(fmaxf(m, s[15]));
}

struct K2B { lds_ptr r0, r1, r2; const char* wsrc; unsigned wdst; int lane, osoff; rsrc_t wsr; };
constexpr int k2_slot(int st) { return st < 3 ? st : st == 3 ? 0 : st == 4 ? 3 : st == 5 ? 1 : st == 6 ? 2 : st == 7 ? 0 : 3; }
__device__ __forceinline__ void k2_wpiece(const K2B& c, int st, int i) {
    glds16c(c.wsrc + st * 40960 + i * 8192, c.lane * 16, c.wdst + k2_slot(st) * 40960 + i * 8192);
}
template <int I, int D, int NP, int OF>
__device__ __forceinline__ void k2_bstep(const K2B& c, f32x4 (&acc)[10], const s16x8 (&w)[5], s16x8 (&n)[5], const s16x8& a0, const s16x8& a1, s16x8 (&of)[10]) {
    constexpr int O = k2_slot((I + 1) >> 1) * 40960 + ((I + 1) & 1) * 20480;
#pragma unroll
    for (int i = 0; i < 10; ++i) {
        if (I > 0 && I < 16 && i < 5) n[i] = *(const LDS_AS s16x8*)((O < 61440 ? c.r0 : O < 122880 ? c.r1 : c.r2) + O % 61440 + i * 1024);
        acc[i] = MFMA16(w[i >> 1], (i & 1) ? a1 : a0, acc[i]);
        PIN(acc[i]);
        if (OF >= 0 && !(i & 1)) { const int k = (OF < 0 ? 0 : OF) + (i >> 1); of[k] = LD16(c.wsr, c.lane * 16, c.osoff + ((k & 1) * 20 + (k >> 1) * 4) * 1024); MEMFENCE(); }
        if ((i & 1) && (i >> 1) < NP) k2_wpiece(c, D, i >> 1);
        SBAR();
    }
}

__global__ __launch_bounds__(512, 2) void fused_block(const char* __restrict__ x, const char* __restrict__ wqkv, const char* __restrict__ wg, const char* __restrict__ wu,
                                                      const char* __restrict__ wd, const char* __restrict__ wo, char* __restrict__ ws, char* __restrict__ out) {
    extern __shared__ __attribute__((aligned(16))) char smem[];
    STAMP_DECL; asm volatile("s_memrealtime %0\n\ts_waitcnt lgkmcnt(0)" : "=s"(str_[1])); __builtin_amdgcn_sched_barrier(0);
    asm volatile("" :: "s"(x), "s"(wqkv), "s"(wg), "s"(wu), "s"(wd), "s"(wo), "s"(ws), "s"(out));
    const int wid = __builtin_amdgcn_readfirstlane(threadIdx.x >> 6);
    if (threadIdx.x == 0) __hip_atomic_store((gu64*)(ws + (32 << 20)) + blockIdx.x, str_[1], __ATOMIC_RELAXED, __HIP_MEMORY_SCOPE_AGENT);
    unsigned long long tag = 0;
    if (wid < 4) {
        __builtin_amdgcn_s_barrier();
        if (CODEWARM && wid == 3) {
            unsigned off = ((blockIdx.x >> 3) * 12 + min(threadIdx.x & 63, 11)) * 128;
            asm volatile("s_getpc_b64 vcc\n.Lfb_begin:\n\tv_min_u32 %0, .Lfb_end-.Lfb_begin, %0\n\ts_mov_b32 m0, %1\n\ts_nop 0\n\tglobal_load_lds_dword %0, vcc"
                         : "+v"(off) : "s"((unsigned)(uintptr_t)(lds_ptr)smem + LDS_JUNK) : "vcc", "memory");
        }
    }
    const rsrc_t wsr = __builtin_amdgcn_make_buffer_rsrc(ws, 0, (int)WS_END, 0x00020000);
    const int bid = blockIdx.x, head = (bid & 7) * 4 + ((bid >> 3) & 3), par = wid >> 2, qt0 = (bid >> 5) * 8 + (wid & 3) * 2;
    const char* kv = ws + WS_KV + head * KV_HEAD;
    {
        const int bi = bid >> 3, panel = (bid & 1) * 8 + (bi & 7), grp = ((bid & 7) >> 1) * 4 + (bi >> 3), l0 = threadIdx.x & 63;
        const int rb = K1_ROT ? (5 * (bi & 7)) >> 3 : 0, ra = ROTA == 1 ? (rb + (bi >> 3)) % 5 : ROTA == 2 ? ((bi & 1) * 2 + ((bi >> 3) & 1) * 2) % 4 : 0;
        if (wid < 4) {
            p1_compute((lds_ptr)smem, wid, l0, panel, grp, ra, rb, wsr, Pack{wd, wo, wid * 256 + bid} STAMP_PASS);
            const u32x2 e = *(const LDS_AS u32x2*)((lds_ptr)smem + P1_TAG);
            tag = ((unsigned long long)(unsigned)__builtin_amdgcn_readfirstlane(e[1]) << 32) | (unsigned)__builtin_amdgcn_readfirstlane(e[0]);
        } else {
            tag = p1_helper((lds_ptr)smem, wid - 4, l0, x + (size_t)panel * 128 * (D_ * 2), wqkv + min(grp, 14) * 256, wg + grp * 192, wu + grp * 192, ra, rb,
                            ws, kv, ws + WS_Q + head * Q_HEAD + (bid >> 5) * 8 * Q_TILE);
        }
    }
    int lane = __builtin_amdgcn_mbcnt_hi(~0u, __builtin_amdgcn_mbcnt_lo(~0u, 0u));
    PIN(lane);
    if (wid < 4) __builtin_amdgcn_s_setprio(3); else __builtin_amdgcn_s_setprio(0);
    STAMP(4);
    lds_ptr lds = (lds_ptr)smem + K2_KV0;
    const unsigned lds0 = (unsigned)(uintptr_t)lds;
    const int tp = ((blockIdx.x & 7) >> 1) * 8 + ((blockIdx.x >> 3) & 7), cg = (blockIdx.x & 1) * 4 + (blockIdx.x >> 6);
    K2B kb;
    kb.wsrc = ws + WS_WP + cg * 68 * 5120 + wid * 1024; kb.wdst = (unsigned)(uintptr_t)(lds_ptr)smem + wid * 1024; kb.lane = lane;
    const int hoff = WS_H16 + ((tp * 4 + (wid & 1) * 2) * 48 + (wid >> 1)) * 1024;
    s16x8 hf[24];
    const int h = lane >> 5, l31 = lane & 31, l16 = lane * 16;
    const char* src = kv + wid * 1024 + 3 * K2_CHUNK;
    const int k1off = h * 512 + l31 * 8, k2off = h ? KV_V + 5 * 256 : 1024 + l31 * 8;
    const int vq = min(l31 >> 2, 5), voff = KV_V + vq * 256 + ((h ^ (vq & 3)) << 6) + (l31 < 24 ? l31 & 3 : 1) * 16;

    f32x16 oa[2] = {f32x16{}, f32x16{}}, sc[2];
    s16x8 k1[2], v0[2], v1[2]; s16x4 k2[2];
    const lds_ptr c0 = (lds_ptr)smem + K2_C0;
    lds_ptr pk1 = c0 + k1off + par * KV_TILE, pk2 = c0 + k2off + par * KV_TILE, pv = c0 + voff + par * KV_TILE, pw = c0 + (voff ^ 128) + par * KV_TILE;
    lds_ptr pk3 = pk1 + 256;
    asm volatile("" : "+v"(pk3));
#define LDS8(p) (*(const LDS_AS s16x4*)(p))
#define RD_K(t, j) do { k1[(t) & 1] = CAT8(LDS8(pk1 + (j) * 2 * KV_TILE), LDS8(pk3 + (j) * 2 * KV_TILE)); k2[(t) & 1] = LDS8(pk2 + (j) * 2 * KV_TILE); } while (0)
#define RD_V(t, j) do { v0[(t) & 1] = *(const LDS_AS s16x8*)(pv + (j) * 2 * KV_TILE); v1[(t) & 1] = *(const LDS_AS s16x8*)(pw + (j) * 2 * KV_TILE); } while (0)
    s16x8 q1[2]; s16x4 q2[2];
#pragma unroll
    for (int b = 0; b < 2; ++b) {
        const lds_ptr qp = (lds_ptr)smem + K2_Q0 + ((wid & 3) * 2 + b) * Q_TILE;
        q1[b] = CAT8(LDS8(qp + k1off), LDS8(qp + k1off + 256)); q2[b] = LDS8(qp + 1024 + l31 * 8);
    }
    {
        const s16x8 ka = CAT8(LDS8(c0 + k1off), LDS8(c0 + k1off + 256));
        const s16x4 kb = LDS8(c0 + k2off);
        RD_K(0, 0); RD_V(0, 0);
#pragma unroll
        for (int b = 0; b < 2; ++b) {
            if (h) q2[b] = s16x4{0, 0, 0, 0};
            f32x16 s = MFMA32(ka, q1[b], f32x16{});
            s = MFMA8(kb, q2[b], s);
            const float m = rbf(-max16(s));
            if (h) q2[b][0] = (short)(__float_as_uint(m) >> 16);
        }
        sc[0] = MFMA32(k1[0], q1[0], f32x16{});
        sc[0] = MFMA8(k2[0], q2[0], sc[0]);
        sc[1] = f32x16{}; v1[1] = s16x8{};
    }
    STAMP(5); PSTAMP(42); PSTAMP(43);
#define EXP4(S, i) do { S[i] = __builtin_amdgcn_exp2f(S[i]); S[i + 1] = __builtin_amdgcn_exp2f(S[i + 1]); S[i + 2] = __builtin_amdgcn_exp2f(S[i + 2]); S[i + 3] = __builtin_amdgcn_exp2f(S[i + 3]); PIN(S); SBAR(); } while (0)
    Poll pl = {};
    auto request = [&](int hs) {
        const int p = (hs - 6) >> 3, r = (hs - 6) & 7;
        const int w = (hs >= 9 && hs < 14) ? hs - 9 : (hs >= 17 && hs < 22) ? hs - 12 : (hs >= 54 && hs < 57) ? hs - 44 : hs >= 62 ? hs - 49 : -1;
        const int e = (p >= 2 && p <= 4 && r >= 3) ? (p - 2) * 5 + r - 3 : p == 5 ? 15 + r : hs == 57 ? 23 : -1;
        if (hs < 3) glds16c(src - K2_CHUNK + hs * 8192, l16, lds0 + 2 * K2_SLOTB + wid * 1024 + hs * 8192);
        else if (hs >= 6 && p < 5 && r < 3) glds16c(src + p * K2_CHUNK + r * 8192, l16, lds0 + (p % 3) * K2_SLOTB + wid * 1024 + r * 8192);
        else if (w >= 0) glds16c(kb.wsrc + w * 8192, l16, kb.wdst + w * 8192);
        else if (e >= 0) { hf[e] = LD16(wsr, l16, hoff + e * 4096); MEMFENCE(); }
        if (hs == 6 && wid == 4 && lane == 0) raise_flag(ws, WS_FB, tag);
        if (hs == 16 && wid == 4) poll_request(pl, ws + WS_FB, l16);
        SBAR();
    };
#pragma unroll
    for (int c = 0; c < 8; ++c) {
#pragma unroll
        for (int j = 0; j < 4; ++j) {
            if (j == 3) {
                PSTAMP(44 + 2 * c);
                if (c == 2 && wid == 4) poll_wait(pl, ws + WS_FB, l16, tag);
                if (c == 0) { asm volatile("s_waitcnt vmcnt(0)" ::: "memory"); LGKM_BAR(); } else if (c == 7) WAIT_BAR(0); else WAIT_BAR(7);
                PSTAMP(45 + 2 * c);
                __builtin_amdgcn_s_setprio(2);
                const int dsl = c == 0 ? K2_KV0 + K2_SLOTB - K2_C0 : (c % 3 == 2) ? -2 * K2_SLOTB : K2_SLOTB;
                pk1 += dsl; pk2 += dsl; pk3 += dsl; pv += dsl; pw += dsl;
                asm volatile("" : "+v"(pk1), "+v"(pk2), "+v"(pk3), "+v"(pv), "+v"(pw));
                SBAR();
            }
            RD_K(j + 1, (j + 1) & 3); SBAR();
            oa[1] = SMF32(v1[(j + 1) & 1], hi8(sc[1]), oa[1]); PIN(oa[1]); SBAR();
            EXP4(sc[0], 0);
            sc[1] = MFMA32(k1[j & 1], q1[1], f32x16{}); PIN(sc[1]); SBAR();
            request(8 * c + 2 * j);
            EXP4(sc[0], 4);
            sc[1] = MFMA8(k2[j & 1], q2[1], sc[1]); PIN(sc[1]); SBAR();
            EXP4(sc[0], 8);
            oa[0] = SMF32(v0[j & 1], lo8(sc[0]), oa[0]); PIN(oa[0]); SBAR();
            EXP4(sc[0], 12);
            RD_V(j + 1, (j + 1) & 3);
            if (j == 1 && c > 0) __builtin_amdgcn_s_setprio(1);
            if (j == 2 && c > 0) __builtin_amdgcn_s_setprio(0);
            SBAR();
            oa[0] = SMF32(v1[j & 1], hi8(sc[0]), oa[0]); PIN(oa[0]); SBAR();
            EXP4(sc[1], 0);
            sc[0] = MFMA32(k1[(j + 1) & 1], q1[0], f32x16{}); PIN(sc[0]); SBAR();
            request(8 * c + 2 * j + 1);
            EXP4(sc[1], 4);
            sc[0] = MFMA8(k2[(j + 1) & 1], q2[0], sc[0]); PIN(sc[0]); SBAR();
            EXP4(sc[1], 8);
            oa[1] = SMF32(v0[j & 1], lo8(sc[1]), oa[1]); PIN(oa[1]); SBAR();
            EXP4(sc[1], 12);
        }
    }
    oa[1] = SMF32(v1[1], hi8(sc[1]), oa[1]);
    unsigned qs_ = 0; (void)qs_;
    STAMP(2);
    PSTAMP(60); QSTAMP(60);
    int ln = __builtin_amdgcn_mbcnt_hi(~0u, __builtin_amdgcn_mbcnt_lo(~0u, 0u));
    PIN(ln);
    const int h2 = ln >> 5, l2 = ln & 31;
    const int mt = wid & 1, kq = wid >> 1;
    kb.lane = ln; kb.r0 = (lds_ptr)smem + kq * 5120 + ln * 16; kb.r1 = kb.r0 + 61440; kb.r2 = kb.r0 + 122880;
    asm volatile("" : "+v"(kb.r1), "+v"(kb.r2));
    kb.wsr = wsr; kb.osoff = WS_O16 + ((tp * 4 + mt * 2) * 20 + kq) * 1024;
    s16x8 wa[5], wb[5];
#pragma unroll
    for (int i = 0; i < 5; ++i) { wa[i] = *(const LDS_AS s16x8*)(kb.r0 + i * 1024); wb[i] = *(const LDS_AS s16x8*)(kb.r0 + 20480 + i * 1024); }
    {
        const lds_ptr xp_ = lds + K2_XCH + (wid & 3) * 6144 + ln * 16;
        if (par) {
#pragma unroll
            for (int i = 0; i < 6; ++i) { const f32x16& g = oa[i / 3]; const int r = 4 * (i % 3); *(LDS_AS f32x4*)(xp_ + i * 1024) = f32x4{g[r], g[r + 1], g[r + 2], g[r + 3]}; }
        }
        LGKM_BAR();
        QSTAMP(21);
        if (!par) {
            __builtin_amdgcn_s_setprio(3);
            f32x4 tt[6];
#pragma unroll
            for (int i = 0; i < 6; ++i) tt[i] = *(const LDS_AS f32x4*)(xp_ + i * 1024);
            SBAR();
#pragma unroll
            for (int i = 0; i < 6; ++i) { const f32x4 t = tt[i]; f32x16& g = oa[i / 3]; const int r = 4 * (i % 3); g[r] = add1(g[r], t[0]); g[r + 1] = add1(g[r + 1], t[1]); g[r + 2] = add1(g[r + 2], t[2]); g[r + 3] = add1(g[r + 3], t[3]); }
            const int ob = (l2 >> 4) * 20 * 1024 + (l2 & 15) * 16, os = WS_O16 + qt0 * 2 * 20 * 1024;
            const int o1 = ob + h2 * 256 + os + (head >> 1) * 1024 + (head & 1) * 512, o2 = os + (16 + (head >> 3)) * 1024 + ((head >> 1) & 3) * 256 + (head & 1) * 8;
            auto put = [&](const f32x16& o, int b, u32x2& e) {
                const float l = __uint_as_float(__builtin_amdgcn_permlane32_swap(__float_as_uint(o[8]), __float_as_uint(o[8]), false, false)[1]);
                const float rl = __builtin_amdgcn_rcpf(l);
                unsigned w[6];
#pragma unroll
                for (int i = 0; i < 6; ++i) w[i] = cvtpk(mul1(o[2 * i], rl), mul1(o[2 * i + 1], rl));
                ST16(wsr, (u32x4{w[0], w[1], w[2], w[3]}), o1, b * 40 * 1024);
                e = u32x2{w[4], w[5]};
                return l;
            };
            u32x2 e0, e1;
            QSTAMP(22);
            const float l0 = put(oa[0], 0, e0), l1 = put(oa[1], 1, e1);
            ST8(wsr, (u32x2{__builtin_amdgcn_permlane32_swap(e0[0], e1[0], false, false)[0], __builtin_amdgcn_permlane32_swap(e0[1], e1[1], false, false)[0]}), ob + h2 * 40 * 1024, o2);
            MEMFENCE(); SBAR();
            QSTAMP(23);
            float big = 0.f;
#pragma unroll
            for (int r = 0; r < 12; ++r) big = add1(big, add1(fabsf(oa[0][r]), fabsf(oa[1][r])));
            if (__builtin_expect(__any(!(big < 1e30f) | !(fminf(l0, l1) > 1e-30f)), 0)) {
                const int g1 = h2 * 512 + l2 * 8, g2 = 1024 + l2 * 8, gq = min(l2 >> 2, 5), gv = KV_V + gq * 256 + ((h2 ^ (gq & 3)) << 6) + (l2 & 3) * 16;
                asm volatile("s_waitcnt vmcnt(0)" ::: "memory");
#pragma unroll 1
                for (int b = 0; b < 2; ++b) {
                    const s16x8 qf1 = b ? q1[1] : q1[0];
                    s16x4 qf2 = b ? q2[1] : q2[0];
                    if (h2) qf2[0] = 0;
                    float m = -INFINITY;
                    f32x16 negm, o = f32x16{};
#pragma unroll 1
                    for (int t = 0; t < 64; ++t) {
                        const int g = WS_KV + head * KV_HEAD + t * KV_TILE;
                        f32x16 s = MFMA32(CAT8(LD8(wsr, g1, g), LD8(wsr, g1 + 256, g)), qf1, f32x16{});
                        s = MFMA8(LD8(wsr, g2, g), qf2, s);
                        m = fmaxf(m, max16(s));
                    }
#pragma unroll
                    for (int r = 0; r < 16; ++r) negm[r] = -m;
#pragma unroll 1
                    for (int t = 0; t < 64; ++t) {
                        const int g = WS_KV + head * KV_HEAD + t * KV_TILE;
                        f32x16 s = MFMA32(CAT8(LD8(wsr, g1, g), LD8(wsr, g1 + 256, g)), qf1, negm);
                        s = MFMA8(LD8(wsr, g2, g), qf2, s);
#pragma unroll
                        for (int r = 0; r < 16; ++r) s[r] = __builtin_amdgcn_exp2f(s[r]);
                        o = SMF32(LD16(wsr, gv, g), lo8(s), o);
                        o = SMF32(LD16(wsr, gv ^ 128, g), hi8(s), o);
                    }
                    u32x2 e;
                    put(o, b, e);
                    if (!h2) ST8(wsr, e, ob + b * 40 * 1024, o2);
                }
            }
        }
    }
    PSTAMP(61); QSTAMP(61);
    if (DIAG_PF2) { PFLUSH(); }
    __builtin_amdgcn_s_setprio(0);
    const int ch0 = wid * 128 + ln, ch1 = ch0 + 64, er0 = ch0 / 10, er1 = ch1 / 10, ec0 = ch0 - er0 * 10, ec1 = ch1 - er1 * 10;
    const size_t eb = (size_t)tp * 64 * (D_ * 2) + cg * 160;
    const unsigned eo0 = er0 * (D_ * 2) + ec0 * 16, eo1 = er1 * (D_ * 2) + ec1 * 16;
    f32x4 acc[10];
#pragma unroll
    for (int i = 0; i < 10; ++i) acc[i] = f32x4{};
    s16x8 of[10];
    QSTAMP(0); k2_bstep<0, 0, 0, -1>(kb, acc, wa, wb, hf[0], hf[12], of);
    QSTAMP(1); k2_bstep<1, 0, 0, -1>(kb, acc, wb, wa, hf[1], hf[13], of);
    QSTAMP(2); k2_bstep<2, 0, 0, -1>(kb, acc, wa, wb, hf[2], hf[14], of);
    if (!par) {
        asm volatile("s_waitcnt vmcnt(0)" ::: "memory");
        if (ln == 0) __hip_atomic_store((gu64*)(ws + WS_FC) + (qt0 >> 1) * 32 + head, tag, __ATOMIC_RELAXED, __HIP_MEMORY_SCOPE_AGENT);
    } else {
#pragma unroll
        for (int i = 0; i < 10; ++i) glds16c(kb.wsrc + 3 * 40960 - 4096 + i * 4096, ln * 16, kb.wdst - 4096 + i * 4096);
    }
    WAIT_BAR(10);
    QSTAMP(3); k2_bstep<3, 4, 5, -1>(kb, acc, wb, wa, hf[3], hf[15], of);
    QSTAMP(4); k2_bstep<4, 5, 5, -1>(kb, acc, wa, wb, hf[4], hf[16], of);
    WAIT_BAR(10);
    QSTAMP(5); k2_bstep<5, 6, 5, -1>(kb, acc, wb, wa, hf[5], hf[17], of);
    unsigned long long seen = 0;
    const char* cw = ws + WS_FC + tp * 256;
    const unsigned cvo = l2 * 8;
    if (wid == 4) asm volatile("global_load_dwordx2 %0, %1, %2 sc1" : "+v"(seen) : "v"(cvo), "s"(cw) : "memory");
    SBAR();
    QSTAMP(6); k2_bstep<6, 0, 0, -1>(kb, acc, wa, wb, hf[6], hf[18], of);
    if (wid == 4) {
        asm volatile("s_waitcnt vmcnt(0)" : "+v"(seen) :: "memory");
#pragma unroll 1
        for (unsigned spin = 0; spin < (1u << 18) && !__all(seen == tag); ++spin) {
            __builtin_amdgcn_s_sleep(1);
            asm volatile("global_load_dwordx2 %0, %1, %2 sc1\n\ts_waitcnt vmcnt(0)" : "=&v"(seen) : "v"(cvo), "s"(cw) : "memory");
        }
    }
    WAIT_BAR(10);
    QSTAMP(7); k2_bstep<7, 0, 0, 0>(kb, acc, wb, wa, hf[7], hf[19], of);
    QSTAMP(8); k2_bstep<8, 0, 0, 5>(kb, acc, wa, wb, hf[8], hf[20], of);
    WAIT_BAR(15);
    QSTAMP(9); k2_bstep<9, 7, 5, -1>(kb, acc, wb, wa, hf[9], hf[21], of);
    QSTAMP(10); k2_bstep<10, 8, 3, -1>(kb, acc, wa, wb, hf[10], hf[22], of);
    WAIT_BAR(8);
    QSTAMP(11); k2_bstep<11, 0, 0, -1>(kb, acc, wb, wa, hf[11], hf[23], of);
    QSTAMP(12); k2_bstep<12, 0, 0, -1>(kb, acc, wa, wb, of[0], of[1], of);
    WAIT_BAR(3);
    QSTAMP(13); k2_bstep<13, 0, 0, -1>(kb, acc, wb, wa, of[2], of[3], of);
    QSTAMP(14); k2_bstep<14, 0, 0, -1>(kb, acc, wa, wb, of[4], of[5], of);
    WAIT_BAR(0);
    u32x4 xv0 = u32x4{}, xv1 = u32x4{};
    if (wid < 5) { xv0 = *(const u32x4*)(x + eb + eo0); xv1 = *(const u32x4*)(x + eb + eo1); }
    MEMFENCE(); SBAR();
    QSTAMP(15); k2_bstep<15, 0, 0, -1>(kb, acc, wb, wa, of[6], of[7], of);
    QSTAMP(16); k2_bstep<16, 0, 0, -1>(kb, acc, wa, wb, of[8], of[9], of);
    QSTAMP(17);
    {
        const int tk = ln & 15;
        const lds_ptr pp = (lds_ptr)smem + kq * 20480 + (mt * 32 + tk) * 320 + (((ln >> 4) ^ ((tk >> 1) & 3)) << 4);
#pragma unroll
        for (int i = 0; i < 10; ++i) *(LDS_AS f32x4*)(pp + (i & 1) * 16 * 320 + (i >> 1) * 64) = acc[i];
        QSTAMP(18);
        LGKM_BAR();
        QSTAMP(19);
        if (wid < 5) {
#pragma unroll
            for (int e = 0; e < 2; ++e) {
                const int er = e ? er1 : er0, ec = e ? ec1 : ec0, sw = (er >> 1) & 3;
                const lds_ptr p0 = (lds_ptr)smem + er * 320 + (((2 * ec) ^ sw) << 4), p1 = (lds_ptr)smem + er * 320 + (((2 * ec + 1) ^ sw) << 4);
                f32x4 a = *(const LDS_AS f32x4*)p0, b = *(const LDS_AS f32x4*)p1;
#pragma unroll
                for (int k = 1; k < 4; ++k) { a += *(const LDS_AS f32x4*)(p0 + k * 20480); b += *(const LDS_AS f32x4*)(p1 + k * 20480); }
                const u32x4 xv = e ? xv1 : xv0;
                const u32x4 ov = u32x4{cvtpk(a[0] + bf_lo(xv[0]), a[1] + bf_hi(xv[0])), cvtpk(a[2] + bf_lo(xv[1]), a[3] + bf_hi(xv[1])),
                                       cvtpk(b[0] + bf_lo(xv[2]), b[1] + bf_hi(xv[2])), cvtpk(b[2] + bf_lo(xv[3]), b[3] + bf_hi(xv[3]))};
                __builtin_amdgcn_raw_buffer_store_b128(ov, __builtin_amdgcn_make_buffer_rsrc(out, 0, S_ * D_ * 2, 0x00020000), (int)eb + (e ? eo1 : eo0), 0, OUTAUX);
            }
        }
    }
    QSTAMP(20);
    if (CODEWARM) asm volatile(".Lfb_end:");
    STAMP(7); STAMP_FLUSH(0); if (!DIAG_PF2) { PFLUSH(); }
}

extern "C" void kernel_launch(void* const* d_in, const int* in_sizes, int n_in, void* d_out, int out_size, void* d_ws, size_t ws_size,
                              hipStream_t stream) {
    const char* x = (const char*)d_in[0];
    const char* wqkv = (const char*)d_in[1];
    const char* wo = (const char*)d_in[2];
    const char* wg = (const char*)d_in[4];
    const char* wu = (const char*)d_in[5];
    const char* wd = (const char*)d_in[6];
    char* ws = (char*)d_ws;
    (void)hipFuncSetAttribute((const void*)fused_block, hipFuncAttributeMaxDynamicSharedMemorySize, LDS_BYTES);
    fused_block<<<256, 512, LDS_BYTES, stream>>>(x, wqkv, wg, wu, wd, wo, ws, (char*)d_out);
}
```

```cpp
#include <hip/hip_runtime.h>
#include <hip/hip_bf16.h>
#include <stdint.h>

typedef __bf16 bfx4 __attribute__((ext_vector_type(4)));
typedef __bf16 bfx8 __attribute__((ext_vector_type(8)));
typedef __bf16 bfx16 __attribute__((ext_vector_type(16)));
typedef short s16x4 __attribute__((ext_vector_type(4)));
typedef short s16x8 __attribute__((ext_vector_type(8)));
typedef short s16x16 __attribute__((ext_vector_type(16)));
typedef float f32x2 __attribute__((ext_vector_type(2)));
typedef float f32x4 __attribute__((ext_vector_type(4)));
typedef float f32x8 __attribute__((ext_vector_type(8)));
typedef float f32x16 __attribute__((ext_vector_type(16)));
typedef unsigned u32x2 __attribute__((ext_vector_type(2)));
typedef unsigned u32x4 __attribute__((ext_vector_type(4)));
typedef unsigned u32x8 __attribute__((ext_vector_type(8)));
#define LDS_AS __attribute__((address_space(3)))
typedef LDS_AS char* lds_ptr;

#define ABL 0
#define DPRIO 1
#define CODEWARM 1
#define EVEN 1
#define HPRIO 3
#define CPRIO 0
#define WPWARM 0
#define UNR2 2
#define HUNR 1
#define FA_POS 14
#define ROTA 2
#define OUTAUX 16
#define P3ROT 1
#define WOWARM 1
#define K1_ROT 1
constexpr int S_ = 2048, D_ = 640, H_ = 32, F_ = 1536;

constexpr int Q_TILE = 1280, Q_HEAD = 64 * Q_TILE, KV_TILE = 2816, KV_V = 1280, KV_HEAD = 64 * KV_TILE;
constexpr int WS_Q = 0;
constexpr int WS_KV = WS_Q + H_ * Q_HEAD;
constexpr int WS_O16 = WS_KV + H_ * KV_HEAD + 8192;
constexpr int WS_H16 = WS_O16 + 128 * 20 * 1024;
constexpr int WS_WP = WS_H16 + 128 * 48 * 1024;
constexpr int WS_EPOCH = WS_WP + 8 * 68 * 5120 + 8192;
constexpr int WS_FA = WS_EPOCH + 256, WS_FB = WS_FA + 2048, WS_FC = WS_FB + 2048, WS_END = WS_FC + 8192;

#define PSTAMP(i) do {} while (0)
#define PFLUSH() do {} while (0)
#define DIAG_PF2 0
#define QSTAMP(i) do {} while (0)
#define STAMP_ARG
#define STAMP_PASS
#define STAMP_DECL do {} while (0)
#define STAMP(i) do {} while (0)
#define STAMP_FLUSH(kern) do {} while (0)
#define SBAR() __builtin_amdgcn_sched_barrier(0)
template <int V> struct IC { static constexpr int v = V; };
template <int I, int N, class F> __device__ __forceinline__ void static_for(F&& f) { if constexpr (I < N) { f(IC<I>{}); static_for<I + 1, N>(f); } }
#define MFMA32(a, b, c) __builtin_amdgcn_mfma_f32_32x32x16_bf16((bfx8)(a), (bfx8)(b), (c), 0, 0, 0)

__device__ __forceinline__ unsigned cvtpk(float lo, float hi) {
    typedef __bf16 bfx2 __attribute__((ext_vector_type(2)));
    bfx2 r = {(__bf16)lo, (__bf16)hi};
    return __builtin_bit_cast(unsigned, r);
}
__device__ __forceinline__ void glds16(const void* g, unsigned lds_base) {
    unsigned sv;
    asm volatile("s_mov_b32 %0, m0\n\ts_mov_b32 m0, %2\n\ts_nop 0\n\tglobal_load_lds_dwordx4 %1, off\n\ts_mov_b32 m0, %0"
                 : "=&s"(sv) : "v"(g), "s"(lds_base) : "memory");
}
__device__ __forceinline__ s16x4 trread(const LDS_AS char* p) {
    return __builtin_amdgcn_ds_read_tr16_b64_v4i16((LDS_AS s16x4*)p);
}
__device__ __forceinline__ float rbf(float v) { return (float)(__bf16)v; }
__device__ __forceinline__ float bf_lo(unsigned u) { return __uint_as_float(u << 16); }
__device__ __forceinline__ float bf_hi(unsigned u) { return __uint_as_float(u & 0xffff0000u); }
__device__ __forceinline__ float swap32_add(float v) {
    auto r = __builtin_amdgcn_permlane32_swap(__float_as_uint(v), __float_as_uint(v), false, false);
    return __uint_as_float(r[0]) + __uint_as_float(r[1]);
}
__device__ __forceinline__ float swap32_max(float v) {
    auto r = __builtin_amdgcn_permlane32_swap(__float_as_uint(v), __float_as_uint(v), false, false);
    return fmaxf(__uint_as_float(r[0]), __uint_as_float(r[1]));
}

constexpr int P1_WSLOT = 24576, P1_X0 = 3 * P1_WSLOT, P1_XSLOT = 16384, P1_STAT = P1_X0 + 5 * P1_XSLOT;
constexpr int P1_COLA = P1_STAT + 1024, P1_COLB = P1_COLA + 512, P1_TAG = P1_COLB + 1024, LDS_JUNK = 161792, LDS_BYTES = 163840;
constexpr int K2_CHUNK = 8 * KV_TILE, K2_SLOTB = 24576, K2_SLOTS = 3, K2_KV0 = 81920, K2_C0 = 2 * P1_WSLOT, K2_Q0 = 0;
typedef __attribute__((address_space(1))) unsigned long long gu64;

#define PIN(x) asm volatile("" : "+v"(x))
#define MEMFENCE() asm volatile("" ::: "memory")
#define WAIT_BAR(N) do { MEMFENCE(); __builtin_amdgcn_s_waitcnt(0x70 | ((N) & 15) | (((N) >> 4) << 14)); __builtin_amdgcn_s_barrier(); MEMFENCE(); SBAR(); } while (0)
#define LGKM_BAR() do { MEMFENCE(); __builtin_amdgcn_s_waitcnt(0xC07F); __builtin_amdgcn_s_barrier(); MEMFENCE(); SBAR(); } while (0)
#define MFMA16(a, b, c) __builtin_amdgcn_mfma_f32_16x16x32_bf16((bfx8)(a), (bfx8)(b), (c), 0, 0, 0)
#define SMF16(a, b, c, i) __builtin_amdgcn_smfmac_f32_16x16x64_bf16((bfx8)(a), (bfx16)(b), (c), (i), 0, 0)
#define CAT8(a, b) __builtin_shufflevector(a, b, 0, 1, 2, 3, 4, 5, 6, 7)
#define CAT16(a, b) __builtin_shufflevector(a, b, 0, 1, 2, 3, 4, 5, 6, 7, 8, 9, 10, 11, 12, 13, 14, 15)

__device__ __forceinline__ void glds16s(const char* sbase, unsigned voff, unsigned lds_base) {
    unsigned sv;
    asm volatile("s_mov_b32 %0, m0\n\ts_mov_b32 m0, %3\n\ts_nop 0\n\tglobal_load_lds_dwordx4 %1, %2\n\ts_mov_b32 m0, %0"
                 : "=&s"(sv) : "v"(voff), "s"(sbase), "s"(lds_base) : "memory");
}

__device__ __forceinline__ void glds16c(const char* sbase, unsigned voff, unsigned lds_base) {
    asm volatile("s_mov_b32 m0, %2\n\ts_nop 0\n\tglobal_load_lds_dwordx4 %0, %1 sc1" :: "v"(voff), "s"(sbase), "s"(lds_base) : "memory");
}
typedef __amdgpu_buffer_rsrc_t rsrc_t;
#define ST16(rs, v, voff, soff) __builtin_amdgcn_raw_buffer_store_b128((v), (rs), (voff) + (soff), 0, (ABL & 32) ? 0 : 16)
#define ST8(rs, v, voff, soff) __builtin_amdgcn_raw_buffer_store_b64((v), (rs), (voff), (soff), 16)
#define LD16(rs, voff, soff) __builtin_bit_cast(s16x8, __builtin_amdgcn_raw_buffer_load_b128((rs), (voff), (soff), 16))
#define LD8(rs, voff, soff) __builtin_bit_cast(s16x4, __builtin_amdgcn_raw_buffer_load_b64((rs), (voff), (soff), 16))
__device__ __forceinline__ f32x2 pk(const f32x16& a, int i) { return f32x2{a[i], a[i + 1]}; }
__device__ __forceinline__ f32x2 pk4(const f32x4& a, int i) { return f32x2{a[i], a[i + 1]}; }
__device__ __forceinline__ f32x2 pfma(f32x2 a, f32x2 b, f32x2 c) { return __builtin_elementwise_fma(a, b, c); }
__device__ __forceinline__ unsigned cvt2(f32x2 v) { return cvtpk(v[0], v[1]); }
__device__ __forceinline__ float add1(float a, float b) { float r = a + b; asm volatile("" : "+v"(r)); return r; }
__device__ __forceinline__ float mul1(float a, float b) { float r = a * b; asm volatile("" : "+v"(r)); return r; }

__device__ __forceinline__ void raise_flag(char* ws, int arr, unsigned long long tag) {
    __hip_atomic_store((gu64*)(ws + arr) + blockIdx.x, tag, __ATOMIC_RELAXED, __HIP_MEMORY_SCOPE_AGENT);
}
struct Poll { u32x4 a, b; };
__device__ __forceinline__ void poll_request(Poll& p, const char* arr, unsigned voff) {
    asm volatile("global_load_dwordx4 %0, %2, %3 sc1\n\tglobal_load_dwordx4 %1, %2, %3 offset:1024 sc1" : "+v"(p.a), "+v"(p.b) : "v"(voff), "s"(arr) : "memory");
}
__device__ __forceinline__ void poll_wait(Poll& p, const char* arr, unsigned voff, unsigned long long tag) {
    const unsigned tl = (unsigned)tag, th = (unsigned)(tag >> 32);
#pragma unroll 1
    for (unsigned spin = 0; spin < (1u << 18); ++spin) {
        asm volatile("s_waitcnt vmcnt(0)" : "+v"(p.a), "+v"(p.b) :: "memory");
        if (__all(p.a[0] == tl && p.a[1] == th && p.a[2] == tl && p.a[3] == th && p.b[0] == tl && p.b[1] == th && p.b[2] == tl && p.b[3] == th)) break;
        __builtin_amdgcn_s_sleep(1);
        poll_request(p, arr, voff);
    }
    asm volatile("s_waitcnt vmcnt(0)" : "+v"(p.a), "+v"(p.b) :: "memory");
}

template <int MODE> struct P1F { s16x8 x[2]; s16x4 w[MODE == 2 ? 3 : 2][2]; };

template <int MODE>
__device__ __forceinline__ void p1_read(P1F<MODE>& n, lds_ptr xa, lds_ptr w0, lds_ptr w1, int wimm, int i) {
    constexpr int TS = MODE == 2 ? 256 : 512;
    if (i == 0) { n.x[0] = *(const LDS_AS s16x8*)xa; n.w[0][0] = trread(w0 + wimm); n.w[0][1] = trread(w0 + wimm + TS); }
    if (i == 1) { n.x[1] = *(const LDS_AS s16x8*)(xa + 4096); n.w[1][0] = trread(w1 + wimm); n.w[1][1] = trread(w1 + wimm + TS); }
    if (i == 2 && MODE == 2) { n.w[MODE == 2 ? 2 : 0][0] = trread(w0 + wimm + 1024); n.w[MODE == 2 ? 2 : 0][1] = trread(w0 + wimm + 1024 + TS); }
}
template <int MODE, int NR>
__device__ __forceinline__ void p1_step(const P1F<MODE>& f, f32x16 (&acc)[MODE == 2 ? 6 : 4], P1F<MODE>& n0, lds_ptr xa0, int wi0, P1F<MODE>& n1, lds_ptr xa1, int wi1,
                                        lds_ptr w0, lds_ptr w1) {
    constexpr int NM = MODE == 2 ? 6 : 4;
#pragma unroll
    for (int g = 0; g < NM; ++g) {
        const s16x8 w = CAT8(f.w[g >> 1][0], f.w[g >> 1][1]);
#if !(ABL & 1)
        acc[g] = MODE == 1 ? MFMA32(f.x[g & 1], w, acc[g]) : MFMA32(w, f.x[g & 1], acc[g]);
#else
        { s16x8 x_ = f.x[g & 1]; asm volatile("" :: "v"(w), "v"(x_)); }
#endif
        PIN(acc[g]); SBAR();
        if (NR >= 1 && g < NM / 2) p1_read<MODE>(n0, xa0, w0, w1, wi0, g);
        if (NR >= 2 && g >= NM / 2) p1_read<MODE>(n1, xa1, w0, w1, wi1, g - NM / 2);
        SBAR();
    }
}
struct P1X { lds_ptr a0, a1, a2, a3; };
template <int MODE>
__device__ __forceinline__ void p1_first(P1F<MODE> (&f)[4], const P1X& xa, lds_ptr w0, lds_ptr w1) {
    constexpr int KS = MODE == 2 ? 6144 : 4096;
#pragma unroll
    for (int i = 0; i < 3; ++i) p1_read<MODE>(f[0], xa.a0, w0, w1, 0, i);
#pragma unroll
    for (int i = 0; i < 3; ++i) p1_read<MODE>(f[1], xa.a1, w0, w1, KS, i);
    SBAR();
}
template <int MODE>
__device__ __forceinline__ void p1_lo(P1F<MODE> (&f)[4], f32x16 (&acc)[MODE == 2 ? 6 : 4], const P1X& xa, lds_ptr w0, lds_ptr w1) {
    constexpr int KS = MODE == 2 ? 6144 : 4096;
    p1_step<MODE, 1>(f[0], acc, f[2], xa.a2, 2 * KS, f[2], xa.a2, 0, w0, w1);
    p1_step<MODE, 1>(f[1], acc, f[3], xa.a3, 3 * KS, f[3], xa.a3, 0, w0, w1);
}
template <int MODE>
__device__ __forceinline__ void p1_hi(P1F<MODE> (&f)[4], f32x16 (&acc)[MODE == 2 ? 6 : 4], const P1X& xa, lds_ptr w0, lds_ptr w1) {
    constexpr int KS = MODE == 2 ? 6144 : 4096;
    p1_step<MODE, 1>(f[2], acc, f[0], xa.a0, 0, f[0], xa.a0, 0, w0, w1);
    p1_step<MODE, 1>(f[3], acc, f[1], xa.a1, KS, f[1], xa.a1, 0, w0, w1);
}
__device__ __forceinline__ P1X p1_xaddr(lds_ptr lds, int wid, int lane) {
    const int xrow = (wid & 1) * 64 + (lane & 31), xo = P1_X0 + xrow * 128 + (((lane >> 5) ^ ((xrow >> 1) & 7)) << 4);
    return P1X{lds + xo, lds + (xo ^ 32), lds + (xo ^ 64), lds + (xo ^ 96)};
}
template <int MODE>
__device__ __forceinline__ void p1_five(P1F<MODE> (&f)[4], f32x16 (&acc)[MODE == 2 ? 6 : 4], P1X& xa, lds_ptr& w0, lds_ptr& w1, int& ws3, int wrap, int drain, int ps) {
#pragma unroll
    for (int n = 0; n < 5; ++n) {
        const int dw = ws3 == 2 ? -2 * P1_WSLOT : P1_WSLOT, dx = n == wrap ? -4 * P1_XSLOT : P1_XSLOT;
        ws3 = ws3 == 2 ? 0 : ws3 + 1;
        p1_lo<MODE>(f, acc, xa, w0, w1);
        w0 += dw; w1 += dw; xa.a0 += dx; xa.a1 += dx; xa.a2 += dx; xa.a3 += dx;
        asm volatile("" : "+v"(w0), "+v"(w1), "+v"(xa.a0), "+v"(xa.a1), "+v"(xa.a2), "+v"(xa.a3));
        if (n == drain) asm volatile("s_waitcnt vmcnt(0)" ::: "memory");
        PSTAMP(ps + 2 * n);
        LGKM_BAR();
        PSTAMP(ps + 2 * n + 1);
        p1_hi<MODE>(f, acc, xa, w0, w1);
    }
}

struct Pack { const char *wd, *wo; int job; };
__device__ __forceinline__ void pack_fetch(const Pack& pk_, lds_ptr lds, int wid, int lane) {
    const int step = pk_.job / 10, cb = pk_.job - step * 10, r = lane >> 3;
    const unsigned va = ((r & 3) + 8 * (r >> 2)) * (D_ * 2) + (lane & 7) * 16, vb = ((r >> 2) * 20 + 16 + (r & 3)) * (D_ * 2) + (lane & 7) * 16;
#pragma unroll
    for (int g = 0; g < 4; ++g) {
        const int ks = (step - 48) * 2 + (g >> 1), hh = g & 1;
        const char* a = step < 48 ? pk_.wd + (step * 32 + (g >> 1) * 16 + hh * 4) * (D_ * 2) : ks < 32 ? pk_.wo + (ks * 20 + hh * 4) * (D_ * 2) : pk_.wo + ((ks - 32) * 4 + hh * 2) * 20 * (D_ * 2);
        glds16s(a + cb * 128, step < 64 ? va : vb, (unsigned)(uintptr_t)lds + wid * P1_WSLOT + 16384 + g * 1024);
    }
}
__device__ __forceinline__ void pack_read(s16x8 (&pf)[4], lds_ptr lds, int wid, int lane) {
    asm volatile("s_waitcnt vmcnt(0)" ::: "memory");
    const lds_ptr pr = lds + wid * P1_WSLOT + 16384 + (lane >> 4) * 1024 + ((lane >> 2) & 3) * 128 + (lane & 3) * 8;
#pragma unroll
    for (int nt = 0; nt < 4; ++nt) pf[nt] = CAT8(trread(pr + nt * 32), trread(pr + nt * 32 + 512));
}
__device__ __forceinline__ void pack_store(const Pack& pk_, const s16x8 (&pf)[4], int lane, rsrc_t wsr) {
    const int step = pk_.job / 10, cb = pk_.job - step * 10;
#pragma unroll
    for (int nt = 0; nt < 4; ++nt) {
        const int N = cb * 4 + nt, cg = N / 5;
        ST16(wsr, __builtin_bit_cast(u32x4, pf[nt]), lane * 16, WS_WP + ((cg * 68 + step) * 5 + N - cg * 5) * 1024);
    }
}

template <int MODE>
__device__ __forceinline__ void p1a_compute(f32x16 (&acc)[4], lds_ptr lds, int wid, int lane, int ra, const Pack& pk_, s16x8 (&pf)[4]) {
    const int h = lane >> 5, blk = (lane >> 4) & 1, q = (lane >> 2) & 3, fsw = 3 * (q >> 1);
    P1X xa = p1_xaddr(lds + ra * P1_XSLOT, wid, lane);
    const int wb = h * 2048 + (wid >> 1) * 1024 + q * 128 + (lane & 3) * 8;
    lds_ptr w0 = lds + wb + ((blk ^ fsw) << 5), w1 = lds + wb + (((2 + blk) ^ fsw) << 5);
    P1F<MODE> f[4];
    int ws3 = 0;
    PSTAMP(0);
    LGKM_BAR();
    PSTAMP(1);
    p1_first<MODE>(f, xa, w0, w1);
#pragma unroll UNR2
    for (int hf_ = 0; hf_ < 2; ++hf_) {
        p1_five<MODE>(f, acc, xa, w0, w1, ws3, 4 - ra, -1, 2 + 10 * hf_);
        if (hf_ == 0) {
            if (pk_.job < 680) pack_read(pf, lds, wid, lane);
        }
    }
}

__device__ __forceinline__ void p1_compute(lds_ptr lds, int wid, int lane, int panel, int grp, int ra, int rb, rsrc_t wsr, const Pack& pk_ STAMP_ARG) {
    const int h = lane >> 5, l31 = lane & 31, wm = wid & 1, wn = wid >> 1, tt = panel * 4 + wm * 2;
    const LDS_AS f32x2* rowf = (const LDS_AS f32x2*)(lds + P1_STAT);
    __builtin_amdgcn_s_setprio(CPRIO);
    if (pk_.job < 680) pack_fetch(pk_, lds, wid, lane);
    {
        f32x16 acc[4] = {f32x16{}, f32x16{}, f32x16{}, f32x16{}};
        s16x8 pf[4] = {s16x8{}, s16x8{}, s16x8{}, s16x8{}};
        const LDS_AS float* colf = (const LDS_AS float*)(lds + P1_COLA) + wn * 64;
        const int sec = grp / 5, q0 = (grp - sec * 5) * 32 + wn * 16;
        if (sec < 2) {
            p1a_compute<0>(acc, lds, wid, lane, ra, pk_, pf);
            STAMP(1);
            const int pos = sec ? ((l31 & 19) | ((l31 & 4) << 1) | ((l31 & 8) >> 1)) : l31;
            const int hs = sec ? KV_HEAD : Q_HEAD, ts = sec ? KV_TILE : Q_TILE, so = (sec ? WS_KV : WS_Q) + tt * ts;
            const float qs = sec ? 1.0f : 0.22360679774997896f * 1.4426950408889634f;
            f32x2 nm[2], rr[2];
#pragma unroll
            for (int m = 0; m < 2; ++m) { const f32x2 rs = rowf[wm * 64 + m * 32 + l31]; nm[m] = f32x2{rs[1], rs[1]}; rr[m] = f32x2{rs[0] * qs, rs[0] * qs}; }
            const bool odd = lane & 1;
#pragma unroll
            for (int gam = 0; gam < 8; gam += 2) {
                const int qd = q0 + 2 * (gam + (lane & 1)) + h, hd = (qd * 205) >> 10, vo = hd * hs + (qd - hd * 5) * 256 + (pos & ~1) * 8;
                const f32x4 ca = *(const LDS_AS f32x4*)(colf + gam * 8 + h * 4), cb = *(const LDS_AS f32x4*)(colf + gam * 8 + 8 + h * 4);
#pragma unroll
                for (int m = 0; m < 2; ++m) {
                    const f32x16& a = acc[(gam >> 2) * 2 + m];
                    const int r0 = (gam & 3) * 4;
                    const unsigned a0 = cvt2(pfma(pk4(ca, 0), nm[m], pk(a, r0)) * rr[m]), a1 = cvt2(pfma(pk4(ca, 2), nm[m], pk(a, r0 + 2)) * rr[m]);
                    const unsigned b0 = cvt2(pfma(pk4(cb, 0), nm[m], pk(a, r0 + 4)) * rr[m]), b1 = cvt2(pfma(pk4(cb, 2), nm[m], pk(a, r0 + 6)) * rr[m]);
                    const unsigned g0 = __builtin_amdgcn_update_dpp(0, odd ? a0 : b0, 0xB1, 0xF, 0xF, true), g1 = __builtin_amdgcn_update_dpp(0, odd ? a1 : b1, 0xB1, 0xF, 0xF, true);
                    ST16(wsr, (odd ? u32x4{g0, g1, b0, b1} : u32x4{a0, a1, g0, g1}), vo, so + m * ts);
                }
            }
        } else {
            p1a_compute<1>(acc, lds, wid, lane, ra, pk_, pf);
            STAMP(1);
            if (sec == 2) {
                const int so = WS_KV + tt * KV_TILE + KV_V;
#pragma unroll
                for (int m = 0; m < 2; ++m) {
                    f32x2 rr[8], nm[8];
#pragma unroll
                    for (int g = 0; g < 4; ++g) {
                        const LDS_AS f32x2* rp = rowf + wm * 64 + m * 32 + g * 8 + h * 4;
                        const f32x4 a = *(const LDS_AS f32x4*)rp, b = *(const LDS_AS f32x4*)(rp + 2);
                        rr[2 * g] = f32x2{a[0], a[2]}; nm[2 * g] = f32x2{a[1], a[3]}; rr[2 * g + 1] = f32x2{b[0], b[2]}; nm[2 * g + 1] = f32x2{b[1], b[3]};
                    }
#pragma unroll
                    for (int t = 0; t < 2; ++t) {
                        const int qd = q0 + t * 8 + (l31 >> 2), hd = (qd * 205) >> 10, pl = qd - hd * 5;
                        const int vo = hd * KV_HEAD + pl * 256 + ((h ^ (pl & 3)) << 6) + (l31 & 3) * 16;
                        const float c1 = colf[t * 32 + l31];
                        const f32x2 c2 = {c1, c1};
                        unsigned w[8];
#pragma unroll
                        for (int i = 0; i < 8; ++i) w[i] = cvt2(pfma(nm[i], c2, pk(acc[t * 2 + m], 2 * i)) * rr[i]);
                        ST16(wsr, (u32x4{w[0], w[1], w[2], w[3]}), vo, so + m * KV_TILE);
                        ST16(wsr, (u32x4{w[4], w[5], w[6], w[7]}), vo ^ 128, so + m * KV_TILE);
                    }
                }
                const int hd = q0 / 5 + (lane >> 4);
                const unsigned ov = (lane & 3) ? 0u : 0x3f803f80u;
                if (hd * 5 + 4 < q0 + 16) {
                    ST16(wsr, (u32x4{ov, ov, ov, ov}), hd * KV_HEAD + 5 * 256 + (lane & 15) * 16, so);
                    ST16(wsr, (u32x4{ov, ov, ov, ov}), hd * KV_HEAD + 5 * 256 + (lane & 15) * 16, so + KV_TILE);
                }
            }
        }
        if (pk_.job < 680) pack_store(pk_, pf, lane, wsr);
    }
    STAMP(6);
    f32x16 acc[6];
#pragma unroll
    for (int t = 0; t < 6; ++t) acc[t] = f32x16{};
    {
        const int blk = (lane >> 4) & 1, T0 = wn * 3, T1 = T0 + 1;
        P1X xa = p1_xaddr(lds + rb * P1_XSLOT, wid, lane);
        const int wb = P1_WSLOT + 3 * blk * 1024 + h * 512 + ((lane >> 2) & 3) * 64 + (lane & 3) * 8;
        lds_ptr w0 = lds + wb + (T0 >> 1) * 1024 + (((T0 & 1) ^ blk) << 5), w1 = lds + wb + (T1 >> 1) * 1024 + (((T1 & 1) ^ blk) << 5);
        P1F<2> f[4];
        p1_first<2>(f, xa, w0, w1);
        int ws3 = 1;
#pragma unroll UNR2
        for (int hf_ = 0; hf_ < 2; ++hf_) {
            p1_five<2>(f, acc, xa, w0, w1, ws3, 4 - rb, hf_ == 0 ? FA_POS - 11 : -1, 22 + 10 * hf_);
        }
    }
    STAMP(3);
    {
        const LDS_AS float* colf = (const LDS_AS float*)(lds + P1_COLB) + wn * 96;
        const int vo = (l31 >> 4) * 48 * 1024 + (h * 16 + (l31 & 15)) * 16;
        f32x2 nm[2], r2[2];
#pragma unroll
        for (int m = 0; m < 2; ++m) { const f32x2 rs = rowf[wm * 64 + m * 32 + l31]; nm[m] = f32x2{rs[1], rs[1]}; r2[m] = f32x2{rs[0] * rs[0], rs[0] * rs[0]}; }
#pragma unroll
        for (int c = 0; c < 3; ++c) {
            f32x4 cv[4];
#pragma unroll
            for (int g = 0; g < 4; ++g) cv[g] = *(const LDS_AS f32x4*)(colf + c * 32 + g * 8 + h * 4);
            if (c >= 1 && wn) {
#pragma unroll
                for (int g = 0; g < 4; ++g) cv[g] += *(const LDS_AS f32x4*)(colf + 64 + c * 32 + g * 8 + h * 4);
            }
            const int so = WS_H16 + tt * 2 * 48 * 1024 + (grp * 6 + wn * 3 + c) * 512;
#pragma unroll
            for (int m = 0; m < 2; ++m) {
                const f32x16& a = acc[c * 2 + m];
                f32x2 g[4], u[4];
#pragma unroll
                for (int i = 0; i < 4; ++i) { g[i] = pfma(pk4(cv[i >> 1], (i & 1) * 2), nm[m], pk(a, 2 * i)); u[i] = pfma(pk4(cv[2 + (i >> 1)], (i & 1) * 2), nm[m], pk(a, 8 + 2 * i)); }
#pragma unroll
                for (int i = 0; i < 4; ++i) { g[i] = f32x2{fmaxf(g[i][0], 0.f), fmaxf(g[i][1], 0.f)}; u[i] *= r2[m]; }
                ST16(wsr, (u32x4{cvt2(g[0] * u[0]), cvt2(g[1] * u[1]), cvt2(g[2] * u[2]), cvt2(g[3] * u[3])}), vo, so + m * 2 * 48 * 1024);
            }
        }
    }
}

__device__ __forceinline__ void glds_x4(const char* sbase, const unsigned (&v)[4], unsigned lds_base) {
    asm volatile("s_mov_b32 m0, %5\n\ts_nop 0\n\tglobal_load_lds_dwordx4 %0, %4\n\tglobal_load_lds_dwordx4 %1, %4 offset:1024\n\t"
                 "global_load_lds_dwordx4 %2, %4 offset:2048\n\tglobal_load_lds_dwordx4 %3, %4 offset:3072"
                 :: "v"(v[0]), "v"(v[1]), "v"(v[2]), "v"(v[3]), "s"(sbase), "s"(lds_base) : "memory");
}
__device__ __forceinline__ void glds_x3(const char* sbase, const unsigned* v, unsigned lds_base) {
    asm volatile("s_mov_b32 m0, %4\n\ts_nop 0\n\tglobal_load_lds_dwordx4 %0, %3\n\tglobal_load_lds_dwordx4 %1, %3 offset:1024\n\tglobal_load_lds_dwordx4 %2, %3 offset:2048"
                 :: "v"(v[0]), "v"(v[1]), "v"(v[2]), "s"(sbase), "s"(lds_base) : "memory");
}

__device__ __forceinline__ unsigned long long p1_helper(lds_ptr lds, int hw, int lane, const char* xp, const char* wq, const char* wgp, const char* wup, int ra, int rb,
                                          char* ws, const char* kv, const char* qsrc) {
    const unsigned lds0 = (unsigned)(uintptr_t)lds;
    const int r = lane >> 3, pos = lane & 7;
    unsigned xv[4], wa[4], wb[6];
#pragma unroll
    for (int j = 0; j < 4; ++j) {
        xv[j] = 4096 + (hw * 32 + j * 8 + r) * (D_ * 2) + ((pos ^ ((j & 1) * 4 + (r >> 1))) << 4) - j * 1024;
        wa[j] = 4096 + (hw * 16 + (j >> 1) * 8 + r) * (3 * D_ * 2) + (j & 1) * 128 + ((pos ^ (6 * ((r >> 1) & 1))) << 4) - j * 1024;
    }
#pragma unroll
    for (int j = 0; j < 6; ++j) wb[j] = 4096 + (hw * 16 + (lane >> 2)) * (F_ * 2) + (j % 3) * 64 + (((lane & 3) ^ (j < 3 ? 0 : 2)) << 4) - (j % 3) * 1024;
    xp -= 4096; wq -= 4096; wgp -= 4096; wup -= 4096;
    int kb_ = 0, xs = ra, dws = 0, left = 5;
    auto issue1 = [&](bool pa) {
        if (ABL & 4) return;
        if (pa) glds_x4(wq + (unsigned)((kb_ + xs) * 64 * 3 * D_ * 2), wa, lds0 + dws * P1_WSLOT + hw * 4096);
        else glds_x3(wgp + (unsigned)((kb_ + xs) * 64 * F_ * 2), wb, lds0 + dws * P1_WSLOT + hw * 6144);
        SBAR();
    };
    auto issue2 = [&](bool pa, bool xx) {
        SBAR();
        if (ABL & 4) return;
        if (!pa) glds_x3(wup + (unsigned)((kb_ + xs) * 64 * F_ * 2), wb + 3, lds0 + dws * P1_WSLOT + hw * 6144 + 3072);
        xs = xs == 4 ? 0 : xs + 1; dws = dws == 2 ? 0 : dws + 1; --left; kb_ = left == 0 ? 5 - kb_ : kb_; left = left == 0 ? 5 : left;
        if (xx) glds_x4(xp + (kb_ + xs) * 128, xv, lds0 + P1_X0 + xs * P1_XSLOT + hw * 4096);
    };
    glds_x4(xp + ra * 128, xv, lds0 + P1_X0 + ra * P1_XSLOT + hw * 4096);
    issue1(true); issue2(true, true);
    __builtin_amdgcn_s_barrier();
    unsigned long long ep = 0;
    asm volatile("global_load_dwordx2 %0, %1, %2 sc1" : "+v"(ep) : "v"(0u), "s"(ws + WS_EPOCH) : "memory");
    __builtin_amdgcn_s_setprio(HPRIO);
    const int r16 = lane & 15, h = lane >> 5, blk = (lane >> 4) & 1, q = (lane >> 2) & 3, fsw = 3 * (q >> 1);
    const unsigned olo = r16 < 2 ? 0x3f803f80u : 0u, ohi = (r16 >> 1) == 1 ? 0x3f803f80u : 0u;
    const s16x8 ones = __builtin_bit_cast(s16x8, (u32x4{olo, olo, ohi, ohi}));
    const int idx = (r16 & 1) ? 0xEEEE : 0x4444;
    int ws3 = 0;
    {
        lds_ptr b = lds + h * 2048 + (hw >> 1) * 1024 + q * 128 + (lane & 3) * 8 + ((((hw & 1) * 2 + blk) ^ fsw) << 5);
        const int so = P1_X0 + ra * P1_XSLOT + (hw * 32 + r16) * 128 + (((lane >> 4) ^ (r16 >> 1)) << 4);
        lds_ptr sp0 = lds + so, sp1 = lds + (so ^ 64);
        const s16x8 ones2 = __builtin_bit_cast(s16x8, (u32x4{olo, olo, olo, olo}));
        f32x4 cs = f32x4{}, s1[2] = {f32x4{}, f32x4{}}, s2[2] = {f32x4{}, f32x4{}};
        s16x16 wv0, wv1; s16x8 xa[2], xb[2];
        auto col_rd = [&]() {
            wv0 = CAT16(CAT8(trread(b), trread(b + 512)), CAT8(trread(b + 4096), trread(b + 4096 + 512)));
            wv1 = CAT16(CAT8(trread(b + 8192), trread(b + 8192 + 512)), CAT8(trread(b + 12288), trread(b + 12288 + 512)));
            b += ws3 == 2 ? -2 * P1_WSLOT : P1_WSLOT; ws3 = ws3 == 2 ? 0 : ws3 + 1;
        };
        auto row_rd = [&](int dx) {
#pragma unroll
            for (int t = 0; t < 2; ++t) { xa[t] = *(const LDS_AS s16x8*)(sp0 + t * 2048); xb[t] = *(const LDS_AS s16x8*)(sp1 + t * 2048); }
            sp0 += dx; sp1 += dx;
        };
        auto row_mm = [&]() {
#pragma unroll
            for (int t = 0; t < 2; ++t) { s2[t] = MFMA16(xa[t], xa[t], s2[t]); s1[t] = SMF16(ones2, CAT16(xa[t], xb[t]), s1[t], idx); }
#pragma unroll
            for (int t = 0; t < 2; ++t) { s2[t] = MFMA16(xb[t], xb[t], s2[t]); PIN(s1[t]); PIN(s2[t]); }
        };
#pragma unroll
        for (int nn = 0; nn < 9; ++nn) {
            int n = nn;
            if (!HUNR) asm volatile("" : "+s"(n));
            PSTAMP(2 * n);
            if (n == 0) WAIT_BAR(5); else if (n < 8) WAIT_BAR(12); else WAIT_BAR(8);
            PSTAMP(2 * n + 1);
            if (!(ABL & 8) || n == 0) { col_rd(); row_rd(n % 5 == 4 - ra ? -4 * P1_XSLOT : P1_XSLOT); }
            if (n == 0) { issue1(true); issue2(true, true); }
            if (n == 8) { kb_ = 5; xs = rb; }
            issue1(n < 8);
            if (!(ABL & 8)) {
            cs = SMF16(ones, wv0, cs, idx);
            row_mm();
            cs = SMF16(ones, wv1, cs, idx); PIN(cs); }
            issue2(n < 8, n < 7);
        }
        MEMFENCE(); __builtin_amdgcn_s_waitcnt(0xF70 | 10); MEMFENCE();
        row_rd(0); row_mm();
        {
            const int t = blk;
            const float d0 = (lane & 2) ? ((lane & 1) ? s2[0][3] : s2[0][2]) : ((lane & 1) ? s2[0][1] : s2[0][0]);
            const float d1 = (lane & 2) ? ((lane & 1) ? s2[1][3] : s2[1][2]) : ((lane & 1) ? s2[1][1] : s2[1][0]);
            const float a0 = __shfl(s1[0][0] + s1[0][1], r16), a1 = __shfl(s1[1][0] + s1[1][1], r16), c0 = __shfl(d0, r16 + 16 * q), c1 = __shfl(d1, r16 + 16 * q);
            const float a = t ? a1 : a0, c = t ? c1 : c0;
            const float mean = a * (1.0f / D_), var = fmaxf(c * (1.0f / D_) - mean * mean, 0.f);
            const float rstd = __builtin_amdgcn_rcpf(rbf(__builtin_amdgcn_sqrtf(rbf(rbf(var) + 1e-5f))));
            if (lane < 32) ((LDS_AS f32x2*)(lds + P1_STAT))[hw * 32 + lane] = f32x2{rstd, -rbf(mean)};
        }
        PSTAMP(18);
        WAIT_BAR(6);
        PSTAMP(19);
        col_rd();
        issue1(false);
        cs = SMF16(ones, wv0, cs, idx);
        cs = SMF16(ones, wv1, cs, idx); PIN(cs);
        issue2(false, false);
        LDS_AS float* colf = (LDS_AS float*)(lds + P1_COLA) + hw * 32;
        if (lane < 16) { colf[lane] = cs[0] + cs[1]; colf[16 + lane] = cs[2] + cs[3]; }
    }
    asm volatile("" : "+v"(ep));
    const unsigned long long tag = (((unsigned long long)(unsigned)__builtin_amdgcn_readfirstlane((unsigned)(ep >> 32)) << 32) | (unsigned)__builtin_amdgcn_readfirstlane((unsigned)ep)) + 1ull;
    if (hw == 0 && lane == 0) *(LDS_AS unsigned long long*)(lds + P1_TAG) = tag;
    {
        const int ro = P1_WSLOT + 3 * blk * 1024 + h * 512 + q * 64 + (lane & 3) * 8 + (((hw & 1) ^ blk) << 5);
        lds_ptr b = lds + ro + (hw >> 1) * 1024, c = lds + ro + 2048 + (hw >> 1) * 12288;
        f32x4 cs = f32x4{}, ct = f32x4{};
        Poll pl = {};
#pragma unroll
        for (int nn = 10; nn < 20; ++nn) {
            int n = nn;
            if (!HUNR) asm volatile("" : "+s"(n));
            PSTAMP(2 * n);
            if (n < 13 || n == 19) WAIT_BAR(6); else if (n == 13) WAIT_BAR(10); else if (n == 18) WAIT_BAR(12); else WAIT_BAR(14);
            PSTAMP(2 * n + 1);
            const s16x16 wv0 = CAT16(CAT8(trread(b), trread(b + 256)), CAT8(trread(b + 6144), trread(b + 6144 + 256)));
            const s16x16 wv2 = CAT16(CAT8(trread(c), trread(c + 256)), CAT8(trread(c + 6144), trread(c + 6144 + 256)));
            const s16x16 wv1 = CAT16(CAT8(trread(b + 12288), trread(b + 12288 + 256)), CAT8(trread(b + 18432), trread(b + 18432 + 256)));
            if (n == FA_POS && hw == 0 && lane == 0) raise_flag(ws, WS_FA, tag);
            if (n < 18) issue1(false);
            cs = SMF16(ones, wv0, cs, idx);
            ct = SMF16(ones, wv2, ct, idx);
            cs = SMF16(ones, wv1, cs, idx); PIN(cs); PIN(ct);
            if (n < 18) issue2(false, n >= 12 && n < 17);
            if (n == 17) poll_request(pl, ws + WS_FA, lane * 16);
            if (n == 18) {
                poll_wait(pl, ws + WS_FA, lane * 16, tag);
#pragma unroll
                for (int j = 0; j < 6; ++j) glds16c(kv + (hw + 4 * j) * 1024, lane * 16, lds0 + K2_C0 + (hw + 4 * j) * 1024);
            }
            if (n == 19) {
#pragma unroll
                for (int j = 0; j < 3; ++j) glds16c(qsrc + (hw + 4 * j) * 1024, lane * 16, lds0 + K2_Q0 + (hw + 4 * j) * 1024);
            }
            const int dw = ws3 == 2 ? -2 * P1_WSLOT : P1_WSLOT;
            b += dw; c += dw; ws3 = ws3 == 2 ? 0 : ws3 + 1;
        }
        LDS_AS float* colf = (LDS_AS float*)(lds + P1_COLB);
        if (lane < 16) { colf[hw * 32 + lane] = cs[0] + cs[1]; colf[hw * 32 + 16 + lane] = cs[2] + cs[3]; }
        if (lane < 16) { colf[128 + hw * 32 + lane] = ct[0] + ct[1]; colf[128 + hw * 32 + 16 + lane] = ct[2] + ct[3]; }
        WAIT_BAR(0);
        if (blockIdx.x == 0 && hw == 0 && lane == 0) __hip_atomic_store((gu64*)(ws + WS_EPOCH), tag, __ATOMIC_RELAXED, __HIP_MEMORY_SCOPE_AGENT);
#pragma unroll
        for (int j = 0; j < 6; ++j) glds16c(kv + K2_CHUNK + (hw + 4 * j) * 1024, lane * 16, lds0 + K2_KV0 + K2_SLOTB + (hw + 4 * j) * 1024);
    }
    return tag;
}

constexpr int K2_XCH = 2 * K2_SLOTB;

#define SMF32(a, b, c) __builtin_amdgcn_smfmac_f32_32x32x32_bf16((bfx8)(a), __builtin_bit_cast(bfx16, (b)), (c), 0xDDDD, 0, 0)
#define MFMA8(a, b, c) __builtin_amdgcn_mfma_f32_32x32x8bf16_1k((a), (b), (c), 0, 0, 0)
__device__ __forceinline__ f32x8 lo8(const f32x16& v) { return __builtin_shufflevector(v, v, 0, 1, 2, 3, 4, 5, 6, 7); }
__device__ __forceinline__ f32x8 hi8(const f32x16& v) { return __builtin_shufflevector(v, v, 8, 9, 10, 11, 12, 13, 14, 15); }
__device__ __forceinline__ float max16(const f32x16& s) {
    float m = fmaxf(fmaxf(s[0], s[1]), s[2]);
#pragma unroll
    for (int r = 3; r < 15; r += 2) m = fmaxf(fmaxf(m, s[r]), s[r + 1]);
    return swap32_max(fmaxf(m, s[15]));
}

struct K2B { lds_ptr r0, r1, r2; const char *wsrc, *wsa, *wsb; unsigned wdst; int lane, osoff; rsrc_t wsr; };
constexpr int k2_slot(int st) { return st < 3 ? st : st == 3 ? 0 : st == 4 ? 3 : st == 5 ? 1 : st == 6 ? 2 : st == 7 ? 0 : 3; }
__device__ __forceinline__ void k2_wpiece(const K2B& c, int st, int i) {
    glds16c((st < 3 ? c.wsa : st < 6 ? c.wsb : c.wsrc) + st * 40960 + i * 8192, c.lane * 16, c.wdst + k2_slot(st) * 40960 + i * 8192);
}
template <int I, int D, int NP, int OF>
__device__ __forceinline__ void k2_bstep(const K2B& c, f32x4 (&acc)[10], const s16x8 (&w)[5], s16x8 (&n)[5], const s16x8& a0, const s16x8& a1, s16x8 (&of)[10]) {
    constexpr int O = k2_slot((I + 1) >> 1) * 40960 + ((I + 1) & 1) * 20480;
#pragma unroll
    for (int i = 0; i < 10; ++i) {
        if (I > 0 && I < 16 && i < 5) n[i] = *(const LDS_AS s16x8*)((O < 61440 ? c.r0 : O < 122880 ? c.r1 : c.r2) + O % 61440 + i * 1024);
        acc[i] = MFMA16(w[i >> 1], (i & 1) ? a1 : a0, acc[i]);
        PIN(acc[i]);
        if (OF >= 0 && !(i & 1)) { const int k = (OF < 0 ? 0 : OF) + (i >> 1); of[k] = LD16(c.wsr, c.lane * 16, c.osoff + ((k & 1) * 20 + (k >> 1) * 4) * 1024); MEMFENCE(); }
        if ((i & 1) && (i >> 1) < NP) k2_wpiece(c, D, (i >> 1) + (D == 4));
        SBAR();
    }
}

__global__ __launch_bounds__(512, 2) void fused_block(const char* __restrict__ x, const char* __restrict__ wqkv, const char* __restrict__ wg, const char* __restrict__ wu,
                                                      const char* __restrict__ wd, const char* __restrict__ wo, char* __restrict__ ws, char* __restrict__ out) {
    extern __shared__ __attribute__((aligned(16))) char smem[];
    asm volatile("" :: "s"(x), "s"(wqkv), "s"(wg), "s"(wu), "s"(wd), "s"(wo), "s"(ws), "s"(out));
    const int wid = __builtin_amdgcn_readfirstlane(threadIdx.x >> 6);
    STAMP_DECL; STAMP(0); PSTAMP(63);
    unsigned long long tag = 0;
    if (wid < 4) {
        __builtin_amdgcn_s_barrier();
        if (CODEWARM && wid == 3) {
            unsigned off = ((blockIdx.x >> 3) * 12 + min(threadIdx.x & 63, 11)) * 128;
            asm volatile("s_getpc_b64 vcc\n.Lfb_begin:\n\tv_min_u32 %0, .Lfb_end-.Lfb_begin, %0\n\ts_mov_b32 m0, %1\n\ts_nop 0\n\tglobal_load_lds_dword %0, vcc"
                         : "+v"(off) : "s"((unsigned)(uintptr_t)(lds_ptr)smem + LDS_JUNK) : "vcc", "memory");
        }
    }
    const rsrc_t wsr = __builtin_amdgcn_make_buffer_rsrc(ws, 0, (int)WS_END, 0x00020000);
    const int bid = blockIdx.x, head = (bid & 7) * 4 + ((bid >> 3) & 3), par = wid >> 2, qt0 = (bid >> 5) * 8 + (wid & 3) * 2;
    const char* kv = ws + WS_KV + head * KV_HEAD;
    {
        const int bi = bid >> 3, panel = (bid & 1) * 8 + (bi & 7), grp = ((bid & 7) >> 1) * 4 + (bi >> 3), l0 = threadIdx.x & 63;
        const int rb = K1_ROT ? (5 * (bi & 7)) >> 3 : 0, ra = ROTA == 1 ? (rb + (bi >> 3)) % 5 : ROTA == 2 ? ((bi & 1) * 2 + ((bi >> 3) & 1) * 2) % 4 : 0;
        if (wid < 4) {
            p1_compute((lds_ptr)smem, wid, l0, panel, grp, ra, rb, wsr, Pack{wd, wo, wid * 256 + bid} STAMP_PASS);
            const u32x2 e = *(const LDS_AS u32x2*)((lds_ptr)smem + P1_TAG);
            tag = ((unsigned long long)(unsigned)__builtin_amdgcn_readfirstlane(e[1]) << 32) | (unsigned)__builtin_amdgcn_readfirstlane(e[0]);
        } else {
            tag = p1_helper((lds_ptr)smem, wid - 4, l0, x + (size_t)panel * 128 * (D_ * 2), wqkv + min(grp, 14) * 256, wg + grp * 192, wu + grp * 192, ra, rb,
                            ws, kv, ws + WS_Q + head * Q_HEAD + (bid >> 5) * 8 * Q_TILE);
        }
    }
    int lane = __builtin_amdgcn_mbcnt_hi(~0u, __builtin_amdgcn_mbcnt_lo(~0u, 0u));
    PIN(lane);
    if (wid < 4) __builtin_amdgcn_s_setprio(3); else __builtin_amdgcn_s_setprio(0);
    STAMP(4);
    lds_ptr lds = (lds_ptr)smem + K2_KV0;
    const unsigned lds0 = (unsigned)(uintptr_t)lds;
    const int tp = ((blockIdx.x & 7) >> 1) * 8 + ((blockIdx.x >> 3) & 7), cg = (blockIdx.x & 1) * 4 + (blockIdx.x >> 6);
    K2B kb;
    kb.wsrc = ws + WS_WP + cg * 68 * 5120 + wid * 1024; kb.wdst = (unsigned)(uintptr_t)(lds_ptr)smem + wid * 1024; kb.lane = lane;
    const int sw3 = P3ROT ? tp & 1 : 0;
    kb.wsa = kb.wsrc + sw3 * 122880; kb.wsb = kb.wsrc - sw3 * 122880;
    const int hoff = WS_H16 + ((tp * 4 + (wid & 1) * 2) * 48 + (wid >> 1) + sw3 * 24) * 1024, hoff1 = hoff + (1 - 2 * sw3) * 24576;
    s16x8 hf[24];
    const int h = lane >> 5, l31 = lane & 31, l16 = lane * 16;
    const char* src = kv + wid * 1024 + 3 * K2_CHUNK;
    const int k1off = h * 512 + l31 * 8, k2off = h ? KV_V + 5 * 256 : 1024 + l31 * 8;
    const int vq = min(l31 >> 2, 5), voff = KV_V + vq * 256 + ((h ^ (vq & 3)) << 6) + (l31 < 24 ? l31 & 3 : 1) * 16;

    f32x16 oa[2] = {f32x16{}, f32x16{}}, sc[2];
    s16x8 k1[2], v0[2], v1[2]; s16x4 k2[2];
    const lds_ptr c0 = (lds_ptr)smem + K2_C0;
    lds_ptr pk1 = c0 + k1off + par * KV_TILE, pk2 = c0 + k2off + par * KV_TILE, pv = c0 + voff + par * KV_TILE, pw = c0 + (voff ^ 128) + par * KV_TILE;
    lds_ptr pk3 = pk1 + 256;
    asm volatile("" : "+v"(pk3));
#define LDS8(p) (*(const LDS_AS s16x4*)(p))
#define RD_K(t, j) do { k1[(t) & 1] = CAT8(LDS8(pk1 + (j) * 2 * KV_TILE), LDS8(pk3 + (j) * 2 * KV_TILE)); k2[(t) & 1] = LDS8(pk2 + (j) * 2 * KV_TILE); } while (0)
#define RD_V(t, j) do { v0[(t) & 1] = *(const LDS_AS s16x8*)(pv + (j) * 2 * KV_TILE); v1[(t) & 1] = *(const LDS_AS s16x8*)(pw + (j) * 2 * KV_TILE); } while (0)
    s16x8 q1[2]; s16x4 q2[2];
#pragma unroll
    for (int b = 0; b < 2; ++b) {
        const lds_ptr qp = (lds_ptr)smem + K2_Q0 + ((wid & 3) * 2 + b) * Q_TILE;
        q1[b] = CAT8(LDS8(qp + k1off), LDS8(qp + k1off + 256)); q2[b] = LDS8(qp + 1024 + l31 * 8);
    }
    {
        const s16x8 ka = CAT8(LDS8(c0 + k1off), LDS8(c0 + k1off + 256));
        const s16x4 kb = LDS8(c0 + k2off);
        RD_K(0, 0); RD_V(0, 0);
#pragma unroll
        for (int b = 0; b < 2; ++b) {
            if (h) q2[b] = s16x4{0, 0, 0, 0};
            f32x16 s = MFMA32(ka, q1[b], f32x16{});
            s = MFMA8(kb, q2[b], s);
            const float m = rbf(-max16(s));
            if (h) q2[b][0] = (short)(__float_as_uint(m) >> 16);
        }
        sc[0] = MFMA32(k1[0], q1[0], f32x16{});
        sc[0] = MFMA8(k2[0], q2[0], sc[0]);
        sc[1] = f32x16{}; v1[1] = s16x8{};
    }
    STAMP(5); PSTAMP(42); PSTAMP(43);
#define EXP4(S, i) do { S[i] = __builtin_amdgcn_exp2f(S[i]); S[i + 1] = __builtin_amdgcn_exp2f(S[i + 1]); S[i + 2] = __builtin_amdgcn_exp2f(S[i + 2]); S[i + 3] = __builtin_amdgcn_exp2f(S[i + 3]); PIN(S); SBAR(); } while (0)
    Poll pl = {};
    auto request = [&](auto HS) __attribute__((always_inline)) {
        constexpr int hs = HS.v, p = (hs - 6) >> 3, r = (hs - 6) & 7;
        constexpr int w = (hs >= 9 && hs < 14) ? hs - 9 : (hs >= 17 && hs < 22) ? hs - 12 : (hs >= 54 && hs < 57) ? hs - 44 : hs >= 62 ? hs - 49 : -1;
        constexpr int e = (p >= 2 && p <= 4 && r >= 3) ? (p - 2) * 5 + r - 3 : p == 5 ? 15 + r : hs == 57 ? 23 : -1;
        if constexpr (hs < 3) glds16c(src - K2_CHUNK + hs * 8192, l16, lds0 + 2 * K2_SLOTB + wid * 1024 + hs * 8192);
        else if constexpr (hs >= 6 && p < 5 && r < 3) glds16c(src + p * K2_CHUNK + r * 8192, l16, lds0 + (p % 3) * K2_SLOTB + wid * 1024 + r * 8192);
        else if constexpr (w >= 0) k2_wpiece(kb, w / 5, w % 5);
        else if constexpr (e >= 0) { hf[e] = LD16(wsr, l16, ((e % 12) < 6 ? hoff : hoff1) + (e / 12) * 49152 + (e % 6) * 4096); MEMFENCE(); }
        if constexpr (hs == 58) k2_wpiece(kb, 4, 4);
        if constexpr (hs == 63) k2_wpiece(kb, 4, 0);
        if constexpr (WOWARM && hs == 3) {
            const unsigned wo_ = 6 * 40960 + min((((blockIdx.x >> 3) & 7) * 8 + wid) * 13 + min(lane, 12), 799) * 128;
            asm volatile("s_mov_b32 m0, %2\n\ts_nop 0\n\tglobal_load_lds_dword %0, %1 sc1" :: "v"(wo_), "s"(kb.wsrc - wid * 1024), "s"(kb.wdst - wid * 768 + LDS_JUNK) : "memory");
        }
        if (hs == 6 && wid == 4 && lane == 0) raise_flag(ws, WS_FB, tag);
        if (hs == 16 && wid == 4) poll_request(pl, ws + WS_FB, l16);
        SBAR();
    };
    static_for<0, 32>([&](auto T) __attribute__((always_inline)) {
        {
            constexpr int c = T.v >> 2, j = T.v & 3;
            if (j == 3) {
                PSTAMP(44 + 2 * c);
                if (c == 2 && wid == 4) poll_wait(pl, ws + WS_FB, l16, tag);
                if (c == 0) { asm volatile("s_waitcnt vmcnt(0)" ::: "memory"); LGKM_BAR(); } else if (c == 7) WAIT_BAR(0); else WAIT_BAR(7);
                PSTAMP(45 + 2 * c);
                __builtin_amdgcn_s_setprio(2);
                const int dsl = c == 0 ? K2_KV0 + K2_SLOTB - K2_C0 : (c % 3 == 2) ? -2 * K2_SLOTB : K2_SLOTB;
                pk1 += dsl; pk2 += dsl; pk3 += dsl; pv += dsl; pw += dsl;
                asm volatile("" : "+v"(pk1), "+v"(pk2), "+v"(pk3), "+v"(pv), "+v"(pw));
                SBAR();
            }
            RD_K(j + 1, (j + 1) & 3); SBAR();
            oa[1] = SMF32(v1[(j + 1) & 1], hi8(sc[1]), oa[1]); PIN(oa[1]); SBAR();
            EXP4(sc[0], 0);
            sc[1] = MFMA32(k1[j & 1], q1[1], f32x16{}); PIN(sc[1]); SBAR();
            request(IC<8 * c + 2 * j>{});
            EXP4(sc[0], 4);
            sc[1] = MFMA8(k2[j & 1], q2[1], sc[1]); PIN(sc[1]); SBAR();
            EXP4(sc[0], 8);
            oa[0] = SMF32(v0[j & 1], lo8(sc[0]), oa[0]); PIN(oa[0]); SBAR();
            EXP4(sc[0], 12);
            RD_V(j + 1, (j + 1) & 3);
            if (j == 1 && c > 0) __builtin_amdgcn_s_setprio(1);
            if (j == 2 && c > 0) __builtin_amdgcn_s_setprio(0);
            SBAR();
            oa[0] = SMF32(v1[j & 1], hi8(sc[0]), oa[0]); PIN(oa[0]); SBAR();
            EXP4(sc[1], 0);
            sc[0] = MFMA32(k1[(j + 1) & 1], q1[0], f32x16{}); PIN(sc[0]); SBAR();
            request(IC<8 * c + 2 * j + 1>{});
            EXP4(sc[1], 4);
            sc[0] = MFMA8(k2[(j + 1) & 1], q2[0], sc[0]); PIN(sc[0]); SBAR();
            EXP4(sc[1], 8);
            oa[1] = SMF32(v0[j & 1], lo8(sc[1]), oa[1]); PIN(oa[1]); SBAR();
            EXP4(sc[1], 12);
        }
    });
    oa[1] = SMF32(v1[1], hi8(sc[1]), oa[1]);
    unsigned qs_ = 0; (void)qs_;
    STAMP(2);
    PSTAMP(60); QSTAMP(60);
    int ln = __builtin_amdgcn_mbcnt_hi(~0u, __builtin_amdgcn_mbcnt_lo(~0u, 0u));
    PIN(ln);
    const int h2 = ln >> 5, l2 = ln & 31;
    const int mt = wid & 1, kq = wid >> 1;
    kb.lane = ln; kb.r0 = (lds_ptr)smem + kq * 5120 + ln * 16; kb.r1 = kb.r0 + 61440; kb.r2 = kb.r0 + 122880;
    asm volatile("" : "+v"(kb.r1), "+v"(kb.r2));
    kb.wsr = wsr; kb.osoff = WS_O16 + ((tp * 4 + mt * 2) * 20 + kq) * 1024;
    s16x8 wa[5], wb[5];
#pragma unroll
    for (int i = 0; i < 5; ++i) { wa[i] = *(const LDS_AS s16x8*)(kb.r0 + i * 1024); wb[i] = *(const LDS_AS s16x8*)(kb.r0 + 20480 + i * 1024); }
    {
        const lds_ptr xp_ = lds + K2_XCH + (wid & 3) * 6144 + ln * 16;
        if (par) {
#pragma unroll
            for (int i = 0; i < 6; ++i) { const f32x16& g = oa[i / 3]; const int r = 4 * (i % 3); *(LDS_AS f32x4*)(xp_ + i * 1024) = f32x4{g[r], g[r + 1], g[r + 2], g[r + 3]}; }
        }
        LGKM_BAR();
        QSTAMP(21);
        if (!par) {
            __builtin_amdgcn_s_setprio(3);
            f32x4 tt[6];
#pragma unroll
            for (int i = 0; i < 6; ++i) tt[i] = *(const LDS_AS f32x4*)(xp_ + i * 1024);
            SBAR();
#pragma unroll
            for (int i = 0; i < 6; ++i) { const f32x4 t = tt[i]; f32x16& g = oa[i / 3]; const int r = 4 * (i % 3); g[r] = add1(g[r], t[0]); g[r + 1] = add1(g[r + 1], t[1]); g[r + 2] = add1(g[r + 2], t[2]); g[r + 3] = add1(g[r + 3], t[3]); }
            const int ob = (l2 >> 4) * 20 * 1024 + (l2 & 15) * 16, os = WS_O16 + qt0 * 2 * 20 * 1024;
            const int o1 = ob + h2 * 256 + os + (head >> 1) * 1024 + (head & 1) * 512, o2 = os + (16 + (head >> 3)) * 1024 + ((head >> 1) & 3) * 256 + (head & 1) * 8;
            auto put = [&](const f32x16& o, int b, u32x2& e) {
                const float l = __uint_as_float(__builtin_amdgcn_permlane32_swap(__float_as_uint(o[8]), __float_as_uint(o[8]), false, false)[1]);
                const float rl = __builtin_amdgcn_rcpf(l);
                unsigned w[6];
#pragma unroll
                for (int i = 0; i < 6; ++i) w[i] = cvtpk(mul1(o[2 * i], rl), mul1(o[2 * i + 1], rl));
                ST16(wsr, (u32x4{w[0], w[1], w[2], w[3]}), o1, b * 40 * 1024);
                e = u32x2{w[4], w[5]};
                return l;
            };
            u32x2 e0, e1;
            QSTAMP(22);
            const float l0 = put(oa[0], 0, e0), l1 = put(oa[1], 1, e1);
            ST8(wsr, (u32x2{__builtin_amdgcn_permlane32_swap(e0[0], e1[0], false, false)[0], __builtin_amdgcn_permlane32_swap(e0[1], e1[1], false, false)[0]}), ob + h2 * 40 * 1024, o2);
            MEMFENCE(); SBAR();
            QSTAMP(23);
            float big = 0.f;
#pragma unroll
            for (int r = 0; r < 12; ++r) big = add1(big, add1(fabsf(oa[0][r]), fabsf(oa[1][r])));
            if (__builtin_expect(__any(!(big < 1e30f) | !(fminf(l0, l1) > 1e-30f)), 0)) {
                const int g1 = h2 * 512 + l2 * 8, g2 = 1024 + l2 * 8, gq = min(l2 >> 2, 5), gv = KV_V + gq * 256 + ((h2 ^ (gq & 3)) << 6) + (l2 & 3) * 16;
                asm volatile("s_waitcnt vmcnt(0)" ::: "memory");
#pragma unroll 1
                for (int b = 0; b < 2; ++b) {
                    const s16x8 qf1 = b ? q1[1] : q1[0];
                    s16x4 qf2 = b ? q2[1] : q2[0];
                    if (h2) qf2[0] = 0;
                    float m = -INFINITY;
                    f32x16 negm, o = f32x16{};
#pragma unroll 1
                    for (int t = 0; t < 64; ++t) {
                        const int g = WS_KV + head * KV_HEAD + t * KV_TILE;
                        f32x16 s = MFMA32(CAT8(LD8(wsr, g1, g), LD8(wsr, g1 + 256, g)), qf1, f32x16{});
                        s = MFMA8(LD8(wsr, g2, g), qf2, s);
                        m = fmaxf(m, max16(s));
                    }
#pragma unroll
                    for (int r = 0; r < 16; ++r) negm[r] = -m;
#pragma unroll 1
                    for (int t = 0; t < 64; ++t) {
                        const int g = WS_KV + head * KV_HEAD + t * KV_TILE;
                        f32x16 s = MFMA32(CAT8(LD8(wsr, g1, g), LD8(wsr, g1 + 256, g)), qf1, negm);
                        s = MFMA8(LD8(wsr, g2, g), qf2, s);
#pragma unroll
                        for (int r = 0; r < 16; ++r) s[r] = __builtin_amdgcn_exp2f(s[r]);
                        o = SMF32(LD16(wsr, gv, g), lo8(s), o);
                        o = SMF32(LD16(wsr, gv ^ 128, g), hi8(s), o);
                    }
                    u32x2 e;
                    put(o, b, e);
                    if (!h2) ST8(wsr, e, ob + b * 40 * 1024, o2);
                }
            }
        }
    }
    PSTAMP(61); QSTAMP(61);
    if (DIAG_PF2) { PFLUSH(); }
    __builtin_amdgcn_s_setprio(0);
    const int ch0 = wid * 128 + ln, ch1 = ch0 + 64, er0 = ch0 / 10, er1 = ch1 / 10, ec0 = ch0 - er0 * 10, ec1 = ch1 - er1 * 10;
    const size_t eb = (size_t)tp * 64 * (D_ * 2) + cg * 160;
    const unsigned eo0 = er0 * (D_ * 2) + ec0 * 16, eo1 = er1 * (D_ * 2) + ec1 * 16;
    f32x4 acc[10];
#pragma unroll
    for (int i = 0; i < 10; ++i) acc[i] = f32x4{};
    s16x8 of[10];
    QSTAMP(0); k2_bstep<0, 0, 0, -1>(kb, acc, wa, wb, hf[0], hf[12], of);
    QSTAMP(1); k2_bstep<1, 0, 0, -1>(kb, acc, wb, wa, hf[1], hf[13], of);
    QSTAMP(2); k2_bstep<2, 0, 0, -1>(kb, acc, wa, wb, hf[2], hf[14], of);
    if (!par) {
        asm volatile("s_waitcnt vmcnt(0)" ::: "memory");
        if (ln == 0) __hip_atomic_store((gu64*)(ws + WS_FC) + (qt0 >> 1) * 32 + head, tag, __ATOMIC_RELAXED, __HIP_MEMORY_SCOPE_AGENT);
    } else {
#pragma unroll
        for (int i = 0; i < 10; ++i) glds16c(kb.wsb + 3 * 40960 - 4096 + i * 4096, ln * 16, kb.wdst - 4096 + i * 4096);
    }
    WAIT_BAR(10);
    QSTAMP(3); k2_bstep<3, 4, 3, -1>(kb, acc, wb, wa, hf[3], hf[15], of);
    QSTAMP(4); k2_bstep<4, 5, 5, -1>(kb, acc, wa, wb, hf[4], hf[16], of);
    WAIT_BAR(8);
    QSTAMP(5); k2_bstep<5, 6, 5, -1>(kb, acc, wb, wa, hf[5], hf[17], of);
    QSTAMP(6); k2_bstep<6, 0, 0, -1>(kb, acc, wa, wb, hf[6], hf[18], of);
    WAIT_BAR(10);
    QSTAMP(7); k2_bstep<7, 7, 5, -1>(kb, acc, wb, wa, hf[7], hf[19], of);
    unsigned long long seen = 0;
    const char* cw = ws + WS_FC + tp * 256;
    const unsigned cvo = l2 * 8;
    if (wid == 4) asm volatile("global_load_dwordx2 %0, %1, %2 sc1" : "+v"(seen) : "v"(cvo), "s"(cw) : "memory");
    SBAR();
    QSTAMP(8); k2_bstep<8, 0, 0, -1>(kb, acc, wa, wb, hf[8], hf[20], of);
    if (wid == 4) {
        asm volatile("s_waitcnt vmcnt(0)" : "+v"(seen) :: "memory");
#pragma unroll 1
        for (unsigned spin = 0; spin < (1u << 18) && !__all(seen == tag); ++spin) {
            __builtin_amdgcn_s_sleep(1);
            asm volatile("global_load_dwordx2 %0, %1, %2 sc1\n\ts_waitcnt vmcnt(0)" : "=&v"(seen) : "v"(cvo), "s"(cw) : "memory");
        }
    }
    WAIT_BAR(10);
    QSTAMP(9); k2_bstep<9, 0, 0, 0>(kb, acc, wb, wa, hf[9], hf[21], of);
    QSTAMP(10); k2_bstep<10, 0, 0, 5>(kb, acc, wa, wb, hf[10], hf[22], of);
    WAIT_BAR(10);
    QSTAMP(11); k2_bstep<11, 8, 3, -1>(kb, acc, wb, wa, hf[11], hf[23], of);
    QSTAMP(12); k2_bstep<12, 0, 0, -1>(kb, acc, wa, wb, of[0], of[1], of);
    QSTAMP(13); k2_bstep<13, 0, 0, -1>(kb, acc, wb, wa, of[2], of[3], of);
    QSTAMP(14); k2_bstep<14, 0, 0, -1>(kb, acc, wa, wb, of[4], of[5], of);
    WAIT_BAR(0);
    u32x4 xv0 = u32x4{}, xv1 = u32x4{};
    if (wid < 5) { xv0 = *(const u32x4*)(x + eb + eo0); xv1 = *(const u32x4*)(x + eb + eo1); }
    MEMFENCE(); SBAR();
    QSTAMP(15); k2_bstep<15, 0, 0, -1>(kb, acc, wb, wa, of[6], of[7], of);
    QSTAMP(16); k2_bstep<16, 0, 0, -1>(kb, acc, wa, wb, of[8], of[9], of);
    QSTAMP(17);
    {
        const int tk = ln & 15;
        const lds_ptr pp = (lds_ptr)smem + kq * 20480 + (mt * 32 + tk) * 320 + (((ln >> 4) ^ ((tk >> 1) & 3)) << 4);
#pragma unroll
        for (int i = 0; i < 10; ++i) *(LDS_AS f32x4*)(pp + (i & 1) * 16 * 320 + (i >> 1) * 64) = acc[i];
        QSTAMP(18);
        LGKM_BAR();
        QSTAMP(19);
        if (wid < 5) {
#pragma unroll
            for (int e = 0; e < 2; ++e) {
                const int er = e ? er1 : er0, ec = e ? ec1 : ec0, sw = (er >> 1) & 3;
                const lds_ptr p0 = (lds_ptr)smem + er * 320 + (((2 * ec) ^ sw) << 4), p1 = (lds_ptr)smem + er * 320 + (((2 * ec + 1) ^ sw) << 4);
                f32x4 a = *(const LDS_AS f32x4*)p0, b = *(const LDS_AS f32x4*)p1;
#pragma unroll
                for (int k = 1; k < 4; ++k) { a += *(const LDS_AS f32x4*)(p0 + k * 20480); b += *(const LDS_AS f32x4*)(p1 + k * 20480); }
                const u32x4 xv = e ? xv1 : xv0;
                const u32x4 ov = u32x4{cvtpk(a[0] + bf_lo(xv[0]), a[1] + bf_hi(xv[0])), cvtpk(a[2] + bf_lo(xv[1]), a[3] + bf_hi(xv[1])),
                                       cvtpk(b[0] + bf_lo(xv[2]), b[1] + bf_hi(xv[2])), cvtpk(b[2] + bf_lo(xv[3]), b[3] + bf_hi(xv[3]))};
                __builtin_amdgcn_raw_buffer_store_b128(ov, __builtin_amdgcn_make_buffer_rsrc(out, 0, S_ * D_ * 2, 0x00020000), (int)eb + (e ? eo1 : eo0), 0, OUTAUX);
            }
        }
    }
    QSTAMP(20);
    if (CODEWARM) asm volatile(".Lfb_end:");
    STAMP(7); STAMP_FLUSH(0); if (!DIAG_PF2) { PFLUSH(); }
}

extern "C" void kernel_launch(void* const* d_in, const int* in_sizes, int n_in, void* d_out, int out_size, void* d_ws, size_t ws_size,
                              hipStream_t stream) {
    const char* x = (const char*)d_in[0];
    const char* wqkv = (const char*)d_in[1];
    const char* wo = (const char*)d_in[2];
    const char* wg = (const char*)d_in[4];
    const char* wu = (const char*)d_in[5];
    const char* wd = (const char*)d_in[6];
    char* ws = (char*)d_ws;
    (void)hipFuncSetAttribute((const void*)fused_block, hipFuncAttributeMaxDynamicSharedMemorySize, LDS_BYTES);
    fused_block<<<256, 512, LDS_BYTES, stream>>>(x, wqkv, wg, wu, wd, wo, ws, (char*)d_out);
}
```

```cpp
#include <hip/hip_runtime.h>
#include <hip/hip_bf16.h>
#include <stdint.h>

typedef __bf16 bfx4 __attribute__((ext_vector_type(4)));
typedef __bf16 bfx8 __attribute__((ext_vector_type(8)));
typedef __bf16 bfx16 __attribute__((ext_vector_type(16)));
typedef short s16x4 __attribute__((ext_vector_type(4)));
typedef short s16x8 __attribute__((ext_vector_type(8)));
typedef short s16x16 __attribute__((ext_vector_type(16)));
typedef float f32x2 __attribute__((ext_vector_type(2)));
typedef float f32x4 __attribute__((ext_vector_type(4)));
typedef float f32x8 __attribute__((ext_vector_type(8)));
typedef float f32x16 __attribute__((ext_vector_type(16)));
typedef unsigned u32x2 __attribute__((ext_vector_type(2)));
typedef unsigned u32x4 __attribute__((ext_vector_type(4)));
typedef unsigned u32x8 __attribute__((ext_vector_type(8)));
#define LDS_AS __attribute__((address_space(3)))
typedef LDS_AS char* lds_ptr;

#define ABL 0
#define DPRIO 1
#define CODEWARM 1
#define EVEN 1
#define HPRIO 3
#define CPRIO 0
#define WPWARM 0
#define UNR2 2
#define HUNR 1
#define FA_POS 14
#define ROTA 2
#define OUTAUX 16
#define K1_ROT 1
constexpr int S_ = 2048, D_ = 640, H_ = 32, F_ = 1536;

constexpr int Q_TILE = 1280, Q_HEAD = 64 * Q_TILE, KV_TILE = 2816, KV_V = 1280, KV_HEAD = 64 * KV_TILE;
constexpr int WS_Q = 0;
constexpr int WS_KV = WS_Q + H_ * Q_HEAD;
constexpr int WS_O16 = WS_KV + H_ * KV_HEAD + 8192;
constexpr int WS_H16 = WS_O16 + 128 * 20 * 1024;
constexpr int WS_WP = WS_H16 + 128 * 48 * 1024;
constexpr int WS_EPOCH = WS_WP + 8 * 68 * 5120 + 8192;
constexpr int WS_FA = WS_EPOCH + 256, WS_FB = WS_FA + 2048, WS_FC = WS_FB + 2048, WS_END = WS_FC + 8192;

#define PSTAMP(i) do {} while (0)
#define PFLUSH() do {} while (0)
#define DIAG_PF2 0
#define QSTAMP(i) do {} while (0)
#define STAMP_ARG , unsigned long long (&str_)[2]
#define STAMP_PASS , str_
#define STAMP_DECL unsigned long long str_[2] = {0, 0}
#define STAMP(i) do { if ((i) == 0) { str_[0] = __builtin_amdgcn_s_memrealtime(); str_[1] = str_[0]; } else if ((i) == 0) str_[1] = __builtin_amdgcn_s_memrealtime(); } while (0)
#define STAMP_FLUSH(kern) do { const unsigned long long ta_ = __hip_atomic_load((gu64*)(ws + (32 << 20)), __ATOMIC_RELAXED, __HIP_MEMORY_SCOPE_AGENT); \
    for (int i_ = 0; i_ < 20000 && __builtin_amdgcn_s_memrealtime() < ta_ + 6000; ++i_) __builtin_amdgcn_s_sleep(1); } while (0)
#define SBAR() __builtin_amdgcn_sched_barrier(0)
#define MFMA32(a, b, c) __builtin_amdgcn_mfma_f32_32x32x16_bf16((bfx8)(a), (bfx8)(b), (c), 0, 0, 0)

__device__ __forceinline__ unsigned cvtpk(float lo, float hi) {
    typedef __bf16 bfx2 __attribute__((ext_vector_type(2)));
    bfx2 r = {(__bf16)lo, (__bf16)hi};
    return __builtin_bit_cast(unsigned, r);
}
__device__ __forceinline__ void glds16(const void* g, unsigned lds_base) {
    unsigned sv;
    asm volatile("s_mov_b32 %0, m0\n\ts_mov_b32 m0, %2\n\ts_nop 0\n\tglobal_load_lds_dwordx4 %1, off\n\ts_mov_b32 m0, %0"
                 : "=&s"(sv) : "v"(g), "s"(lds_base) : "memory");
}
__device__ __forceinline__ s16x4 trread(const LDS_AS char* p) {
    return __builtin_amdgcn_ds_read_tr16_b64_v4i16((LDS_AS s16x4*)p);
}
__device__ __forceinline__ float rbf(float v) { return (float)(__bf16)v; }
__device__ __forceinline__ float bf_lo(unsigned u) { return __uint_as_float(u << 16); }
__device__ __forceinline__ float bf_hi(unsigned u) { return __uint_as_float(u & 0xffff0000u); }
__device__ __forceinline__ float swap32_add(float v) {
    auto r = __builtin_amdgcn_permlane32_swap(__float_as_uint(v), __float_as_uint(v), false, false);
    return __uint_as_float(r[0]) + __uint_as_float(r[1]);
}
__device__ __forceinline__ float swap32_max(float v) {
    auto r = __builtin_amdgcn_permlane32_swap(__float_as_uint(v), __float_as_uint(v), false, false);
    return fmaxf(__uint_as_float(r[0]), __uint_as_float(r[1]));
}

constexpr int P1_WSLOT = 24576, P1_X0 = 3 * P1_WSLOT, P1_XSLOT = 16384, P1_STAT = P1_X0 + 5 * P1_XSLOT;
constexpr int P1_COLA = P1_STAT + 1024, P1_COLB = P1_COLA + 512, P1_TAG = P1_COLB + 1024, LDS_JUNK = 161792, LDS_BYTES = 163840;
constexpr int K2_CHUNK = 8 * KV_TILE, K2_SLOTB = 24576, K2_SLOTS = 3, K2_KV0 = 81920, K2_C0 = 2 * P1_WSLOT, K2_Q0 = 0;
typedef __attribute__((address_space(1))) unsigned long long gu64;

#define PIN(x) asm volatile("" : "+v"(x))
#define MEMFENCE() asm volatile("" ::: "memory")
#define WAIT_BAR(N) do { MEMFENCE(); __builtin_amdgcn_s_waitcnt(0x70 | ((N) & 15) | (((N) >> 4) << 14)); __builtin_amdgcn_s_barrier(); MEMFENCE(); SBAR(); } while (0)
#define LGKM_BAR() do { MEMFENCE(); __builtin_amdgcn_s_waitcnt(0xC07F); __builtin_amdgcn_s_barrier(); MEMFENCE(); SBAR(); } while (0)
#define MFMA16(a, b, c) __builtin_amdgcn_mfma_f32_16x16x32_bf16((bfx8)(a), (bfx8)(b), (c), 0, 0, 0)
#define SMF16(a, b, c, i) __builtin_amdgcn_smfmac_f32_16x16x64_bf16((bfx8)(a), (bfx16)(b), (c), (i), 0, 0)
#define CAT8(a, b) __builtin_shufflevector(a, b, 0, 1, 2, 3, 4, 5, 6, 7)
#define CAT16(a, b) __builtin_shufflevector(a, b, 0, 1, 2, 3, 4, 5, 6, 7, 8, 9, 10, 11, 12, 13, 14, 15)

__device__ __forceinline__ void glds16s(const char* sbase, unsigned voff, unsigned lds_base) {
    unsigned sv;
    asm volatile("s_mov_b32 %0, m0\n\ts_mov_b32 m0, %3\n\ts_nop 0\n\tglobal_load_lds_dwordx4 %1, %2\n\ts_mov_b32 m0, %0"
                 : "=&s"(sv) : "v"(voff), "s"(sbase), "s"(lds_base) : "memory");
}

__device__ __forceinline__ void glds16c(const char* sbase, unsigned voff, unsigned lds_base) {
    asm volatile("s_mov_b32 m0, %2\n\ts_nop 0\n\tglobal_load_lds_dwordx4 %0, %1 sc1" :: "v"(voff), "s"(sbase), "s"(lds_base) : "memory");
}
typedef __amdgpu_buffer_rsrc_t rsrc_t;
#define ST16(rs, v, voff, soff) __builtin_amdgcn_raw_buffer_store_b128((v), (rs), (voff) + (soff), 0, (ABL & 32) ? 0 : 16)
#define ST8(rs, v, voff, soff) __builtin_amdgcn_raw_buffer_store_b64((v), (rs), (voff), (soff), 16)
#define LD16(rs, voff, soff) __builtin_bit_cast(s16x8, __builtin_amdgcn_raw_buffer_load_b128((rs), (voff), (soff), 16))
#define LD8(rs, voff, soff) __builtin_bit_cast(s16x4, __builtin_amdgcn_raw_buffer_load_b64((rs), (voff), (soff), 16))
__device__ __forceinline__ f32x2 pk(const f32x16& a, int i) { return f32x2{a[i], a[i + 1]}; }
__device__ __forceinline__ f32x2 pk4(const f32x4& a, int i) { return f32x2{a[i], a[i + 1]}; }
__device__ __forceinline__ f32x2 pfma(f32x2 a, f32x2 b, f32x2 c) { return __builtin_elementwise_fma(a, b, c); }
__device__ __forceinline__ unsigned cvt2(f32x2 v) { return cvtpk(v[0], v[1]); }
__device__ __forceinline__ float add1(float a, float b) { float r = a + b; asm volatile("" : "+v"(r)); return r; }
__device__ __forceinline__ float mul1(float a, float b) { float r = a * b; asm volatile("" : "+v"(r)); return r; }

__device__ __forceinline__ void raise_flag(char* ws, int arr, unsigned long long tag) {
    __hip_atomic_store((gu64*)(ws + arr) + blockIdx.x, tag, __ATOMIC_RELAXED, __HIP_MEMORY_SCOPE_AGENT);
}
struct Poll { u32x4 a, b; };
__device__ __forceinline__ void poll_request(Poll& p, const char* arr, unsigned voff) {
    asm volatile("global_load_dwordx4 %0, %2, %3 sc1\n\tglobal_load_dwordx4 %1, %2, %3 offset:1024 sc1" : "+v"(p.a), "+v"(p.b) : "v"(voff), "s"(arr) : "memory");
}
__device__ __forceinline__ void poll_wait(Poll& p, const char* arr, unsigned voff, unsigned long long tag) {
    const unsigned tl = (unsigned)tag, th = (unsigned)(tag >> 32);
#pragma unroll 1
    for (unsigned spin = 0; spin < (1u << 18); ++spin) {
        asm volatile("s_waitcnt vmcnt(0)" : "+v"(p.a), "+v"(p.b) :: "memory");
        if (__all(p.a[0] == tl && p.a[1] == th && p.a[2] == tl && p.a[3] == th && p.b[0] == tl && p.b[1] == th && p.b[2] == tl && p.b[3] == th)) break;
        __builtin_amdgcn_s_sleep(1);
        poll_request(p, arr, voff);
    }
    asm volatile("s_waitcnt vmcnt(0)" : "+v"(p.a), "+v"(p.b) :: "memory");
}

template <int MODE> struct P1F { s16x8 x[2]; s16x4 w[MODE == 2 ? 3 : 2][2]; };

template <int MODE>
__device__ __forceinline__ void p1_read(P1F<MODE>& n, lds_ptr xa, lds_ptr w0, lds_ptr w1, int wimm, int i) {
    constexpr int TS = MODE == 2 ? 256 : 512;
    if (i == 0) { n.x[0] = *(const LDS_AS s16x8*)xa; n.w[0][0] = trread(w0 + wimm); n.w[0][1] = trread(w0 + wimm + TS); }
    if (i == 1) { n.x[1] = *(const LDS_AS s16x8*)(xa + 4096); n.w[1][0] = trread(w1 + wimm); n.w[1][1] = trread(w1 + wimm + TS); }
    if (i == 2 && MODE == 2) { n.w[MODE == 2 ? 2 : 0][0] = trread(w0 + wimm + 1024); n.w[MODE == 2 ? 2 : 0][1] = trread(w0 + wimm + 1024 + TS); }
}
template <int MODE, int NR>
__device__ __forceinline__ void p1_step(const P1F<MODE>& f, f32x16 (&acc)[MODE == 2 ? 6 : 4], P1F<MODE>& n0, lds_ptr xa0, int wi0, P1F<MODE>& n1, lds_ptr xa1, int wi1,
                                        lds_ptr w0, lds_ptr w1) {
    constexpr int NM = MODE == 2 ? 6 : 4;
#pragma unroll
    for (int g = 0; g < NM; ++g) {
        const s16x8 w = CAT8(f.w[g >> 1][0], f.w[g >> 1][1]);
#if !(ABL & 1)
        acc[g] = MODE == 1 ? MFMA32(f.x[g & 1], w, acc[g]) : MFMA32(w, f.x[g & 1], acc[g]);
#else
        { s16x8 x_ = f.x[g & 1]; asm volatile("" :: "v"(w), "v"(x_)); }
#endif
        PIN(acc[g]); SBAR();
        if (NR >= 1 && g < NM / 2) p1_read<MODE>(n0, xa0, w0, w1, wi0, g);
        if (NR >= 2 && g >= NM / 2) p1_read<MODE>(n1, xa1, w0, w1, wi1, g - NM / 2);
        SBAR();
    }
}
struct P1X { lds_ptr a0, a1, a2, a3; };
template <int MODE>
__device__ __forceinline__ void p1_first(P1F<MODE> (&f)[4], const P1X& xa, lds_ptr w0, lds_ptr w1) {
    constexpr int KS = MODE == 2 ? 6144 : 4096;
#pragma unroll
    for (int i = 0; i < 3; ++i) p1_read<MODE>(f[0], xa.a0, w0, w1, 0, i);
#pragma unroll
    for (int i = 0; i < 3; ++i) p1_read<MODE>(f[1], xa.a1, w0, w1, KS, i);
    SBAR();
}
template <int MODE>
__device__ __forceinline__ void p1_lo(P1F<MODE> (&f)[4], f32x16 (&acc)[MODE == 2 ? 6 : 4], const P1X& xa, lds_ptr w0, lds_ptr w1) {
    constexpr int KS = MODE == 2 ? 6144 : 4096;
    p1_step<MODE, 1>(f[0], acc, f[2], xa.a2, 2 * KS, f[2], xa.a2, 0, w0, w1);
    p1_step<MODE, 1>(f[1], acc, f[3], xa.a3, 3 * KS, f[3], xa.a3, 0, w0, w1);
}
template <int MODE>
__device__ __forceinline__ void p1_hi(P1F<MODE> (&f)[4], f32x16 (&acc)[MODE == 2 ? 6 : 4], const P1X& xa, lds_ptr w0, lds_ptr w1) {
    constexpr int KS = MODE == 2 ? 6144 : 4096;
    p1_step<MODE, 1>(f[2], acc, f[0], xa.a0, 0, f[0], xa.a0, 0, w0, w1);
    p1_step<MODE, 1>(f[3], acc, f[1], xa.a1, KS, f[1], xa.a1, 0, w0, w1);
}
__device__ __forceinline__ P1X p1_xaddr(lds_ptr lds, int wid, int lane) {
    const int xrow = (wid & 1) * 64 + (lane & 31), xo = P1_X0 + xrow * 128 + (((lane >> 5) ^ ((xrow >> 1) & 7)) << 4);
    return P1X{lds + xo, lds + (xo ^ 32), lds + (xo ^ 64), lds + (xo ^ 96)};
}
template <int MODE>
__device__ __forceinline__ void p1_five(P1F<MODE> (&f)[4], f32x16 (&acc)[MODE == 2 ? 6 : 4], P1X& xa, lds_ptr& w0, lds_ptr& w1, int& ws3, int wrap, int drain, int ps) {
#pragma unroll
    for (int n = 0; n < 5; ++n) {
        const int dw = ws3 == 2 ? -2 * P1_WSLOT : P1_WSLOT, dx = n == wrap ? -4 * P1_XSLOT : P1_XSLOT;
        ws3 = ws3 == 2 ? 0 : ws3 + 1;
        p1_lo<MODE>(f, acc, xa, w0, w1);
        w0 += dw; w1 += dw; xa.a0 += dx; xa.a1 += dx; xa.a2 += dx; xa.a3 += dx;
        asm volatile("" : "+v"(w0), "+v"(w1), "+v"(xa.a0), "+v"(xa.a1), "+v"(xa.a2), "+v"(xa.a3));
        if (n == drain) asm volatile("s_waitcnt vmcnt(0)" ::: "memory");
        PSTAMP(ps + 2 * n);
        LGKM_BAR();
        PSTAMP(ps + 2 * n + 1);
        p1_hi<MODE>(f, acc, xa, w0, w1);
    }
}

struct Pack { const char *wd, *wo; int job; };
__device__ __forceinline__ void pack_fetch(const Pack& pk_, lds_ptr lds, int wid, int lane) {
    const int step = pk_.job / 10, cb = pk_.job - step * 10, r = lane >> 3;
    const unsigned va = ((r & 3) + 8 * (r >> 2)) * (D_ * 2) + (lane & 7) * 16, vb = ((r >> 2) * 20 + 16 + (r & 3)) * (D_ * 2) + (lane & 7) * 16;
#pragma unroll
    for (int g = 0; g < 4; ++g) {
        const int ks = (step - 48) * 2 + (g >> 1), hh = g & 1;
        const char* a = step < 48 ? pk_.wd + (step * 32 + (g >> 1) * 16 + hh * 4) * (D_ * 2) : ks < 32 ? pk_.wo + (ks * 20 + hh * 4) * (D_ * 2) : pk_.wo + ((ks - 32) * 4 + hh * 2) * 20 * (D_ * 2);
        glds16s(a + cb * 128, step < 64 ? va : vb, (unsigned)(uintptr_t)lds + wid * P1_WSLOT + 16384 + g * 1024);
    }
}
__device__ __forceinline__ void pack_read(s16x8 (&pf)[4], lds_ptr lds, int wid, int lane) {
    asm volatile("s_waitcnt vmcnt(0)" ::: "memory");
    const lds_ptr pr = lds + wid * P1_WSLOT + 16384 + (lane >> 4) * 1024 + ((lane >> 2) & 3) * 128 + (lane & 3) * 8;
#pragma unroll
    for (int nt = 0; nt < 4; ++nt) pf[nt] = CAT8(trread(pr + nt * 32), trread(pr + nt * 32 + 512));
}
__device__ __forceinline__ void pack_store(const Pack& pk_, const s16x8 (&pf)[4], int lane, rsrc_t wsr) {
    const int step = pk_.job / 10, cb = pk_.job - step * 10;
#pragma unroll
    for (int nt = 0; nt < 4; ++nt) {
        const int N = cb * 4 + nt, cg = N / 5;
        ST16(wsr, __builtin_bit_cast(u32x4, pf[nt]), lane * 16, WS_WP + ((cg * 68 + step) * 5 + N - cg * 5) * 1024);
    }
}

template <int MODE>
__device__ __forceinline__ void p1a_compute(f32x16 (&acc)[4], lds_ptr lds, int wid, int lane, int ra, const Pack& pk_, s16x8 (&pf)[4]) {
    const int h = lane >> 5, blk = (lane >> 4) & 1, q = (lane >> 2) & 3, fsw = 3 * (q >> 1);
    P1X xa = p1_xaddr(lds + ra * P1_XSLOT, wid, lane);
    const int wb = h * 2048 + (wid >> 1) * 1024 + q * 128 + (lane & 3) * 8;
    lds_ptr w0 = lds + wb + ((blk ^ fsw) << 5), w1 = lds + wb + (((2 + blk) ^ fsw) << 5);
    P1F<MODE> f[4];
    int ws3 = 0;
    PSTAMP(0);
    LGKM_BAR();
    PSTAMP(1);
    p1_first<MODE>(f, xa, w0, w1);
#pragma unroll UNR2
    for (int hf_ = 0; hf_ < 2; ++hf_) {
        p1_five<MODE>(f, acc, xa, w0, w1, ws3, 4 - ra, -1, 2 + 10 * hf_);
        if (hf_ == 0) {
            if (pk_.job < 680) pack_read(pf, lds, wid, lane);
        }
    }
}

__device__ __forceinline__ void p1_compute(lds_ptr lds, int wid, int lane, int panel, int grp, int ra, int rb, rsrc_t wsr, const Pack& pk_ STAMP_ARG) {
    const int h = lane >> 5, l31 = lane & 31, wm = wid & 1, wn = wid >> 1, tt = panel * 4 + wm * 2;
    const LDS_AS f32x2* rowf = (const LDS_AS f32x2*)(lds + P1_STAT);
    __builtin_amdgcn_s_setprio(CPRIO);
    if (pk_.job < 680) pack_fetch(pk_, lds, wid, lane);
    {
        f32x16 acc[4] = {f32x16{}, f32x16{}, f32x16{}, f32x16{}};
        s16x8 pf[4] = {s16x8{}, s16x8{}, s16x8{}, s16x8{}};
        const LDS_AS float* colf = (const LDS_AS float*)(lds + P1_COLA) + wn * 64;
        const int sec = grp / 5, q0 = (grp - sec * 5) * 32 + wn * 16;
        if (sec < 2) {
            p1a_compute<0>(acc, lds, wid, lane, ra, pk_, pf);
            STAMP(1);
            const int pos = sec ? ((l31 & 19) | ((l31 & 4) << 1) | ((l31 & 8) >> 1)) : l31;
            const int hs = sec ? KV_HEAD : Q_HEAD, ts = sec ? KV_TILE : Q_TILE, so = (sec ? WS_KV : WS_Q) + tt * ts;
            const float qs = sec ? 1.0f : 0.22360679774997896f * 1.4426950408889634f;
            f32x2 nm[2], rr[2];
#pragma unroll
            for (int m = 0; m < 2; ++m) { const f32x2 rs = rowf[wm * 64 + m * 32 + l31]; nm[m] = f32x2{rs[1], rs[1]}; rr[m] = f32x2{rs[0] * qs, rs[0] * qs}; }
            const bool odd = lane & 1;
#pragma unroll
            for (int gam = 0; gam < 8; gam += 2) {
                const int qd = q0 + 2 * (gam + (lane & 1)) + h, hd = (qd * 205) >> 10, vo = hd * hs + (qd - hd * 5) * 256 + (pos & ~1) * 8;
                const f32x4 ca = *(const LDS_AS f32x4*)(colf + gam * 8 + h * 4), cb = *(const LDS_AS f32x4*)(colf + gam * 8 + 8 + h * 4);
#pragma unroll
                for (int m = 0; m < 2; ++m) {
                    const f32x16& a = acc[(gam >> 2) * 2 + m];
                    const int r0 = (gam & 3) * 4;
                    const unsigned a0 = cvt2(pfma(pk4(ca, 0), nm[m], pk(a, r0)) * rr[m]), a1 = cvt2(pfma(pk4(ca, 2), nm[m], pk(a, r0 + 2)) * rr[m]);
                    const unsigned b0 = cvt2(pfma(pk4(cb, 0), nm[m], pk(a, r0 + 4)) * rr[m]), b1 = cvt2(pfma(pk4(cb, 2), nm[m], pk(a, r0 + 6)) * rr[m]);
                    const unsigned g0 = __builtin_amdgcn_update_dpp(0, odd ? a0 : b0, 0xB1, 0xF, 0xF, true), g1 = __builtin_amdgcn_update_dpp(0, odd ? a1 : b1, 0xB1, 0xF, 0xF, true);
                    ST16(wsr, (odd ? u32x4{g0, g1, b0, b1} : u32x4{a0, a1, g0, g1}), vo, so + m * ts);
                }
            }
        } else {
            p1a_compute<1>(acc, lds, wid, lane, ra, pk_, pf);
            STAMP(1);
            if (sec == 2) {
                const int so = WS_KV + tt * KV_TILE + KV_V;
#pragma unroll
                for (int m = 0; m < 2; ++m) {
                    f32x2 rr[8], nm[8];
#pragma unroll
                    for (int g = 0; g < 4; ++g) {
                        const LDS_AS f32x2* rp = rowf + wm * 64 + m * 32 + g * 8 + h * 4;
                        const f32x4 a = *(const LDS_AS f32x4*)rp, b = *(const LDS_AS f32x4*)(rp + 2);
                        rr[2 * g] = f32x2{a[0], a[2]}; nm[2 * g] = f32x2{a[1], a[3]}; rr[2 * g + 1] = f32x2{b[0], b[2]}; nm[2 * g + 1] = f32x2{b[1], b[3]};
                    }
#pragma unroll
                    for (int t = 0; t < 2; ++t) {
                        const int qd = q0 + t * 8 + (l31 >> 2), hd = (qd * 205) >> 10, pl = qd - hd * 5;
                        const int vo = hd * KV_HEAD + pl * 256 + ((h ^ (pl & 3)) << 6) + (l31 & 3) * 16;
                        const float c1 = colf[t * 32 + l31];
                        const f32x2 c2 = {c1, c1};
                        unsigned w[8];
#pragma unroll
                        for (int i = 0; i < 8; ++i) w[i] = cvt2(pfma(nm[i], c2, pk(acc[t * 2 + m], 2 * i)) * rr[i]);
                        ST16(wsr, (u32x4{w[0], w[1], w[2], w[3]}), vo, so + m * KV_TILE);
                        ST16(wsr, (u32x4{w[4], w[5], w[6], w[7]}), vo ^ 128, so + m * KV_TILE);
                    }
                }
                const int hd = q0 / 5 + (lane >> 4);
                const unsigned ov = (lane & 3) ? 0u : 0x3f803f80u;
                if (hd * 5 + 4 < q0 + 16) {
                    ST16(wsr, (u32x4{ov, ov, ov, ov}), hd * KV_HEAD + 5 * 256 + (lane & 15) * 16, so);
                    ST16(wsr, (u32x4{ov, ov, ov, ov}), hd * KV_HEAD + 5 * 256 + (lane & 15) * 16, so + KV_TILE);
                }
            }
        }
        if (pk_.job < 680) pack_store(pk_, pf, lane, wsr);
    }
    STAMP(6);
    f32x16 acc[6];
#pragma unroll
    for (int t = 0; t < 6; ++t) acc[t] = f32x16{};
    {
        const int blk = (lane >> 4) & 1, T0 = wn * 3, T1 = T0 + 1;
        P1X xa = p1_xaddr(lds + rb * P1_XSLOT, wid, lane);
        const int wb = P1_WSLOT + 3 * blk * 1024 + h * 512 + ((lane >> 2) & 3) * 64 + (lane & 3) * 8;
        lds_ptr w0 = lds + wb + (T0 >> 1) * 1024 + (((T0 & 1) ^ blk) << 5), w1 = lds + wb + (T1 >> 1) * 1024 + (((T1 & 1) ^ blk) << 5);
        P1F<2> f[4];
        p1_first<2>(f, xa, w0, w1);
        int ws3 = 1;
#pragma unroll UNR2
        for (int hf_ = 0; hf_ < 2; ++hf_) {
            p1_five<2>(f, acc, xa, w0, w1, ws3, 4 - rb, hf_ == 0 ? FA_POS - 11 : -1, 22 + 10 * hf_);
        }
    }
    STAMP(3);
    {
        const LDS_AS float* colf = (const LDS_AS float*)(lds + P1_COLB) + wn * 96;
        const int vo = (l31 >> 4) * 48 * 1024 + (h * 16 + (l31 & 15)) * 16;
        f32x2 nm[2], r2[2];
#pragma unroll
        for (int m = 0; m < 2; ++m) { const f32x2 rs = rowf[wm * 64 + m * 32 + l31]; nm[m] = f32x2{rs[1], rs[1]}; r2[m] = f32x2{rs[0] * rs[0], rs[0] * rs[0]}; }
#pragma unroll
        for (int c = 0; c < 3; ++c) {
            f32x4 cv[4];
#pragma unroll
            for (int g = 0; g < 4; ++g) cv[g] = *(const LDS_AS f32x4*)(colf + c * 32 + g * 8 + h * 4);
            if (c >= 1 && wn) {
#pragma unroll
                for (int g = 0; g < 4; ++g) cv[g] += *(const LDS_AS f32x4*)(colf + 64 + c * 32 + g * 8 + h * 4);
            }
            const int so = WS_H16 + tt * 2 * 48 * 1024 + (grp * 6 + wn * 3 + c) * 512;
#pragma unroll
            for (int m = 0; m < 2; ++m) {
                const f32x16& a = acc[c * 2 + m];
                f32x2 g[4], u[4];
#pragma unroll
                for (int i = 0; i < 4; ++i) { g[i] = pfma(pk4(cv[i >> 1], (i & 1) * 2), nm[m], pk(a, 2 * i)); u[i] = pfma(pk4(cv[2 + (i >> 1)], (i & 1) * 2), nm[m], pk(a, 8 + 2 * i)); }
#pragma unroll
                for (int i = 0; i < 4; ++i) { g[i] = f32x2{fmaxf(g[i][0], 0.f), fmaxf(g[i][1], 0.f)}; u[i] *= r2[m]; }
                ST16(wsr, (u32x4{cvt2(g[0] * u[0]), cvt2(g[1] * u[1]), cvt2(g[2] * u[2]), cvt2(g[3] * u[3])}), vo, so + m * 2 * 48 * 1024);
            }
        }
    }
}

__device__ __forceinline__ void glds_x4(const char* sbase, const unsigned (&v)[4], unsigned lds_base) {
    asm volatile("s_mov_b32 m0, %5\n\ts_nop 0\n\tglobal_load_lds_dwordx4 %0, %4\n\tglobal_load_lds_dwordx4 %1, %4 offset:1024\n\t"
                 "global_load_lds_dwordx4 %2, %4 offset:2048\n\tglobal_load_lds_dwordx4 %3, %4 offset:3072"
                 :: "v"(v[0]), "v"(v[1]), "v"(v[2]), "v"(v[3]), "s"(sbase), "s"(lds_base) : "memory");
}
__device__ __forceinline__ void glds_x3(const char* sbase, const unsigned* v, unsigned lds_base) {
    asm volatile("s_mov_b32 m0, %4\n\ts_nop 0\n\tglobal_load_lds_dwordx4 %0, %3\n\tglobal_load_lds_dwordx4 %1, %3 offset:1024\n\tglobal_load_lds_dwordx4 %2, %3 offset:2048"
                 :: "v"(v[0]), "v"(v[1]), "v"(v[2]), "s"(sbase), "s"(lds_base) : "memory");
}

__device__ __forceinline__ unsigned long long p1_helper(lds_ptr lds, int hw, int lane, const char* xp, const char* wq, const char* wgp, const char* wup, int ra, int rb,
                                          char* ws, const char* kv, const char* qsrc) {
    const unsigned lds0 = (unsigned)(uintptr_t)lds;
    const int r = lane >> 3, pos = lane & 7;
    unsigned xv[4], wa[4], wb[6];
#pragma unroll
    for (int j = 0; j < 4; ++j) {
        xv[j] = 4096 + (hw * 32 + j * 8 + r) * (D_ * 2) + ((pos ^ ((j & 1) * 4 + (r >> 1))) << 4) - j * 1024;
        wa[j] = 4096 + (hw * 16 + (j >> 1) * 8 + r) * (3 * D_ * 2) + (j & 1) * 128 + ((pos ^ (6 * ((r >> 1) & 1))) << 4) - j * 1024;
    }
#pragma unroll
    for (int j = 0; j < 6; ++j) wb[j] = 4096 + (hw * 16 + (lane >> 2)) * (F_ * 2) + (j % 3) * 64 + (((lane & 3) ^ (j < 3 ? 0 : 2)) << 4) - (j % 3) * 1024;
    xp -= 4096; wq -= 4096; wgp -= 4096; wup -= 4096;
    int kb_ = 0, xs = ra, dws = 0, left = 5;
    auto issue1 = [&](bool pa) {
        if (ABL & 4) return;
        if (pa) glds_x4(wq + (unsigned)((kb_ + xs) * 64 * 3 * D_ * 2), wa, lds0 + dws * P1_WSLOT + hw * 4096);
        else glds_x3(wgp + (unsigned)((kb_ + xs) * 64 * F_ * 2), wb, lds0 + dws * P1_WSLOT + hw * 6144);
        SBAR();
    };
    auto issue2 = [&](bool pa, bool xx) {
        SBAR();
        if (ABL & 4) return;
        if (!pa) glds_x3(wup + (unsigned)((kb_ + xs) * 64 * F_ * 2), wb + 3, lds0 + dws * P1_WSLOT + hw * 6144 + 3072);
        xs = xs == 4 ? 0 : xs + 1; dws = dws == 2 ? 0 : dws + 1; --left; kb_ = left == 0 ? 5 - kb_ : kb_; left = left == 0 ? 5 : left;
        if (xx) glds_x4(xp + (kb_ + xs) * 128, xv, lds0 + P1_X0 + xs * P1_XSLOT + hw * 4096);
    };
    glds_x4(xp + ra * 128, xv, lds0 + P1_X0 + ra * P1_XSLOT + hw * 4096);
    issue1(true); issue2(true, true);
    __builtin_amdgcn_s_barrier();
    unsigned long long ep = 0;
    asm volatile("global_load_dwordx2 %0, %1, %2 sc1" : "+v"(ep) : "v"(0u), "s"(ws + WS_EPOCH) : "memory");
    __builtin_amdgcn_s_setprio(HPRIO);
    const int r16 = lane & 15, h = lane >> 5, blk = (lane >> 4) & 1, q = (lane >> 2) & 3, fsw = 3 * (q >> 1);
    const unsigned olo = r16 < 2 ? 0x3f803f80u : 0u, ohi = (r16 >> 1) == 1 ? 0x3f803f80u : 0u;
    const s16x8 ones = __builtin_bit_cast(s16x8, (u32x4{olo, olo, ohi, ohi}));
    const int idx = (r16 & 1) ? 0xEEEE : 0x4444;
    int ws3 = 0;
    {
        lds_ptr b = lds + h * 2048 + (hw >> 1) * 1024 + q * 128 + (lane & 3) * 8 + ((((hw & 1) * 2 + blk) ^ fsw) << 5);
        const int so = P1_X0 + ra * P1_XSLOT + (hw * 32 + r16) * 128 + (((lane >> 4) ^ (r16 >> 1)) << 4);
        lds_ptr sp0 = lds + so, sp1 = lds + (so ^ 64);
        const s16x8 ones2 = __builtin_bit_cast(s16x8, (u32x4{olo, olo, olo, olo}));
        f32x4 cs = f32x4{}, s1[2] = {f32x4{}, f32x4{}}, s2[2] = {f32x4{}, f32x4{}};
        s16x16 wv0, wv1; s16x8 xa[2], xb[2];
        auto col_rd = [&]() {
            wv0 = CAT16(CAT8(trread(b), trread(b + 512)), CAT8(trread(b + 4096), trread(b + 4096 + 512)));
            wv1 = CAT16(CAT8(trread(b + 8192), trread(b + 8192 + 512)), CAT8(trread(b + 12288), trread(b + 12288 + 512)));
            b += ws3 == 2 ? -2 * P1_WSLOT : P1_WSLOT; ws3 = ws3 == 2 ? 0 : ws3 + 1;
        };
        auto row_rd = [&](int dx) {
#pragma unroll
            for (int t = 0; t < 2; ++t) { xa[t] = *(const LDS_AS s16x8*)(sp0 + t * 2048); xb[t] = *(const LDS_AS s16x8*)(sp1 + t * 2048); }
            sp0 += dx; sp1 += dx;
        };
        auto row_mm = [&]() {
#pragma unroll
            for (int t = 0; t < 2; ++t) { s2[t] = MFMA16(xa[t], xa[t], s2[t]); s1[t] = SMF16(ones2, CAT16(xa[t], xb[t]), s1[t], idx); }
#pragma unroll
            for (int t = 0; t < 2; ++t) { s2[t] = MFMA16(xb[t], xb[t], s2[t]); PIN(s1[t]); PIN(s2[t]); }
        };
#pragma unroll
        for (int nn = 0; nn < 9; ++nn) {
            int n = nn;
            if (!HUNR) asm volatile("" : "+s"(n));
            PSTAMP(2 * n);
            if (n == 0) WAIT_BAR(5); else if (n < 8) WAIT_BAR(12); else WAIT_BAR(8);
            PSTAMP(2 * n + 1);
            if (!(ABL & 8) || n == 0) { col_rd(); row_rd(n % 5 == 4 - ra ? -4 * P1_XSLOT : P1_XSLOT); }
            if (n == 0) { issue1(true); issue2(true, true); }
            if (n == 8) { kb_ = 5; xs = rb; }
            issue1(n < 8);
            if (!(ABL & 8)) {
            cs = SMF16(ones, wv0, cs, idx);
            row_mm();
            cs = SMF16(ones, wv1, cs, idx); PIN(cs); }
            issue2(n < 8, n < 7);
        }
        MEMFENCE(); __builtin_amdgcn_s_waitcnt(0xF70 | 10); MEMFENCE();
        row_rd(0); row_mm();
        {
            const int t = blk;
            const float d0 = (lane & 2) ? ((lane & 1) ? s2[0][3] : s2[0][2]) : ((lane & 1) ? s2[0][1] : s2[0][0]);
            const float d1 = (lane & 2) ? ((lane & 1) ? s2[1][3] : s2[1][2]) : ((lane & 1) ? s2[1][1] : s2[1][0]);
            const float a0 = __shfl(s1[0][0] + s1[0][1], r16), a1 = __shfl(s1[1][0] + s1[1][1], r16), c0 = __shfl(d0, r16 + 16 * q), c1 = __shfl(d1, r16 + 16 * q);
            const float a = t ? a1 : a0, c = t ? c1 : c0;
            const float mean = a * (1.0f / D_), var = fmaxf(c * (1.0f / D_) - mean * mean, 0.f);
            const float rstd = __builtin_amdgcn_rcpf(rbf(__builtin_amdgcn_sqrtf(rbf(rbf(var) + 1e-5f))));
            if (lane < 32) ((LDS_AS f32x2*)(lds + P1_STAT))[hw * 32 + lane] = f32x2{rstd, -rbf(mean)};
        }
        PSTAMP(18);
        WAIT_BAR(6);
        PSTAMP(19);
        col_rd();
        issue1(false);
        cs = SMF16(ones, wv0, cs, idx);
        cs = SMF16(ones, wv1, cs, idx); PIN(cs);
        issue2(false, false);
        LDS_AS float* colf = (LDS_AS float*)(lds + P1_COLA) + hw * 32;
        if (lane < 16) { colf[lane] = cs[0] + cs[1]; colf[16 + lane] = cs[2] + cs[3]; }
    }
    asm volatile("" : "+v"(ep));
    const unsigned long long tag = (((unsigned long long)(unsigned)__builtin_amdgcn_readfirstlane((unsigned)(ep >> 32)) << 32) | (unsigned)__builtin_amdgcn_readfirstlane((unsigned)ep)) + 1ull;
    if (hw == 0 && lane == 0) *(LDS_AS unsigned long long*)(lds + P1_TAG) = tag;
    {
        const int ro = P1_WSLOT + 3 * blk * 1024 + h * 512 + q * 64 + (lane & 3) * 8 + (((hw & 1) ^ blk) << 5);
        lds_ptr b = lds + ro + (hw >> 1) * 1024, c = lds + ro + 2048 + (hw >> 1) * 12288;
        f32x4 cs = f32x4{}, ct = f32x4{};
        Poll pl = {};
#pragma unroll
        for (int nn = 10; nn < 20; ++nn) {
            int n = nn;
            if (!HUNR) asm volatile("" : "+s"(n));
            PSTAMP(2 * n);
            if (n < 13 || n == 19) WAIT_BAR(6); else if (n == 13) WAIT_BAR(10); else if (n == 18) WAIT_BAR(12); else WAIT_BAR(14);
            PSTAMP(2 * n + 1);
            const s16x16 wv0 = CAT16(CAT8(trread(b), trread(b + 256)), CAT8(trread(b + 6144), trread(b + 6144 + 256)));
            const s16x16 wv2 = CAT16(CAT8(trread(c), trread(c + 256)), CAT8(trread(c + 6144), trread(c + 6144 + 256)));
            const s16x16 wv1 = CAT16(CAT8(trread(b + 12288), trread(b + 12288 + 256)), CAT8(trread(b + 18432), trread(b + 18432 + 256)));
            if (n == FA_POS && hw == 0 && lane == 0) raise_flag(ws, WS_FA, tag);
            if (n < 18) issue1(false);
            cs = SMF16(ones, wv0, cs, idx);
            ct = SMF16(ones, wv2, ct, idx);
            cs = SMF16(ones, wv1, cs, idx); PIN(cs); PIN(ct);
            if (n < 18) issue2(false, n >= 12 && n < 17);
            if (n == 17) poll_request(pl, ws + WS_FA, lane * 16);
            if (n == 18) {
                poll_wait(pl, ws + WS_FA, lane * 16, tag);
#pragma unroll
                for (int j = 0; j < 6; ++j) glds16c(kv + (hw + 4 * j) * 1024, lane * 16, lds0 + K2_C0 + (hw + 4 * j) * 1024);
            }
            if (n == 19) {
#pragma unroll
                for (int j = 0; j < 3; ++j) glds16c(qsrc + (hw + 4 * j) * 1024, lane * 16, lds0 + K2_Q0 + (hw + 4 * j) * 1024);
            }
            const int dw = ws3 == 2 ? -2 * P1_WSLOT : P1_WSLOT;
            b += dw; c += dw; ws3 = ws3 == 2 ? 0 : ws3 + 1;
        }
        LDS_AS float* colf = (LDS_AS float*)(lds + P1_COLB);
        if (lane < 16) { colf[hw * 32 + lane] = cs[0] + cs[1]; colf[hw * 32 + 16 + lane] = cs[2] + cs[3]; }
        if (lane < 16) { colf[128 + hw * 32 + lane] = ct[0] + ct[1]; colf[128 + hw * 32 + 16 + lane] = ct[2] + ct[3]; }
        WAIT_BAR(0);
        if (blockIdx.x == 0 && hw == 0 && lane == 0) __hip_atomic_store((gu64*)(ws + WS_EPOCH), tag, __ATOMIC_RELAXED, __HIP_MEMORY_SCOPE_AGENT);
#pragma unroll
        for (int j = 0; j < 6; ++j) glds16c(kv + K2_CHUNK + (hw + 4 * j) * 1024, lane * 16, lds0 + K2_KV0 + K2_SLOTB + (hw + 4 * j) * 1024);
    }
    return tag;
}

constexpr int K2_XCH = 2 * K2_SLOTB;

#define SMF32(a, b, c) __builtin_amdgcn_smfmac_f32_32x32x32_bf16((bfx8)(a), __builtin_bit_cast(bfx16, (b)), (c), 0xDDDD, 0, 0)
#define MFMA8(a, b, c) __builtin_amdgcn_mfma_f32_32x32x8bf16_1k((a), (b), (c), 0, 0, 0)
__device__ __forceinline__ f32x8 lo8(const f32x16& v) { return __builtin_shufflevector(v, v, 0, 1, 2, 3, 4, 5, 6, 7); }
__device__ __forceinline__ f32x8 hi8(const f32x16& v) { return __builtin_shufflevector(v, v, 8, 9, 10, 11, 12, 13, 14, 15); }
__device__ __forceinline__ float max16(const f32x16& s) {
    float m = fmaxf(fmaxf(s[0], s[1]), s[2]);
#pragma unroll
    for (int r = 3; r < 15; r += 2) m = fmaxf(fmaxf(m, s[r]), s[r + 1]);
    return swap32_max(fmaxf(m, s[15]));
}

struct K2B { lds_ptr r0, r1, r2; const char* wsrc; unsigned wdst; int lane, osoff; rsrc_t wsr; };
constexpr int k2_slot(int st) { return st < 3 ? st : st == 3 ? 0 : st == 4 ? 3 : st == 5 ? 1 : st == 6 ? 2 : st == 7 ? 0 : 3; }
__device__ __forceinline__ void k2_wpiece(const K2B& c, int st, int i) {
    glds16c(c.wsrc + st * 40960 + i * 8192, c.lane * 16, c.wdst + k2_slot(st) * 40960 + i * 8192);
}
template <int I, int D, int NP, int OF>
__device__ __forceinline__ void k2_bstep(const K2B& c, f32x4 (&acc)[10], const s16x8 (&w)[5], s16x8 (&n)[5], const s16x8& a0, const s16x8& a1, s16x8 (&of)[10]) {
    constexpr int O = k2_slot((I + 1) >> 1) * 40960 + ((I + 1) & 1) * 20480;
#pragma unroll
    for (int i = 0; i < 10; ++i) {
        if (I > 0 && I < 16 && i < 5) n[i] = *(const LDS_AS s16x8*)((O < 61440 ? c.r0 : O < 122880 ? c.r1 : c.r2) + O % 61440 + i * 1024);
        acc[i] = MFMA16(w[i >> 1], (i & 1) ? a1 : a0, acc[i]);
        PIN(acc[i]);
        if (OF >= 0 && !(i & 1)) { const int k = (OF < 0 ? 0 : OF) + (i >> 1); of[k] = LD16(c.wsr, c.lane * 16, c.osoff + ((k & 1) * 20 + (k >> 1) * 4) * 1024); MEMFENCE(); }
        if ((i & 1) && (i >> 1) < NP) k2_wpiece(c, D, i >> 1);
        SBAR();
    }
}

__global__ __launch_bounds__(512, 2) void fused_block(const char* __restrict__ x, const char* __restrict__ wqkv, const char* __restrict__ wg, const char* __restrict__ wu,
                                                      const char* __restrict__ wd, const char* __restrict__ wo, char* __restrict__ ws, char* __restrict__ out) {
    extern __shared__ __attribute__((aligned(16))) char smem[];
    STAMP_DECL; asm volatile("s_memrealtime %0\n\ts_waitcnt lgkmcnt(0)" : "=s"(str_[1])); __builtin_amdgcn_sched_barrier(0);
    asm volatile("" :: "s"(x), "s"(wqkv), "s"(wg), "s"(wu), "s"(wd), "s"(wo), "s"(ws), "s"(out));
    const int wid = __builtin_amdgcn_readfirstlane(threadIdx.x >> 6);
    unsigned long long tag = 0;
    if (wid < 4) {
        __builtin_amdgcn_s_barrier();
        if (CODEWARM && wid == 3) {
            unsigned off = ((blockIdx.x >> 3) * 12 + min(threadIdx.x & 63, 11)) * 128;
            asm volatile("s_getpc_b64 vcc\n.Lfb_begin:\n\tv_min_u32 %0, .Lfb_end-.Lfb_begin, %0\n\ts_mov_b32 m0, %1\n\ts_nop 0\n\tglobal_load_lds_dword %0, vcc"
                         : "+v"(off) : "s"((unsigned)(uintptr_t)(lds_ptr)smem + LDS_JUNK) : "vcc", "memory");
        }
    }
    const rsrc_t wsr = __builtin_amdgcn_make_buffer_rsrc(ws, 0, (int)WS_END, 0x00020000);
    const int bid = blockIdx.x, head = (bid & 7) * 4 + ((bid >> 3) & 3), par = wid >> 2, qt0 = (bid >> 5) * 8 + (wid & 3) * 2;
    const char* kv = ws + WS_KV + head * KV_HEAD;
    {
        const int bi = bid >> 3, panel = (bid & 1) * 8 + (bi & 7), grp = ((bid & 7) >> 1) * 4 + (bi >> 3), l0 = threadIdx.x & 63;
        const int rb = K1_ROT ? (5 * (bi & 7)) >> 3 : 0, ra = ROTA == 1 ? (rb + (bi >> 3)) % 5 : ROTA == 2 ? ((bi & 1) * 2 + ((bi >> 3) & 1) * 2) % 4 : 0;
        if (wid < 4) {
            p1_compute((lds_ptr)smem, wid, l0, panel, grp, ra, rb, wsr, Pack{wd, wo, wid * 256 + bid} STAMP_PASS);
            const u32x2 e = *(const LDS_AS u32x2*)((lds_ptr)smem + P1_TAG);
            tag = ((unsigned long long)(unsigned)__builtin_amdgcn_readfirstlane(e[1]) << 32) | (unsigned)__builtin_amdgcn_readfirstlane(e[0]);
        } else {
            tag = p1_helper((lds_ptr)smem, wid - 4, l0, x + (size_t)panel * 128 * (D_ * 2), wqkv + min(grp, 14) * 256, wg + grp * 192, wu + grp * 192, ra, rb,
                            ws, kv, ws + WS_Q + head * Q_HEAD + (bid >> 5) * 8 * Q_TILE);
        }
    }
    int lane = __builtin_amdgcn_mbcnt_hi(~0u, __builtin_amdgcn_mbcnt_lo(~0u, 0u));
    PIN(lane);
    if (wid < 4) __builtin_amdgcn_s_setprio(3); else __builtin_amdgcn_s_setprio(0);
    STAMP(4);
    lds_ptr lds = (lds_ptr)smem + K2_KV0;
    const unsigned lds0 = (unsigned)(uintptr_t)lds;
    const int tp = ((blockIdx.x & 7) >> 1) * 8 + ((blockIdx.x >> 3) & 7), cg = (blockIdx.x & 1) * 4 + (blockIdx.x >> 6);
    K2B kb;
    kb.wsrc = ws + WS_WP + cg * 68 * 5120 + wid * 1024; kb.wdst = (unsigned)(uintptr_t)(lds_ptr)smem + wid * 1024; kb.lane = lane;
    const int hoff = WS_H16 + ((tp * 4 + (wid & 1) * 2) * 48 + (wid >> 1)) * 1024;
    s16x8 hf[24];
    const int h = lane >> 5, l31 = lane & 31, l16 = lane * 16;
    const char* src = kv + wid * 1024 + 3 * K2_CHUNK;
    const int k1off = h * 512 + l31 * 8, k2off = h ? KV_V + 5 * 256 : 1024 + l31 * 8;
    const int vq = min(l31 >> 2, 5), voff = KV_V + vq * 256 + ((h ^ (vq & 3)) << 6) + (l31 < 24 ? l31 & 3 : 1) * 16;

    f32x16 oa[2] = {f32x16{}, f32x16{}}, sc[2];
    s16x8 k1[2], v0[2], v1[2]; s16x4 k2[2];
    const lds_ptr c0 = (lds_ptr)smem + K2_C0;
    lds_ptr pk1 = c0 + k1off + par * KV_TILE, pk2 = c0 + k2off + par * KV_TILE, pv = c0 + voff + par * KV_TILE, pw = c0 + (voff ^ 128) + par * KV_TILE;
    lds_ptr pk3 = pk1 + 256;
    asm volatile("" : "+v"(pk3));
#define LDS8(p) (*(const LDS_AS s16x4*)(p))
#define RD_K(t, j) do { k1[(t) & 1] = CAT8(LDS8(pk1 + (j) * 2 * KV_TILE), LDS8(pk3 + (j) * 2 * KV_TILE)); k2[(t) & 1] = LDS8(pk2 + (j) * 2 * KV_TILE); } while (0)
#define RD_V(t, j) do { v0[(t) & 1] = *(const LDS_AS s16x8*)(pv + (j) * 2 * KV_TILE); v1[(t) & 1] = *(const LDS_AS s16x8*)(pw + (j) * 2 * KV_TILE); } while (0)
    s16x8 q1[2]; s16x4 q2[2];
#pragma unroll
    for (int b = 0; b < 2; ++b) {
        const lds_ptr qp = (lds_ptr)smem + K2_Q0 + ((wid & 3) * 2 + b) * Q_TILE;
        q1[b] = CAT8(LDS8(qp + k1off), LDS8(qp + k1off + 256)); q2[b] = LDS8(qp + 1024 + l31 * 8);
    }
    {
        const s16x8 ka = CAT8(LDS8(c0 + k1off), LDS8(c0 + k1off + 256));
        const s16x4 kb = LDS8(c0 + k2off);
        RD_K(0, 0); RD_V(0, 0);
#pragma unroll
        for (int b = 0; b < 2; ++b) {
            if (h) q2[b] = s16x4{0, 0, 0, 0};
            f32x16 s = MFMA32(ka, q1[b], f32x16{});
            s = MFMA8(kb, q2[b], s);
            const float m = rbf(-max16(s));
            if (h) q2[b][0] = (short)(__float_as_uint(m) >> 16);
        }
        sc[0] = MFMA32(k1[0], q1[0], f32x16{});
        sc[0] = MFMA8(k2[0], q2[0], sc[0]);
        sc[1] = f32x16{}; v1[1] = s16x8{};
    }
    STAMP(5); PSTAMP(42); PSTAMP(43);
#define EXP4(S, i) do { S[i] = __builtin_amdgcn_exp2f(S[i]); S[i + 1] = __builtin_amdgcn_exp2f(S[i + 1]); S[i + 2] = __builtin_amdgcn_exp2f(S[i + 2]); S[i + 3] = __builtin_amdgcn_exp2f(S[i + 3]); PIN(S); SBAR(); } while (0)
    Poll pl = {};
    auto request = [&](int hs) {
        const int p = (hs - 6) >> 3, r = (hs - 6) & 7;
        const int w = (hs >= 9 && hs < 14) ? hs - 9 : (hs >= 17 && hs < 22) ? hs - 12 : (hs >= 54 && hs < 57) ? hs - 44 : hs >= 62 ? hs - 49 : -1;
        const int e = (p >= 2 && p <= 4 && r >= 3) ? (p - 2) * 5 + r - 3 : p == 5 ? 15 + r : hs == 57 ? 23 : -1;
        if (hs < 3) glds16c(src - K2_CHUNK + hs * 8192, l16, lds0 + 2 * K2_SLOTB + wid * 1024 + hs * 8192);
        else if (hs >= 6 && p < 5 && r < 3) glds16c(src + p * K2_CHUNK + r * 8192, l16, lds0 + (p % 3) * K2_SLOTB + wid * 1024 + r * 8192);
        else if (w >= 0) glds16c(kb.wsrc + w * 8192, l16, kb.wdst + w * 8192);
        else if (e >= 0) { hf[e] = LD16(wsr, l16, hoff + e * 4096); MEMFENCE(); }
        if (hs == 6 && wid == 4 && lane == 0) raise_flag(ws, WS_FB, tag);
        if (hs == 16 && wid == 4) poll_request(pl, ws + WS_FB, l16);
        SBAR();
    };
#pragma unroll
    for (int c = 0; c < 8; ++c) {
#pragma unroll
        for (int j = 0; j < 4; ++j) {
            if (j == 3) {
                PSTAMP(44 + 2 * c);
                if (c == 2 && wid == 4) poll_wait(pl, ws + WS_FB, l16, tag);
                if (c == 0) { asm volatile("s_waitcnt vmcnt(0)" ::: "memory"); LGKM_BAR(); } else if (c == 7) WAIT_BAR(0); else WAIT_BAR(7);
                PSTAMP(45 + 2 * c);
                __builtin_amdgcn_s_setprio(2);
                const int dsl = c == 0 ? K2_KV0 + K2_SLOTB - K2_C0 : (c % 3 == 2) ? -2 * K2_SLOTB : K2_SLOTB;
                pk1 += dsl; pk2 += dsl; pk3 += dsl; pv += dsl; pw += dsl;
                asm volatile("" : "+v"(pk1), "+v"(pk2), "+v"(pk3), "+v"(pv), "+v"(pw));
                SBAR();
            }
            RD_K(j + 1, (j + 1) & 3); SBAR();
            oa[1] = SMF32(v1[(j + 1) & 1], hi8(sc[1]), oa[1]); PIN(oa[1]); SBAR();
            EXP4(sc[0], 0);
            sc[1] = MFMA32(k1[j & 1], q1[1], f32x16{}); PIN(sc[1]); SBAR();
            request(8 * c + 2 * j);
            EXP4(sc[0], 4);
            sc[1] = MFMA8(k2[j & 1], q2[1], sc[1]); PIN(sc[1]); SBAR();
            EXP4(sc[0], 8);
            oa[0] = SMF32(v0[j & 1], lo8(sc[0]), oa[0]); PIN(oa[0]); SBAR();
            EXP4(sc[0], 12);
            RD_V(j + 1, (j + 1) & 3);
            if (j == 1 && c > 0) __builtin_amdgcn_s_setprio(1);
            if (j == 2 && c > 0) __builtin_amdgcn_s_setprio(0);
            SBAR();
            oa[0] = SMF32(v1[j & 1], hi8(sc[0]), oa[0]); PIN(oa[0]); SBAR();
            EXP4(sc[1], 0);
            sc[0] = MFMA32(k1[(j + 1) & 1], q1[0], f32x16{}); PIN(sc[0]); SBAR();
            request(8 * c + 2 * j + 1);
            EXP4(sc[1], 4);
            sc[0] = MFMA8(k2[(j + 1) & 1], q2[0], sc[0]); PIN(sc[0]); SBAR();
            EXP4(sc[1], 8);
            oa[1] = SMF32(v0[j & 1], lo8(sc[1]), oa[1]); PIN(oa[1]); SBAR();
            EXP4(sc[1], 12);
        }
    }
    oa[1] = SMF32(v1[1], hi8(sc[1]), oa[1]);
    unsigned qs_ = 0; (void)qs_;
    STAMP(2);
    PSTAMP(60); QSTAMP(60);
    int ln = __builtin_amdgcn_mbcnt_hi(~0u, __builtin_amdgcn_mbcnt_lo(~0u, 0u));
    PIN(ln);
    const int h2 = ln >> 5, l2 = ln & 31;
    const int mt = wid & 1, kq = wid >> 1;
    kb.lane = ln; kb.r0 = (lds_ptr)smem + kq * 5120 + ln * 16; kb.r1 = kb.r0 + 61440; kb.r2 = kb.r0 + 122880;
    asm volatile("" : "+v"(kb.r1), "+v"(kb.r2));
    kb.wsr = wsr; kb.osoff = WS_O16 + ((tp * 4 + mt * 2) * 20 + kq) * 1024;
    s16x8 wa[5], wb[5];
#pragma unroll
    for (int i = 0; i < 5; ++i) { wa[i] = *(const LDS_AS s16x8*)(kb.r0 + i * 1024); wb[i] = *(const LDS_AS s16x8*)(kb.r0 + 20480 + i * 1024); }
    {
        const lds_ptr xp_ = lds + K2_XCH + (wid & 3) * 6144 + ln * 16;
        if (par) {
#pragma unroll
            for (int i = 0; i < 6; ++i) { const f32x16& g = oa[i / 3]; const int r = 4 * (i % 3); *(LDS_AS f32x4*)(xp_ + i * 1024) = f32x4{g[r], g[r + 1], g[r + 2], g[r + 3]}; }
        }
        LGKM_BAR();
        QSTAMP(21);
        if (!par) {
            __builtin_amdgcn_s_setprio(3);
            f32x4 tt[6];
#pragma unroll
            for (int i = 0; i < 6; ++i) tt[i] = *(const LDS_AS f32x4*)(xp_ + i * 1024);
            SBAR();
#pragma unroll
            for (int i = 0; i < 6; ++i) { const f32x4 t = tt[i]; f32x16& g = oa[i / 3]; const int r = 4 * (i % 3); g[r] = add1(g[r], t[0]); g[r + 1] = add1(g[r + 1], t[1]); g[r + 2] = add1(g[r + 2], t[2]); g[r + 3] = add1(g[r + 3], t[3]); }
            const int ob = (l2 >> 4) * 20 * 1024 + (l2 & 15) * 16, os = WS_O16 + qt0 * 2 * 20 * 1024;
            const int o1 = ob + h2 * 256 + os + (head >> 1) * 1024 + (head & 1) * 512, o2 = os + (16 + (head >> 3)) * 1024 + ((head >> 1) & 3) * 256 + (head & 1) * 8;
            auto put = [&](const f32x16& o, int b, u32x2& e) {
                const float l = __uint_as_float(__builtin_amdgcn_permlane32_swap(__float_as_uint(o[8]), __float_as_uint(o[8]), false, false)[1]);
                const float rl = __builtin_amdgcn_rcpf(l);
                unsigned w[6];
#pragma unroll
                for (int i = 0; i < 6; ++i) w[i] = cvtpk(mul1(o[2 * i], rl), mul1(o[2 * i + 1], rl));
                ST16(wsr, (u32x4{w[0], w[1], w[2], w[3]}), o1, b * 40 * 1024);
                e = u32x2{w[4], w[5]};
                return l;
            };
            u32x2 e0, e1;
            QSTAMP(22);
            const float l0 = put(oa[0], 0, e0), l1 = put(oa[1], 1, e1);
            ST8(wsr, (u32x2{__builtin_amdgcn_permlane32_swap(e0[0], e1[0], false, false)[0], __builtin_amdgcn_permlane32_swap(e0[1], e1[1], false, false)[0]}), ob + h2 * 40 * 1024, o2);
            MEMFENCE(); SBAR();
            QSTAMP(23);
            float big = 0.f;
#pragma unroll
            for (int r = 0; r < 12; ++r) big = add1(big, add1(fabsf(oa[0][r]), fabsf(oa[1][r])));
            if (__builtin_expect(__any(!(big < 1e30f) | !(fminf(l0, l1) > 1e-30f)), 0)) {
                const int g1 = h2 * 512 + l2 * 8, g2 = 1024 + l2 * 8, gq = min(l2 >> 2, 5), gv = KV_V + gq * 256 + ((h2 ^ (gq & 3)) << 6) + (l2 & 3) * 16;
                asm volatile("s_waitcnt vmcnt(0)" ::: "memory");
#pragma unroll 1
                for (int b = 0; b < 2; ++b) {
                    const s16x8 qf1 = b ? q1[1] : q1[0];
                    s16x4 qf2 = b ? q2[1] : q2[0];
                    if (h2) qf2[0] = 0;
                    float m = -INFINITY;
                    f32x16 negm, o = f32x16{};
#pragma unroll 1
                    for (int t = 0; t < 64; ++t) {
                        const int g = WS_KV + head * KV_HEAD + t * KV_TILE;
                        f32x16 s = MFMA32(CAT8(LD8(wsr, g1, g), LD8(wsr, g1 + 256, g)), qf1, f32x16{});
                        s = MFMA8(LD8(wsr, g2, g), qf2, s);
                        m = fmaxf(m, max16(s));
                    }
#pragma unroll
                    for (int r = 0; r < 16; ++r) negm[r] = -m;
#pragma unroll 1
                    for (int t = 0; t < 64; ++t) {
                        const int g = WS_KV + head * KV_HEAD + t * KV_TILE;
                        f32x16 s = MFMA32(CAT8(LD8(wsr, g1, g), LD8(wsr, g1 + 256, g)), qf1, negm);
                        s = MFMA8(LD8(wsr, g2, g), qf2, s);
#pragma unroll
                        for (int r = 0; r < 16; ++r) s[r] = __builtin_amdgcn_exp2f(s[r]);
                        o = SMF32(LD16(wsr, gv, g), lo8(s), o);
                        o = SMF32(LD16(wsr, gv ^ 128, g), hi8(s), o);
                    }
                    u32x2 e;
                    put(o, b, e);
                    if (!h2) ST8(wsr, e, ob + b * 40 * 1024, o2);
                }
            }
        }
    }
    PSTAMP(61); QSTAMP(61);
    if (DIAG_PF2) { PFLUSH(); }
    __builtin_amdgcn_s_setprio(0);
    const int ch0 = wid * 128 + ln, ch1 = ch0 + 64, er0 = ch0 / 10, er1 = ch1 / 10, ec0 = ch0 - er0 * 10, ec1 = ch1 - er1 * 10;
    const size_t eb = (size_t)tp * 64 * (D_ * 2) + cg * 160;
    const unsigned eo0 = er0 * (D_ * 2) + ec0 * 16, eo1 = er1 * (D_ * 2) + ec1 * 16;
    f32x4 acc[10];
#pragma unroll
    for (int i = 0; i < 10; ++i) acc[i] = f32x4{};
    s16x8 of[10];
    QSTAMP(0); k2_bstep<0, 0, 0, -1>(kb, acc, wa, wb, hf[0], hf[12], of);
    QSTAMP(1); k2_bstep<1, 0, 0, -1>(kb, acc, wb, wa, hf[1], hf[13], of);
    QSTAMP(2); k2_bstep<2, 0, 0, -1>(kb, acc, wa, wb, hf[2], hf[14], of);
    if (!par) {
        asm volatile("s_waitcnt vmcnt(0)" ::: "memory");
        if (ln == 0) __hip_atomic_store((gu64*)(ws + WS_FC) + (qt0 >> 1) * 32 + head, tag, __ATOMIC_RELAXED, __HIP_MEMORY_SCOPE_AGENT);
    } else {
#pragma unroll
        for (int i = 0; i < 10; ++i) glds16c(kb.wsrc + 3 * 40960 - 4096 + i * 4096, ln * 16, kb.wdst - 4096 + i * 4096);
    }
    WAIT_BAR(10);
    QSTAMP(3); k2_bstep<3, 4, 5, -1>(kb, acc, wb, wa, hf[3], hf[15], of);
    QSTAMP(4); k2_bstep<4, 5, 5, -1>(kb, acc, wa, wb, hf[4], hf[16], of);
    WAIT_BAR(10);
    QSTAMP(5); k2_bstep<5, 6, 5, -1>(kb, acc, wb, wa, hf[5], hf[17], of);
    unsigned long long seen = 0;
    const char* cw = ws + WS_FC + tp * 256;
    const unsigned cvo = l2 * 8;
    if (wid == 4) asm volatile("global_load_dwordx2 %0, %1, %2 sc1" : "+v"(seen) : "v"(cvo), "s"(cw) : "memory");
    SBAR();
    QSTAMP(6); k2_bstep<6, 0, 0, -1>(kb, acc, wa, wb, hf[6], hf[18], of);
    if (wid == 4) {
        asm volatile("s_waitcnt vmcnt(0)" : "+v"(seen) :: "memory");
#pragma unroll 1
        for (unsigned spin = 0; spin < (1u << 18) && !__all(seen == tag); ++spin) {
            __builtin_amdgcn_s_sleep(1);
            asm volatile("global_load_dwordx2 %0, %1, %2 sc1\n\ts_waitcnt vmcnt(0)" : "=&v"(seen) : "v"(cvo), "s"(cw) : "memory");
        }
    }
    WAIT_BAR(10);
    QSTAMP(7); k2_bstep<7, 0, 0, 0>(kb, acc, wb, wa, hf[7], hf[19], of);
    QSTAMP(8); k2_bstep<8, 0, 0, 5>(kb, acc, wa, wb, hf[8], hf[20], of);
    WAIT_BAR(15);
    QSTAMP(9); k2_bstep<9, 7, 5, -1>(kb, acc, wb, wa, hf[9], hf[21], of);
    QSTAMP(10); k2_bstep<10, 8, 3, -1>(kb, acc, wa, wb, hf[10], hf[22], of);
    WAIT_BAR(8);
    QSTAMP(11); k2_bstep<11, 0, 0, -1>(kb, acc, wb, wa, hf[11], hf[23], of);
    QSTAMP(12); k2_bstep<12, 0, 0, -1>(kb, acc, wa, wb, of[0], of[1], of);
    WAIT_BAR(3);
    QSTAMP(13); k2_bstep<13, 0, 0, -1>(kb, acc, wb, wa, of[2], of[3], of);
    QSTAMP(14); k2_bstep<14, 0, 0, -1>(kb, acc, wa, wb, of[4], of[5], of);
    WAIT_BAR(0);
    u32x4 xv0 = u32x4{}, xv1 = u32x4{};
    if (wid < 5) { xv0 = *(const u32x4*)(x + eb + eo0); xv1 = *(const u32x4*)(x + eb + eo1); }
    MEMFENCE(); SBAR();
    QSTAMP(15); k2_bstep<15, 0, 0, -1>(kb, acc, wb, wa, of[6], of[7], of);
    QSTAMP(16); k2_bstep<16, 0, 0, -1>(kb, acc, wa, wb, of[8], of[9], of);
    QSTAMP(17);
    {
        const int tk = ln & 15;
        const lds_ptr pp = (lds_ptr)smem + kq * 20480 + (mt * 32 + tk) * 320 + (((ln >> 4) ^ ((tk >> 1) & 3)) << 4);
#pragma unroll
        for (int i = 0; i < 10; ++i) *(LDS_AS f32x4*)(pp + (i & 1) * 16 * 320 + (i >> 1) * 64) = acc[i];
        QSTAMP(18);
        LGKM_BAR();
        QSTAMP(19);
        if (wid < 5) {
#pragma unroll
            for (int e = 0; e < 2; ++e) {
                const int er = e ? er1 : er0, ec = e ? ec1 : ec0, sw = (er >> 1) & 3;
                const lds_ptr p0 = (lds_ptr)smem + er * 320 + (((2 * ec) ^ sw) << 4), p1 = (lds_ptr)smem + er * 320 + (((2 * ec + 1) ^ sw) << 4);
                f32x4 a = *(const LDS_AS f32x4*)p0, b = *(const LDS_AS f32x4*)p1;
#pragma unroll
                for (int k = 1; k < 4; ++k) { a += *(const LDS_AS f32x4*)(p0 + k * 20480); b += *(const LDS_AS f32x4*)(p1 + k * 20480); }
                const u32x4 xv = e ? xv1 : xv0;
                const u32x4 ov = u32x4{cvtpk(a[0] + bf_lo(xv[0]), a[1] + bf_hi(xv[0])), cvtpk(a[2] + bf_lo(xv[1]), a[3] + bf_hi(xv[1])),
                                       cvtpk(b[0] + bf_lo(xv[2]), b[1] + bf_hi(xv[2])), cvtpk(b[2] + bf_lo(xv[3]), b[3] + bf_hi(xv[3]))};
                __builtin_amdgcn_raw_buffer_store_b128(ov, __builtin_amdgcn_make_buffer_rsrc(out, 0, S_ * D_ * 2, 0x00020000), (int)eb + (e ? eo1 : eo0), 0, OUTAUX);
            }
        }
    }
    QSTAMP(20);
    if (CODEWARM) asm volatile(".Lfb_end:");
    STAMP(7); STAMP_FLUSH(0); if (!DIAG_PF2) { PFLUSH(); }
}

__global__ void tiny(char* ws) { if (threadIdx.x == 0) *(unsigned long long*)(ws + (32 << 20)) = __builtin_amdgcn_s_memrealtime(); }
extern "C" void kernel_launch(void* const* d_in, const int* in_sizes, int n_in, void* d_out, int out_size, void* d_ws, size_t ws_size,
                              hipStream_t stream) {
    const char* x = (const char*)d_in[0];
    const char* wqkv = (const char*)d_in[1];
    const char* wo = (const char*)d_in[2];
    const char* wg = (const char*)d_in[4];
    const char* wu = (const char*)d_in[5];
    const char* wd = (const char*)d_in[6];
    char* ws = (char*)d_ws;
    (void)hipFuncSetAttribute((const void*)fused_block, hipFuncAttributeMaxDynamicSharedMemorySize, LDS_BYTES);
    tiny<<<1, 64, 0, stream>>>(ws);
    fused_block<<<256, 512, LDS_BYTES, stream>>>(x, wqkv, wg, wu, wd, wo, ws, (char*)d_out);
}
```

```cpp
#include <hip/hip_runtime.h>
#include <hip/hip_bf16.h>
#include <stdint.h>

typedef __bf16 bfx4 __attribute__((ext_vector_type(4)));
typedef __bf16 bfx8 __attribute__((ext_vector_type(8)));
typedef __bf16 bfx16 __attribute__((ext_vector_type(16)));
typedef short s16x4 __attribute__((ext_vector_type(4)));
typedef short s16x8 __attribute__((ext_vector_type(8)));
typedef short s16x16 __attribute__((ext_vector_type(16)));
typedef float f32x2 __attribute__((ext_vector_type(2)));
typedef float f32x4 __attribute__((ext_vector_type(4)));
typedef float f32x8 __attribute__((ext_vector_type(8)));
typedef float f32x16 __attribute__((ext_vector_type(16)));
typedef unsigned u32x2 __attribute__((ext_vector_type(2)));
typedef unsigned u32x4 __attribute__((ext_vector_type(4)));
typedef unsigned u32x8 __attribute__((ext_vector_type(8)));
#define LDS_AS __attribute__((address_space(3)))
typedef LDS_AS char* lds_ptr;

#define ABL 0
#define DPRIO 1
#define CODEWARM 1
#define EVEN 1
#define HPRIO 3
#define CPRIO 0
#define WPWARM 0
#define UNR2 2
#define HUNR 1
#define FA_POS 14
#define ROTA 2
#define OUTAUX 16
#define P3ROT 1
#define WOWARM 1
#define K1_ROT 1
constexpr int S_ = 2048, D_ = 640, H_ = 32, F_ = 1536;

constexpr int Q_TILE = 1280, Q_HEAD = 64 * Q_TILE, KV_TILE = 2816, KV_V = 2560, KV_VT = 1536, KV_HEAD = 64 * KV_TILE;
constexpr int WS_Q = 0;
constexpr int WS_KV = WS_Q + H_ * Q_HEAD;
constexpr int WS_O16 = WS_KV + H_ * KV_HEAD + 8192;
constexpr int WS_H16 = WS_O16 + 128 * 20 * 1024;
constexpr int WS_WP = WS_H16 + 128 * 48 * 1024;
constexpr int WS_EPOCH = WS_WP + 8 * 68 * 5120 + 8192;
constexpr int WS_FA = WS_EPOCH + 256, WS_FB = WS_FA + 2048, WS_FC = WS_FB + 2048, WS_END = WS_FC + 8192;

#define PSTAMP(i) do {} while (0)
#define PFLUSH() do {} while (0)
#define DIAG_PF2 0
#define QFLUSH() do {} while (0)
#define QSTAMP(i) do {} while (0)
#define STAMP_ARG
#define STAMP_PASS
#define STAMP_DECL do {} while (0)
#define STAMP(i) do {} while (0)
#define STAMP_FLUSH(kern) do {} while (0)
#define SBAR() __builtin_amdgcn_sched_barrier(0)
template <int V> struct IC { static constexpr int v = V; };
template <int I, int N, class F> __device__ __forceinline__ void static_for(F&& f) { if constexpr (I < N) { f(IC<I>{}); static_for<I + 1, N>(f); } }
#define MFMA32(a, b, c) __builtin_amdgcn_mfma_f32_32x32x16_bf16((bfx8)(a), (bfx8)(b), (c), 0, 0, 0)

__device__ __forceinline__ unsigned cvtpk(float lo, float hi) {
    typedef __bf16 bfx2 __attribute__((ext_vector_type(2)));
    bfx2 r = {(__bf16)lo, (__bf16)hi};
    return __builtin_bit_cast(unsigned, r);
}
__device__ __forceinline__ void glds16(const void* g, unsigned lds_base) {
    unsigned sv;
    asm volatile("s_mov_b32 %0, m0\n\ts_mov_b32 m0, %2\n\ts_nop 0\n\tglobal_load_lds_dwordx4 %1, off\n\ts_mov_b32 m0, %0"
                 : "=&s"(sv) : "v"(g), "s"(lds_base) : "memory");
}
__device__ __forceinline__ s16x4 trread(const LDS_AS char* p) {
    return __builtin_amdgcn_ds_read_tr16_b64_v4i16((LDS_AS s16x4*)p);
}
__device__ __forceinline__ float rbf(float v) { return (float)(__bf16)v; }
__device__ __forceinline__ float bf_lo(unsigned u) { return __uint_as_float(u << 16); }
__device__ __forceinline__ float bf_hi(unsigned u) { return __uint_as_float(u & 0xffff0000u); }
__device__ __forceinline__ float swap32_add(float v) {
    auto r = __builtin_amdgcn_permlane32_swap(__float_as_uint(v), __float_as_uint(v), false, false);
    return __uint_as_float(r[0]) + __uint_as_float(r[1]);
}
__device__ __forceinline__ float swap32_max(float v) {
    auto r = __builtin_amdgcn_permlane32_swap(__float_as_uint(v), __float_as_uint(v), false, false);
    return fmaxf(__uint_as_float(r[0]), __uint_as_float(r[1]));
}

constexpr int P1_WSLOT = 24576, P1_X0 = 3 * P1_WSLOT, P1_XSLOT = 16384, P1_STAT = P1_X0 + 5 * P1_XSLOT;
constexpr int P1_COLA = P1_STAT + 1024, P1_COLB = P1_COLA + 512, P1_TAG = P1_COLB + 1024, LDS_JUNK = 161792, LDS_BYTES = 163840;
constexpr int K2_CHUNK = 8 * KV_TILE, K2_SLOTB = 24576, K2_SLOTS = 3, K2_KV0 = 81920, K2_C0 = 2 * P1_WSLOT, K2_Q0 = 0;
typedef __attribute__((address_space(1))) unsigned long long gu64;

#define PIN(x) asm volatile("" : "+v"(x))
#define MEMFENCE() asm volatile("" ::: "memory")
#define WAIT_BAR(N) do { MEMFENCE(); __builtin_amdgcn_s_waitcnt(0x70 | ((N) & 15) | (((N) >> 4) << 14)); __builtin_amdgcn_s_barrier(); MEMFENCE(); SBAR(); } while (0)
#define LGKM_BAR() do { MEMFENCE(); __builtin_amdgcn_s_waitcnt(0xC07F); __builtin_amdgcn_s_barrier(); MEMFENCE(); SBAR(); } while (0)
#define MFMA16(a, b, c) __builtin_amdgcn_mfma_f32_16x16x32_bf16((bfx8)(a), (bfx8)(b), (c), 0, 0, 0)
#define SMF16(a, b, c, i) __builtin_amdgcn_smfmac_f32_16x16x64_bf16((bfx8)(a), (bfx16)(b), (c), (i), 0, 0)
#define CAT8(a, b) __builtin_shufflevector(a, b, 0, 1, 2, 3, 4, 5, 6, 7)
#define CAT16(a, b) __builtin_shufflevector(a, b, 0, 1, 2, 3, 4, 5, 6, 7, 8, 9, 10, 11, 12, 13, 14, 15)

__device__ __forceinline__ void glds16s(const char* sbase, unsigned voff, unsigned lds_base) {
    unsigned sv;
    asm volatile("s_mov_b32 %0, m0\n\ts_mov_b32 m0, %3\n\ts_nop 0\n\tglobal_load_lds_dwordx4 %1, %2\n\ts_mov_b32 m0, %0"
                 : "=&s"(sv) : "v"(voff), "s"(sbase), "s"(lds_base) : "memory");
}

__device__ __forceinline__ void glds16c(const char* sbase, unsigned voff, unsigned lds_base) {
    asm volatile("s_mov_b32 m0, %2\n\ts_nop 0\n\tglobal_load_lds_dwordx4 %0, %1 sc1" :: "v"(voff), "s"(sbase), "s"(lds_base) : "memory");
}
typedef __amdgpu_buffer_rsrc_t rsrc_t;
#define ST16(rs, v, voff, soff) __builtin_amdgcn_raw_buffer_store_b128((v), (rs), (voff) + (soff), 0, (ABL & 32) ? 0 : 16)
#define ST8(rs, v, voff, soff) __builtin_amdgcn_raw_buffer_store_b64((v), (rs), (voff), (soff), 16)
#define LD16(rs, voff, soff) __builtin_bit_cast(s16x8, __builtin_amdgcn_raw_buffer_load_b128((rs), (voff), (soff), 16))
#define LD8(rs, voff, soff) __builtin_bit_cast(s16x4, __builtin_amdgcn_raw_buffer_load_b64((rs), (voff), (soff), 16))
__device__ __forceinline__ f32x2 pk(const f32x16& a, int i) { return f32x2{a[i], a[i + 1]}; }
__device__ __forceinline__ f32x2 pk4(const f32x4& a, int i) { return f32x2{a[i], a[i + 1]}; }
__device__ __forceinline__ f32x2 pfma(f32x2 a, f32x2 b, f32x2 c) { return __builtin_elementwise_fma(a, b, c); }
__device__ __forceinline__ unsigned cvt2(f32x2 v) { return cvtpk(v[0], v[1]); }
__device__ __forceinline__ float add1(float a, float b) { float r = a + b; asm volatile("" : "+v"(r)); return r; }
__device__ __forceinline__ float fma1(float a, float b, float c) { float r = fmaf(a, b, c); asm volatile("" : "+v"(r)); return r; }
__device__ __forceinline__ float mul1(float a, float b) { float r = a * b; asm volatile("" : "+v"(r)); return r; }

__device__ __forceinline__ void raise_flag(char* ws, int arr, unsigned long long tag) {
    __hip_atomic_store((gu64*)(ws + arr) + blockIdx.x, tag, __ATOMIC_RELAXED, __HIP_MEMORY_SCOPE_AGENT);
}
struct Poll { u32x4 a, b; };
__device__ __forceinline__ void poll_request(Poll& p, const char* arr, unsigned voff) {
    asm volatile("global_load_dwordx4 %0, %2, %3 sc1\n\tglobal_load_dwordx4 %1, %2, %3 offset:1024 sc1" : "+v"(p.a), "+v"(p.b) : "v"(voff), "s"(arr) : "memory");
}
__device__ __forceinline__ void poll_wait(Poll& p, const char* arr, unsigned voff, unsigned long long tag) {
    const unsigned tl = (unsigned)tag, th = (unsigned)(tag >> 32);
#pragma unroll 1
    for (unsigned spin = 0; spin < (1u << 18); ++spin) {
        asm volatile("s_waitcnt vmcnt(0)" : "+v"(p.a), "+v"(p.b) :: "memory");
        if (__all(p.a[0] == tl && p.a[1] == th && p.a[2] == tl && p.a[3] == th && p.b[0] == tl && p.b[1] == th && p.b[2] == tl && p.b[3] == th)) break;
        __builtin_amdgcn_s_sleep(1);
        poll_request(p, arr, voff);
    }
    asm volatile("s_waitcnt vmcnt(0)" : "+v"(p.a), "+v"(p.b) :: "memory");
}

template <int MODE, class FM>
__device__ __forceinline__ void p1_pass(f32x4 (&acc)[4][MODE == 2 ? 6 : 4], lds_ptr x0, lds_ptr x1, lds_ptr (&wp)[MODE == 2 ? 2 : 4], int start, FM&& mid) {
    constexpr int NC = MODE == 2 ? 6 : 4, NR = 4 + 2 * NC, WS0 = MODE == 2 ? 1 : 0;
    struct F { s16x8 x[4]; s16x4 w[NC][2]; } f[2];
    auto rd = [&](F& n, int ks, int i) __attribute__((always_inline)) {
        if (i == 0 || i > 2 * NC) { const int m = i == 0 ? 0 : i - 2 * NC; n.x[m] = *(const LDS_AS s16x8*)((ks ? x1 : x0) + m * 2048); }
        else {
            const int t = (i - 1) >> 1, e = (i - 1) & 1, c = t % 3;
            n.w[t][e] = trread(MODE == 2 ? wp[c & 1] + ks * 12288 + (t / 3) * 3072 + (c >> 1) * 1024 + e * 256 : wp[MODE == 2 ? 0 : t] + ks * 8192 + e * 512);
        }
    };
    auto step = [&](const F& c, F& n, int ks, bool more) __attribute__((always_inline)) {
#pragma unroll
        for (int i = 0; i < 4 * NC; ++i) {
            const s16x8 w = CAT8(c.w[i % NC][0], c.w[i % NC][1]);
            f32x4& a = acc[i / NC][i % NC];
            a = MODE == 1 ? MFMA16(c.x[i / NC], w, a) : MFMA16(w, c.x[i / NC], a);
            PIN(a); SBAR();
            if (more && i < NR) { rd(n, ks, i); SBAR(); }
        }
    };
#pragma unroll
    for (int i = 0; i < NR; ++i) rd(f[0], 0, i);
    SBAR();
    static_for<0, 10>([&](auto N) __attribute__((always_inline)) {
        constexpr int n = N.v, dw = (WS0 + n) % 3 == 2 ? -2 * P1_WSLOT : P1_WSLOT;
        const int dx = n % 5 == 4 - start ? -4 * P1_XSLOT : P1_XSLOT;
        step(f[0], f[1], 1, true);
        x0 += dx; x1 += dx; PIN(x0); PIN(x1);
#pragma unroll
        for (int i = 0; i < (MODE == 2 ? 2 : 4); ++i) { wp[i] += dw; PIN(wp[i]); }
        mid(N);
        PSTAMP((MODE == 2 ? 22 : 2) + 2 * n);
        LGKM_BAR();
        PSTAMP((MODE == 2 ? 23 : 3) + 2 * n);
        step(f[1], f[0], 0, n < 9);
    });
}

struct Pack { const char *wd, *wo; int job; };
__device__ __forceinline__ void pack_fetch(const Pack& pk_, lds_ptr lds, int wid, int lane) {
    const int step = pk_.job / 10, cb = pk_.job - step * 10, r = lane >> 3;
    const unsigned va = ((r & 3) + (step < 48 ? 16 : 8) * (r >> 2)) * (D_ * 2) + (lane & 7) * 16, vb = ((r >> 2) * 20 + 16 + (r & 3)) * (D_ * 2) + (lane & 7) * 16;
#pragma unroll
    for (int g = 0; g < 4; ++g) {
        const int ks = (step - 48) * 2 + (g >> 1), hh = g & 1;
        const char* a = step < 48 ? pk_.wd + (step * 32 + g * 4) * (D_ * 2) : ks < 32 ? pk_.wo + (ks * 20 + hh * 4) * (D_ * 2) : pk_.wo + ((ks - 32) * 4 + hh * 2) * 20 * (D_ * 2);
        glds16s(a + cb * 128, step < 64 ? va : vb, (unsigned)(uintptr_t)lds + wid * P1_WSLOT + 16384 + g * 1024);
    }
}
__device__ __forceinline__ void pack_read(s16x8 (&pf)[4], lds_ptr lds, int wid, int lane) {
    asm volatile("s_waitcnt vmcnt(0)" ::: "memory");
    const lds_ptr pr = lds + wid * P1_WSLOT + 16384 + (lane >> 4) * 1024 + ((lane >> 2) & 3) * 128 + (lane & 3) * 8;
#pragma unroll
    for (int nt = 0; nt < 4; ++nt) pf[nt] = CAT8(trread(pr + nt * 32), trread(pr + nt * 32 + 512));
}
__device__ __forceinline__ void pack_store(const Pack& pk_, const s16x8 (&pf)[4], int lane, rsrc_t wsr) {
    const int step = pk_.job / 10, cb = pk_.job - step * 10;
#pragma unroll
    for (int nt = 0; nt < 4; ++nt) {
        const int N = cb * 4 + nt, cg = N / 5;
        ST16(wsr, __builtin_bit_cast(u32x4, pf[nt]), lane * 16, WS_WP + ((cg * 68 + step) * 5 + N - cg * 5) * 1024);
    }
}

constexpr int OOB = 0x7ffffff0;

__device__ __forceinline__ void p1_compute(lds_ptr lds, int wid, int lane, int panel, int grp, int ra, int rb, rsrc_t wsr, const Pack& pk_ STAMP_ARG) {
    const int l15 = lane & 15, g4 = lane >> 4, q = (lane >> 2) & 3, wm = wid & 1, wn = wid >> 1, tt = panel * 4 + wm * 2;
    const LDS_AS float* rstd = (const LDS_AS float*)(lds + P1_STAT) + wm * 64;
    const int xo = P1_X0 + (wm * 64 + l15) * 128 + ((g4 ^ (l15 >> 1)) << 4);
    if (pk_.job < 680) pack_fetch(pk_, lds, wid, lane);
    {
        f32x4 acc[4][4];
#pragma unroll
        for (int i = 0; i < 16; ++i) acc[i >> 2][i & 3] = f32x4{};
        s16x8 pf[4];
        const LDS_AS float* colf = (const LDS_AS float*)(lds + P1_COLA) + wn * 64;
        const int sec = grp / 5, q0 = (grp - sec * 5) * 32 + wn * 16;
        lds_ptr wp[4];
#pragma unroll
        for (int t = 0; t < 4; ++t) wp[t] = lds + g4 * 2048 + wn * 1024 + q * 128 + (lane & 3) * 8 + ((t ^ (q >> 1) ^ ((g4 & 1) << 1)) << 5);
        auto mid = [&](auto N) __attribute__((always_inline)) { if constexpr (N.v == 5) pack_read(pf, lds, min(wid, 2), lane); };
        PSTAMP(0);
        LGKM_BAR();
        PSTAMP(1);
        f32x2 t[32];
        int vo[4], vs;
        if (sec < 2) {
            p1_pass<0>(acc, lds + xo + ra * P1_XSLOT, lds + (xo ^ 64) + ra * P1_XSLOT, wp, ra, mid);
            STAMP(1);
            f32x4 cv[4]; float rs[4], nm[4];
#pragma unroll
            for (int i = 0; i < 4; ++i) { cv[i] = *(const LDS_AS f32x4*)(colf + i * 16 + g4 * 4); nm[i] = rstd[128 + i * 16 + l15]; rs[i] = rstd[i * 16 + l15]; }
            const float qs = sec ? 1.0f : 0.22360679774997896f * 1.4426950408889634f;
#pragma unroll
            for (int c = 0; c < 4; ++c) { const int qd = q0 + 4 * c + g4, hd = (qd * 205) >> 10; vo[c] = (sec ? WS_KV + tt * KV_TILE + hd * KV_HEAD : WS_Q + tt * Q_TILE + hd * Q_HEAD) + (qd - hd * 5) * 512 + l15 * 16; }
            vs = 256;
#pragma unroll
            for (int k = 0; k < 32; ++k) { const int m = (k & 2) + ((k >> 2) & 1); t[k] = pfma(pk4(cv[k >> 3], (k & 1) * 2), f32x2{nm[m], nm[m]}, pk4(acc[m][k >> 3], (k & 1) * 2)); }
            SBAR();
#pragma unroll
            for (int k = 0; k < 32; ++k) { const float r = rs[(k & 2) + ((k >> 2) & 1)] * qs; t[k] *= f32x2{r, r}; }
        } else {
            p1_pass<1>(acc, lds + xo + ra * P1_XSLOT, lds + (xo ^ 64) + ra * P1_XSLOT, wp, ra, mid);
            STAMP(1);
            f32x4 rs[4], nm[4]; float cv[4];
#pragma unroll
            for (int i = 0; i < 4; ++i) { cv[i] = colf[i * 16 + l15]; nm[i] = *(const LDS_AS f32x4*)(rstd + 128 + i * 16 + g4 * 4); rs[i] = *(const LDS_AS f32x4*)(rstd + i * 16 + g4 * 4); }
            const int so = WS_KV + tt * KV_TILE + KV_V;
#pragma unroll
            for (int c = 0; c < 4; ++c) { const int qd = q0 + 4 * c + q, hd = (qd * 205) >> 10, pl = qd - hd * 5; vo[c] = sec == 2 ? so + hd * KV_HEAD + pl * 256 + ((g4 ^ (pl & 3)) << 6) + (lane & 3) * 16 : OOB - KV_VT; }
            vs = KV_VT;
#pragma unroll
            for (int k = 0; k < 32; ++k) { const int m = (k >> 1) & 3; t[k] = pfma(pk4(nm[m], (k & 1) * 2), f32x2{cv[k >> 3], cv[k >> 3]}, pk4(acc[m][k >> 3], (k & 1) * 2)); }
            SBAR();
#pragma unroll
            for (int k = 0; k < 32; ++k) t[k] *= pk4(rs[(k >> 1) & 3], (k & 1) * 2);
            const int hd = q0 / 5 + g4, oo = sec == 2 && hd * 5 + 4 < q0 + 16 ? so + hd * KV_HEAD + 5 * 256 + l15 * 16 : OOB - KV_VT;
            const unsigned ov = (lane & 3) ? 0u : 0x3f803f80u;
            ST16(wsr, (u32x4{ov, ov, ov, ov}), oo, 0); ST16(wsr, (u32x4{ov, ov, ov, ov}), oo, KV_VT);
        }
        SBAR();
#pragma unroll
        for (int i = 0; i < 8; ++i) ST16(wsr, (u32x4{cvt2(t[4 * i]), cvt2(t[4 * i + 1]), cvt2(t[4 * i + 2]), cvt2(t[4 * i + 3])}), vo[i >> 1], (i & 1) * vs);
        if (pk_.job < 680) pack_store(pk_, pf, lane, wsr);
    }
    STAMP(6);
    f32x4 acc[4][6];
#pragma unroll
    for (int i = 0; i < 24; ++i) acc[i / 6][i % 6] = f32x4{};
    {
        const int wo0 = P1_WSLOT + (g4 >> 1) * 6144 + (g4 & 1) * 512 + q * 64 + (lane & 3) * 8, T0 = wn * 3, T1 = T0 + 1;
        lds_ptr wp[2] = {lds + wo0 + (T0 >> 1) * 1024 + (((T0 ^ g4) & 1) << 5), lds + wo0 + (T1 >> 1) * 1024 + (((T1 ^ g4) & 1) << 5)};
        p1_pass<2>(acc, lds + xo + rb * P1_XSLOT, lds + (xo ^ 64) + rb * P1_XSLOT, wp, rb, [&](auto N) __attribute__((always_inline)) {
            if constexpr (N.v == FA_POS - 11) asm volatile("s_waitcnt vmcnt(0)" ::: "memory");
        });
    }
    STAMP(3);
    {
        const LDS_AS float* colf = (const LDS_AS float*)(lds + P1_COLB) + wn * 48 + g4 * 4;
        f32x4 cg[3], cu[3];
#pragma unroll
        for (int c = 0; c < 3; ++c) { cg[c] = *(const LDS_AS f32x4*)(colf + c * 16); cu[c] = *(const LDS_AS f32x4*)(colf + 96 + c * 16); }
        float nm[4], r2[4];
#pragma unroll
        for (int m = 0; m < 4; ++m) { const float r = rstd[m * 16 + l15]; nm[m] = rstd[128 + m * 16 + l15]; r2[m] = r * r; }
        const int vo = lane * 16 + WS_H16 + (tt * 96 + grp * 6 + wn * 3) * 1024;
#pragma unroll
        for (int mm = 0; mm < 2; ++mm)
#pragma unroll
            for (int c = 0; c < 3; ++c) {
                float g[8], u[8];
#pragma unroll
                for (int i = 0; i < 8; ++i) { const int m = mm * 2 + (i >> 2); g[i] = fma1(cg[c][i & 3], nm[m], acc[m][c][i & 3]); u[i] = fma1(cu[c][i & 3], nm[m], acc[m][3 + c][i & 3]); }
#pragma unroll
                for (int i = 0; i < 8; ++i) { g[i] = fmaxf(g[i], 0.f); u[i] = mul1(u[i], r2[mm * 2 + (i >> 2)]); }
#pragma unroll
                for (int i = 0; i < 8; ++i) g[i] = mul1(g[i], u[i]);
                ST16(wsr, (u32x4{cvtpk(g[0], g[1]), cvtpk(g[2], g[3]), cvtpk(g[4], g[5]), cvtpk(g[6], g[7])}), vo, (mm * 96 + c) * 1024);
            }
    }
}

__device__ __forceinline__ void glds_x4(const char* sbase, const unsigned (&v)[4], unsigned lds_base) {
    asm volatile("s_mov_b32 m0, %5\n\ts_nop 0\n\tglobal_load_lds_dwordx4 %0, %4\n\tglobal_load_lds_dwordx4 %1, %4 offset:1024\n\t"
                 "global_load_lds_dwordx4 %2, %4 offset:2048\n\tglobal_load_lds_dwordx4 %3, %4 offset:3072"
                 :: "v"(v[0]), "v"(v[1]), "v"(v[2]), "v"(v[3]), "s"(sbase), "s"(lds_base) : "memory");
}
__device__ __forceinline__ void glds_x3(const char* sbase, const unsigned* v, unsigned lds_base) {
    asm volatile("s_mov_b32 m0, %4\n\ts_nop 0\n\tglobal_load_lds_dwordx4 %0, %3\n\tglobal_load_lds_dwordx4 %1, %3 offset:1024\n\tglobal_load_lds_dwordx4 %2, %3 offset:2048"
                 :: "v"(v[0]), "v"(v[1]), "v"(v[2]), "s"(sbase), "s"(lds_base) : "memory");
}

__device__ __forceinline__ unsigned long long p1_helper(lds_ptr lds, int hw, int lane, const char* xp, const char* wq, const char* wgp, const char* wup, int ra, int rb,
                                          char* ws, const char* kv, const char* qsrc) {
    const unsigned lds0 = (unsigned)(uintptr_t)lds;
    const int r = lane >> 3, pos = lane & 7;
    unsigned xv[4], wa[4], wb[6];
#pragma unroll
    for (int j = 0; j < 4; ++j) {
        xv[j] = 4096 + (hw * 32 + j * 8 + r) * (D_ * 2) + ((pos ^ ((j & 1) * 4 + (r >> 1))) << 4) - j * 1024;
        wa[j] = 4096 + (hw * 16 + (j >> 1) * 8 + r) * (3 * D_ * 2) + (j & 1) * 128 + ((pos ^ (((r >> 1) & 1) * 2 + (j >> 1) * 4)) << 4) - j * 1024;
    }
#pragma unroll
    for (int j = 0; j < 6; ++j) wb[j] = 4096 + (hw * 16 + (lane >> 2)) * (F_ * 2) + (j % 3) * 64 + (((lane & 3) ^ ((lane >> 5) * 2)) << 4) - (j % 3) * 1024;
    xp -= 4096; wq -= 4096; wgp -= 4096; wup -= 4096;
    int kb_ = 0, xs = ra, dws = 0, left = 5;
    auto issue1 = [&](bool pa) {
        if (ABL & 4) return;
        if (pa) glds_x4(wq + (unsigned)((kb_ + xs) * 64 * 3 * D_ * 2), wa, lds0 + dws * P1_WSLOT + hw * 4096);
        else glds_x3(wgp + (unsigned)((kb_ + xs) * 64 * F_ * 2), wb, lds0 + dws * P1_WSLOT + hw * 6144);
        SBAR();
    };
    auto issue2 = [&](bool pa, bool xx) {
        SBAR();
        if (ABL & 4) return;
        if (!pa) glds_x3(wup + (unsigned)((kb_ + xs) * 64 * F_ * 2), wb + 3, lds0 + dws * P1_WSLOT + hw * 6144 + 3072);
        xs = xs == 4 ? 0 : xs + 1; dws = dws == 2 ? 0 : dws + 1; --left; kb_ = left == 0 ? 5 - kb_ : kb_; left = left == 0 ? 5 : left;
        if (xx) glds_x4(xp + (kb_ + xs) * 128, xv, lds0 + P1_X0 + xs * P1_XSLOT + hw * 4096);
    };
    glds_x4(xp + ra * 128, xv, lds0 + P1_X0 + ra * P1_XSLOT + hw * 4096);
    issue1(true); issue2(true, true);
    __builtin_amdgcn_s_barrier();
    unsigned long long ep = 0;
    asm volatile("global_load_dwordx2 %0, %1, %2 sc1" : "+v"(ep) : "v"(0u), "s"(ws + WS_EPOCH) : "memory");
    __builtin_amdgcn_s_setprio(HPRIO);
    const int r16 = lane & 15, h = lane >> 5, blk = (lane >> 4) & 1, q = (lane >> 2) & 3;
    const unsigned olo = r16 < 2 ? 0x3f803f80u : 0u;
    const s16x8 ones2 = __builtin_bit_cast(s16x8, (u32x4{olo, olo, olo, olo}));
    const int idx = (r16 & 1) ? 0xEEEE : 0x4444;
    int ws3 = 0;
    {
        const int bo = (lane >> 4) * 2048 + (hw >> 1) * 1024 + q * 128 + (lane & 3) * 8 + ((((hw & 1) * 2) ^ (q >> 1) ^ (blk << 1)) << 5);
        lds_ptr b = lds + bo, b1 = lds + (bo ^ 32);
        const int so = P1_X0 + ra * P1_XSLOT + (hw * 32 + r16) * 128 + (((lane >> 4) ^ (r16 >> 1)) << 4);
        lds_ptr sp0 = lds + so, sp1 = lds + (so ^ 64);
        f32x4 cs = f32x4{}, cs1 = f32x4{}, s1[2] = {f32x4{}, f32x4{}}, s2[2] = {f32x4{}, f32x4{}};
        s16x16 wv0, wv1; s16x8 xa[2], xb[2];
        auto col_rd = [&]() {
            wv0 = CAT16(CAT8(trread(b), trread(b + 512)), CAT8(trread(b + 8192), trread(b + 8192 + 512)));
            wv1 = CAT16(CAT8(trread(b1), trread(b1 + 512)), CAT8(trread(b1 + 8192), trread(b1 + 8192 + 512)));
            const int dw = ws3 == 2 ? -2 * P1_WSLOT : P1_WSLOT;
            b += dw; b1 += dw; ws3 = ws3 == 2 ? 0 : ws3 + 1;
        };
        auto row_rd = [&](int dx) {
#pragma unroll
            for (int t = 0; t < 2; ++t) { xa[t] = *(const LDS_AS s16x8*)(sp0 + t * 2048); xb[t] = *(const LDS_AS s16x8*)(sp1 + t * 2048); }
            sp0 += dx; sp1 += dx;
        };
        auto row_mm = [&]() {
#pragma unroll
            for (int t = 0; t < 2; ++t) { s2[t] = MFMA16(xa[t], xa[t], s2[t]); s1[t] = SMF16(ones2, CAT16(xa[t], xb[t]), s1[t], idx); }
#pragma unroll
            for (int t = 0; t < 2; ++t) { s2[t] = MFMA16(xb[t], xb[t], s2[t]); PIN(s1[t]); PIN(s2[t]); }
        };
#pragma unroll
        for (int nn = 0; nn < 9; ++nn) {
            int n = nn;
            if (!HUNR) asm volatile("" : "+s"(n));
            PSTAMP(2 * n);
            if (n == 0) WAIT_BAR(5); else if (n < 8) WAIT_BAR(12); else WAIT_BAR(8);
            PSTAMP(2 * n + 1);
            if (!(ABL & 8) || n == 0) { col_rd(); row_rd(n % 5 == 4 - ra ? -4 * P1_XSLOT : P1_XSLOT); }
            if (n == 0) { issue1(true); issue2(true, true); }
            if (n == 8) { kb_ = 5; xs = rb; }
            issue1(n < 8);
            if (!(ABL & 8)) {
            cs = SMF16(ones2, wv0, cs, idx);
            row_mm();
            cs1 = SMF16(ones2, wv1, cs1, idx); PIN(cs); PIN(cs1); }
            issue2(n < 8, n < 7);
        }
        MEMFENCE(); __builtin_amdgcn_s_waitcnt(0xF70 | 10); MEMFENCE();
        row_rd(0); row_mm();
        {
            const int t = blk;
            const float d0 = (lane & 2) ? ((lane & 1) ? s2[0][3] : s2[0][2]) : ((lane & 1) ? s2[0][1] : s2[0][0]);
            const float d1 = (lane & 2) ? ((lane & 1) ? s2[1][3] : s2[1][2]) : ((lane & 1) ? s2[1][1] : s2[1][0]);
            const float a0 = __shfl(s1[0][0] + s1[0][1], r16), a1 = __shfl(s1[1][0] + s1[1][1], r16), c0 = __shfl(d0, r16 + 16 * q), c1 = __shfl(d1, r16 + 16 * q);
            const float a = t ? a1 : a0, c = t ? c1 : c0;
            const float mean = a * (1.0f / D_), var = fmaxf(c * (1.0f / D_) - mean * mean, 0.f);
            const float rstd = __builtin_amdgcn_rcpf(rbf(__builtin_amdgcn_sqrtf(rbf(rbf(var) + 1e-5f))));
            ((LDS_AS float*)(lds + P1_STAT))[h * 128 + hw * 32 + (lane & 31)] = h ? -rbf(mean) : rstd;
        }
        PSTAMP(18);
        WAIT_BAR(6);
        PSTAMP(19);
        col_rd();
        issue1(false);
        cs = SMF16(ones2, wv0, cs, idx);
        cs1 = SMF16(ones2, wv1, cs1, idx); PIN(cs); PIN(cs1);
        issue2(false, false);
        LDS_AS float* colf = (LDS_AS float*)(lds + P1_COLA) + hw * 32;
        if (lane < 16) { colf[lane] = cs[0] + cs[1]; colf[16 + lane] = cs1[0] + cs1[1]; }
    }
    asm volatile("" : "+v"(ep));
    const unsigned long long tag = (((unsigned long long)(unsigned)__builtin_amdgcn_readfirstlane((unsigned)(ep >> 32)) << 32) | (unsigned)__builtin_amdgcn_readfirstlane((unsigned)ep)) + 1ull;
    if (hw == 0 && lane == 0) *(LDS_AS unsigned long long*)(lds + P1_TAG) = tag;
    {
        const int g4 = lane >> 4, ro = P1_WSLOT + (g4 >> 1) * 6144 + (g4 & 1) * 512 + q * 64 + (lane & 3) * 8, T0 = hw * 3, T1 = T0 + 1;
        lds_ptr b = lds + ro + (T0 >> 1) * 1024 + (((T0 ^ g4) & 1) << 5), c = lds + ro + (T1 >> 1) * 1024 + (((T1 ^ g4) & 1) << 5);
        f32x4 cs = f32x4{}, ct = f32x4{}, cu = f32x4{};
        Poll pl = {};
#pragma unroll
        for (int nn = 10; nn < 20; ++nn) {
            int n = nn;
            if (!HUNR) asm volatile("" : "+s"(n));
            PSTAMP(2 * n);
            if (n < 13 || n == 19) WAIT_BAR(6); else if (n == 13) WAIT_BAR(10); else if (n == 18) WAIT_BAR(12); else WAIT_BAR(14);
            PSTAMP(2 * n + 1);
            const s16x16 wv0 = CAT16(CAT8(trread(b), trread(b + 256)), CAT8(trread(b + 12288), trread(b + 12288 + 256)));
            const s16x16 wv1 = CAT16(CAT8(trread(c), trread(c + 256)), CAT8(trread(c + 12288), trread(c + 12288 + 256)));
            const s16x16 wv2 = CAT16(CAT8(trread(b + 1024), trread(b + 1024 + 256)), CAT8(trread(b + 13312), trread(b + 13312 + 256)));
            if (n == FA_POS && hw == 0 && lane == 0) raise_flag(ws, WS_FA, tag);
            if (n < 18) issue1(false);
            cs = SMF16(ones2, wv0, cs, idx);
            ct = SMF16(ones2, wv1, ct, idx);
            cu = SMF16(ones2, wv2, cu, idx); PIN(cs); PIN(ct); PIN(cu);
            if (n < 18) issue2(false, n >= 12 && n < 17);
            if (n == 17) poll_request(pl, ws + WS_FA, lane * 16);
            if (n == 18) {
                poll_wait(pl, ws + WS_FA, lane * 16, tag);
#pragma unroll
                for (int j = 0; j < 6; ++j) glds16c(kv + (hw + 4 * j) * 1024, lane * 16, lds0 + K2_C0 + (hw + 4 * j) * 1024);
            }
            if (n == 19) {
#pragma unroll
                for (int j = 0; j < 3; ++j) glds16c(qsrc + (hw + 4 * j) * 1024, lane * 16, lds0 + K2_Q0 + (hw + 4 * j) * 1024);
            }
            const int dw = ws3 == 2 ? -2 * P1_WSLOT : P1_WSLOT;
            b += dw; c += dw; ws3 = ws3 == 2 ? 0 : ws3 + 1;
        }
        LDS_AS float* colf = (LDS_AS float*)(lds + P1_COLB) + hw * 48;
        if (lane < 16) { colf[lane] = cs[0] + cs[1]; colf[16 + lane] = ct[0] + ct[1]; colf[32 + lane] = cu[0] + cu[1]; }
        WAIT_BAR(0);
        if (blockIdx.x == 0 && hw == 0 && lane == 0) __hip_atomic_store((gu64*)(ws + WS_EPOCH), tag, __ATOMIC_RELAXED, __HIP_MEMORY_SCOPE_AGENT);
#pragma unroll
        for (int j = 0; j < 6; ++j) glds16c(kv + K2_CHUNK + (hw + 4 * j) * 1024, lane * 16, lds0 + K2_KV0 + K2_SLOTB + (hw + 4 * j) * 1024);
    }
    return tag;
}

constexpr int K2_XCH = 2 * K2_SLOTB;

#define SMF32(a, b, c) __builtin_amdgcn_smfmac_f32_32x32x32_bf16((bfx8)(a), __builtin_bit_cast(bfx16, (b)), (c), 0xDDDD, 0, 0)
#define MFMA8(a, b, c) __builtin_amdgcn_mfma_f32_32x32x8bf16_1k((a), (b), (c), 0, 0, 0)
__device__ __forceinline__ f32x8 lo8(const f32x16& v) { return __builtin_shufflevector(v, v, 0, 1, 2, 3, 4, 5, 6, 7); }
__device__ __forceinline__ f32x8 hi8(const f32x16& v) { return __builtin_shufflevector(v, v, 8, 9, 10, 11, 12, 13, 14, 15); }
__device__ __forceinline__ float max16(const f32x16& s) {
    float m = fmaxf(fmaxf(s[0], s[1]), s[2]);
#pragma unroll
    for (int r = 3; r < 15; r += 2) m = fmaxf(fmaxf(m, s[r]), s[r + 1]);
    return swap32_max(fmaxf(m, s[15]));
}

struct K2B { lds_ptr r0, r1, r2; const char *wsrc, *wsa, *wsb; unsigned wdst; int lane, osoff; rsrc_t wsr; };
constexpr int k2_slot(int st) { return st < 3 ? st : st == 3 ? 0 : st == 4 ? 3 : st == 5 ? 1 : st == 6 ? 2 : st == 7 ? 0 : 3; }
__device__ __forceinline__ void k2_wpiece(const K2B& c, int st, int i) {
    glds16c((st < 3 ? c.wsa : st < 6 ? c.wsb : c.wsrc) + st * 40960 + i * 8192, c.lane * 16, c.wdst + k2_slot(st) * 40960 + i * 8192);
}
template <int I, int D, int NP, int OF>
__device__ __forceinline__ void k2_bstep(const K2B& c, f32x4 (&acc)[10], const s16x8 (&w)[5], s16x8 (&n)[5], const s16x8& a0, const s16x8& a1, s16x8 (&of)[10]) {
    constexpr int O = k2_slot((I + 1) >> 1) * 40960 + ((I + 1) & 1) * 20480;
#pragma unroll
    for (int i = 0; i < 10; ++i) {
        if (I > 0 && I < 16 && i < 5) n[i] = *(const LDS_AS s16x8*)((O < 61440 ? c.r0 : O < 122880 ? c.r1 : c.r2) + O % 61440 + i * 1024);
        acc[i] = MFMA16(w[i >> 1], (i & 1) ? a1 : a0, acc[i]);
        PIN(acc[i]);
        if (OF >= 0 && !(i & 1)) { const int k = (OF < 0 ? 0 : OF) + (i >> 1); of[k] = LD16(c.wsr, c.lane * 16, c.osoff + ((k & 1) * 20 + (k >> 1) * 4) * 1024); MEMFENCE(); }
        if ((i & 1) && (i >> 1) < NP) k2_wpiece(c, D, (i >> 1) + (D == 4));
        SBAR();
    }
}

__global__ __launch_bounds__(512, 2) void fused_block(const char* __restrict__ x, const char* __restrict__ wqkv, const char* __restrict__ wg, const char* __restrict__ wu,
                                                      const char* __restrict__ wd, const char* __restrict__ wo, char* __restrict__ ws, char* __restrict__ out) {
    extern __shared__ __attribute__((aligned(16))) char smem[];
    asm volatile("" :: "s"(x), "s"(wqkv), "s"(wg), "s"(wu), "s"(wd), "s"(wo), "s"(ws), "s"(out));
    const int wid = __builtin_amdgcn_readfirstlane(threadIdx.x >> 6);
    STAMP_DECL; STAMP(0); PSTAMP(63);
    unsigned long long tag = 0;
    if (wid < 4) {
        __builtin_amdgcn_s_barrier();
        if (CODEWARM && wid == 3) {
            unsigned off = ((blockIdx.x >> 3) * 13 + min(threadIdx.x & 63, 12)) * 128;
            asm volatile("s_getpc_b64 vcc\n.Lfb_begin:\n\tv_min_u32 %0, .Lfb_end-.Lfb_begin, %0\n\ts_mov_b32 m0, %1\n\ts_nop 0\n\tglobal_load_lds_dword %0, vcc"
                         : "+v"(off) : "s"((unsigned)(uintptr_t)(lds_ptr)smem + LDS_JUNK) : "vcc", "memory");
        }
    }
    const rsrc_t wsr = __builtin_amdgcn_make_buffer_rsrc(ws, 0, (int)WS_END, 0x00020000);
    const int bid = blockIdx.x, head = (bid & 7) * 4 + ((bid >> 3) & 3), par = wid >> 2, qt0 = (bid >> 5) * 8 + (wid & 3) * 2;
    const char* kv = ws + WS_KV + head * KV_HEAD;
    {
        const int bi = bid >> 3, panel = (bid & 1) * 8 + (bi & 7), grp = ((bid & 7) >> 1) * 4 + (bi >> 3), l0 = threadIdx.x & 63;
        const int rb = K1_ROT ? (5 * (bi & 7)) >> 3 : 0, ra = ROTA == 1 ? (rb + (bi >> 3)) % 5 : ROTA == 2 ? ((bi & 1) * 2 + ((bi >> 3) & 1) * 2) % 4 : 0;
        if (wid < 4) {
            p1_compute((lds_ptr)smem, wid, l0, panel, grp, ra, rb, wsr, Pack{wd, wo, wid * 256 + bid} STAMP_PASS);
            const u32x2 e = *(const LDS_AS u32x2*)((lds_ptr)smem + P1_TAG);
            tag = ((unsigned long long)(unsigned)__builtin_amdgcn_readfirstlane(e[1]) << 32) | (unsigned)__builtin_amdgcn_readfirstlane(e[0]);
        } else {
            tag = p1_helper((lds_ptr)smem, wid - 4, l0, x + (size_t)panel * 128 * (D_ * 2), wqkv + min(grp, 14) * 256, wg + grp * 192, wu + grp * 192, ra, rb,
                            ws, kv, ws + WS_Q + head * Q_HEAD + (bid >> 5) * 8 * Q_TILE);
        }
    }
    int lane = __builtin_amdgcn_mbcnt_hi(~0u, __builtin_amdgcn_mbcnt_lo(~0u, 0u));
    PIN(lane);
    if (wid < 4) __builtin_amdgcn_s_setprio(3); else __builtin_amdgcn_s_setprio(0);
    STAMP(4);
    lds_ptr lds = (lds_ptr)smem + K2_KV0;
    const unsigned lds0 = (unsigned)(uintptr_t)lds;
    const int tp = ((blockIdx.x & 7) >> 1) * 8 + ((blockIdx.x >> 3) & 7), cg = (blockIdx.x & 1) * 4 + (blockIdx.x >> 6);
    K2B kb;
    kb.wsrc = ws + WS_WP + cg * 68 * 5120 + wid * 1024; kb.wdst = (unsigned)(uintptr_t)(lds_ptr)smem + wid * 1024; kb.lane = lane;
    const int sw3 = P3ROT ? tp & 1 : 0;
    kb.wsa = kb.wsrc + sw3 * 122880; kb.wsb = kb.wsrc - sw3 * 122880;
    const int hoff = WS_H16 + ((tp * 2 + (wid & 1)) * 96 + (wid >> 1) * 2 + sw3 * 48) * 1024, hoff1 = hoff + (1 - 2 * sw3) * 49152;
    s16x8 hf[24];
    const int h = lane >> 5, l31 = lane & 31, l16 = lane * 16;
    const char* src = kv + wid * 1024 + 3 * K2_CHUNK;
    const int kr = ((l31 & 4) << 2) | ((l31 >> 1) & 12) | (l31 & 3);
    const int k1off = h * 1024 + kr * 16, k2off = h ? KV_V + 5 * 256 : 2048 + kr * 16;
    const int vq = min(l31 >> 2, 5), voff = KV_V + vq * 256 + ((h ^ (vq & 3)) << 6) + (l31 < 24 ? l31 & 3 : 1) * 16;

    f32x16 oa[2] = {f32x16{}, f32x16{}}, sc[2];
    s16x8 k1[2], v0[2], v1[2]; s16x4 k2[2];
    const lds_ptr c0 = (lds_ptr)smem + K2_C0;
    lds_ptr pk1 = c0 + k1off + par * 8, pk2 = c0 + k2off + par * 8, pv = c0 + voff + par * KV_VT, pw = c0 + (voff ^ 128) + par * KV_VT;
    lds_ptr pk3 = pk1 + 512;
    asm volatile("" : "+v"(pk3));
#define LDS8(p) (*(const LDS_AS s16x4*)(p))
#define RD_K(t, j) do { k1[(t) & 1] = CAT8(LDS8(pk1 + (j) * 2 * KV_TILE), LDS8(pk3 + (j) * 2 * KV_TILE)); k2[(t) & 1] = LDS8(pk2 + (j) * 2 * KV_TILE); } while (0)
#define RD_V(t, j) do { v0[(t) & 1] = *(const LDS_AS s16x8*)(pv + (j) * 2 * KV_TILE); v1[(t) & 1] = *(const LDS_AS s16x8*)(pw + (j) * 2 * KV_TILE); } while (0)
    s16x8 q1[2]; s16x4 q2[2];
#pragma unroll
    for (int b = 0; b < 2; ++b) {
        const lds_ptr qp = (lds_ptr)smem + K2_Q0 + (wid & 3) * 2 * Q_TILE + b * 8 + l31 * 16;
        q1[b] = CAT8(LDS8(qp + h * 1024), LDS8(qp + h * 1024 + 512)); q2[b] = LDS8(qp + 2048);
    }
    {
        const s16x8 ka = CAT8(LDS8(c0 + k1off), LDS8(c0 + k1off + 512));
        const s16x4 kb = LDS8(c0 + k2off);
        RD_K(0, 0); RD_V(0, 0);
#pragma unroll
        for (int b = 0; b < 2; ++b) {
            if (h) q2[b] = s16x4{0, 0, 0, 0};
            f32x16 s = MFMA32(ka, q1[b], f32x16{});
            s = MFMA8(kb, q2[b], s);
            const float m = rbf(-max16(s));
            if (h) q2[b][0] = (short)(__float_as_uint(m) >> 16);
        }
        sc[0] = MFMA32(k1[0], q1[0], f32x16{});
        sc[0] = MFMA8(k2[0], q2[0], sc[0]);
        sc[1] = f32x16{}; v1[1] = s16x8{};
    }
    STAMP(5); PSTAMP(42); PSTAMP(43);
#define EXP4(S, i) do { S[i] = __builtin_amdgcn_exp2f(S[i]); S[i + 1] = __builtin_amdgcn_exp2f(S[i + 1]); S[i + 2] = __builtin_amdgcn_exp2f(S[i + 2]); S[i + 3] = __builtin_amdgcn_exp2f(S[i + 3]); PIN(S); SBAR(); } while (0)
    Poll pl = {};
    auto request = [&](auto HS) __attribute__((always_inline)) {
        constexpr int hs = HS.v, p = (hs - 6) >> 3, r = (hs - 6) & 7;
        constexpr int w = (hs >= 9 && hs < 14) ? hs - 9 : (hs >= 17 && hs < 22) ? hs - 12 : (hs >= 54 && hs < 57) ? hs - 44 : hs >= 62 ? hs - 49 : -1;
        constexpr int e = (p >= 2 && p <= 4 && r >= 3) ? (p - 2) * 5 + r - 3 : p == 5 ? 15 + r : hs == 57 ? 23 : -1;
        if constexpr (hs < 3) glds16c(src - K2_CHUNK + hs * 8192, l16, lds0 + 2 * K2_SLOTB + wid * 1024 + hs * 8192);
        else if constexpr (hs >= 6 && p < 5 && r < 3) glds16c(src + p * K2_CHUNK + r * 8192, l16, lds0 + (p % 3) * K2_SLOTB + wid * 1024 + r * 8192);
        else if constexpr (w >= 0) k2_wpiece(kb, w / 5, w % 5);
        else if constexpr (e >= 0) { hf[e] = LD16(wsr, l16, (e < 12 ? hoff : hoff1) + ((e % 12) >> 1) * 8192 + (e & 1) * 1024); MEMFENCE(); }
        if constexpr (hs == 58) { if (DIAG_PF2) { PFLUSH(); } k2_wpiece(kb, 4, 4); }
        if constexpr (hs == 63) k2_wpiece(kb, 4, 0);
        if constexpr (WOWARM && hs == 3) {
            const unsigned wo_ = 6 * 40960 + min((((blockIdx.x >> 3) & 7) * 8 + wid) * 13 + min(lane, 12), 799) * 128;
            asm volatile("s_mov_b32 m0, %2\n\ts_nop 0\n\tglobal_load_lds_dword %0, %1 sc1" :: "v"(wo_), "s"(kb.wsrc - wid * 1024), "s"(kb.wdst - wid * 768 + LDS_JUNK) : "memory");
        }
        if (hs == 6 && wid == 4 && lane == 0) raise_flag(ws, WS_FB, tag);
        if (hs == 16 && wid == 4) poll_request(pl, ws + WS_FB, l16);
        SBAR();
    };
    static_for<0, 32>([&](auto T) __attribute__((always_inline)) {
        {
            constexpr int c = T.v >> 2, j = T.v & 3;
            if (j == 3) {
                PSTAMP(44 + 2 * c);
                if (c == 2 && wid == 4) poll_wait(pl, ws + WS_FB, l16, tag);
                if (c == 0) { asm volatile("s_waitcnt vmcnt(0)" ::: "memory"); LGKM_BAR(); } else if (c == 7) WAIT_BAR(0); else WAIT_BAR(7);
                PSTAMP(45 + 2 * c);
                __builtin_amdgcn_s_setprio(2);
                const int dsl = c == 0 ? K2_KV0 + K2_SLOTB - K2_C0 : (c % 3 == 2) ? -2 * K2_SLOTB : K2_SLOTB;
                pk1 += dsl; pk2 += dsl; pk3 += dsl; pv += dsl; pw += dsl;
                asm volatile("" : "+v"(pk1), "+v"(pk2), "+v"(pk3), "+v"(pv), "+v"(pw));
                SBAR();
            }
            RD_K(j + 1, (j + 1) & 3); SBAR();
            oa[1] = SMF32(v1[(j + 1) & 1], hi8(sc[1]), oa[1]); PIN(oa[1]); SBAR();
            EXP4(sc[0], 0);
            sc[1] = MFMA32(k1[j & 1], q1[1], f32x16{}); PIN(sc[1]); SBAR();
            request(IC<8 * c + 2 * j>{});
            EXP4(sc[0], 4);
            sc[1] = MFMA8(k2[j & 1], q2[1], sc[1]); PIN(sc[1]); SBAR();
            EXP4(sc[0], 8);
            oa[0] = SMF32(v0[j & 1], lo8(sc[0]), oa[0]); PIN(oa[0]); SBAR();
            EXP4(sc[0], 12);
            RD_V(j + 1, (j + 1) & 3);
            if (j == 1 && c > 0) __builtin_amdgcn_s_setprio(1);
            if (j == 2 && c > 0) __builtin_amdgcn_s_setprio(0);
            SBAR();
            oa[0] = SMF32(v1[j & 1], hi8(sc[0]), oa[0]); PIN(oa[0]); SBAR();
            EXP4(sc[1], 0);
            sc[0] = MFMA32(k1[(j + 1) & 1], q1[0], f32x16{}); PIN(sc[0]); SBAR();
            request(IC<8 * c + 2 * j + 1>{});
            EXP4(sc[1], 4);
            sc[0] = MFMA8(k2[(j + 1) & 1], q2[0], sc[0]); PIN(sc[0]); SBAR();
            EXP4(sc[1], 8);
            oa[1] = SMF32(v0[j & 1], lo8(sc[1]), oa[1]); PIN(oa[1]); SBAR();
            EXP4(sc[1], 12);
        }
    });
    oa[1] = SMF32(v1[1], hi8(sc[1]), oa[1]);
    unsigned qs_ = 0; (void)qs_;
    STAMP(2);
    PSTAMP(60); QSTAMP(60);
    int ln = __builtin_amdgcn_mbcnt_hi(~0u, __builtin_amdgcn_mbcnt_lo(~0u, 0u));
    PIN(ln);
    const int h2 = ln >> 5, l2 = ln & 31;
    const int mt = wid & 1, kq = wid >> 1;
    kb.lane = ln; kb.r0 = (lds_ptr)smem + kq * 5120 + ln * 16; kb.r1 = kb.r0 + 61440; kb.r2 = kb.r0 + 122880;
    asm volatile("" : "+v"(kb.r1), "+v"(kb.r2));
    kb.wsr = wsr; kb.osoff = WS_O16 + ((tp * 4 + mt * 2) * 20 + kq) * 1024;
    s16x8 wa[5], wb[5];
#pragma unroll
    for (int i = 0; i < 5; ++i) { wa[i] = *(const LDS_AS s16x8*)(kb.r0 + i * 1024); wb[i] = *(const LDS_AS s16x8*)(kb.r0 + 20480 + i * 1024); }
    {
        const lds_ptr xp_ = lds + K2_XCH + (wid & 3) * 6144 + ln * 16;
        if (par) {
#pragma unroll
            for (int i = 0; i < 6; ++i) { const f32x16& g = oa[i / 3]; const int r = 4 * (i % 3); *(LDS_AS f32x4*)(xp_ + i * 1024) = f32x4{g[r], g[r + 1], g[r + 2], g[r + 3]}; }
        }
        LGKM_BAR();
        QSTAMP(21);
        if (!par) {
            __builtin_amdgcn_s_setprio(3);
            f32x4 tt[6];
#pragma unroll
            for (int i = 0; i < 6; ++i) tt[i] = *(const LDS_AS f32x4*)(xp_ + i * 1024);
            SBAR();
#pragma unroll
            for (int i = 0; i < 6; ++i) { const f32x4 t = tt[i]; f32x16& g = oa[i / 3]; const int r = 4 * (i % 3); g[r] = add1(g[r], t[0]); g[r + 1] = add1(g[r + 1], t[1]); g[r + 2] = add1(g[r + 2], t[2]); g[r + 3] = add1(g[r + 3], t[3]); }
            const int ob = (l2 >> 4) * 20 * 1024 + (l2 & 15) * 16, os = WS_O16 + qt0 * 2 * 20 * 1024;
            const int o1 = ob + h2 * 256 + os + (head >> 1) * 1024 + (head & 1) * 512, o2 = os + (16 + (head >> 3)) * 1024 + ((head >> 1) & 3) * 256 + (head & 1) * 8;
            auto put = [&](const f32x16& o, int b, u32x2& e) {
                const float l = __uint_as_float(__builtin_amdgcn_permlane32_swap(__float_as_uint(o[8]), __float_as_uint(o[8]), false, false)[1]);
                const float rl = __builtin_amdgcn_rcpf(l);
                unsigned w[6];
#pragma unroll
                for (int i = 0; i < 6; ++i) w[i] = cvtpk(mul1(o[2 * i], rl), mul1(o[2 * i + 1], rl));
                ST16(wsr, (u32x4{w[0], w[1], w[2], w[3]}), o1, b * 40 * 1024);
                e = u32x2{w[4], w[5]};
                return l;
            };
            u32x2 e0, e1;
            QSTAMP(22);
            const float l0 = put(oa[0], 0, e0), l1 = put(oa[1], 1, e1);
            ST8(wsr, (u32x2{__builtin_amdgcn_permlane32_swap(e0[0], e1[0], false, false)[0], __builtin_amdgcn_permlane32_swap(e0[1], e1[1], false, false)[0]}), ob + h2 * 40 * 1024, o2);
            MEMFENCE(); SBAR();
            QSTAMP(23);
            float big = 0.f;
#pragma unroll
            for (int r = 0; r < 12; ++r) big = add1(big, add1(fabsf(oa[0][r]), fabsf(oa[1][r])));
            if (__builtin_expect(__any(!(big < 1e30f) | !(fminf(l0, l1) > 1e-30f)), 0)) {
                const int gr = ((l2 & 4) << 2) | ((l2 >> 1) & 12) | (l2 & 3), g1 = h2 * 1024 + gr * 16, g2 = 2048 + gr * 16, gq = min(l2 >> 2, 5), gv = KV_V + gq * 256 + ((h2 ^ (gq & 3)) << 6) + (l2 & 3) * 16;
                asm volatile("s_waitcnt vmcnt(0)" ::: "memory");
#pragma unroll 1
                for (int b = 0; b < 2; ++b) {
                    const s16x8 qf1 = b ? q1[1] : q1[0];
                    s16x4 qf2 = b ? q2[1] : q2[0];
                    if (h2) qf2[0] = 0;
                    float m = -INFINITY;
                    f32x16 negm, o = f32x16{};
#pragma unroll 1
                    for (int t = 0; t < 64; ++t) {
                        const int g = WS_KV + head * KV_HEAD + (t >> 1) * 2 * KV_TILE + (t & 1) * 8;
                        f32x16 s = MFMA32(CAT8(LD8(wsr, g1, g), LD8(wsr, g1 + 512, g)), qf1, f32x16{});
                        s = MFMA8(LD8(wsr, g2, g), qf2, s);
                        m = fmaxf(m, max16(s));
                    }
#pragma unroll
                    for (int r = 0; r < 16; ++r) negm[r] = -m;
#pragma unroll 1
                    for (int t = 0; t < 64; ++t) {
                        const int g = WS_KV + head * KV_HEAD + (t >> 1) * 2 * KV_TILE + (t & 1) * 8;
                        f32x16 s = MFMA32(CAT8(LD8(wsr, g1, g), LD8(wsr, g1 + 512, g)), qf1, negm);
                        s = MFMA8(LD8(wsr, g2, g), qf2, s);
#pragma unroll
                        for (int r = 0; r < 16; ++r) s[r] = __builtin_amdgcn_exp2f(s[r]);
                        o = SMF32(LD16(wsr, gv, g + (t & 1) * (KV_VT - 8)), lo8(s), o);
                        o = SMF32(LD16(wsr, gv ^ 128, g + (t & 1) * (KV_VT - 8)), hi8(s), o);
                    }
                    u32x2 e;
                    put(o, b, e);
                    if (!h2) ST8(wsr, e, ob + b * 40 * 1024, o2);
                }
            }
        }
    }
    PSTAMP(61); QSTAMP(61);
    __builtin_amdgcn_s_setprio(0);
    const int ch0 = wid * 128 + ln, ch1 = ch0 + 64, er0 = ch0 / 10, er1 = ch1 / 10, ec0 = ch0 - er0 * 10, ec1 = ch1 - er1 * 10;
    const size_t eb = (size_t)tp * 64 * (D_ * 2) + cg * 160;
    const unsigned eo0 = er0 * (D_ * 2) + ec0 * 16, eo1 = er1 * (D_ * 2) + ec1 * 16;
    f32x4 acc[10];
#pragma unroll
    for (int i = 0; i < 10; ++i) acc[i] = f32x4{};
    s16x8 of[10];
#define HLO(i) __builtin_shufflevector(hf[2 * (i)], hf[2 * (i) + 1], 0, 1, 2, 3, 8, 9, 10, 11)
#define HHI(i) __builtin_shufflevector(hf[2 * (i)], hf[2 * (i) + 1], 4, 5, 6, 7, 12, 13, 14, 15)
    QSTAMP(0); k2_bstep<0, 0, 0, -1>(kb, acc, wa, wb, HLO(0), HHI(0), of);
    QSTAMP(1); k2_bstep<1, 0, 0, -1>(kb, acc, wb, wa, HLO(1), HHI(1), of);
    QSTAMP(2); k2_bstep<2, 0, 0, -1>(kb, acc, wa, wb, HLO(2), HHI(2), of);
    if (!par) {
        asm volatile("s_waitcnt vmcnt(0)" ::: "memory");
        if (ln == 0) __hip_atomic_store((gu64*)(ws + WS_FC) + (qt0 >> 1) * 32 + head, tag, __ATOMIC_RELAXED, __HIP_MEMORY_SCOPE_AGENT);
    } else {
#pragma unroll
        for (int i = 0; i < 10; ++i) glds16c(kb.wsb + 3 * 40960 - 4096 + i * 4096, ln * 16, kb.wdst - 4096 + i * 4096);
    }
    WAIT_BAR(10);
    QSTAMP(3); k2_bstep<3, 4, 3, -1>(kb, acc, wb, wa, HLO(3), HHI(3), of);
    QSTAMP(4); k2_bstep<4, 5, 5, -1>(kb, acc, wa, wb, HLO(4), HHI(4), of);
    WAIT_BAR(8);
    QSTAMP(5); k2_bstep<5, 6, 5, -1>(kb, acc, wb, wa, HLO(5), HHI(5), of);
    QSTAMP(6); k2_bstep<6, 0, 0, -1>(kb, acc, wa, wb, HLO(6), HHI(6), of);
    WAIT_BAR(10);
    QSTAMP(7); k2_bstep<7, 7, 5, -1>(kb, acc, wb, wa, HLO(7), HHI(7), of);
    unsigned long long seen = 0;
    const char* cw = ws + WS_FC + tp * 256;
    const unsigned cvo = l2 * 8;
    if (wid == 4) asm volatile("global_load_dwordx2 %0, %1, %2 sc1" : "+v"(seen) : "v"(cvo), "s"(cw) : "memory");
    SBAR();
    QSTAMP(8); k2_bstep<8, 0, 0, -1>(kb, acc, wa, wb, HLO(8), HHI(8), of);
    if (wid == 4) {
        asm volatile("s_waitcnt vmcnt(0)" : "+v"(seen) :: "memory");
#pragma unroll 1
        for (unsigned spin = 0; spin < (1u << 18) && !__all(seen == tag); ++spin) {
            __builtin_amdgcn_s_sleep(1);
            asm volatile("global_load_dwordx2 %0, %1, %2 sc1\n\ts_waitcnt vmcnt(0)" : "=&v"(seen) : "v"(cvo), "s"(cw) : "memory");
        }
    }
    WAIT_BAR(10);
    QSTAMP(9); k2_bstep<9, 0, 0, 0>(kb, acc, wb, wa, HLO(9), HHI(9), of);
    QSTAMP(10); k2_bstep<10, 0, 0, 5>(kb, acc, wa, wb, HLO(10), HHI(10), of);
    WAIT_BAR(10);
    QSTAMP(11); k2_bstep<11, 8, 3, -1>(kb, acc, wb, wa, HLO(11), HHI(11), of);
    QSTAMP(12); k2_bstep<12, 0, 0, -1>(kb, acc, wa, wb, of[0], of[1], of);
    QSTAMP(13); k2_bstep<13, 0, 0, -1>(kb, acc, wb, wa, of[2], of[3], of);
    QSTAMP(14); k2_bstep<14, 0, 0, -1>(kb, acc, wa, wb, of[4], of[5], of);
    WAIT_BAR(0);
    u32x4 xv0 = u32x4{}, xv1 = u32x4{};
    if (wid < 5) { xv0 = *(const u32x4*)(x + eb + eo0); xv1 = *(const u32x4*)(x + eb + eo1); }
    MEMFENCE(); SBAR();
    QSTAMP(15); k2_bstep<15, 0, 0, -1>(kb, acc, wb, wa, of[6], of[7], of);
    QSTAMP(16); k2_bstep<16, 0, 0, -1>(kb, acc, wa, wb, of[8], of[9], of);
    QSTAMP(17);
    {
        const int tk = ln & 15;
        const lds_ptr pp = (lds_ptr)smem + kq * 20480 + (mt * 32 + tk) * 320 + (((ln >> 4) ^ ((tk >> 1) & 3)) << 4);
#pragma unroll
        for (int i = 0; i < 10; ++i) *(LDS_AS f32x4*)(pp + (i & 1) * 16 * 320 + (i >> 1) * 64) = acc[i];
        QSTAMP(18);
        LGKM_BAR();
        QSTAMP(19);
        if (wid < 5) {
#pragma unroll
            for (int e = 0; e < 2; ++e) {
                const int er = e ? er1 : er0, ec = e ? ec1 : ec0, sw = (er >> 1) & 3;
                const lds_ptr p0 = (lds_ptr)smem + er * 320 + (((2 * ec) ^ sw) << 4), p1 = (lds_ptr)smem + er * 320 + (((2 * ec + 1) ^ sw) << 4);
                f32x4 a = *(const LDS_AS f32x4*)p0, b = *(const LDS_AS f32x4*)p1;
#pragma unroll
                for (int k = 1; k < 4; ++k) { a += *(const LDS_AS f32x4*)(p0 + k * 20480); b += *(const LDS_AS f32x4*)(p1 + k * 20480); }
                const u32x4 xv = e ? xv1 : xv0;
                const u32x4 ov = u32x4{cvtpk(a[0] + bf_lo(xv[0]), a[1] + bf_hi(xv[0])), cvtpk(a[2] + bf_lo(xv[1]), a[3] + bf_hi(xv[1])),
                                       cvtpk(b[0] + bf_lo(xv[2]), b[1] + bf_hi(xv[2])), cvtpk(b[2] + bf_lo(xv[3]), b[3] + bf_hi(xv[3]))};
                __builtin_amdgcn_raw_buffer_store_b128(ov, __builtin_amdgcn_make_buffer_rsrc(out, 0, S_ * D_ * 2, 0x00020000), (int)eb + (e ? eo1 : eo0), 0, OUTAUX);
            }
        }
    }
    QSTAMP(20);
    if (CODEWARM) asm volatile(".Lfb_end:");
    STAMP(7); STAMP_FLUSH(0); QFLUSH();
}

extern "C" void kernel_launch(void* const* d_in, const int* in_sizes, int n_in, void* d_out, int out_size, void* d_ws, size_t ws_size,
                              hipStream_t stream) {
    const char* x = (const char*)d_in[0];
    const char* wqkv = (const char*)d_in[1];
    const char* wo = (const char*)d_in[2];
    const char* wg = (const char*)d_in[4];
    const char* wu = (const char*)d_in[5];
    const char* wd = (const char*)d_in[6];
    char* ws = (char*)d_ws;
    (void)hipFuncSetAttribute((const void*)fused_block, hipFuncAttributeMaxDynamicSharedMemorySize, LDS_BYTES);
    fused_block<<<256, 512, LDS_BYTES, stream>>>(x, wqkv, wg, wu, wd, wo, ws, (char*)d_out);
}
```

```cpp
#include <hip/hip_runtime.h>
#include <hip/hip_bf16.h>
#include <stdint.h>

typedef __bf16 bfx4 __attribute__((ext_vector_type(4)));
typedef __bf16 bfx8 __attribute__((ext_vector_type(8)));
typedef __bf16 bfx16 __attribute__((ext_vector_type(16)));
typedef short s16x4 __attribute__((ext_vector_type(4)));
typedef short s16x8 __attribute__((ext_vector_type(8)));
typedef short s16x16 __attribute__((ext_vector_type(16)));
typedef float f32x2 __attribute__((ext_vector_type(2)));
typedef float f32x4 __attribute__((ext_vector_type(4)));
typedef float f32x8 __attribute__((ext_vector_type(8)));
typedef float f32x16 __attribute__((ext_vector_type(16)));
typedef unsigned u32x2 __attribute__((ext_vector_type(2)));
typedef unsigned u32x4 __attribute__((ext_vector_type(4)));
typedef unsigned u32x8 __attribute__((ext_vector_type(8)));
#define LDS_AS __attribute__((address_space(3)))
typedef LDS_AS char* lds_ptr;

#define ABL 0
#define DPRIO 1
#define CODEWARM 1
#define EVEN 1
#define HPRIO 3
#define CPRIO 0
#define WPWARM 0
#define UNR2 2
#define HUNR 1
#define FA_POS 14
#define ROTA 2
#define OUTAUX 16
#define P3ROT 1
#define WOWARM 1
#define K1_ROT 1
constexpr int S_ = 2048, D_ = 640, H_ = 32, F_ = 1536;

constexpr int Q_TILE = 1280, Q_HEAD = 64 * Q_TILE, KV_TILE = 2816, KV_V = 2560, KV_VT = 1536, KV_HEAD = 64 * KV_TILE;
constexpr int WS_Q = 0;
constexpr int WS_KV = WS_Q + H_ * Q_HEAD;
constexpr int WS_O16 = WS_KV + H_ * KV_HEAD + 8192;
constexpr int WS_H16 = WS_O16 + 128 * 20 * 1024;
constexpr int WS_WP = WS_H16 + 128 * 48 * 1024;
constexpr int WS_EPOCH = WS_WP + 8 * 68 * 5120 + 8192;
constexpr int WS_FA = WS_EPOCH + 256, WS_FB = WS_FA + 2048, WS_FC = WS_FB + 2048, WS_END = WS_FC + 8192;

#define PSTAMP(i) do {} while (0)
#define PFLUSH() do {} while (0)
#define DIAG_PF2 0
#define QFLUSH() do {} while (0)
#define QSTAMP(i) do {} while (0)
#define STAMP_ARG
#define STAMP_PASS
#define STAMP_DECL do {} while (0)
#define STAMP(i) do {} while (0)
#define STAMP_FLUSH(kern) do {} while (0)
#define SBAR() __builtin_amdgcn_sched_barrier(0)
template <int V> struct IC { static constexpr int v = V; };
template <int I, int N, class F> __device__ __forceinline__ void static_for(F&& f) { if constexpr (I < N) { f(IC<I>{}); static_for<I + 1, N>(f); } }
#define MFMA32(a, b, c) __builtin_amdgcn_mfma_f32_32x32x16_bf16((bfx8)(a), (bfx8)(b), (c), 0, 0, 0)

__device__ __forceinline__ unsigned cvtpk(float lo, float hi) {
    typedef __bf16 bfx2 __attribute__((ext_vector_type(2)));
    bfx2 r = {(__bf16)lo, (__bf16)hi};
    return __builtin_bit_cast(unsigned, r);
}
__device__ __forceinline__ void glds16(const void* g, unsigned lds_base) {
    unsigned sv;
    asm volatile("s_mov_b32 %0, m0\n\ts_mov_b32 m0, %2\n\ts_nop 0\n\tglobal_load_lds_dwordx4 %1, off\n\ts_mov_b32 m0, %0"
                 : "=&s"(sv) : "v"(g), "s"(lds_base) : "memory");
}
__device__ __forceinline__ s16x4 trread(const LDS_AS char* p) {
    return __builtin_amdgcn_ds_read_tr16_b64_v4i16((LDS_AS s16x4*)p);
}
__device__ __forceinline__ float rbf(float v) { return (float)(__bf16)v; }
__device__ __forceinline__ float bf_lo(unsigned u) { return __uint_as_float(u << 16); }
__device__ __forceinline__ float bf_hi(unsigned u) { return __uint_as_float(u & 0xffff0000u); }
__device__ __forceinline__ float swap32_add(float v) {
    auto r = __builtin_amdgcn_permlane32_swap(__float_as_uint(v), __float_as_uint(v), false, false);
    return __uint_as_float(r[0]) + __uint_as_float(r[1]);
}
__device__ __forceinline__ float swap32_max(float v) {
    auto r = __builtin_amdgcn_permlane32_swap(__float_as_uint(v), __float_as_uint(v), false, false);
    return fmaxf(__uint_as_float(r[0]), __uint_as_float(r[1]));
}

constexpr int P1_WSLOT = 24576, P1_X0 = 3 * P1_WSLOT, P1_XSLOT = 16384, P1_STAT = P1_X0 + 5 * P1_XSLOT;
constexpr int P1_COLA = P1_STAT + 1024, P1_COLB = P1_COLA + 512, P1_TAG = P1_COLB + 1024, LDS_JUNK = 161792, LDS_BYTES = 163840;
constexpr int K2_CHUNK = 8 * KV_TILE, K2_SLOTB = 24576, K2_SLOTS = 3, K2_KV0 = 81920, K2_C0 = 2 * P1_WSLOT, K2_Q0 = 0;
typedef __attribute__((address_space(1))) unsigned long long gu64;

#define PIN(x) asm volatile("" : "+v"(x))
#define MEMFENCE() asm volatile("" ::: "memory")
#define WAIT_BAR(N) do { MEMFENCE(); __builtin_amdgcn_s_waitcnt(0x70 | ((N) & 15) | (((N) >> 4) << 14)); __builtin_amdgcn_s_barrier(); MEMFENCE(); SBAR(); } while (0)
#define LGKM_BAR() do { MEMFENCE(); __builtin_amdgcn_s_waitcnt(0xC07F); __builtin_amdgcn_s_barrier(); MEMFENCE(); SBAR(); } while (0)
#define MFMA16(a, b, c) __builtin_amdgcn_mfma_f32_16x16x32_bf16((bfx8)(a), (bfx8)(b), (c), 0, 0, 0)
#define SMF16(a, b, c, i) __builtin_amdgcn_smfmac_f32_16x16x64_bf16((bfx8)(a), (bfx16)(b), (c), (i), 0, 0)
#define CAT8(a, b) __builtin_shufflevector(a, b, 0, 1, 2, 3, 4, 5, 6, 7)
#define CAT16(a, b) __builtin_shufflevector(a, b, 0, 1, 2, 3, 4, 5, 6, 7, 8, 9, 10, 11, 12, 13, 14, 15)

__device__ __forceinline__ void glds16s(const char* sbase, unsigned voff, unsigned lds_base) {
    unsigned sv;
    asm volatile("s_mov_b32 %0, m0\n\ts_mov_b32 m0, %3\n\ts_nop 0\n\tglobal_load_lds_dwordx4 %1, %2\n\ts_mov_b32 m0, %0"
                 : "=&s"(sv) : "v"(voff), "s"(sbase), "s"(lds_base) : "memory");
}

__device__ __forceinline__ void glds16c(const char* sbase, unsigned voff, unsigned lds_base) {
    asm volatile("s_mov_b32 m0, %2\n\ts_nop 0\n\tglobal_load_lds_dwordx4 %0, %1 sc1" :: "v"(voff), "s"(sbase), "s"(lds_base) : "memory");
}
typedef __amdgpu_buffer_rsrc_t rsrc_t;
#define ST16(rs, v, voff, soff) __builtin_amdgcn_raw_buffer_store_b128((v), (rs), (voff) + (soff), 0, (ABL & 32) ? 0 : 16)
#define ST8(rs, v, voff, soff) __builtin_amdgcn_raw_buffer_store_b64((v), (rs), (voff), (soff), 16)
#define LD16(rs, voff, soff) __builtin_bit_cast(s16x8, __builtin_amdgcn_raw_buffer_load_b128((rs), (voff), (soff), 16))
#define LD8(rs, voff, soff) __builtin_bit_cast(s16x4, __builtin_amdgcn_raw_buffer_load_b64((rs), (voff), (soff), 16))
__device__ __forceinline__ f32x2 pk(const f32x16& a, int i) { return f32x2{a[i], a[i + 1]}; }
__device__ __forceinline__ f32x2 pk4(const f32x4& a, int i) { return f32x2{a[i], a[i + 1]}; }
__device__ __forceinline__ f32x2 pfma(f32x2 a, f32x2 b, f32x2 c) { return __builtin_elementwise_fma(a, b, c); }
__device__ __forceinline__ unsigned cvt2(f32x2 v) { return cvtpk(v[0], v[1]); }
__device__ __forceinline__ float add1(float a, float b) { float r = a + b; asm volatile("" : "+v"(r)); return r; }
__device__ __forceinline__ float fma1(float a, float b, float c) { float r = fmaf(a, b, c); asm volatile("" : "+v"(r)); return r; }
__device__ __forceinline__ float mul1(float a, float b) { float r = a * b; asm volatile("" : "+v"(r)); return r; }

__device__ __forceinline__ void raise_flag(char* ws, int arr, unsigned long long tag) {
    __hip_atomic_store((gu64*)(ws + arr) + blockIdx.x, tag, __ATOMIC_RELAXED, __HIP_MEMORY_SCOPE_AGENT);
}
struct Poll { u32x4 a, b; };
__device__ __forceinline__ void poll_request(Poll& p, const char* arr, unsigned voff) {
    asm volatile("global_load_dwordx4 %0, %2, %3 sc1\n\tglobal_load_dwordx4 %1, %2, %3 offset:1024 sc1" : "+v"(p.a), "+v"(p.b) : "v"(voff), "s"(arr) : "memory");
}
__device__ __forceinline__ bool poll_check(Poll& p, unsigned long long tag) {
    const unsigned tl = (unsigned)tag, th = (unsigned)(tag >> 32);
    asm volatile("s_waitcnt vmcnt(0)" : "+v"(p.a), "+v"(p.b) :: "memory");
    return __all(p.a[0] == tl && p.a[1] == th && p.a[2] == tl && p.a[3] == th && p.b[0] == tl && p.b[1] == th && p.b[2] == tl && p.b[3] == th);
}
__device__ __forceinline__ void poll_wait(Poll& p, const char* arr, unsigned voff, unsigned long long tag) {
#pragma unroll 1
    for (unsigned spin = 0; spin < (1u << 18) && !poll_check(p, tag); ++spin) {
        __builtin_amdgcn_s_sleep(4);
        poll_request(p, arr, voff);
    }
    asm volatile("s_waitcnt vmcnt(0)" : "+v"(p.a), "+v"(p.b) :: "memory");
}

template <int MODE, class FM>
__device__ __forceinline__ void p1_pass(f32x4 (&acc)[4][MODE == 2 ? 6 : 4], lds_ptr x0, lds_ptr x1, lds_ptr (&wp)[MODE == 2 ? 2 : 4], int start, FM&& mid) {
    constexpr int NC = MODE == 2 ? 6 : 4, NR = 4 + 2 * NC, WS0 = MODE == 2 ? 1 : 0;
    struct F { s16x8 x[4]; s16x4 w[NC][2]; } f[2];
    auto rd = [&](F& n, int ks, int i) __attribute__((always_inline)) {
        if (i == 0 || i > 2 * NC) { const int m = i == 0 ? 0 : i - 2 * NC; n.x[m] = *(const LDS_AS s16x8*)((ks ? x1 : x0) + m * 2048); }
        else {
            const int t = (i - 1) >> 1, e = (i - 1) & 1, c = t % 3;
            n.w[t][e] = trread(MODE == 2 ? wp[c & 1] + ks * 12288 + (t / 3) * 3072 + (c >> 1) * 1024 + e * 256 : wp[MODE == 2 ? 0 : t] + ks * 8192 + e * 512);
        }
    };
    auto step = [&](const F& c, F& n, int ks, bool more) __attribute__((always_inline)) {
#pragma unroll
        for (int i = 0; i < 4 * NC; ++i) {
            const s16x8 w = CAT8(c.w[i % NC][0], c.w[i % NC][1]);
            f32x4& a = acc[i / NC][i % NC];
            a = MODE == 1 ? MFMA16(c.x[i / NC], w, a) : MFMA16(w, c.x[i / NC], a);
            PIN(a); SBAR();
            if (more && i < NR) { rd(n, ks, i); SBAR(); }
        }
    };
#pragma unroll
    for (int i = 0; i < NR; ++i) rd(f[0], 0, i);
    SBAR();
    static_for<0, 10>([&](auto N) __attribute__((always_inline)) {
        constexpr int n = N.v, dw = (WS0 + n) % 3 == 2 ? -2 * P1_WSLOT : P1_WSLOT;
        const int dx = n % 5 == 4 - start ? -4 * P1_XSLOT : P1_XSLOT;
        step(f[0], f[1], 1, true);
        x0 += dx; x1 += dx; PIN(x0); PIN(x1);
#pragma unroll
        for (int i = 0; i < (MODE == 2 ? 2 : 4); ++i) { wp[i] += dw; PIN(wp[i]); }
        mid(N);
        PSTAMP((MODE == 2 ? 22 : 2) + 2 * n);
        LGKM_BAR();
        PSTAMP((MODE == 2 ? 23 : 3) + 2 * n);
        step(f[1], f[0], 0, n < 9);
    });
}

struct Pack { const char *wd, *wo; int job; };
__device__ __forceinline__ void pack_fetch(const Pack& pk_, lds_ptr lds, int wid, int lane) {
    const int step = pk_.job / 10, cb = pk_.job - step * 10, r = lane >> 3;
    const unsigned va = ((r & 3) + (step < 48 ? 16 : 8) * (r >> 2)) * (D_ * 2) + (lane & 7) * 16, vb = ((r >> 2) * 20 + 16 + (r & 3)) * (D_ * 2) + (lane & 7) * 16;
#pragma unroll
    for (int g = 0; g < 4; ++g) {
        const int ks = (step - 48) * 2 + (g >> 1), hh = g & 1;
        const char* a = step < 48 ? pk_.wd + (step * 32 + g * 4) * (D_ * 2) : ks < 32 ? pk_.wo + (ks * 20 + hh * 4) * (D_ * 2) : pk_.wo + ((ks - 32) * 4 + hh * 2) * 20 * (D_ * 2);
        glds16s(a + cb * 128, step < 64 ? va : vb, (unsigned)(uintptr_t)lds + wid * P1_WSLOT + 16384 + g * 1024);
    }
}
__device__ __forceinline__ void pack_read(s16x8 (&pf)[4], lds_ptr lds, int wid, int lane) {
    asm volatile("s_waitcnt vmcnt(0)" ::: "memory");
    const lds_ptr pr = lds + wid * P1_WSLOT + 16384 + (lane >> 4) * 1024 + ((lane >> 2) & 3) * 128 + (lane & 3) * 8;
#pragma unroll
    for (int nt = 0; nt < 4; ++nt) pf[nt] = CAT8(trread(pr + nt * 32), trread(pr + nt * 32 + 512));
}
__device__ __forceinline__ void pack_store(const Pack& pk_, const s16x8 (&pf)[4], int lane, rsrc_t wsr) {
    const int step = pk_.job / 10, cb = pk_.job - step * 10;
#pragma unroll
    for (int nt = 0; nt < 4; ++nt) {
        const int N = cb * 4 + nt, cg = N / 5;
        ST16(wsr, __builtin_bit_cast(u32x4, pf[nt]), lane * 16, WS_WP + ((cg * 68 + step) * 5 + N - cg * 5) * 1024);
    }
}

constexpr int OOB = 0x7ffffff0;

__device__ __forceinline__ void p1_compute(lds_ptr lds, int wid, int lane, int panel, int grp, int ra, int rb, rsrc_t wsr, const Pack& pk_ STAMP_ARG) {
    const int l15 = lane & 15, g4 = lane >> 4, q = (lane >> 2) & 3, wm = wid & 1, wn = wid >> 1, tt = panel * 4 + wm * 2;
    const LDS_AS float* rstd = (const LDS_AS float*)(lds + P1_STAT) + wm * 64;
    const int xo = P1_X0 + (wm * 64 + l15) * 128 + ((g4 ^ (l15 >> 1)) << 4);
    if (pk_.job < 680) pack_fetch(pk_, lds, wid, lane);
    {
        f32x4 acc[4][4];
#pragma unroll
        for (int i = 0; i < 16; ++i) acc[i >> 2][i & 3] = f32x4{};
        s16x8 pf[4];
        const LDS_AS float* colf = (const LDS_AS float*)(lds + P1_COLA) + wn * 64;
        const int sec = grp / 5, q0 = (grp - sec * 5) * 32 + wn * 16;
        lds_ptr wp[4];
#pragma unroll
        for (int t = 0; t < 4; ++t) wp[t] = lds + g4 * 2048 + wn * 1024 + q * 128 + (lane & 3) * 8 + ((t ^ (q >> 1) ^ ((g4 & 1) << 1)) << 5);
        auto mid = [&](auto N) __attribute__((always_inline)) { if constexpr (N.v == 5) pack_read(pf, lds, min(wid, 2), lane); };
        PSTAMP(0);
        LGKM_BAR();
        PSTAMP(1);
        f32x2 t[32];
        int vo[4], vs;
        if (sec < 2) {
            p1_pass<0>(acc, lds + xo + ra * P1_XSLOT, lds + (xo ^ 64) + ra * P1_XSLOT, wp, ra, mid);
            STAMP(1);
            f32x4 cv[4]; float rs[4], nm[4];
#pragma unroll
            for (int i = 0; i < 4; ++i) { cv[i] = *(const LDS_AS f32x4*)(colf + i * 16 + g4 * 4); nm[i] = rstd[128 + i * 16 + l15]; rs[i] = rstd[i * 16 + l15]; }
            const float qs = sec ? 1.0f : 0.22360679774997896f * 1.4426950408889634f;
#pragma unroll
            for (int c = 0; c < 4; ++c) { const int qd = q0 + 4 * c + g4, hd = (qd * 205) >> 10; vo[c] = (sec ? WS_KV + tt * KV_TILE + hd * KV_HEAD : WS_Q + tt * Q_TILE + hd * Q_HEAD) + (qd - hd * 5) * 512 + l15 * 16; }
            vs = 256;
#pragma unroll
            for (int k = 0; k < 32; ++k) { const int m = (k & 2) + ((k >> 2) & 1); t[k] = pfma(pk4(cv[k >> 3], (k & 1) * 2), f32x2{nm[m], nm[m]}, pk4(acc[m][k >> 3], (k & 1) * 2)); }
            SBAR();
#pragma unroll
            for (int k = 0; k < 32; ++k) { const float r = rs[(k & 2) + ((k >> 2) & 1)] * qs; t[k] *= f32x2{r, r}; }
        } else {
            if (sec == 2) p1_pass<1>(acc, lds + xo + ra * P1_XSLOT, lds + (xo ^ 64) + ra * P1_XSLOT, wp, ra, mid);
            else static_for<0, 10>([&](auto N) __attribute__((always_inline)) { mid(N); LGKM_BAR(); });
            STAMP(1);
            f32x4 rs[4], nm[4]; float cv[4];
#pragma unroll
            for (int i = 0; i < 4; ++i) { cv[i] = colf[i * 16 + l15]; nm[i] = *(const LDS_AS f32x4*)(rstd + 128 + i * 16 + g4 * 4); rs[i] = *(const LDS_AS f32x4*)(rstd + i * 16 + g4 * 4); }
            const int so = WS_KV + tt * KV_TILE + KV_V;
#pragma unroll
            for (int c = 0; c < 4; ++c) { const int qd = q0 + 4 * c + q, hd = (qd * 205) >> 10, pl = qd - hd * 5; vo[c] = sec == 2 ? so + hd * KV_HEAD + pl * 256 + ((g4 ^ (pl & 3)) << 6) + (lane & 3) * 16 : OOB - KV_VT; }
            vs = KV_VT;
#pragma unroll
            for (int k = 0; k < 32; ++k) { const int m = (k >> 1) & 3; t[k] = pfma(pk4(nm[m], (k & 1) * 2), f32x2{cv[k >> 3], cv[k >> 3]}, pk4(acc[m][k >> 3], (k & 1) * 2)); }
            SBAR();
#pragma unroll
            for (int k = 0; k < 32; ++k) t[k] *= pk4(rs[(k >> 1) & 3], (k & 1) * 2);
            const int hd = q0 / 5 + g4, oo = sec == 2 && hd * 5 + 4 < q0 + 16 ? so + hd * KV_HEAD + 5 * 256 + l15 * 16 : OOB - KV_VT;
            const unsigned ov = (lane & 3) ? 0u : 0x3f803f80u;
            ST16(wsr, (u32x4{ov, ov, ov, ov}), oo, 0); ST16(wsr, (u32x4{ov, ov, ov, ov}), oo, KV_VT);
        }
        SBAR();
#pragma unroll
        for (int i = 0; i < 8; ++i) ST16(wsr, (u32x4{cvt2(t[4 * i]), cvt2(t[4 * i + 1]), cvt2(t[4 * i + 2]), cvt2(t[4 * i + 3])}), vo[i >> 1], (i & 1) * vs);
        if (pk_.job < 680) pack_store(pk_, pf, lane, wsr);
    }
    STAMP(6);
    f32x4 acc[4][6];
#pragma unroll
    for (int i = 0; i < 24; ++i) acc[i / 6][i % 6] = f32x4{};
    {
        const int wo0 = P1_WSLOT + (g4 >> 1) * 6144 + (g4 & 1) * 512 + q * 64 + (lane & 3) * 8, T0 = wn * 3, T1 = T0 + 1;
        lds_ptr wp[2] = {lds + wo0 + (T0 >> 1) * 1024 + (((T0 ^ g4) & 1) << 5), lds + wo0 + (T1 >> 1) * 1024 + (((T1 ^ g4) & 1) << 5)};
        p1_pass<2>(acc, lds + xo + rb * P1_XSLOT, lds + (xo ^ 64) + rb * P1_XSLOT, wp, rb, [&](auto N) __attribute__((always_inline)) {
            if constexpr (N.v == FA_POS - 11) asm volatile("s_waitcnt vmcnt(0)" ::: "memory");
        });
    }
    STAMP(3);
    {
        const LDS_AS float* colf = (const LDS_AS float*)(lds + P1_COLB) + wn * 48 + g4 * 4;
        f32x4 cg[3], cu[3];
#pragma unroll
        for (int c = 0; c < 3; ++c) { cg[c] = *(const LDS_AS f32x4*)(colf + c * 16); cu[c] = *(const LDS_AS f32x4*)(colf + 96 + c * 16); }
        float nm[4], r2[4];
#pragma unroll
        for (int m = 0; m < 4; ++m) { const float r = rstd[m * 16 + l15]; nm[m] = rstd[128 + m * 16 + l15]; r2[m] = r * r; }
        const int vo = lane * 16 + WS_H16 + (tt * 96 + grp * 6 + wn * 3) * 1024;
#pragma unroll
        for (int mm = 0; mm < 2; ++mm)
#pragma unroll
            for (int c = 0; c < 3; ++c) {
                float g[8], u[8];
#pragma unroll
                for (int i = 0; i < 8; ++i) { const int m = mm * 2 + (i >> 2); g[i] = fma1(cg[c][i & 3], nm[m], acc[m][c][i & 3]); u[i] = fma1(cu[c][i & 3], nm[m], acc[m][3 + c][i & 3]); }
#pragma unroll
                for (int i = 0; i < 8; ++i) { g[i] = fmaxf(g[i], 0.f); u[i] = mul1(u[i], r2[mm * 2 + (i >> 2)]); }
#pragma unroll
                for (int i = 0; i < 8; ++i) g[i] = mul1(g[i], u[i]);
                ST16(wsr, (u32x4{cvtpk(g[0], g[1]), cvtpk(g[2], g[3]), cvtpk(g[4], g[5]), cvtpk(g[6], g[7])}), vo, (mm * 96 + c) * 1024);
            }
    }
}

__device__ __forceinline__ void glds_x4(const char* sbase, const unsigned (&v)[4], unsigned lds_base) {
    asm volatile("s_mov_b32 m0, %5\n\ts_nop 0\n\tglobal_load_lds_dwordx4 %0, %4\n\tglobal_load_lds_dwordx4 %1, %4 offset:1024\n\t"
                 "global_load_lds_dwordx4 %2, %4 offset:2048\n\tglobal_load_lds_dwordx4 %3, %4 offset:3072"
                 :: "v"(v[0]), "v"(v[1]), "v"(v[2]), "v"(v[3]), "s"(sbase), "s"(lds_base) : "memory");
}
__device__ __forceinline__ void glds_x3(const char* sbase, const unsigned* v, unsigned lds_base) {
    asm volatile("s_mov_b32 m0, %4\n\ts_nop 0\n\tglobal_load_lds_dwordx4 %0, %3\n\tglobal_load_lds_dwordx4 %1, %3 offset:1024\n\tglobal_load_lds_dwordx4 %2, %3 offset:2048"
                 :: "v"(v[0]), "v"(v[1]), "v"(v[2]), "s"(sbase), "s"(lds_base) : "memory");
}

__device__ __forceinline__ unsigned long long p1_helper(lds_ptr lds, int hw, int lane, const char* xp, const char* wq, const char* wgp, const char* wup, int ra, int rb,
                                          char* ws, const char* kv, const char* qsrc) {
    const unsigned lds0 = (unsigned)(uintptr_t)lds;
    const int r = lane >> 3, pos = lane & 7;
    unsigned xv[4], wa[4], wb[6];
#pragma unroll
    for (int j = 0; j < 4; ++j) {
        xv[j] = 4096 + (hw * 32 + j * 8 + r) * (D_ * 2) + ((pos ^ ((j & 1) * 4 + (r >> 1))) << 4) - j * 1024;
        wa[j] = 4096 + (hw * 16 + (j >> 1) * 8 + r) * (3 * D_ * 2) + (j & 1) * 128 + ((pos ^ (((r >> 1) & 1) * 2 + (j >> 1) * 4)) << 4) - j * 1024;
    }
#pragma unroll
    for (int j = 0; j < 6; ++j) wb[j] = 4096 + (hw * 16 + (lane >> 2)) * (F_ * 2) + (j % 3) * 64 + (((lane & 3) ^ ((lane >> 5) * 2)) << 4) - (j % 3) * 1024;
    xp -= 4096; wq -= 4096; wgp -= 4096; wup -= 4096;
    int kb_ = 0, xs = ra, dws = 0, left = 5;
    auto issue1 = [&](bool pa) {
        if (ABL & 4) return;
        if (pa) glds_x4(wq + (unsigned)((kb_ + xs) * 64 * 3 * D_ * 2), wa, lds0 + dws * P1_WSLOT + hw * 4096);
        else glds_x3(wgp + (unsigned)((kb_ + xs) * 64 * F_ * 2), wb, lds0 + dws * P1_WSLOT + hw * 6144);
        SBAR();
    };
    auto issue2 = [&](bool pa, bool xx) {
        SBAR();
        if (ABL & 4) return;
        if (!pa) glds_x3(wup + (unsigned)((kb_ + xs) * 64 * F_ * 2), wb + 3, lds0 + dws * P1_WSLOT + hw * 6144 + 3072);
        xs = xs == 4 ? 0 : xs + 1; dws = dws == 2 ? 0 : dws + 1; --left; kb_ = left == 0 ? 5 - kb_ : kb_; left = left == 0 ? 5 : left;
        if (xx) glds_x4(xp + (kb_ + xs) * 128, xv, lds0 + P1_X0 + xs * P1_XSLOT + hw * 4096);
    };
    glds_x4(xp + ra * 128, xv, lds0 + P1_X0 + ra * P1_XSLOT + hw * 4096);
    issue1(true); issue2(true, true);
    __builtin_amdgcn_s_barrier();
    unsigned long long ep = 0;
    asm volatile("global_load_dwordx2 %0, %1, %2 sc1" : "+v"(ep) : "v"(0u), "s"(ws + WS_EPOCH) : "memory");
    __builtin_amdgcn_s_setprio(HPRIO);
    const int r16 = lane & 15, h = lane >> 5, blk = (lane >> 4) & 1, q = (lane >> 2) & 3;
    const unsigned olo = r16 < 2 ? 0x3f803f80u : 0u;
    const s16x8 ones2 = __builtin_bit_cast(s16x8, (u32x4{olo, olo, olo, olo}));
    const int idx = (r16 & 1) ? 0xEEEE : 0x4444;
    int ws3 = 0;
    {
        const int bo = (lane >> 4) * 2048 + (hw >> 1) * 1024 + q * 128 + (lane & 3) * 8 + ((((hw & 1) * 2) ^ (q >> 1) ^ (blk << 1)) << 5);
        lds_ptr b = lds + bo, b1 = lds + (bo ^ 32);
        const int so = P1_X0 + ra * P1_XSLOT + (hw * 32 + r16) * 128 + (((lane >> 4) ^ (r16 >> 1)) << 4);
        lds_ptr sp0 = lds + so, sp1 = lds + (so ^ 64);
        f32x4 cs = f32x4{}, cs1 = f32x4{}, s1[2] = {f32x4{}, f32x4{}}, s2[2] = {f32x4{}, f32x4{}};
        s16x16 wv0, wv1; s16x8 xa[2], xb[2];
        auto col_rd = [&]() {
            wv0 = CAT16(CAT8(trread(b), trread(b + 512)), CAT8(trread(b + 8192), trread(b + 8192 + 512)));
            wv1 = CAT16(CAT8(trread(b1), trread(b1 + 512)), CAT8(trread(b1 + 8192), trread(b1 + 8192 + 512)));
            const int dw = ws3 == 2 ? -2 * P1_WSLOT : P1_WSLOT;
            b += dw; b1 += dw; ws3 = ws3 == 2 ? 0 : ws3 + 1;
        };
        auto row_rd = [&](int dx) {
#pragma unroll
            for (int t = 0; t < 2; ++t) { xa[t] = *(const LDS_AS s16x8*)(sp0 + t * 2048); xb[t] = *(const LDS_AS s16x8*)(sp1 + t * 2048); }
            sp0 += dx; sp1 += dx;
        };
        auto row_mm = [&]() {
#pragma unroll
            for (int t = 0; t < 2; ++t) { s2[t] = MFMA16(xa[t], xa[t], s2[t]); s1[t] = SMF16(ones2, CAT16(xa[t], xb[t]), s1[t], idx); }
#pragma unroll
            for (int t = 0; t < 2; ++t) { s2[t] = MFMA16(xb[t], xb[t], s2[t]); PIN(s1[t]); PIN(s2[t]); }
        };
#pragma unroll
        for (int nn = 0; nn < 9; ++nn) {
            int n = nn;
            if (!HUNR) asm volatile("" : "+s"(n));
            PSTAMP(2 * n);
            if (n == 0) WAIT_BAR(5); else if (n < 8) WAIT_BAR(12); else WAIT_BAR(8);
            PSTAMP(2 * n + 1);
            if (!(ABL & 8) || n == 0) { col_rd(); row_rd(n % 5 == 4 - ra ? -4 * P1_XSLOT : P1_XSLOT); }
            if (n == 0) { issue1(true); issue2(true, true); }
            if (n == 8) { kb_ = 5; xs = rb; }
            issue1(n < 8);
            if (!(ABL & 8)) {
            cs = SMF16(ones2, wv0, cs, idx);
            row_mm();
            cs1 = SMF16(ones2, wv1, cs1, idx); PIN(cs); PIN(cs1); }
            issue2(n < 8, n < 7);
        }
        MEMFENCE(); __builtin_amdgcn_s_waitcnt(0xF70 | 10); MEMFENCE();
        row_rd(0); row_mm();
        {
            const int t = blk;
            const float d0 = (lane & 2) ? ((lane & 1) ? s2[0][3] : s2[0][2]) : ((lane & 1) ? s2[0][1] : s2[0][0]);
            const float d1 = (lane & 2) ? ((lane & 1) ? s2[1][3] : s2[1][2]) : ((lane & 1) ? s2[1][1] : s2[1][0]);
            const float a0 = __shfl(s1[0][0] + s1[0][1], r16), a1 = __shfl(s1[1][0] + s1[1][1], r16), c0 = __shfl(d0, r16 + 16 * q), c1 = __shfl(d1, r16 + 16 * q);
            const float a = t ? a1 : a0, c = t ? c1 : c0;
            const float mean = a * (1.0f / D_), var = fmaxf(c * (1.0f / D_) - mean * mean, 0.f);
            const float rstd = __builtin_amdgcn_rcpf(rbf(__builtin_amdgcn_sqrtf(rbf(rbf(var) + 1e-5f))));
            ((LDS_AS float*)(lds + P1_STAT))[h * 128 + hw * 32 + (lane & 31)] = h ? -rbf(mean) : rstd;
        }
        PSTAMP(18);
        WAIT_BAR(6);
        PSTAMP(19);
        col_rd();
        issue1(false);
        cs = SMF16(ones2, wv0, cs, idx);
        cs1 = SMF16(ones2, wv1, cs1, idx); PIN(cs); PIN(cs1);
        issue2(false, false);
        LDS_AS float* colf = (LDS_AS float*)(lds + P1_COLA) + hw * 32;
        if (lane < 16) { colf[lane] = cs[0] + cs[1]; colf[16 + lane] = cs1[0] + cs1[1]; }
    }
    asm volatile("" : "+v"(ep));
    const unsigned long long tag = (((unsigned long long)(unsigned)__builtin_amdgcn_readfirstlane((unsigned)(ep >> 32)) << 32) | (unsigned)__builtin_amdgcn_readfirstlane((unsigned)ep)) + 1ull;
    if (hw == 0 && lane == 0) *(LDS_AS unsigned long long*)(lds + P1_TAG) = tag;
    {
        const int g4 = lane >> 4, ro = P1_WSLOT + (g4 >> 1) * 6144 + (g4 & 1) * 512 + q * 64 + (lane & 3) * 8, T0 = hw * 3, T1 = T0 + 1;
        lds_ptr b = lds + ro + (T0 >> 1) * 1024 + (((T0 ^ g4) & 1) << 5), c = lds + ro + (T1 >> 1) * 1024 + (((T1 ^ g4) & 1) << 5);
        f32x4 cs = f32x4{}, ct = f32x4{}, cu = f32x4{};
        Poll pl = {};
        bool early = false;
#pragma unroll
        for (int nn = 10; nn < 20; ++nn) {
            int n = nn;
            if (!HUNR) asm volatile("" : "+s"(n));
            PSTAMP(2 * n);
            if (n < 13 || n == 19) WAIT_BAR(6); else if (n == 13) WAIT_BAR(10); else if (n == 18) WAIT_BAR(12); else WAIT_BAR(14);
            PSTAMP(2 * n + 1);
            const s16x16 wv0 = CAT16(CAT8(trread(b), trread(b + 256)), CAT8(trread(b + 12288), trread(b + 12288 + 256)));
            const s16x16 wv1 = CAT16(CAT8(trread(c), trread(c + 256)), CAT8(trread(c + 12288), trread(c + 12288 + 256)));
            const s16x16 wv2 = CAT16(CAT8(trread(b + 1024), trread(b + 1024 + 256)), CAT8(trread(b + 13312), trread(b + 13312 + 256)));
            if (n == FA_POS && hw == 0 && lane == 0) raise_flag(ws, WS_FA, tag);
            if (n < 18) issue1(false);
            cs = SMF16(ones2, wv0, cs, idx);
            ct = SMF16(ones2, wv1, ct, idx);
            cu = SMF16(ones2, wv2, cu, idx); PIN(cs); PIN(ct); PIN(cu);
            if (n < 18) issue2(false, n >= 12 && n < 17);
            if (n == 17) poll_request(pl, ws + WS_FA, lane * 16);
            if (n == 18) { early = poll_check(pl, tag); if (!early) poll_request(pl, ws + WS_FA, lane * 16); }
            if (n == 19 && !early) poll_wait(pl, ws + WS_FA, lane * 16, tag);
            if (n >= 18 && early == (n == 18)) {
#pragma unroll
                for (int j = 0; j < 6; ++j) glds16c(kv + (hw + 4 * j) * 1024, lane * 16, lds0 + K2_C0 + (hw + 4 * j) * 1024);
            }
            if (n == 19) {
#pragma unroll
                for (int j = 0; j < 3; ++j) glds16c(qsrc + (hw + 4 * j) * 1024, lane * 16, lds0 + K2_Q0 + (hw + 4 * j) * 1024);
            }
            const int dw = ws3 == 2 ? -2 * P1_WSLOT : P1_WSLOT;
            b += dw; c += dw; ws3 = ws3 == 2 ? 0 : ws3 + 1;
        }
        LDS_AS float* colf = (LDS_AS float*)(lds + P1_COLB) + hw * 48;
        if (lane < 16) { colf[lane] = cs[0] + cs[1]; colf[16 + lane] = ct[0] + ct[1]; colf[32 + lane] = cu[0] + cu[1]; }
        WAIT_BAR(0);
        if (blockIdx.x == 0 && hw == 0 && lane == 0) __hip_atomic_store((gu64*)(ws + WS_EPOCH), tag, __ATOMIC_RELAXED, __HIP_MEMORY_SCOPE_AGENT);
#pragma unroll
        for (int j = 0; j < 6; ++j) glds16c(kv + K2_CHUNK + (hw + 4 * j) * 1024, lane * 16, lds0 + K2_KV0 + K2_SLOTB + (hw + 4 * j) * 1024);
    }
    return tag;
}

constexpr int K2_XCH = 2 * K2_SLOTB;

#define SMF32(a, b, c) __builtin_amdgcn_smfmac_f32_32x32x32_bf16((bfx8)(a), __builtin_bit_cast(bfx16, (b)), (c), 0xDDDD, 0, 0)
#define MFMA8(a, b, c) __builtin_amdgcn_mfma_f32_32x32x8bf16_1k((a), (b), (c), 0, 0, 0)
__device__ __forceinline__ f32x8 lo8(const f32x16& v) { return __builtin_shufflevector(v, v, 0, 1, 2, 3, 4, 5, 6, 7); }
__device__ __forceinline__ f32x8 hi8(const f32x16& v) { return __builtin_shufflevector(v, v, 8, 9, 10, 11, 12, 13, 14, 15); }
__device__ __forceinline__ float max16(const f32x16& s) {
    float m = fmaxf(fmaxf(s[0], s[1]), s[2]);
#pragma unroll
    for (int r = 3; r < 15; r += 2) m = fmaxf(fmaxf(m, s[r]), s[r + 1]);
    return swap32_max(fmaxf(m, s[15]));
}

struct K2B { lds_ptr r0, r1, r2; const char *wsrc, *wsa, *wsb; unsigned wdst; int lane, osoff; rsrc_t wsr; };
constexpr int k2_slot(int st) { return st < 3 ? st : st == 3 ? 0 : st == 4 ? 3 : st == 5 ? 1 : st == 6 ? 2 : st == 7 ? 0 : 3; }
__device__ __forceinline__ void k2_wpiece(const K2B& c, int st, int i) {
    glds16c((st < 3 ? c.wsa : st < 6 ? c.wsb : c.wsrc) + st * 40960 + i * 8192, c.lane * 16, c.wdst + k2_slot(st) * 40960 + i * 8192);
}
template <int I, int D, int NP, int OF>
__device__ __forceinline__ void k2_bstep(const K2B& c, f32x4 (&acc)[10], const s16x8 (&w)[5], s16x8 (&n)[5], const s16x8& a0, const s16x8& a1, s16x8 (&of)[10]) {
    constexpr int O = k2_slot((I + 1) >> 1) * 40960 + ((I + 1) & 1) * 20480;
#pragma unroll
    for (int i = 0; i < 10; ++i) {
        if (I > 0 && I < 16 && i < 5) n[i] = *(const LDS_AS s16x8*)((O < 61440 ? c.r0 : O < 122880 ? c.r1 : c.r2) + O % 61440 + i * 1024);
        acc[i] = MFMA16(w[i >> 1], (i & 1) ? a1 : a0, acc[i]);
        PIN(acc[i]);
        if (OF >= 0 && !(i & 1)) { const int k = (OF < 0 ? 0 : OF) + (i >> 1); of[k] = LD16(c.wsr, c.lane * 16, c.osoff + ((k & 1) * 20 + (k >> 1) * 4) * 1024); MEMFENCE(); }
        if ((i & 1) && (i >> 1) < NP) k2_wpiece(c, D, (i >> 1) + (D == 4));
        SBAR();
    }
}

__global__ __launch_bounds__(512, 2) void fused_block(const char* __restrict__ x, const char* __restrict__ wqkv, const char* __restrict__ wg, const char* __restrict__ wu,
                                                      const char* __restrict__ wd, const char* __restrict__ wo, char* __restrict__ ws, char* __restrict__ out) {
    extern __shared__ __attribute__((aligned(16))) char smem[];
    asm volatile("" :: "s"(x), "s"(wqkv), "s"(wg), "s"(wu), "s"(wd), "s"(wo), "s"(ws), "s"(out));
    const int wid = __builtin_amdgcn_readfirstlane(threadIdx.x >> 6);
    STAMP_DECL; STAMP(0); PSTAMP(63);
    unsigned long long tag = 0;
    if (wid < 4) {
        __builtin_amdgcn_s_barrier();
        if (CODEWARM && wid == 3) {
            unsigned off = ((blockIdx.x >> 3) * 13 + min(threadIdx.x & 63, 12)) * 128;
            asm volatile("s_getpc_b64 vcc\n.Lfb_begin:\n\tv_min_u32 %0, .Lfb_end-.Lfb_begin, %0\n\ts_mov_b32 m0, %1\n\ts_nop 0\n\tglobal_load_lds_dword %0, vcc"
                         : "+v"(off) : "s"((unsigned)(uintptr_t)(lds_ptr)smem + LDS_JUNK) : "vcc", "memory");
        }
    }
    const rsrc_t wsr = __builtin_amdgcn_make_buffer_rsrc(ws, 0, (int)WS_END, 0x00020000);
    const int bid = blockIdx.x, head = (bid & 7) * 4 + ((bid >> 3) & 3), par = wid >> 2, qt0 = (bid >> 5) * 8 + (wid & 3) * 2;
    const char* kv = ws + WS_KV + head * KV_HEAD;
    {
        const int bi = bid >> 3, panel = (bid & 1) * 8 + (bi & 7), grp = ((bid & 7) >> 1) * 4 + (bi >> 3), l0 = threadIdx.x & 63;
        const int rb = K1_ROT ? (5 * (bi & 7)) >> 3 : 0, ra = ROTA == 1 ? (rb + (bi >> 3)) % 5 : ROTA == 2 ? ((bi & 1) * 2 + ((bi >> 3) & 1) * 2) % 4 : 0;
        if (wid < 4) {
            p1_compute((lds_ptr)smem, wid, l0, panel, grp, ra, rb, wsr, Pack{wd, wo, wid * 256 + bid} STAMP_PASS);
            const u32x2 e = *(const LDS_AS u32x2*)((lds_ptr)smem + P1_TAG);
            tag = ((unsigned long long)(unsigned)__builtin_amdgcn_readfirstlane(e[1]) << 32) | (unsigned)__builtin_amdgcn_readfirstlane(e[0]);
        } else {
            tag = p1_helper((lds_ptr)smem, wid - 4, l0, x + (size_t)panel * 128 * (D_ * 2), wqkv + min(grp, 14) * 256, wg + grp * 192, wu + grp * 192, ra, rb,
                            ws, kv, ws + WS_Q + head * Q_HEAD + (bid >> 5) * 8 * Q_TILE);
        }
    }
    int lane = __builtin_amdgcn_mbcnt_hi(~0u, __builtin_amdgcn_mbcnt_lo(~0u, 0u));
    PIN(lane);
    if (wid < 4) __builtin_amdgcn_s_setprio(3); else __builtin_amdgcn_s_setprio(0);
    STAMP(4);
    lds_ptr lds = (lds_ptr)smem + K2_KV0;
    const unsigned lds0 = (unsigned)(uintptr_t)lds;
    const int tp = ((blockIdx.x & 7) >> 1) * 8 + ((blockIdx.x >> 3) & 7), cg = (blockIdx.x & 1) * 4 + (blockIdx.x >> 6);
    K2B kb;
    kb.wsrc = ws + WS_WP + cg * 68 * 5120 + wid * 1024; kb.wdst = (unsigned)(uintptr_t)(lds_ptr)smem + wid * 1024; kb.lane = lane;
    const int sw3 = P3ROT ? tp & 1 : 0;
    kb.wsa = kb.wsrc + sw3 * 122880; kb.wsb = kb.wsrc - sw3 * 122880;
    const int hoff = WS_H16 + ((tp * 2 + (wid & 1)) * 96 + (wid >> 1) * 2 + sw3 * 48) * 1024, hoff1 = hoff + (1 - 2 * sw3) * 49152;
    s16x8 hf[24];
    const int h = lane >> 5, l31 = lane & 31, l16 = lane * 16;
    const char* src = kv + wid * 1024 + 3 * K2_CHUNK;
    const int kr = ((l31 & 4) << 2) | ((l31 >> 1) & 12) | (l31 & 3);
    const int k1off = h * 1024 + kr * 16, k2off = h ? KV_V + 5 * 256 : 2048 + kr * 16;
    const int vq = min(l31 >> 2, 5), voff = KV_V + vq * 256 + ((h ^ (vq & 3)) << 6) + (l31 < 24 ? l31 & 3 : 1) * 16;

    f32x16 oa[2] = {f32x16{}, f32x16{}}, sc[2];
    s16x8 k1[2], v0[2], v1[2]; s16x4 k2[2];
    const lds_ptr c0 = (lds_ptr)smem + K2_C0;
    lds_ptr pk1 = c0 + k1off + par * 8, pk2 = c0 + k2off + par * 8, pv = c0 + voff + par * KV_VT, pw = c0 + (voff ^ 128) + par * KV_VT;
    lds_ptr pk3 = pk1 + 512;
    asm volatile("" : "+v"(pk3));
#define LDS8(p) (*(const LDS_AS s16x4*)(p))
#define RD_K(t, j) do { k1[(t) & 1] = CAT8(LDS8(pk1 + (j) * 2 * KV_TILE), LDS8(pk3 + (j) * 2 * KV_TILE)); k2[(t) & 1] = LDS8(pk2 + (j) * 2 * KV_TILE); } while (0)
#define RD_V(t, j) do { v0[(t) & 1] = *(const LDS_AS s16x8*)(pv + (j) * 2 * KV_TILE); v1[(t) & 1] = *(const LDS_AS s16x8*)(pw + (j) * 2 * KV_TILE); } while (0)
    s16x8 q1[2]; s16x4 q2[2];
#pragma unroll
    for (int b = 0; b < 2; ++b) {
        const lds_ptr qp = (lds_ptr)smem + K2_Q0 + (wid & 3) * 2 * Q_TILE + b * 8 + l31 * 16;
        q1[b] = CAT8(LDS8(qp + h * 1024), LDS8(qp + h * 1024 + 512)); q2[b] = LDS8(qp + 2048);
    }
    {
        const s16x8 ka = CAT8(LDS8(c0 + k1off), LDS8(c0 + k1off + 512));
        const s16x4 kb = LDS8(c0 + k2off);
        RD_K(0, 0); RD_V(0, 0);
#pragma unroll
        for (int b = 0; b < 2; ++b) {
            if (h) q2[b] = s16x4{0, 0, 0, 0};
            f32x16 s = MFMA32(ka, q1[b], f32x16{});
            s = MFMA8(kb, q2[b], s);
            const float m = rbf(-max16(s));
            if (h) q2[b][0] = (short)(__float_as_uint(m) >> 16);
        }
        sc[0] = MFMA32(k1[0], q1[0], f32x16{});
        sc[0] = MFMA8(k2[0], q2[0], sc[0]);
    }
    STAMP(5); PSTAMP(42); PSTAMP(43);
#define EXP4(S, i) do { S[i] = __builtin_amdgcn_exp2f(S[i]); S[i + 1] = __builtin_amdgcn_exp2f(S[i + 1]); S[i + 2] = __builtin_amdgcn_exp2f(S[i + 2]); S[i + 3] = __builtin_amdgcn_exp2f(S[i + 3]); PIN(S); SBAR(); } while (0)
    Poll pl = {};
    s16x8 wa[5], wb[5];
    kb.r0 = (lds_ptr)smem + (wid >> 1) * 5120 + l16;
    auto request = [&](auto HS) __attribute__((always_inline)) {
        constexpr int hs = HS.v, p = (hs - 6) >> 3, r = (hs - 6) & 7;
        constexpr int w = (hs >= 9 && hs < 14) ? hs - 9 : (hs >= 17 && hs < 22) ? hs - 12 : (hs >= 54 && hs < 57) ? hs - 44 : hs >= 62 ? hs - 49 : -1;
        constexpr int e = (p >= 2 && p <= 4 && r >= 3) ? (p - 2) * 5 + r - 3 : p == 5 ? 15 + r : hs == 57 ? 23 : -1;
        if constexpr (hs < 3) glds16c(src - K2_CHUNK + hs * 8192, l16, lds0 + 2 * K2_SLOTB + wid * 1024 + hs * 8192);
        else if constexpr (hs >= 6 && p < 5 && r < 3) glds16c(src + p * K2_CHUNK + r * 8192, l16, lds0 + (p % 3) * K2_SLOTB + wid * 1024 + r * 8192);
        else if constexpr (w >= 0) k2_wpiece(kb, w / 5, w % 5);
        else if constexpr (e >= 0) { hf[e] = LD16(wsr, l16, (e < 12 ? hoff : hoff1) + ((e % 12) >> 1) * 8192 + (e & 1) * 1024); MEMFENCE(); }
        if constexpr (hs == 58) { if (DIAG_PF2) { PFLUSH(); } k2_wpiece(kb, 4, 4); }
        if constexpr (hs == 63) k2_wpiece(kb, 4, 0);
        if constexpr (WOWARM && hs == 3) {
            const unsigned wo_ = 6 * 40960 + min((((blockIdx.x >> 3) & 7) * 8 + wid) * 13 + min(lane, 12), 799) * 128;
            asm volatile("s_mov_b32 m0, %2\n\ts_nop 0\n\tglobal_load_lds_dword %0, %1 sc1" :: "v"(wo_), "s"(kb.wsrc - wid * 1024), "s"(kb.wdst - wid * 768 + LDS_JUNK) : "memory");
        }
        if (hs == 6 && wid == 4 && lane == 0) raise_flag(ws, WS_FB, tag);
        if (hs == 16 && wid == 4) poll_request(pl, ws + WS_FB, l16);
        SBAR();
    };
    static_for<0, 32>([&](auto T) __attribute__((always_inline)) {
        {
            constexpr int c = T.v >> 2, j = T.v & 3;
            if (j == 3) {
                PSTAMP(44 + 2 * c);
                if (c == 2 && wid == 4) poll_wait(pl, ws + WS_FB, l16, tag);
                if (c == 0) { asm volatile("s_waitcnt vmcnt(0)" ::: "memory"); LGKM_BAR(); } else if (c == 7) WAIT_BAR(0); else WAIT_BAR(7);
                PSTAMP(45 + 2 * c);
                if (c == 7 && par) __builtin_amdgcn_s_setprio(3); else __builtin_amdgcn_s_setprio(2);
                const int dsl = c == 0 ? K2_KV0 + K2_SLOTB - K2_C0 : (c % 3 == 2) ? -2 * K2_SLOTB : K2_SLOTB;
                pk1 += dsl; pk2 += dsl; pk3 += dsl; pv += dsl; pw += dsl;
                asm volatile("" : "+v"(pk1), "+v"(pk2), "+v"(pk3), "+v"(pv), "+v"(pw));
                SBAR();
            }
            if constexpr (T.v < 31) RD_K(j + 1, (j + 1) & 3);
            SBAR();
            if constexpr (T.v > 0) { oa[1] = SMF32(v1[(j + 1) & 1], hi8(sc[1]), oa[1]); PIN(oa[1]); }
            SBAR();
            EXP4(sc[0], 0);
            sc[1] = MFMA32(k1[j & 1], q1[1], f32x16{}); PIN(sc[1]); SBAR();
            request(IC<8 * c + 2 * j>{});
            EXP4(sc[0], 4);
            sc[1] = MFMA8(k2[j & 1], q2[1], sc[1]); PIN(sc[1]); SBAR();
            EXP4(sc[0], 8);
            oa[0] = SMF32(v0[j & 1], lo8(sc[0]), oa[0]); PIN(oa[0]); SBAR();
            EXP4(sc[0], 12);
            if constexpr (T.v < 31) RD_V(j + 1, (j + 1) & 3);
            if (j == 1 && c > 0) __builtin_amdgcn_s_setprio(1);
            if (j == 2 && c > 0) __builtin_amdgcn_s_setprio(0);
            SBAR();
            oa[0] = SMF32(v1[j & 1], hi8(sc[0]), oa[0]); PIN(oa[0]); SBAR();
            EXP4(sc[1], 0);
            if constexpr (T.v < 31) { sc[0] = MFMA32(k1[(j + 1) & 1], q1[0], f32x16{}); PIN(sc[0]); }
            SBAR();
            request(IC<8 * c + 2 * j + 1>{});
            EXP4(sc[1], 4);
            if constexpr (T.v < 31) { sc[0] = MFMA8(k2[(j + 1) & 1], q2[0], sc[0]); PIN(sc[0]); }
            else {
#pragma unroll
                for (int i = 0; i < 5; ++i) { wa[i] = *(const LDS_AS s16x8*)(kb.r0 + i * 1024); wb[i] = *(const LDS_AS s16x8*)(kb.r0 + 20480 + i * 1024); }
                if (par) {
#pragma unroll
                    for (int i = 0; i < 3; ++i) *(LDS_AS f32x4*)(lds + K2_XCH + (wid & 3) * 6144 + l16 + i * 1024) = f32x4{oa[0][4 * i], oa[0][4 * i + 1], oa[0][4 * i + 2], oa[0][4 * i + 3]};
                }
            }
            SBAR();
            EXP4(sc[1], 8);
            oa[1] = SMF32(v0[j & 1], lo8(sc[1]), oa[1]); PIN(oa[1]); SBAR();
            EXP4(sc[1], 12);
        }
    });
    oa[1] = SMF32(v1[1], hi8(sc[1]), oa[1]);
    unsigned qs_ = 0; (void)qs_;
    STAMP(2);
    PSTAMP(60); QSTAMP(60);
    int ln = __builtin_amdgcn_mbcnt_hi(~0u, __builtin_amdgcn_mbcnt_lo(~0u, 0u));
    PIN(ln);
    const int h2 = ln >> 5, l2 = ln & 31;
    const int mt = wid & 1, kq = wid >> 1;
    kb.lane = ln; kb.r0 = (lds_ptr)smem + kq * 5120 + ln * 16; kb.r1 = kb.r0 + 61440; kb.r2 = kb.r0 + 122880;
    asm volatile("" : "+v"(kb.r1), "+v"(kb.r2));
    kb.wsr = wsr; kb.osoff = WS_O16 + ((tp * 4 + mt * 2) * 20 + kq) * 1024;
    {
        const lds_ptr xp_ = lds + K2_XCH + (wid & 3) * 6144 + ln * 16;
        if (par) {
#pragma unroll
            for (int i = 3; i < 6; ++i) { const f32x16& g = oa[i / 3]; const int r = 4 * (i % 3); *(LDS_AS f32x4*)(xp_ + i * 1024) = f32x4{g[r], g[r + 1], g[r + 2], g[r + 3]}; }
        }
        LGKM_BAR();
        QSTAMP(21);
        if (!par) {
            __builtin_amdgcn_s_setprio(3);
            f32x4 tt[6];
#pragma unroll
            for (int i = 0; i < 6; ++i) tt[i] = *(const LDS_AS f32x4*)(xp_ + i * 1024);
            SBAR();
#pragma unroll
            for (int i = 0; i < 6; ++i) { const f32x4 t = tt[i]; f32x16& g = oa[i / 3]; const int r = 4 * (i % 3); g[r] = add1(g[r], t[0]); g[r + 1] = add1(g[r + 1], t[1]); g[r + 2] = add1(g[r + 2], t[2]); g[r + 3] = add1(g[r + 3], t[3]); }
            const int ob = (l2 >> 4) * 20 * 1024 + (l2 & 15) * 16, os = WS_O16 + qt0 * 2 * 20 * 1024;
            const int o1 = ob + h2 * 256 + os + (head >> 1) * 1024 + (head & 1) * 512, o2 = os + (16 + (head >> 3)) * 1024 + ((head >> 1) & 3) * 256 + (head & 1) * 8;
            auto put = [&](const f32x16& o, int b, u32x2& e) {
                const float l = __uint_as_float(__builtin_amdgcn_permlane32_swap(__float_as_uint(o[8]), __float_as_uint(o[8]), false, false)[1]);
                const float rl = __builtin_amdgcn_rcpf(l);
                unsigned w[6];
#pragma unroll
                for (int i = 0; i < 6; ++i) w[i] = cvtpk(mul1(o[2 * i], rl), mul1(o[2 * i + 1], rl));
                ST16(wsr, (u32x4{w[0], w[1], w[2], w[3]}), o1, b * 40 * 1024);
                e = u32x2{w[4], w[5]};
                return l;
            };
            u32x2 e0, e1;
            QSTAMP(22);
            const float l0 = put(oa[0], 0, e0), l1 = put(oa[1], 1, e1);
            ST8(wsr, (u32x2{__builtin_amdgcn_permlane32_swap(e0[0], e1[0], false, false)[0], __builtin_amdgcn_permlane32_swap(e0[1], e1[1], false, false)[0]}), ob + h2 * 40 * 1024, o2);
            MEMFENCE(); SBAR();
            QSTAMP(23);
            float big = 0.f;
#pragma unroll
            for (int r = 0; r < 12; ++r) big = add1(big, add1(fabsf(oa[0][r]), fabsf(oa[1][r])));
            if (__builtin_expect(__any(!(big < 1e30f) | !(fminf(l0, l1) > 1e-30f)), 0)) {
                const int gr = ((l2 & 4) << 2) | ((l2 >> 1) & 12) | (l2 & 3), g1 = h2 * 1024 + gr * 16, g2 = 2048 + gr * 16, gq = min(l2 >> 2, 5), gv = KV_V + gq * 256 + ((h2 ^ (gq & 3)) << 6) + (l2 & 3) * 16;
                asm volatile("s_waitcnt vmcnt(0)" ::: "memory");
#pragma unroll 1
                for (int b = 0; b < 2; ++b) {
                    const s16x8 qf1 = b ? q1[1] : q1[0];
                    s16x4 qf2 = b ? q2[1] : q2[0];
                    if (h2) qf2[0] = 0;
                    float m = -INFINITY;
                    f32x16 negm, o = f32x16{};
#pragma unroll 1
                    for (int t = 0; t < 64; ++t) {
                        const int g = WS_KV + head * KV_HEAD + (t >> 1) * 2 * KV_TILE + (t & 1) * 8;
                        f32x16 s = MFMA32(CAT8(LD8(wsr, g1, g), LD8(wsr, g1 + 512, g)), qf1, f32x16{});
                        s = MFMA8(LD8(wsr, g2, g), qf2, s);
                        m = fmaxf(m, max16(s));
                    }
#pragma unroll
                    for (int r = 0; r < 16; ++r) negm[r] = -m;
#pragma unroll 1
                    for (int t = 0; t < 64; ++t) {
                        const int g = WS_KV + head * KV_HEAD + (t >> 1) * 2 * KV_TILE + (t & 1) * 8;
                        f32x16 s = MFMA32(CAT8(LD8(wsr, g1, g), LD8(wsr, g1 + 512, g)), qf1, negm);
                        s = MFMA8(LD8(wsr, g2, g), qf2, s);
#pragma unroll
                        for (int r = 0; r < 16; ++r) s[r] = __builtin_amdgcn_exp2f(s[r]);
                        o = SMF32(LD16(wsr, gv, g + (t & 1) * (KV_VT - 8)), lo8(s), o);
                        o = SMF32(LD16(wsr, gv ^ 128, g + (t & 1) * (KV_VT - 8)), hi8(s), o);
                    }
                    u32x2 e;
                    put(o, b, e);
                    if (!h2) ST8(wsr, e, ob + b * 40 * 1024, o2);
                }
            }
        }
    }
    PSTAMP(61); QSTAMP(61);
    __builtin_amdgcn_s_setprio(0);
    const int ch0 = wid * 128 + ln, ch1 = ch0 + 64, er0 = ch0 / 10, er1 = ch1 / 10, ec0 = ch0 - er0 * 10, ec1 = ch1 - er1 * 10;
    const size_t eb = (size_t)tp * 64 * (D_ * 2) + cg * 160;
    const unsigned eo0 = er0 * (D_ * 2) + ec0 * 16, eo1 = er1 * (D_ * 2) + ec1 * 16;
    f32x4 acc[10];
#pragma unroll
    for (int i = 0; i < 10; ++i) acc[i] = f32x4{};
    s16x8 of[10];
#define HLO(i) __builtin_shufflevector(hf[2 * (i)], hf[2 * (i) + 1], 0, 1, 2, 3, 8, 9, 10, 11)
#define HHI(i) __builtin_shufflevector(hf[2 * (i)], hf[2 * (i) + 1], 4, 5, 6, 7, 12, 13, 14, 15)
    QSTAMP(0); k2_bstep<0, 0, 0, -1>(kb, acc, wa, wb, HLO(0), HHI(0), of);
    QSTAMP(1); k2_bstep<1, 0, 0, -1>(kb, acc, wb, wa, HLO(1), HHI(1), of);
    QSTAMP(2); k2_bstep<2, 0, 0, -1>(kb, acc, wa, wb, HLO(2), HHI(2), of);
    if (!par) {
        asm volatile("s_waitcnt vmcnt(0)" ::: "memory");
        if (ln == 0) __hip_atomic_store((gu64*)(ws + WS_FC) + (qt0 >> 1) * 32 + head, tag, __ATOMIC_RELAXED, __HIP_MEMORY_SCOPE_AGENT);
    } else {
#pragma unroll
        for (int i = 0; i < 10; ++i) glds16c(kb.wsb + 3 * 40960 - 4096 + i * 4096, ln * 16, kb.wdst - 4096 + i * 4096);
    }
    WAIT_BAR(10);
    QSTAMP(3); k2_bstep<3, 4, 3, -1>(kb, acc, wb, wa, HLO(3), HHI(3), of);
    QSTAMP(4); k2_bstep<4, 5, 5, -1>(kb, acc, wa, wb, HLO(4), HHI(4), of);
    WAIT_BAR(8);
    QSTAMP(5); k2_bstep<5, 6, 5, -1>(kb, acc, wb, wa, HLO(5), HHI(5), of);
    QSTAMP(6); k2_bstep<6, 0, 0, -1>(kb, acc, wa, wb, HLO(6), HHI(6), of);
    WAIT_BAR(10);
    QSTAMP(7); k2_bstep<7, 7, 5, -1>(kb, acc, wb, wa, HLO(7), HHI(7), of);
    unsigned long long seen = 0;
    const char* cw = ws + WS_FC + tp * 256;
    const unsigned cvo = l2 * 8;
    if (wid == 4) asm volatile("global_load_dwordx2 %0, %1, %2 sc1" : "+v"(seen) : "v"(cvo), "s"(cw) : "memory");
    SBAR();
    QSTAMP(8); k2_bstep<8, 0, 0, -1>(kb, acc, wa, wb, HLO(8), HHI(8), of);
    if (wid == 4) {
        asm volatile("s_waitcnt vmcnt(0)" : "+v"(seen) :: "memory");
#pragma unroll 1
        for (unsigned spin = 0; spin < (1u << 18) && !__all(seen == tag); ++spin) {
            __builtin_amdgcn_s_sleep(1);
            asm volatile("global_load_dwordx2 %0, %1, %2 sc1\n\ts_waitcnt vmcnt(0)" : "=&v"(seen) : "v"(cvo), "s"(cw) : "memory");
        }
    }
    WAIT_BAR(10);
    QSTAMP(9); k2_bstep<9, 0, 0, 0>(kb, acc, wb, wa, HLO(9), HHI(9), of);
    QSTAMP(10); k2_bstep<10, 0, 0, 5>(kb, acc, wa, wb, HLO(10), HHI(10), of);
    WAIT_BAR(10);
    QSTAMP(11); k2_bstep<11, 8, 3, -1>(kb, acc, wb, wa, HLO(11), HHI(11), of);
    u32x4 xv0 = u32x4{}, xv1 = u32x4{};
    if (wid < 5) { xv0 = *(const u32x4*)(x + eb + eo0); xv1 = *(const u32x4*)(x + eb + eo1); }
    MEMFENCE(); SBAR();
    QSTAMP(12); k2_bstep<12, 0, 0, -1>(kb, acc, wa, wb, of[0], of[1], of);
    QSTAMP(13); k2_bstep<13, 0, 0, -1>(kb, acc, wb, wa, of[2], of[3], of);
    QSTAMP(14); k2_bstep<14, 0, 0, -1>(kb, acc, wa, wb, of[4], of[5], of);
    WAIT_BAR(0);
    QSTAMP(15); k2_bstep<15, 0, 0, -1>(kb, acc, wb, wa, of[6], of[7], of);
    QSTAMP(16); k2_bstep<16, 0, 0, -1>(kb, acc, wa, wb, of[8], of[9], of);
    QSTAMP(17);
    {
        const int tk = ln & 15;
        const lds_ptr pp = (lds_ptr)smem + kq * 20480 + (mt * 32 + tk) * 320 + (((ln >> 4) ^ ((tk >> 1) & 3)) << 4);
#pragma unroll
        for (int i = 0; i < 10; ++i) *(LDS_AS f32x4*)(pp + (i & 1) * 16 * 320 + (i >> 1) * 64) = acc[i];
        QSTAMP(18);
        LGKM_BAR();
        QSTAMP(19);
        if (wid < 5) {
#pragma unroll
            for (int e = 0; e < 2; ++e) {
                const int er = e ? er1 : er0, ec = e ? ec1 : ec0, sw = (er >> 1) & 3;
                const lds_ptr p0 = (lds_ptr)smem + er * 320 + (((2 * ec) ^ sw) << 4), p1 = (lds_ptr)smem + er * 320 + (((2 * ec + 1) ^ sw) << 4);
                f32x4 a = *(const LDS_AS f32x4*)p0, b = *(const LDS_AS f32x4*)p1;
#pragma unroll
                for (int k = 1; k < 4; ++k) { a += *(const LDS_AS f32x4*)(p0 + k * 20480); b += *(const LDS_AS f32x4*)(p1 + k * 20480); }
                const u32x4 xv = e ? xv1 : xv0;
                const u32x4 ov = u32x4{cvtpk(a[0] + bf_lo(xv[0]), a[1] + bf_hi(xv[0])), cvtpk(a[2] + bf_lo(xv[1]), a[3] + bf_hi(xv[1])),
                                       cvtpk(b[0] + bf_lo(xv[2]), b[1] + bf_hi(xv[2])), cvtpk(b[2] + bf_lo(xv[3]), b[3] + bf_hi(xv[3]))};
                __builtin_amdgcn_raw_buffer_store_b128(ov, __builtin_amdgcn_make_buffer_rsrc(out, 0, S_ * D_ * 2, 0x00020000), (int)eb + (e ? eo1 : eo0), 0, OUTAUX);
            }
        }
    }
    QSTAMP(20);
    if (CODEWARM) asm volatile(".Lfb_end:");
    STAMP(7); STAMP_FLUSH(0); QFLUSH();
}

extern "C" void kernel_launch(void* const* d_in, const int* in_sizes, int n_in, void* d_out, int out_size, void* d_ws, size_t ws_size,
                              hipStream_t stream) {
    const char* x = (const char*)d_in[0];
    const char* wqkv = (const char*)d_in[1];
    const char* wo = (const char*)d_in[2];
    const char* wg = (const char*)d_in[4];
    const char* wu = (const char*)d_in[5];
    const char* wd = (const char*)d_in[6];
    char* ws = (char*)d_ws;
    (void)hipFuncSetAttribute((const void*)fused_block, hipFuncAttributeMaxDynamicSharedMemorySize, LDS_BYTES);
    fused_block<<<256, 512, LDS_BYTES, stream>>>(x, wqkv, wg, wu, wd, wo, ws, (char*)d_out);
}
```

```cpp
#include <hip/hip_runtime.h>
#include <hip/hip_bf16.h>
#include <stdint.h>

typedef __bf16 bfx8 __attribute__((ext_vector_type(8)));
typedef __bf16 bfx16 __attribute__((ext_vector_type(16)));
typedef short s16x4 __attribute__((ext_vector_type(4)));
typedef short s16x8 __attribute__((ext_vector_type(8)));
typedef short s16x16 __attribute__((ext_vector_type(16)));
typedef float f32x2 __attribute__((ext_vector_type(2)));
typedef float f32x4 __attribute__((ext_vector_type(4)));
typedef float f32x8 __attribute__((ext_vector_type(8)));
typedef float f32x16 __attribute__((ext_vector_type(16)));
typedef unsigned u32x2 __attribute__((ext_vector_type(2)));
typedef unsigned u32x4 __attribute__((ext_vector_type(4)));
#define LDS_AS __attribute__((address_space(3)))
typedef LDS_AS char* lds_ptr;

constexpr int S_ = 2048, D_ = 640, H_ = 32, F_ = 1536;

constexpr int Q_TILE = 1280, Q_HEAD = 64 * Q_TILE, KV_TILE = 2816, KV_V = 2560, KV_VT = 1536, KV_HEAD = 64 * KV_TILE;
constexpr int WS_Q = 0;
constexpr int WS_KV = WS_Q + H_ * Q_HEAD;
constexpr int WS_O16 = WS_KV + H_ * KV_HEAD + 8192;
constexpr int WS_H16 = WS_O16 + 128 * 20 * 1024;
constexpr int WS_WP = WS_H16 + 128 * 48 * 1024;
constexpr int WS_EPOCH = WS_WP + 8 * 68 * 5120 + 8192;
constexpr int WS_FA = WS_EPOCH + 256, WS_FB = WS_FA + 2048, WS_FC = WS_FB + 2048, WS_END = WS_FC + 8192;

#define SBAR() __builtin_amdgcn_sched_barrier(0)
template <int V> struct IC { static constexpr int v = V; };
template <int I, int N, class F> __device__ __forceinline__ void static_for(F&& f) { if constexpr (I < N) { f(IC<I>{}); static_for<I + 1, N>(f); } }
#define MFMA32(a, b, c) __builtin_amdgcn_mfma_f32_32x32x16_bf16((bfx8)(a), (bfx8)(b), (c), 0, 0, 0)

__device__ __forceinline__ unsigned cvtpk(float lo, float hi) {
    typedef __bf16 bfx2 __attribute__((ext_vector_type(2)));
    bfx2 r = {(__bf16)lo, (__bf16)hi};
    return __builtin_bit_cast(unsigned, r);
}
__device__ __forceinline__ s16x4 trread(const LDS_AS char* p) {
    return __builtin_amdgcn_ds_read_tr16_b64_v4i16((LDS_AS s16x4*)p);
}
__device__ __forceinline__ float rbf(float v) { return (float)(__bf16)v; }
__device__ __forceinline__ float bf_lo(unsigned u) { return __uint_as_float(u << 16); }
__device__ __forceinline__ float bf_hi(unsigned u) { return __uint_as_float(u & 0xffff0000u); }
__device__ __forceinline__ float swap32_max(float v) {
    auto r = __builtin_amdgcn_permlane32_swap(__float_as_uint(v), __float_as_uint(v), false, false);
    return fmaxf(__uint_as_float(r[0]), __uint_as_float(r[1]));
}

constexpr int FA_POS = 14;
constexpr int P1_WSLOT = 24576, P1_X0 = 3 * P1_WSLOT, P1_XSLOT = 16384, P1_STAT = P1_X0 + 5 * P1_XSLOT;
constexpr int P1_COLA = P1_STAT + 1024, P1_COLB = P1_COLA + 512, P1_TAG = P1_COLB + 1024, LDS_JUNK = 161792, LDS_BYTES = 163840;
constexpr int K2_CHUNK = 8 * KV_TILE, K2_SLOTB = 24576, K2_KV0 = 81920, K2_C0 = 2 * P1_WSLOT, K2_Q0 = 0;
typedef __attribute__((address_space(1))) unsigned long long gu64;

#define PIN(x) asm volatile("" : "+v"(x))
#define MEMFENCE() asm volatile("" ::: "memory")
#define WAIT_BAR(N) do { MEMFENCE(); __builtin_amdgcn_s_waitcnt(0x70 | ((N) & 15) | (((N) >> 4) << 14)); __builtin_amdgcn_s_barrier(); MEMFENCE(); SBAR(); } while (0)
#define LGKM_BAR() do { MEMFENCE(); __builtin_amdgcn_s_waitcnt(0xC07F); __builtin_amdgcn_s_barrier(); MEMFENCE(); SBAR(); } while (0)
#define MFMA16(a, b, c) __builtin_amdgcn_mfma_f32_16x16x32_bf16((bfx8)(a), (bfx8)(b), (c), 0, 0, 0)
#define SMF16(a, b, c, i) __builtin_amdgcn_smfmac_f32_16x16x64_bf16((bfx8)(a), (bfx16)(b), (c), (i), 0, 0)
#define CAT8(a, b) __builtin_shufflevector(a, b, 0, 1, 2, 3, 4, 5, 6, 7)
#define CAT16(a, b) __builtin_shufflevector(a, b, 0, 1, 2, 3, 4, 5, 6, 7, 8, 9, 10, 11, 12, 13, 14, 15)

__device__ __forceinline__ void glds16s(const char* sbase, unsigned voff, unsigned lds_base) {
    asm volatile("s_mov_b32 m0, %2\n\ts_nop 0\n\tglobal_load_lds_dwordx4 %0, %1" :: "v"(voff), "s"(sbase), "s"(lds_base) : "memory");
}
__device__ __forceinline__ void glds16c(const char* sbase, unsigned voff, unsigned lds_base) {
    asm volatile("s_mov_b32 m0, %2\n\ts_nop 0\n\tglobal_load_lds_dwordx4 %0, %1 sc1" :: "v"(voff), "s"(sbase), "s"(lds_base) : "memory");
}
typedef __amdgpu_buffer_rsrc_t rsrc_t;
#define ST16(rs, v, voff, soff) __builtin_amdgcn_raw_buffer_store_b128((v), (rs), (voff) + (soff), 0, 16)
#define ST8(rs, v, voff, soff) __builtin_amdgcn_raw_buffer_store_b64((v), (rs), (voff), (soff), 16)
#define LD16(rs, voff, soff) __builtin_bit_cast(s16x8, __builtin_amdgcn_raw_buffer_load_b128((rs), (voff), (soff), 16))
#define LD8(rs, voff, soff) __builtin_bit_cast(s16x4, __builtin_amdgcn_raw_buffer_load_b64((rs), (voff), (soff), 16))
__device__ __forceinline__ f32x2 pk4(const f32x4& a, int i) { return f32x2{a[i], a[i + 1]}; }
__device__ __forceinline__ f32x2 pfma(f32x2 a, f32x2 b, f32x2 c) { return __builtin_elementwise_fma(a, b, c); }
__device__ __forceinline__ unsigned cvt2(f32x2 v) { return cvtpk(v[0], v[1]); }
__device__ __forceinline__ float add1(float a, float b) { float r = a + b; asm volatile("" : "+v"(r)); return r; }
__device__ __forceinline__ float fma1(float a, float b, float c) { float r = fmaf(a, b, c); asm volatile("" : "+v"(r)); return r; }
__device__ __forceinline__ float mul1(float a, float b) { float r = a * b; asm volatile("" : "+v"(r)); return r; }

__device__ __forceinline__ void raise_flag(char* ws, int arr, unsigned long long tag) {
    __hip_atomic_store((gu64*)(ws + arr) + blockIdx.x, tag, __ATOMIC_RELAXED, __HIP_MEMORY_SCOPE_AGENT);
}
struct Poll { u32x4 a, b; };
__device__ __forceinline__ void poll_request(Poll& p, const char* arr, unsigned voff) {
    asm volatile("global_load_dwordx4 %0, %2, %3 sc1\n\tglobal_load_dwordx4 %1, %2, %3 offset:1024 sc1" : "+v"(p.a), "+v"(p.b) : "v"(voff), "s"(arr) : "memory");
}
__device__ __forceinline__ bool poll_check(Poll& p, unsigned long long tag) {
    const unsigned tl = (unsigned)tag, th = (unsigned)(tag >> 32);
    asm volatile("s_waitcnt vmcnt(0)" : "+v"(p.a), "+v"(p.b) :: "memory");
    return __all(p.a[0] == tl && p.a[1] == th && p.a[2] == tl && p.a[3] == th && p.b[0] == tl && p.b[1] == th && p.b[2] == tl && p.b[3] == th);
}
__device__ __forceinline__ void poll_wait(Poll& p, const char* arr, unsigned voff, unsigned long long tag) {
#pragma unroll 1
    for (unsigned spin = 0; spin < (1u << 18) && !poll_check(p, tag); ++spin) {
        __builtin_amdgcn_s_sleep(4);
        poll_request(p, arr, voff);
    }
    asm volatile("s_waitcnt vmcnt(0)" : "+v"(p.a), "+v"(p.b) :: "memory");
}

template <int MODE, class FM>
__device__ __forceinline__ void p1_pass(f32x4 (&acc)[4][MODE == 2 ? 6 : 4], lds_ptr x0, lds_ptr x1, lds_ptr (&wp)[MODE == 2 ? 2 : 4], int start, FM&& mid) {
    constexpr int NC = MODE == 2 ? 6 : 4, NR = 4 + 2 * NC, WS0 = MODE == 2 ? 1 : 0;
    struct F { s16x8 x[4]; s16x4 w[NC][2]; } f[2];
    auto rd = [&](F& n, int ks, int i) __attribute__((always_inline)) {
        if (i == 0 || i > 2 * NC) { const int m = i == 0 ? 0 : i - 2 * NC; n.x[m] = *(const LDS_AS s16x8*)((ks ? x1 : x0) + m * 2048); }
        else {
            const int t = (i - 1) >> 1, e = (i - 1) & 1, c = t % 3;
            n.w[t][e] = trread(MODE == 2 ? wp[c & 1] + ks * 12288 + (t / 3) * 3072 + (c >> 1) * 1024 + e * 256 : wp[MODE == 2 ? 0 : t] + ks * 8192 + e * 512);
        }
    };
    auto step = [&](const F& c, F& n, int ks, bool more) __attribute__((always_inline)) {
#pragma unroll
        for (int i = 0; i < 4 * NC; ++i) {
            const s16x8 w = CAT8(c.w[i % NC][0], c.w[i % NC][1]);
            f32x4& a = acc[i / NC][i % NC];
            a = MODE == 1 ? MFMA16(c.x[i / NC], w, a) : MFMA16(w, c.x[i / NC], a);
            PIN(a); SBAR();
            if (more && i < NR) { rd(n, ks, i); SBAR(); }
        }
    };
#pragma unroll
    for (int i = 0; i < NR; ++i) rd(f[0], 0, i);
    SBAR();
    static_for<0, 10>([&](auto N) __attribute__((always_inline)) {
        constexpr int n = N.v, dw = (WS0 + n) % 3 == 2 ? -2 * P1_WSLOT : P1_WSLOT;
        const int dx = n % 5 == 4 - start ? -4 * P1_XSLOT : P1_XSLOT;
        step(f[0], f[1], 1, true);
        x0 += dx; x1 += dx; PIN(x0); PIN(x1);
#pragma unroll
        for (int i = 0; i < (MODE == 2 ? 2 : 4); ++i) { wp[i] += dw; PIN(wp[i]); }
        mid(N);
        LGKM_BAR();
        step(f[1], f[0], 0, n < 9);
    });
}

struct Pack { const char *wd, *wo; int job; };
__device__ __forceinline__ void pack_fetch(const Pack& pk_, lds_ptr lds, int wid, int lane) {
    const int step = pk_.job / 10, cb = pk_.job - step * 10, r = lane >> 3;
    const unsigned va = ((r & 3) + (step < 48 ? 16 : 8) * (r >> 2)) * (D_ * 2) + (lane & 7) * 16, vb = ((r >> 2) * 20 + 16 + (r & 3)) * (D_ * 2) + (lane & 7) * 16;
#pragma unroll
    for (int g = 0; g < 4; ++g) {
        const int ks = (step - 48) * 2 + (g >> 1), hh = g & 1;
        const char* a = step < 48 ? pk_.wd + (step * 32 + g * 4) * (D_ * 2) : ks < 32 ? pk_.wo + (ks * 20 + hh * 4) * (D_ * 2) : pk_.wo + ((ks - 32) * 4 + hh * 2) * 20 * (D_ * 2);
        glds16s(a + cb * 128, step < 64 ? va : vb, (unsigned)(uintptr_t)lds + wid * P1_WSLOT + 16384 + g * 1024);
    }
}
__device__ __forceinline__ void pack_read(s16x8 (&pf)[4], lds_ptr lds, int wid, int lane) {
    asm volatile("s_waitcnt vmcnt(0)" ::: "memory");
    const lds_ptr pr = lds + wid * P1_WSLOT + 16384 + (lane >> 4) * 1024 + ((lane >> 2) & 3) * 128 + (lane & 3) * 8;
#pragma unroll
    for (int nt = 0; nt < 4; ++nt) pf[nt] = CAT8(trread(pr + nt * 32), trread(pr + nt * 32 + 512));
}
__device__ __forceinline__ void pack_store(const Pack& pk_, const s16x8 (&pf)[4], int lane, rsrc_t wsr) {
    const int step = pk_.job / 10, cb = pk_.job - step * 10;
#pragma unroll
    for (int nt = 0; nt < 4; ++nt) {
        const int N = cb * 4 + nt, cg = N / 5;
        ST16(wsr, __builtin_bit_cast(u32x4, pf[nt]), lane * 16, WS_WP + ((cg * 68 + step) * 5 + N - cg * 5) * 1024);
    }
}

constexpr int OOB = 0x7ffffff0;

__device__ __forceinline__ void p1_compute(lds_ptr lds, int wid, int lane, int panel, int grp, int ra, int rb, rsrc_t wsr, const Pack& pk_) {
    const int l15 = lane & 15, g4 = lane >> 4, q = (lane >> 2) & 3, wm = wid & 1, wn = wid >> 1, tt = panel * 4 + wm * 2;
    const LDS_AS float* rstd = (const LDS_AS float*)(lds + P1_STAT) + wm * 64;
    const int xo = P1_X0 + (wm * 64 + l15) * 128 + ((g4 ^ (l15 >> 1)) << 4);
    if (pk_.job < 680) pack_fetch(pk_, lds, wid, lane);
    {
        f32x4 acc[4][4];
#pragma unroll
        for (int i = 0; i < 16; ++i) acc[i >> 2][i & 3] = f32x4{};
        s16x8 pf[4];
        const LDS_AS float* colf = (const LDS_AS float*)(lds + P1_COLA) + wn * 64;
        const int sec = grp / 5, q0 = (grp - sec * 5) * 32 + wn * 16;
        lds_ptr wp[4];
#pragma unroll
        for (int t = 0; t < 4; ++t) wp[t] = lds + g4 * 2048 + wn * 1024 + q * 128 + (lane & 3) * 8 + ((t ^ (q >> 1) ^ ((g4 & 1) << 1)) << 5);
        auto mid = [&](auto N) __attribute__((always_inline)) { if constexpr (N.v == 5) pack_read(pf, lds, min(wid, 2), lane); };
        LGKM_BAR();
        f32x2 t[32];
        int vo[4], vs;
        if (sec < 2) {
            p1_pass<0>(acc, lds + xo + ra * P1_XSLOT, lds + (xo ^ 64) + ra * P1_XSLOT, wp, ra, mid);
            f32x4 cv[4]; float rs[4], nm[4];
#pragma unroll
            for (int i = 0; i < 4; ++i) { cv[i] = *(const LDS_AS f32x4*)(colf + i * 16 + g4 * 4); nm[i] = rstd[128 + i * 16 + l15]; rs[i] = rstd[i * 16 + l15]; }
            const float qs = sec ? 1.0f : 0.22360679774997896f * 1.4426950408889634f;
#pragma unroll
            for (int c = 0; c < 4; ++c) { const int qd = q0 + 4 * c + g4, hd = (qd * 205) >> 10; vo[c] = (sec ? WS_KV + tt * KV_TILE + hd * KV_HEAD : WS_Q + tt * Q_TILE + hd * Q_HEAD) + (qd - hd * 5) * 512 + l15 * 16; }
            vs = 256;
#pragma unroll
            for (int k = 0; k < 32; ++k) { const int m = (k & 2) + ((k >> 2) & 1); t[k] = pfma(pk4(cv[k >> 3], (k & 1) * 2), f32x2{nm[m], nm[m]}, pk4(acc[m][k >> 3], (k & 1) * 2)); }
            SBAR();
#pragma unroll
            for (int k = 0; k < 32; ++k) { const float r = rs[(k & 2) + ((k >> 2) & 1)] * qs; t[k] *= f32x2{r, r}; }
        } else {
            if (sec == 2) p1_pass<1>(acc, lds + xo + ra * P1_XSLOT, lds + (xo ^ 64) + ra * P1_XSLOT, wp, ra, mid);
            else static_for<0, 10>([&](auto N) __attribute__((always_inline)) { mid(N); LGKM_BAR(); });
            f32x4 rs[4], nm[4]; float cv[4];
#pragma unroll
            for (int i = 0; i < 4; ++i) { cv[i] = colf[i * 16 + l15]; nm[i] = *(const LDS_AS f32x4*)(rstd + 128 + i * 16 + g4 * 4); rs[i] = *(const LDS_AS f32x4*)(rstd + i * 16 + g4 * 4); }
            const int so = WS_KV + tt * KV_TILE + KV_V;
#pragma unroll
            for (int c = 0; c < 4; ++c) { const int qd = q0 + 4 * c + q, hd = (qd * 205) >> 10, pl = qd - hd * 5; vo[c] = sec == 2 ? so + hd * KV_HEAD + pl * 256 + ((g4 ^ (pl & 3)) << 6) + (lane & 3) * 16 : OOB - KV_VT; }
            vs = KV_VT;
#pragma unroll
            for (int k = 0; k < 32; ++k) { const int m = (k >> 1) & 3; t[k] = pfma(pk4(nm[m], (k & 1) * 2), f32x2{cv[k >> 3], cv[k >> 3]}, pk4(acc[m][k >> 3], (k & 1) * 2)); }
            SBAR();
#pragma unroll
            for (int k = 0; k < 32; ++k) t[k] *= pk4(rs[(k >> 1) & 3], (k & 1) * 2);
            const int hd = q0 / 5 + g4, oo = sec == 2 && hd * 5 + 4 < q0 + 16 ? so + hd * KV_HEAD + 5 * 256 + l15 * 16 : OOB - KV_VT;
            const unsigned ov = (lane & 3) ? 0u : 0x3f803f80u;
            ST16(wsr, (u32x4{ov, ov, ov, ov}), oo, 0); ST16(wsr, (u32x4{ov, ov, ov, ov}), oo, KV_VT);
        }
        SBAR();
#pragma unroll
        for (int i = 0; i < 8; ++i) ST16(wsr, (u32x4{cvt2(t[4 * i]), cvt2(t[4 * i + 1]), cvt2(t[4 * i + 2]), cvt2(t[4 * i + 3])}), vo[i >> 1], (i & 1) * vs);
        if (pk_.job < 680) pack_store(pk_, pf, lane, wsr);
    }
    f32x4 acc[4][6];
#pragma unroll
    for (int i = 0; i < 24; ++i) acc[i / 6][i % 6] = f32x4{};
    {
        const int wo0 = P1_WSLOT + (g4 >> 1) * 6144 + (g4 & 1) * 512 + q * 64 + (lane & 3) * 8, T0 = wn * 3, T1 = T0 + 1;
        lds_ptr wp[2] = {lds + wo0 + (T0 >> 1) * 1024 + (((T0 ^ g4) & 1) << 5), lds + wo0 + (T1 >> 1) * 1024 + (((T1 ^ g4) & 1) << 5)};
        p1_pass<2>(acc, lds + xo + rb * P1_XSLOT, lds + (xo ^ 64) + rb * P1_XSLOT, wp, rb, [&](auto N) __attribute__((always_inline)) {
            if constexpr (N.v == FA_POS - 11) asm volatile("s_waitcnt vmcnt(0)" ::: "memory");
        });
    }
    {
        const LDS_AS float* colf = (const LDS_AS float*)(lds + P1_COLB) + wn * 48 + g4 * 4;
        f32x4 cg[3], cu[3];
#pragma unroll
        for (int c = 0; c < 3; ++c) { cg[c] = *(const LDS_AS f32x4*)(colf + c * 16); cu[c] = *(const LDS_AS f32x4*)(colf + 96 + c * 16); }
        float nm[4], r2[4];
#pragma unroll
        for (int m = 0; m < 4; ++m) { const float r = rstd[m * 16 + l15]; nm[m] = rstd[128 + m * 16 + l15]; r2[m] = r * r; }
        const int vo = lane * 16 + WS_H16 + (tt * 96 + grp * 6 + wn * 3) * 1024;
#pragma unroll
        for (int mm = 0; mm < 2; ++mm)
#pragma unroll
            for (int c = 0; c < 3; ++c) {
                float g[8], u[8];
#pragma unroll
                for (int i = 0; i < 8; ++i) { const int m = mm * 2 + (i >> 2); g[i] = fma1(cg[c][i & 3], nm[m], acc[m][c][i & 3]); u[i] = fma1(cu[c][i & 3], nm[m], acc[m][3 + c][i & 3]); }
#pragma unroll
                for (int i = 0; i < 8; ++i) { g[i] = fmaxf(g[i], 0.f); u[i] = mul1(u[i], r2[mm * 2 + (i >> 2)]); }
#pragma unroll
                for (int i = 0; i < 8; ++i) g[i] = mul1(g[i], u[i]);
                ST16(wsr, (u32x4{cvtpk(g[0], g[1]), cvtpk(g[2], g[3]), cvtpk(g[4], g[5]), cvtpk(g[6], g[7])}), vo, (mm * 96 + c) * 1024);
            }
    }
}

__device__ __forceinline__ void glds_x4(const char* sbase, const unsigned (&v)[4], unsigned lds_base) {
    asm volatile("s_mov_b32 m0, %5\n\ts_nop 0\n\tglobal_load_lds_dwordx4 %0, %4\n\tglobal_load_lds_dwordx4 %1, %4 offset:1024\n\t"
                 "global_load_lds_dwordx4 %2, %4 offset:2048\n\tglobal_load_lds_dwordx4 %3, %4 offset:3072"
                 :: "v"(v[0]), "v"(v[1]), "v"(v[2]), "v"(v[3]), "s"(sbase), "s"(lds_base) : "memory");
}
__device__ __forceinline__ void glds_x3(const char* sbase, const unsigned* v, unsigned lds_base) {
    asm volatile("s_mov_b32 m0, %4\n\ts_nop 0\n\tglobal_load_lds_dwordx4 %0, %3\n\tglobal_load_lds_dwordx4 %1, %3 offset:1024\n\tglobal_load_lds_dwordx4 %2, %3 offset:2048"
                 :: "v"(v[0]), "v"(v[1]), "v"(v[2]), "s"(sbase), "s"(lds_base) : "memory");
}

__device__ __forceinline__ unsigned long long p1_helper(lds_ptr lds, int hw, int lane, const char* xp, const char* wq, const char* wgp, const char* wup, int ra, int rb,
                                          char* ws, const char* kv, const char* qsrc, const unsigned (&xv)[4], const unsigned (&wa)[4]) {
    const unsigned lds0 = (unsigned)(uintptr_t)lds;
    unsigned wb[3];
#pragma unroll
    for (int j = 0; j < 3; ++j) wb[j] = 4096 + (hw * 16 + (lane >> 2)) * (F_ * 2) + j * 64 + (((lane & 3) ^ ((lane >> 5) * 2)) << 4) - j * 1024;
    xp -= 4096; wq -= 4096; wgp -= 4096; wup -= 4096;
    int kb_ = 0, xs = ra, dws = 0, left = 5;
    auto issue1 = [&](bool pa) {
        if (pa) glds_x4(wq + (unsigned)((kb_ + xs) * 64 * 3 * D_ * 2), wa, lds0 + dws * P1_WSLOT + hw * 4096);
        else glds_x3(wgp + (unsigned)((kb_ + xs) * 64 * F_ * 2), wb, lds0 + dws * P1_WSLOT + hw * 6144);
        SBAR();
    };
    auto issue2 = [&](bool pa, bool xx) {
        SBAR();
        if (!pa) glds_x3(wup + (unsigned)((kb_ + xs) * 64 * F_ * 2), wb, lds0 + dws * P1_WSLOT + hw * 6144 + 3072);
        xs = xs == 4 ? 0 : xs + 1; dws = dws == 2 ? 0 : dws + 1; --left; kb_ = left == 0 ? 5 - kb_ : kb_; left = left == 0 ? 5 : left;
        if (xx) glds_x4(xp + (kb_ + xs) * 128, xv, lds0 + P1_X0 + xs * P1_XSLOT + hw * 4096);
    };
    issue2(true, true);
    __builtin_amdgcn_s_barrier();
    unsigned long long ep = 0;
    asm volatile("global_load_dwordx2 %0, %1, %2 sc1" : "+v"(ep) : "v"(0u), "s"(ws + WS_EPOCH) : "memory");
    __builtin_amdgcn_s_setprio(3);
    const int r16 = lane & 15, h = lane >> 5, blk = (lane >> 4) & 1, q = (lane >> 2) & 3;
    const unsigned olo = r16 < 2 ? 0x3f803f80u : 0u;
    const s16x8 ones2 = __builtin_bit_cast(s16x8, (u32x4{olo, olo, olo, olo}));
    const int idx = (r16 & 1) ? 0xEEEE : 0x4444;
    int ws3 = 0;
    {
        const int bo = (lane >> 4) * 2048 + (hw >> 1) * 1024 + q * 128 + (lane & 3) * 8 + ((((hw & 1) * 2) ^ (q >> 1) ^ (blk << 1)) << 5);
        lds_ptr b = lds + bo, b1 = lds + (bo ^ 32);
        const int so = P1_X0 + ra * P1_XSLOT + (hw * 32 + r16) * 128 + (((lane >> 4) ^ (r16 >> 1)) << 4);
        lds_ptr sp0 = lds + so, sp1 = lds + (so ^ 64);
        f32x4 cs = f32x4{}, cs1 = f32x4{}, s1[2] = {f32x4{}, f32x4{}}, s2[2] = {f32x4{}, f32x4{}};
        s16x16 wv0, wv1; s16x8 xa[2], xb[2];
        auto col_rd = [&]() {
            wv0 = CAT16(CAT8(trread(b), trread(b + 512)), CAT8(trread(b + 8192), trread(b + 8192 + 512)));
            wv1 = CAT16(CAT8(trread(b1), trread(b1 + 512)), CAT8(trread(b1 + 8192), trread(b1 + 8192 + 512)));
            const int dw = ws3 == 2 ? -2 * P1_WSLOT : P1_WSLOT;
            b += dw; b1 += dw; ws3 = ws3 == 2 ? 0 : ws3 + 1;
        };
        auto row_rd = [&](int dx) {
#pragma unroll
            for (int t = 0; t < 2; ++t) { xa[t] = *(const LDS_AS s16x8*)(sp0 + t * 2048); xb[t] = *(const LDS_AS s16x8*)(sp1 + t * 2048); }
            sp0 += dx; sp1 += dx;
        };
        auto row_mm = [&]() {
#pragma unroll
            for (int t = 0; t < 2; ++t) { s2[t] = MFMA16(xa[t], xa[t], s2[t]); s1[t] = SMF16(ones2, CAT16(xa[t], xb[t]), s1[t], idx); }
#pragma unroll
            for (int t = 0; t < 2; ++t) { s2[t] = MFMA16(xb[t], xb[t], s2[t]); PIN(s1[t]); PIN(s2[t]); }
        };
#pragma unroll
        for (int n = 0; n < 9; ++n) {
            if (n == 0) WAIT_BAR(5); else if (n < 8) WAIT_BAR(12); else WAIT_BAR(8);
            col_rd(); row_rd(n % 5 == 4 - ra ? -4 * P1_XSLOT : P1_XSLOT);
            if (n == 0) { issue1(true); issue2(true, true); }
            if (n == 8) { kb_ = 5; xs = rb; }
            issue1(n < 8);
            cs = SMF16(ones2, wv0, cs, idx);
            row_mm();
            cs1 = SMF16(ones2, wv1, cs1, idx); PIN(cs); PIN(cs1);
            issue2(n < 8, n < 7);
        }
        MEMFENCE(); __builtin_amdgcn_s_waitcnt(0xF70 | 10); MEMFENCE();
        row_rd(0); row_mm();
        {
            const int t = blk;
            const float d0 = (lane & 2) ? ((lane & 1) ? s2[0][3] : s2[0][2]) : ((lane & 1) ? s2[0][1] : s2[0][0]);
            const float d1 = (lane & 2) ? ((lane & 1) ? s2[1][3] : s2[1][2]) : ((lane & 1) ? s2[1][1] : s2[1][0]);
            const float a0 = __shfl(s1[0][0] + s1[0][1], r16), a1 = __shfl(s1[1][0] + s1[1][1], r16), c0 = __shfl(d0, r16 + 16 * q), c1 = __shfl(d1, r16 + 16 * q);
            const float a = t ? a1 : a0, c = t ? c1 : c0;
            const float mean = a * (1.0f / D_), var = fmaxf(c * (1.0f / D_) - mean * mean, 0.f);
            const float rstd = __builtin_amdgcn_rcpf(rbf(__builtin_amdgcn_sqrtf(rbf(rbf(var) + 1e-5f))));
            ((LDS_AS float*)(lds + P1_STAT))[h * 128 + hw * 32 + (lane & 31)] = h ? -rbf(mean) : rstd;
        }
        WAIT_BAR(6);
        col_rd();
        issue1(false);
        cs = SMF16(ones2, wv0, cs, idx);
        cs1 = SMF16(ones2, wv1, cs1, idx); PIN(cs); PIN(cs1);
        issue2(false, false);
        LDS_AS float* colf = (LDS_AS float*)(lds + P1_COLA) + hw * 32;
        if (lane < 16) { colf[lane] = cs[0] + cs[1]; colf[16 + lane] = cs1[0] + cs1[1]; }
    }
    asm volatile("" : "+v"(ep));
    const unsigned long long tag = (((unsigned long long)(unsigned)__builtin_amdgcn_readfirstlane((unsigned)(ep >> 32)) << 32) | (unsigned)__builtin_amdgcn_readfirstlane((unsigned)ep)) * 6364136223846793005ull + 1442695040888963407ull;
    if (hw == 0 && lane == 0) *(LDS_AS unsigned long long*)(lds + P1_TAG) = tag;
    {
        const int g4 = lane >> 4, ro = P1_WSLOT + (g4 >> 1) * 6144 + (g4 & 1) * 512 + q * 64 + (lane & 3) * 8, T0 = hw * 3, T1 = T0 + 1;
        lds_ptr b = lds + ro + (T0 >> 1) * 1024 + (((T0 ^ g4) & 1) << 5), c = lds + ro + (T1 >> 1) * 1024 + (((T1 ^ g4) & 1) << 5);
        f32x4 cs = f32x4{}, ct = f32x4{}, cu = f32x4{};
        Poll pl = {};
        bool early = false;
#pragma unroll
        for (int n = 10; n < 20; ++n) {
            if (n < 13 || n == 19) WAIT_BAR(6); else if (n == 13) WAIT_BAR(10); else if (n == 18) WAIT_BAR(12); else WAIT_BAR(14);
            const s16x16 wv0 = CAT16(CAT8(trread(b), trread(b + 256)), CAT8(trread(b + 12288), trread(b + 12288 + 256)));
            const s16x16 wv1 = CAT16(CAT8(trread(c), trread(c + 256)), CAT8(trread(c + 12288), trread(c + 12288 + 256)));
            const s16x16 wv2 = CAT16(CAT8(trread(b + 1024), trread(b + 1024 + 256)), CAT8(trread(b + 13312), trread(b + 13312 + 256)));
            if (n == FA_POS && hw == 0 && lane == 0) raise_flag(ws, WS_FA, tag);
            if (n < 18) issue1(false);
            cs = SMF16(ones2, wv0, cs, idx);
            ct = SMF16(ones2, wv1, ct, idx);
            cu = SMF16(ones2, wv2, cu, idx); PIN(cs); PIN(ct); PIN(cu);
            if (n < 18) issue2(false, n >= 12 && n < 17);
            if (n == 17) poll_request(pl, ws + WS_FA, lane * 16);
            if (n == 18) { early = poll_check(pl, tag); if (!early) poll_request(pl, ws + WS_FA, lane * 16); }
            if (n == 19 && !early) poll_wait(pl, ws + WS_FA, lane * 16, tag);
            if (n >= 18 && early == (n == 18)) {
#pragma unroll
                for (int j = 0; j < 6; ++j) glds16c(kv + (hw + 4 * j) * 1024, lane * 16, lds0 + K2_C0 + (hw + 4 * j) * 1024);
            }
            if (n == 19) {
#pragma unroll
                for (int j = 0; j < 3; ++j) glds16c(qsrc + (hw + 4 * j) * 1024, lane * 16, lds0 + K2_Q0 + (hw + 4 * j) * 1024);
            }
            const int dw = ws3 == 2 ? -2 * P1_WSLOT : P1_WSLOT;
            b += dw; c += dw; ws3 = ws3 == 2 ? 0 : ws3 + 1;
        }
        LDS_AS float* colf = (LDS_AS float*)(lds + P1_COLB) + hw * 48;
        if (lane < 16) { colf[lane] = cs[0] + cs[1]; colf[16 + lane] = ct[0] + ct[1]; colf[32 + lane] = cu[0] + cu[1]; }
        WAIT_BAR(0);
        if (blockIdx.x == 0 && hw == 0 && lane == 0) __hip_atomic_store((gu64*)(ws + WS_EPOCH), tag, __ATOMIC_RELAXED, __HIP_MEMORY_SCOPE_AGENT);
#pragma unroll
        for (int j = 0; j < 6; ++j) glds16c(kv + K2_CHUNK + (hw + 4 * j) * 1024, lane * 16, lds0 + K2_KV0 + K2_SLOTB + (hw + 4 * j) * 1024);
    }
    return tag;
}

constexpr int K2_XCH = 2 * K2_SLOTB;

#define SMF32(a, b, c) __builtin_amdgcn_smfmac_f32_32x32x32_bf16((bfx8)(a), __builtin_bit_cast(bfx16, (b)), (c), 0xDDDD, 0, 0)
#define MFMA8(a, b, c) __builtin_amdgcn_mfma_f32_32x32x8bf16_1k((a), (b), (c), 0, 0, 0)
__device__ __forceinline__ f32x8 lo8(const f32x16& v) { return __builtin_shufflevector(v, v, 0, 1, 2, 3, 4, 5, 6, 7); }
__device__ __forceinline__ f32x8 hi8(const f32x16& v) { return __builtin_shufflevector(v, v, 8, 9, 10, 11, 12, 13, 14, 15); }
__device__ __forceinline__ float max16(const f32x16& s) {
    float m = fmaxf(fmaxf(s[0], s[1]), s[2]);
#pragma unroll
    for (int r = 3; r < 15; r += 2) m = fmaxf(fmaxf(m, s[r]), s[r + 1]);
    return swap32_max(fmaxf(m, s[15]));
}


struct K2B { lds_ptr r0, r1, r2; const char *wsrc, *wsa, *wsb; unsigned wdst; int lane, osoff; rsrc_t wsr; };
constexpr int k2_slot(int st) { return st < 3 ? st : st == 3 ? 0 : st == 4 ? 3 : st == 5 ? 1 : st == 6 ? 2 : st == 7 ? 0 : 3; }
__device__ __forceinline__ void k2_wpiece(const K2B& c, int st, int i) {
    glds16c((st < 3 ? c.wsa : st < 6 ? c.wsb : c.wsrc) + st * 40960 + i * 8192, c.lane * 16, c.wdst + k2_slot(st) * 40960 + i * 8192);
}
template <int I, int D, int NP, int OF>
__device__ __forceinline__ void k2_bstep(const K2B& c, f32x4 (&acc)[10], const s16x8 (&w)[5], s16x8 (&n)[5], const s16x8& a0, const s16x8& a1, s16x8 (&of)[10]) {
    constexpr int O = k2_slot((I + 1) >> 1) * 40960 + ((I + 1) & 1) * 20480;
#pragma unroll
    for (int i = 0; i < 10; ++i) {
        if (I > 0 && I < 16 && i < 5) n[i] = *(const LDS_AS s16x8*)((O < 61440 ? c.r0 : O < 122880 ? c.r1 : c.r2) + O % 61440 + i * 1024);
        acc[i] = MFMA16(w[i >> 1], (i & 1) ? a1 : a0, acc[i]);
        PIN(acc[i]);
        if (OF >= 0 && !(i & 1)) { const int k = (OF < 0 ? 0 : OF) + (i >> 1); of[k] = LD16(c.wsr, c.lane * 16, c.osoff + ((k & 1) * 20 + (k >> 1) * 4) * 1024); MEMFENCE(); }
        if ((i & 1) && (i >> 1) < NP) k2_wpiece(c, D, (i >> 1) + (D == 4));
        SBAR();
    }
}

__global__ __launch_bounds__(512, 2) void fused_block(const char* __restrict__ x, const char* __restrict__ wqkv, const char* __restrict__ wg, const char* __restrict__ wu,
                                                      const char* __restrict__ wd, const char* __restrict__ wo, char* __restrict__ ws, char* __restrict__ out) {
    extern __shared__ __attribute__((aligned(16))) char smem[];
    const int wid = __builtin_amdgcn_readfirstlane(threadIdx.x >> 6), bid = blockIdx.x;
    const int bi = bid >> 3, panel = (bid & 1) * 8 + (bi & 7), grp = ((bid & 7) >> 1) * 4 + (bi >> 3), l0 = threadIdx.x & 63;
    const int rb = (5 * (bi & 7)) >> 3, ra = ((bi ^ (bi >> 3)) & 1) * 2;
    unsigned xv[4], wa[4];
    {
        const int hw = wid & 3, r = l0 >> 3, pos = l0 & 7;
#pragma unroll
        for (int j = 0; j < 4; ++j) {
            xv[j] = 4096 + (hw * 32 + j * 8 + r) * (D_ * 2) + ((pos ^ ((j & 1) * 4 + (r >> 1))) << 4) - j * 1024;
            wa[j] = 4096 + (hw * 16 + (j >> 1) * 8 + r) * (3 * D_ * 2) + (j & 1) * 128 + ((pos ^ (((r >> 1) & 1) * 2 + (j >> 1) * 4)) << 4) - j * 1024;
            PIN(xv[j]); PIN(wa[j]);
        }
        int xo = panel * 128 * (D_ * 2) + ra * 128 - 4096, wo_ = min(grp, 14) * 256 + ra * 64 * 3 * D_ * 2 - 4096;
        unsigned dx = (unsigned)(uintptr_t)(lds_ptr)smem + P1_X0 + ra * P1_XSLOT + hw * 4096, dw = (unsigned)(uintptr_t)(lds_ptr)smem + hw * 4096;
        asm volatile("" : "+s"(xo), "+s"(wo_), "+s"(dx), "+s"(dw));
        SBAR();
        asm volatile("" :: "s"(x), "s"(wqkv), "s"(wg), "s"(wu), "s"(wd), "s"(wo), "s"(ws), "s"(out));
        if (wid >= 4) { glds_x4(x + xo, xv, dx); glds_x4(wqkv + wo_, wa, dw); }
        SBAR();
    }
    unsigned long long tag = 0;
    if (wid < 4) {
        __builtin_amdgcn_s_barrier();
        if (wid == 3) {
            unsigned off = ((blockIdx.x >> 3) * 13 + min(threadIdx.x & 63u, 12u)) * 128;
            asm volatile("s_getpc_b64 vcc\n.Lfb_begin:\n\tv_min_u32 %0, .Lfb_end-.Lfb_begin, %0\n\ts_mov_b32 m0, %1\n\ts_nop 0\n\tglobal_load_lds_dword %0, vcc"
                         : "+v"(off) : "s"((unsigned)(uintptr_t)(lds_ptr)smem + LDS_JUNK) : "vcc", "memory");
        }
    }
    const rsrc_t wsr = __builtin_amdgcn_make_buffer_rsrc(ws, 0, (int)WS_END, 0x00020000);
    const int head = (bid & 7) * 4 + ((bid >> 3) & 3), par = wid >> 2, qt0 = (bid >> 5) * 8 + (wid & 3) * 2;
    const char* kv = ws + WS_KV + head * KV_HEAD;
    {
        if (wid < 4) {
            p1_compute((lds_ptr)smem, wid, l0, panel, grp, ra, rb, wsr, Pack{wd, wo, wid * 256 + bid});
            const u32x2 e = *(const LDS_AS u32x2*)((lds_ptr)smem + P1_TAG);
            tag = ((unsigned long long)(unsigned)__builtin_amdgcn_readfirstlane(e[1]) << 32) | (unsigned)__builtin_amdgcn_readfirstlane(e[0]);
        } else {
            tag = p1_helper((lds_ptr)smem, wid - 4, l0, x + (size_t)panel * 128 * (D_ * 2), wqkv + min(grp, 14) * 256, wg + grp * 192, wu + grp * 192, ra, rb,
                            ws, kv, ws + WS_Q + head * Q_HEAD + (bid >> 5) * 8 * Q_TILE, xv, wa);
        }
    }
    int lane = __builtin_amdgcn_mbcnt_hi(~0u, __builtin_amdgcn_mbcnt_lo(~0u, 0u));
    PIN(lane);
    if (wid < 4) __builtin_amdgcn_s_setprio(3); else __builtin_amdgcn_s_setprio(0);
    lds_ptr lds = (lds_ptr)smem + K2_KV0;
    const unsigned lds0 = (unsigned)(uintptr_t)lds;
    const int tp = ((blockIdx.x & 7) >> 1) * 8 + ((blockIdx.x >> 3) & 7), cg = (blockIdx.x & 1) * 4 + (blockIdx.x >> 6);
    K2B kb;
    kb.wsrc = ws + WS_WP + cg * 68 * 5120 + wid * 1024; kb.wdst = (unsigned)(uintptr_t)(lds_ptr)smem + wid * 1024; kb.lane = lane;
    const int sw3 = tp & 1;
    kb.wsa = kb.wsrc + sw3 * 122880; kb.wsb = kb.wsrc - sw3 * 122880;
    const int hoff = WS_H16 + ((tp * 2 + (wid & 1)) * 96 + (wid >> 1) * 2 + sw3 * 48) * 1024, hoff1 = hoff + (1 - 2 * sw3) * 49152;
    s16x8 hf[24];
    const int h = lane >> 5, l31 = lane & 31, l16 = lane * 16;
    const char* src = kv + wid * 1024 + 3 * K2_CHUNK;
    const int kr = ((l31 & 4) << 2) | ((l31 >> 1) & 12) | (l31 & 3);
    const int k1off = h * 1024 + kr * 16, k2off = h ? KV_V + 5 * 256 : 2048 + kr * 16;
    const int vq = min(l31 >> 2, 5), voff = KV_V + vq * 256 + ((h ^ (vq & 3)) << 6) + (l31 < 24 ? l31 & 3 : 1) * 16;

    f32x16 oa[2] = {f32x16{}, f32x16{}}, sc[2];
    s16x8 k1[2], v0[2], v1[2]; s16x4 k2[2];
    const lds_ptr c0 = (lds_ptr)smem + K2_C0;
    lds_ptr pk1 = c0 + k1off + par * 8, pk2 = c0 + k2off + par * 8, pv = c0 + voff + par * KV_VT, pw = c0 + (voff ^ 128) + par * KV_VT;
    lds_ptr pk3 = pk1 + 512;
    asm volatile("" : "+v"(pk3));
#define LDS8(p) (*(const LDS_AS s16x4*)(p))
#define RD_K(t, j) do { k1[(t) & 1] = CAT8(LDS8(pk1 + (j) * 2 * KV_TILE), LDS8(pk3 + (j) * 2 * KV_TILE)); k2[(t) & 1] = LDS8(pk2 + (j) * 2 * KV_TILE); } while (0)
#define RD_V(t, j) do { v0[(t) & 1] = *(const LDS_AS s16x8*)(pv + (j) * 2 * KV_TILE); v1[(t) & 1] = *(const LDS_AS s16x8*)(pw + (j) * 2 * KV_TILE); } while (0)
    s16x8 q1[2]; s16x4 q2[2];
#pragma unroll
    for (int b = 0; b < 2; ++b) {
        const lds_ptr qp = (lds_ptr)smem + K2_Q0 + (wid & 3) * 2 * Q_TILE + b * 8 + l31 * 16;
        q1[b] = CAT8(LDS8(qp + h * 1024), LDS8(qp + h * 1024 + 512)); q2[b] = LDS8(qp + 2048);
    }
    {
        const s16x8 ka = CAT8(LDS8(c0 + k1off), LDS8(c0 + k1off + 512));
        const s16x4 kb = LDS8(c0 + k2off);
        RD_K(0, 0); RD_V(0, 0);
#pragma unroll
        for (int b = 0; b < 2; ++b) {
            if (h) q2[b] = s16x4{0, 0, 0, 0};
            f32x16 s = MFMA32(ka, q1[b], f32x16{});
            s = MFMA8(kb, q2[b], s);
            const float m = rbf(-max16(s));
            if (h) q2[b][0] = (short)(__float_as_uint(m) >> 16);
        }
        sc[0] = MFMA32(k1[0], q1[0], f32x16{});
        sc[0] = MFMA8(k2[0], q2[0], sc[0]);
    }
#define EXP4(S, i) do { S[i] = __builtin_amdgcn_exp2f(S[i]); S[i + 1] = __builtin_amdgcn_exp2f(S[i + 1]); S[i + 2] = __builtin_amdgcn_exp2f(S[i + 2]); S[i + 3] = __builtin_amdgcn_exp2f(S[i + 3]); PIN(S); SBAR(); } while (0)
    Poll pl = {};
    s16x8 wfa[5], wfb[5];
    kb.r0 = (lds_ptr)smem + (wid >> 1) * 5120 + l16;
    auto request = [&](auto HS) __attribute__((always_inline)) {
        constexpr int hs = HS.v, p = (hs - 6) >> 3, r = (hs - 6) & 7;
        constexpr int w = (hs >= 9 && hs < 14) ? hs - 9 : (hs >= 17 && hs < 22) ? hs - 12 : (hs >= 54 && hs < 57) ? hs - 44 : hs >= 62 ? hs - 49 : -1;
        constexpr int e = (p >= 2 && p <= 4 && r >= 3) ? (p - 2) * 5 + r - 3 : p == 5 ? 15 + r : hs == 57 ? 23 : -1;
        if constexpr (hs < 3) glds16c(src - K2_CHUNK + hs * 8192, l16, lds0 + 2 * K2_SLOTB + wid * 1024 + hs * 8192);
        else if constexpr (hs >= 6 && p < 5 && r < 3) glds16c(src + p * K2_CHUNK + r * 8192, l16, lds0 + (p % 3) * K2_SLOTB + wid * 1024 + r * 8192);
        else if constexpr (w >= 0) k2_wpiece(kb, w / 5, w % 5);
        else if constexpr (e >= 0) { hf[e] = LD16(wsr, l16, (e < 12 ? hoff : hoff1) + ((e % 12) >> 1) * 8192 + (e & 1) * 1024); MEMFENCE(); }
        if constexpr (hs == 58) k2_wpiece(kb, 4, 4);
        if constexpr (hs == 63) k2_wpiece(kb, 4, 0);
        if constexpr (hs == 3) {
            const unsigned wo_ = 6 * 40960 + min(((bi & 7) * 8 + wid) * 13 + min(lane, 12), 799) * 128;
            asm volatile("s_mov_b32 m0, %2\n\ts_nop 0\n\tglobal_load_lds_dword %0, %1 sc1" :: "v"(wo_), "s"(kb.wsrc - wid * 1024), "s"(kb.wdst - wid * 768 + LDS_JUNK) : "memory");
        }
        if (hs == 6 && wid == 4 && lane == 0) raise_flag(ws, WS_FB, tag);
        if (hs == 16 && wid == 4) poll_request(pl, ws + WS_FB, l16);
        SBAR();
    };
    static_for<0, 32>([&](auto T) __attribute__((always_inline)) {
        {
            constexpr int c = T.v >> 2, j = T.v & 3;
            if (j == 3) {
                if (c == 2 && wid == 4) poll_wait(pl, ws + WS_FB, l16, tag);
                if (c == 0) { asm volatile("s_waitcnt vmcnt(0)" ::: "memory"); LGKM_BAR(); } else if (c == 7) WAIT_BAR(0); else WAIT_BAR(7);
                if (c == 7 && par) __builtin_amdgcn_s_setprio(3); else __builtin_amdgcn_s_setprio(2);
                const int dsl = c == 0 ? K2_KV0 + K2_SLOTB - K2_C0 : (c % 3 == 2) ? -2 * K2_SLOTB : K2_SLOTB;
                pk1 += dsl; pk2 += dsl; pk3 += dsl; pv += dsl; pw += dsl;
                asm volatile("" : "+v"(pk1), "+v"(pk2), "+v"(pk3), "+v"(pv), "+v"(pw));
                SBAR();
            }
            if constexpr (T.v < 31) RD_K(j + 1, (j + 1) & 3);
            SBAR();
            if constexpr (T.v > 0) { oa[1] = SMF32(v1[(j + 1) & 1], hi8(sc[1]), oa[1]); PIN(oa[1]); }
            SBAR();
            EXP4(sc[0], 0);
            sc[1] = MFMA32(k1[j & 1], q1[1], f32x16{}); PIN(sc[1]); SBAR();
            request(IC<8 * c + 2 * j>{});
            EXP4(sc[0], 4);
            sc[1] = MFMA8(k2[j & 1], q2[1], sc[1]); PIN(sc[1]); SBAR();
            EXP4(sc[0], 8);
            oa[0] = SMF32(v0[j & 1], lo8(sc[0]), oa[0]); PIN(oa[0]); SBAR();
            EXP4(sc[0], 12);
            if constexpr (T.v < 31) RD_V(j + 1, (j + 1) & 3);
            if (j == 1 && c > 0) __builtin_amdgcn_s_setprio(1);
            if (j == 2 && c > 0) __builtin_amdgcn_s_setprio(0);
            SBAR();
            oa[0] = SMF32(v1[j & 1], hi8(sc[0]), oa[0]); PIN(oa[0]); SBAR();
            EXP4(sc[1], 0);
            if constexpr (T.v < 31) { sc[0] = MFMA32(k1[(j + 1) & 1], q1[0], f32x16{}); PIN(sc[0]); }
            SBAR();
            request(IC<8 * c + 2 * j + 1>{});
            EXP4(sc[1], 4);
            if constexpr (T.v < 31) { sc[0] = MFMA8(k2[(j + 1) & 1], q2[0], sc[0]); PIN(sc[0]); }
            else {
#pragma unroll
                for (int i = 0; i < 5; ++i) { wfa[i] = *(const LDS_AS s16x8*)(kb.r0 + i * 1024); wfb[i] = *(const LDS_AS s16x8*)(kb.r0 + 20480 + i * 1024); }
                if (par) {
#pragma unroll
                    for (int i = 0; i < 3; ++i) *(LDS_AS f32x4*)(lds + K2_XCH + (wid & 3) * 6144 + l16 + i * 1024) = f32x4{oa[0][4 * i], oa[0][4 * i + 1], oa[0][4 * i + 2], oa[0][4 * i + 3]};
                }
            }
            SBAR();
            EXP4(sc[1], 8);
            oa[1] = SMF32(v0[j & 1], lo8(sc[1]), oa[1]); PIN(oa[1]); SBAR();
            EXP4(sc[1], 12);
        }
    });
    oa[1] = SMF32(v1[1], hi8(sc[1]), oa[1]);
    int ln = __builtin_amdgcn_mbcnt_hi(~0u, __builtin_amdgcn_mbcnt_lo(~0u, 0u));
    PIN(ln);
    const int h2 = ln >> 5, l2 = ln & 31;
    const int mt = wid & 1, kq = wid >> 1;
    kb.lane = ln; kb.r0 = (lds_ptr)smem + kq * 5120 + ln * 16; kb.r1 = kb.r0 + 61440; kb.r2 = kb.r0 + 122880;
    asm volatile("" : "+v"(kb.r1), "+v"(kb.r2));
    kb.wsr = wsr; kb.osoff = WS_O16 + ((tp * 4 + mt * 2) * 20 + kq) * 1024;
    {
        const lds_ptr xp_ = lds + K2_XCH + (wid & 3) * 6144 + ln * 16;
        if (par) {
#pragma unroll
            for (int i = 3; i < 6; ++i) { const f32x16& g = oa[i / 3]; const int r = 4 * (i % 3); *(LDS_AS f32x4*)(xp_ + i * 1024) = f32x4{g[r], g[r + 1], g[r + 2], g[r + 3]}; }
        }
        LGKM_BAR();
        if (!par) {
            __builtin_amdgcn_s_setprio(3);
            f32x4 tt[6];
#pragma unroll
            for (int i = 0; i < 6; ++i) tt[i] = *(const LDS_AS f32x4*)(xp_ + i * 1024);
            SBAR();
#pragma unroll
            for (int i = 0; i < 6; ++i) { const f32x4 t = tt[i]; f32x16& g = oa[i / 3]; const int r = 4 * (i % 3); g[r] = add1(g[r], t[0]); g[r + 1] = add1(g[r + 1], t[1]); g[r + 2] = add1(g[r + 2], t[2]); g[r + 3] = add1(g[r + 3], t[3]); }
            const int ob = (l2 >> 4) * 20 * 1024 + (l2 & 15) * 16, os = WS_O16 + qt0 * 2 * 20 * 1024;
            const int o1 = ob + h2 * 256 + os + (head >> 1) * 1024 + (head & 1) * 512, o2 = os + (16 + (head >> 3)) * 1024 + ((head >> 1) & 3) * 256 + (head & 1) * 8;
            auto put = [&](const f32x16& o, int b, u32x2& e) {
                const float l = __uint_as_float(__builtin_amdgcn_permlane32_swap(__float_as_uint(o[8]), __float_as_uint(o[8]), false, false)[1]);
                const float rl = __builtin_amdgcn_rcpf(l);
                unsigned w[6];
#pragma unroll
                for (int i = 0; i < 6; ++i) w[i] = cvtpk(mul1(o[2 * i], rl), mul1(o[2 * i + 1], rl));
                ST16(wsr, (u32x4{w[0], w[1], w[2], w[3]}), o1, b * 40 * 1024);
                e = u32x2{w[4], w[5]};
                return l;
            };
            u32x2 e0, e1;
            const float l0 = put(oa[0], 0, e0), l1 = put(oa[1], 1, e1);
            ST8(wsr, (u32x2{__builtin_amdgcn_permlane32_swap(e0[0], e1[0], false, false)[0], __builtin_amdgcn_permlane32_swap(e0[1], e1[1], false, false)[0]}), ob + h2 * 40 * 1024, o2);
            MEMFENCE(); SBAR();
            float big = 0.f;
#pragma unroll
            for (int r = 0; r < 12; ++r) big = add1(big, add1(fabsf(oa[0][r]), fabsf(oa[1][r])));
            if (__builtin_expect(__any(!(big < 1e30f) | !(fminf(l0, l1) > 1e-30f)), 0)) {
                const int gr = ((l2 & 4) << 2) | ((l2 >> 1) & 12) | (l2 & 3), g1 = h2 * 1024 + gr * 16, g2 = 2048 + gr * 16, gq = min(l2 >> 2, 5), gv = KV_V + gq * 256 + ((h2 ^ (gq & 3)) << 6) + (l2 & 3) * 16;
                asm volatile("s_waitcnt vmcnt(0)" ::: "memory");
#pragma unroll 1
                for (int b = 0; b < 2; ++b) {
                    const s16x8 qf1 = b ? q1[1] : q1[0];
                    s16x4 qf2 = b ? q2[1] : q2[0];
                    if (h2) qf2[0] = 0;
                    float m = -INFINITY;
                    f32x16 negm, o = f32x16{};
#pragma unroll 1
                    for (int t = 0; t < 64; ++t) {
                        const int g = WS_KV + head * KV_HEAD + (t >> 1) * 2 * KV_TILE + (t & 1) * 8;
                        f32x16 s = MFMA32(CAT8(LD8(wsr, g1, g), LD8(wsr, g1 + 512, g)), qf1, f32x16{});
                        s = MFMA8(LD8(wsr, g2, g), qf2, s);
                        m = fmaxf(m, max16(s));
                    }
#pragma unroll
                    for (int r = 0; r < 16; ++r) negm[r] = -m;
#pragma unroll 1
                    for (int t = 0; t < 64; ++t) {
                        const int g = WS_KV + head * KV_HEAD + (t >> 1) * 2 * KV_TILE + (t & 1) * 8;
                        f32x16 s = MFMA32(CAT8(LD8(wsr, g1, g), LD8(wsr, g1 + 512, g)), qf1, negm);
                        s = MFMA8(LD8(wsr, g2, g), qf2, s);
#pragma unroll
                        for (int r = 0; r < 16; ++r) s[r] = __builtin_amdgcn_exp2f(s[r]);
                        o = SMF32(LD16(wsr, gv, g + (t & 1) * (KV_VT - 8)), lo8(s), o);
                        o = SMF32(LD16(wsr, gv ^ 128, g + (t & 1) * (KV_VT - 8)), hi8(s), o);
                    }
                    u32x2 e;
                    put(o, b, e);
                    if (!h2) ST8(wsr, e, ob + b * 40 * 1024, o2);
                }
            }
        }
    }
    __builtin_amdgcn_s_setprio(0);
    const int ch0 = wid * 128 + ln, ch1 = ch0 + 64, er0 = ch0 / 10, er1 = ch1 / 10, ec0 = ch0 - er0 * 10, ec1 = ch1 - er1 * 10;
    const size_t eb = (size_t)tp * 64 * (D_ * 2) + cg * 160;
    const unsigned eo0 = er0 * (D_ * 2) + ec0 * 16, eo1 = er1 * (D_ * 2) + ec1 * 16;
    f32x4 acc[10];
#pragma unroll
    for (int i = 0; i < 10; ++i) acc[i] = f32x4{};
    s16x8 of[10];
#define HLO(i) __builtin_shufflevector(hf[2 * (i)], hf[2 * (i) + 1], 0, 1, 2, 3, 8, 9, 10, 11)
#define HHI(i) __builtin_shufflevector(hf[2 * (i)], hf[2 * (i) + 1], 4, 5, 6, 7, 12, 13, 14, 15)
    k2_bstep<0, 0, 0, -1>(kb, acc, wfa, wfb, HLO(0), HHI(0), of);
    k2_bstep<1, 0, 0, -1>(kb, acc, wfb, wfa, HLO(1), HHI(1), of);
    k2_bstep<2, 0, 0, -1>(kb, acc, wfa, wfb, HLO(2), HHI(2), of);
    if (!par) {
        asm volatile("s_waitcnt vmcnt(0)" ::: "memory");
        if (ln == 0) __hip_atomic_store((gu64*)(ws + WS_FC) + (qt0 >> 1) * 32 + head, tag, __ATOMIC_RELAXED, __HIP_MEMORY_SCOPE_AGENT);
    } else {
#pragma unroll
        for (int i = 0; i < 10; ++i) glds16c(kb.wsb + 3 * 40960 - 4096 + i * 4096, ln * 16, kb.wdst - 4096 + i * 4096);
    }
    WAIT_BAR(10);
    k2_bstep<3, 4, 3, -1>(kb, acc, wfb, wfa, HLO(3), HHI(3), of);
    k2_bstep<4, 5, 5, -1>(kb, acc, wfa, wfb, HLO(4), HHI(4), of);
    WAIT_BAR(8);
    k2_bstep<5, 6, 5, -1>(kb, acc, wfb, wfa, HLO(5), HHI(5), of);
    k2_bstep<6, 0, 0, -1>(kb, acc, wfa, wfb, HLO(6), HHI(6), of);
    WAIT_BAR(10);
    k2_bstep<7, 7, 5, -1>(kb, acc, wfb, wfa, HLO(7), HHI(7), of);
    unsigned long long seen = 0;
    const char* cw = ws + WS_FC + tp * 256;
    const unsigned cvo = l2 * 8;
    if (wid == 4) asm volatile("global_load_dwordx2 %0, %1, %2 sc1" : "+v"(seen) : "v"(cvo), "s"(cw) : "memory");
    SBAR();
    k2_bstep<8, 0, 0, -1>(kb, acc, wfa, wfb, HLO(8), HHI(8), of);
    if (wid == 4) {
        asm volatile("s_waitcnt vmcnt(0)" : "+v"(seen) :: "memory");
#pragma unroll 1
        for (unsigned spin = 0; spin < (1u << 18) && !__all(seen == tag); ++spin) {
            __builtin_amdgcn_s_sleep(1);
            asm volatile("global_load_dwordx2 %0, %1, %2 sc1\n\ts_waitcnt vmcnt(0)" : "=&v"(seen) : "v"(cvo), "s"(cw) : "memory");
        }
    }
    WAIT_BAR(10);
    k2_bstep<9, 0, 0, 0>(kb, acc, wfb, wfa, HLO(9), HHI(9), of);
    k2_bstep<10, 0, 0, 5>(kb, acc, wfa, wfb, HLO(10), HHI(10), of);
    WAIT_BAR(10);
    k2_bstep<11, 8, 3, -1>(kb, acc, wfb, wfa, HLO(11), HHI(11), of);
    u32x4 xv0 = u32x4{}, xv1 = u32x4{};
    if (wid < 5) { xv0 = *(const u32x4*)(x + eb + eo0); xv1 = *(const u32x4*)(x + eb + eo1); }
    MEMFENCE(); SBAR();
    k2_bstep<12, 0, 0, -1>(kb, acc, wfa, wfb, of[0], of[1], of);
    k2_bstep<13, 0, 0, -1>(kb, acc, wfb, wfa, of[2], of[3], of);
    k2_bstep<14, 0, 0, -1>(kb, acc, wfa, wfb, of[4], of[5], of);
    WAIT_BAR(0);
    k2_bstep<15, 0, 0, -1>(kb, acc, wfb, wfa, of[6], of[7], of);
    k2_bstep<16, 0, 0, -1>(kb, acc, wfa, wfb, of[8], of[9], of);
    {
        const int tk = ln & 15;
        const lds_ptr pp = (lds_ptr)smem + kq * 20480 + (mt * 32 + tk) * 320 + (((ln >> 4) ^ ((tk >> 1) & 3)) << 4);
#pragma unroll
        for (int i = 0; i < 10; ++i) *(LDS_AS f32x4*)(pp + (i & 1) * 16 * 320 + (i >> 1) * 64) = acc[i];
        LGKM_BAR();
        if (wid < 5) {
#pragma unroll
            for (int e = 0; e < 2; ++e) {
                const int er = e ? er1 : er0, ec = e ? ec1 : ec0, sw = (er >> 1) & 3;
                const lds_ptr p0 = (lds_ptr)smem + er * 320 + (((2 * ec) ^ sw) << 4), p1 = (lds_ptr)smem + er * 320 + (((2 * ec + 1) ^ sw) << 4);
                f32x4 a = *(const LDS_AS f32x4*)p0, b = *(const LDS_AS f32x4*)p1;
#pragma unroll
                for (int k = 1; k < 4; ++k) { a += *(const LDS_AS f32x4*)(p0 + k * 20480); b += *(const LDS_AS f32x4*)(p1 + k * 20480); }
                const u32x4 xr = e ? xv1 : xv0;
                const u32x4 ov = u32x4{cvtpk(a[0] + bf_lo(xr[0]), a[1] + bf_hi(xr[0])), cvtpk(a[2] + bf_lo(xr[1]), a[3] + bf_hi(xr[1])),
                                       cvtpk(b[0] + bf_lo(xr[2]), b[1] + bf_hi(xr[2])), cvtpk(b[2] + bf_lo(xr[3]), b[3] + bf_hi(xr[3]))};
                __builtin_amdgcn_raw_buffer_store_b128(ov, __builtin_amdgcn_make_buffer_rsrc(out, 0, S_ * D_ * 2, 0x00020000), (int)eb + (e ? eo1 : eo0), 0, 16);
            }
        }
    }
    asm volatile(".Lfb_end:");
}

extern "C" void kernel_launch(void* const* d_in, const int* in_sizes, int n_in, void* d_out, int out_size, void* d_ws, size_t ws_size,
                              hipStream_t stream) {
    const char* x = (const char*)d_in[0];
    const char* wqkv = (const char*)d_in[1];
    const char* wo = (const char*)d_in[2];
    const char* wg = (const char*)d_in[4];
    const char* wu = (const char*)d_in[5];
    const char* wd = (const char*)d_in[6];
    char* ws = (char*)d_ws;
    (void)hipFuncSetAttribute((const void*)fused_block, hipFuncAttributeMaxDynamicSharedMemorySize, LDS_BYTES);
    fused_block<<<256, 512, LDS_BYTES, stream>>>(x, wqkv, wg, wu, wd, wo, ws, (char*)d_out);
}
```

```cpp
#include <hip/hip_runtime.h>
#include <hip/hip_bf16.h>
#include <stdint.h>

typedef __bf16 bfx8 __attribute__((ext_vector_type(8)));
typedef __bf16 bfx16 __attribute__((ext_vector_type(16)));
typedef short s16x4 __attribute__((ext_vector_type(4)));
typedef short s16x8 __attribute__((ext_vector_type(8)));
typedef short s16x16 __attribute__((ext_vector_type(16)));
typedef float f32x2 __attribute__((ext_vector_type(2)));
typedef float f32x4 __attribute__((ext_vector_type(4)));
typedef float f32x8 __attribute__((ext_vector_type(8)));
typedef float f32x16 __attribute__((ext_vector_type(16)));
typedef unsigned u32x2 __attribute__((ext_vector_type(2)));
typedef unsigned u32x4 __attribute__((ext_vector_type(4)));
#define LDS_AS __attribute__((address_space(3)))
typedef LDS_AS char* lds_ptr;

#define FA_POS 14
constexpr int S_ = 2048, D_ = 640, H_ = 32, F_ = 1536;

constexpr int Q_TILE = 1280, Q_HEAD = 64 * Q_TILE, KV_TILE = 2816, KV_V = 2560, KV_VT = 1536, KV_HEAD = 64 * KV_TILE;
constexpr int WS_Q = 0;
constexpr int WS_KV = WS_Q + H_ * Q_HEAD;
constexpr int WS_O16 = WS_KV + H_ * KV_HEAD + 8192;
constexpr int WS_H16 = WS_O16 + 128 * 20 * 1024;
constexpr int WS_WP = WS_H16 + 128 * 48 * 1024;
constexpr int WS_EPOCH = WS_WP + 8 * 68 * 5120 + 8192;
constexpr int WS_FA = WS_EPOCH + 256, WS_FB = WS_FA + 2048, WS_FC = WS_FB + 2048, WS_END = WS_FC + 8192;

#define PSTAMP(i) do {} while (0)
#define PFLUSH() do {} while (0)
#define DIAG_PF2 0
#define QFLUSH() do {} while (0)
#define QSTAMP(i) do {} while (0)
#define STAMP_ARG
#define STAMP_PASS
#define STAMP_DECL do {} while (0)
#define STAMP(i) do {} while (0)
#define STAMP_FLUSH(kern) do {} while (0)
#define SBAR() __builtin_amdgcn_sched_barrier(0)
template <int V> struct IC { static constexpr int v = V; };
template <int I, int N, class F> __device__ __forceinline__ void static_for(F&& f) { if constexpr (I < N) { f(IC<I>{}); static_for<I + 1, N>(f); } }
#define MFMA32(a, b, c) __builtin_amdgcn_mfma_f32_32x32x16_bf16((bfx8)(a), (bfx8)(b), (c), 0, 0, 0)

__device__ __forceinline__ unsigned cvtpk(float lo, float hi) {
    typedef __bf16 bfx2 __attribute__((ext_vector_type(2)));
    bfx2 r = {(__bf16)lo, (__bf16)hi};
    return __builtin_bit_cast(unsigned, r);
}
__device__ __forceinline__ s16x4 trread(const LDS_AS char* p) {
    return __builtin_amdgcn_ds_read_tr16_b64_v4i16((LDS_AS s16x4*)p);
}
__device__ __forceinline__ float rbf(float v) { return (float)(__bf16)v; }
__device__ __forceinline__ float bf_lo(unsigned u) { return __uint_as_float(u << 16); }
__device__ __forceinline__ float bf_hi(unsigned u) { return __uint_as_float(u & 0xffff0000u); }
__device__ __forceinline__ float swap32_max(float v) {
    auto r = __builtin_amdgcn_permlane32_swap(__float_as_uint(v), __float_as_uint(v), false, false);
    return fmaxf(__uint_as_float(r[0]), __uint_as_float(r[1]));
}

constexpr int P1_WSLOT = 24576, P1_X0 = 3 * P1_WSLOT, P1_XSLOT = 16384, P1_STAT = P1_X0 + 5 * P1_XSLOT;
constexpr int P1_COLA = P1_STAT + 1024, P1_COLB = P1_COLA + 512, P1_TAG = P1_COLB + 1024, LDS_JUNK = 161792, LDS_BYTES = 163840;
constexpr int K2_CHUNK = 8 * KV_TILE, K2_SLOTB = 24576, K2_KV0 = 81920, K2_C0 = 2 * P1_WSLOT, K2_Q0 = 0;
typedef __attribute__((address_space(1))) unsigned long long gu64;

#define PIN(x) asm volatile("" : "+v"(x))
#define MEMFENCE() asm volatile("" ::: "memory")
#define WAIT_BAR(N) do { MEMFENCE(); __builtin_amdgcn_s_waitcnt(0x70 | ((N) & 15) | (((N) >> 4) << 14)); __builtin_amdgcn_s_barrier(); MEMFENCE(); SBAR(); } while (0)
#define LGKM_BAR() do { MEMFENCE(); __builtin_amdgcn_s_waitcnt(0xC07F); __builtin_amdgcn_s_barrier(); MEMFENCE(); SBAR(); } while (0)
#define MFMA16(a, b, c) __builtin_amdgcn_mfma_f32_16x16x32_bf16((bfx8)(a), (bfx8)(b), (c), 0, 0, 0)
#define SMF16(a, b, c, i) __builtin_amdgcn_smfmac_f32_16x16x64_bf16((bfx8)(a), (bfx16)(b), (c), (i), 0, 0)
#define CAT8(a, b) __builtin_shufflevector(a, b, 0, 1, 2, 3, 4, 5, 6, 7)
#define CAT16(a, b) __builtin_shufflevector(a, b, 0, 1, 2, 3, 4, 5, 6, 7, 8, 9, 10, 11, 12, 13, 14, 15)

__device__ __forceinline__ void glds16s(const char* sbase, unsigned voff, unsigned lds_base) {
    asm volatile("s_mov_b32 m0, %2\n\ts_nop 0\n\tglobal_load_lds_dwordx4 %0, %1" :: "v"(voff), "s"(sbase), "s"(lds_base) : "memory");
}
__device__ __forceinline__ void glds16c(const char* sbase, unsigned voff, unsigned lds_base) {
    asm volatile("s_mov_b32 m0, %2\n\ts_nop 0\n\tglobal_load_lds_dwordx4 %0, %1 sc1" :: "v"(voff), "s"(sbase), "s"(lds_base) : "memory");
}
typedef __amdgpu_buffer_rsrc_t rsrc_t;
#define ST16(rs, v, voff, soff) __builtin_amdgcn_raw_buffer_store_b128((v), (rs), (voff) + (soff), 0, 16)
#define ST8(rs, v, voff, soff) __builtin_amdgcn_raw_buffer_store_b64((v), (rs), (voff), (soff), 16)
#define LD16(rs, voff, soff) __builtin_bit_cast(s16x8, __builtin_amdgcn_raw_buffer_load_b128((rs), (voff), (soff), 16))
#define LD8(rs, voff, soff) __builtin_bit_cast(s16x4, __builtin_amdgcn_raw_buffer_load_b64((rs), (voff), (soff), 16))
__device__ __forceinline__ f32x2 pk4(const f32x4& a, int i) { return f32x2{a[i], a[i + 1]}; }
__device__ __forceinline__ f32x2 pfma(f32x2 a, f32x2 b, f32x2 c) { return __builtin_elementwise_fma(a, b, c); }
__device__ __forceinline__ unsigned cvt2(f32x2 v) { return cvtpk(v[0], v[1]); }
__device__ __forceinline__ float add1(float a, float b) { float r = a + b; asm volatile("" : "+v"(r)); return r; }
__device__ __forceinline__ float fma1(float a, float b, float c) { float r = fmaf(a, b, c); asm volatile("" : "+v"(r)); return r; }
__device__ __forceinline__ float mul1(float a, float b) { float r = a * b; asm volatile("" : "+v"(r)); return r; }

__device__ __forceinline__ void raise_flag(char* ws, int arr, unsigned long long tag) {
    __hip_atomic_store((gu64*)(ws + arr) + blockIdx.x, tag, __ATOMIC_RELAXED, __HIP_MEMORY_SCOPE_AGENT);
}
struct Poll { u32x4 a, b; };
__device__ __forceinline__ void poll_request(Poll& p, const char* arr, unsigned voff) {
    asm volatile("global_load_dwordx4 %0, %2, %3 sc1\n\tglobal_load_dwordx4 %1, %2, %3 offset:1024 sc1" : "+v"(p.a), "+v"(p.b) : "v"(voff), "s"(arr) : "memory");
}
__device__ __forceinline__ bool poll_check(Poll& p, unsigned long long tag) {
    const unsigned tl = (unsigned)tag, th = (unsigned)(tag >> 32);
    asm volatile("s_waitcnt vmcnt(0)" : "+v"(p.a), "+v"(p.b) :: "memory");
    return __all(p.a[0] == tl && p.a[1] == th && p.a[2] == tl && p.a[3] == th && p.b[0] == tl && p.b[1] == th && p.b[2] == tl && p.b[3] == th);
}
__device__ __forceinline__ void poll_wait(Poll& p, const char* arr, unsigned voff, unsigned long long tag) {
#pragma unroll 1
    for (unsigned spin = 0; spin < (1u << 18) && !poll_check(p, tag); ++spin) {
        __builtin_amdgcn_s_sleep(4);
        poll_request(p, arr, voff);
    }
    asm volatile("s_waitcnt vmcnt(0)" : "+v"(p.a), "+v"(p.b) :: "memory");
}

template <int MODE, class FM>
__device__ __forceinline__ void p1_pass(f32x4 (&acc)[4][MODE == 2 ? 6 : 4], lds_ptr x0, lds_ptr x1, lds_ptr (&wp)[MODE == 2 ? 2 : 4], int start, FM&& mid) {
    constexpr int NC = MODE == 2 ? 6 : 4, NR = 4 + 2 * NC, WS0 = MODE == 2 ? 1 : 0;
    struct F { s16x8 x[4]; s16x4 w[NC][2]; } f[2];
    auto rd = [&](F& n, int ks, int i) __attribute__((always_inline)) {
        if (i == 0 || i > 2 * NC) { const int m = i == 0 ? 0 : i - 2 * NC; n.x[m] = *(const LDS_AS s16x8*)((ks ? x1 : x0) + m * 2048); }
        else {
            const int t = (i - 1) >> 1, e = (i - 1) & 1, c = t % 3;
            n.w[t][e] = trread(MODE == 2 ? wp[c & 1] + ks * 12288 + (t / 3) * 3072 + (c >> 1) * 1024 + e * 256 : wp[MODE == 2 ? 0 : t] + ks * 8192 + e * 512);
        }
    };
    auto step = [&](const F& c, F& n, int ks, bool more) __attribute__((always_inline)) {
#pragma unroll
        for (int i = 0; i < 4 * NC; ++i) {
            const s16x8 w = CAT8(c.w[i % NC][0], c.w[i % NC][1]);
            f32x4& a = acc[i / NC][i % NC];
            a = MODE == 1 ? MFMA16(c.x[i / NC], w, a) : MFMA16(w, c.x[i / NC], a);
            PIN(a); SBAR();
            if (more && i < NR) { rd(n, ks, i); SBAR(); }
        }
    };
#pragma unroll
    for (int i = 0; i < NR; ++i) rd(f[0], 0, i);
    SBAR();
    static_for<0, 10>([&](auto N) __attribute__((always_inline)) {
        constexpr int n = N.v, dw = (WS0 + n) % 3 == 2 ? -2 * P1_WSLOT : P1_WSLOT;
        const int dx = n % 5 == 4 - start ? -4 * P1_XSLOT : P1_XSLOT;
        step(f[0], f[1], 1, true);
        x0 += dx; x1 += dx; PIN(x0); PIN(x1);
#pragma unroll
        for (int i = 0; i < (MODE == 2 ? 2 : 4); ++i) { wp[i] += dw; PIN(wp[i]); }
        mid(N);
        PSTAMP((MODE == 2 ? 22 : 2) + 2 * n);
        LGKM_BAR();
        PSTAMP((MODE == 2 ? 23 : 3) + 2 * n);
        step(f[1], f[0], 0, n < 9);
    });
}

struct Pack { const char *wd, *wo; int job; };
__device__ __forceinline__ void pack_fetch(const Pack& pk_, lds_ptr lds, int wid, int lane) {
    const int step = pk_.job / 10, cb = pk_.job - step * 10, r = lane >> 3;
    const unsigned va = ((r & 3) + (step < 48 ? 16 : 8) * (r >> 2)) * (D_ * 2) + (lane & 7) * 16, vb = ((r >> 2) * 20 + 16 + (r & 3)) * (D_ * 2) + (lane & 7) * 16;
#pragma unroll
    for (int g = 0; g < 4; ++g) {
        const int ks = (step - 48) * 2 + (g >> 1), hh = g & 1;
        const char* a = step < 48 ? pk_.wd + (step * 32 + g * 4) * (D_ * 2) : ks < 32 ? pk_.wo + (ks * 20 + hh * 4) * (D_ * 2) : pk_.wo + ((ks - 32) * 4 + hh * 2) * 20 * (D_ * 2);
        glds16s(a + cb * 128, step < 64 ? va : vb, (unsigned)(uintptr_t)lds + wid * P1_WSLOT + 16384 + g * 1024);
    }
}
__device__ __forceinline__ void pack_read(s16x8 (&pf)[4], lds_ptr lds, int wid, int lane) {
    asm volatile("s_waitcnt vmcnt(0)" ::: "memory");
    const lds_ptr pr = lds + wid * P1_WSLOT + 16384 + (lane >> 4) * 1024 + ((lane >> 2) & 3) * 128 + (lane & 3) * 8;
#pragma unroll
    for (int nt = 0; nt < 4; ++nt) pf[nt] = CAT8(trread(pr + nt * 32), trread(pr + nt * 32 + 512));
}
__device__ __forceinline__ void pack_store(const Pack& pk_, const s16x8 (&pf)[4], int lane, rsrc_t wsr) {
    const int step = pk_.job / 10, cb = pk_.job - step * 10;
#pragma unroll
    for (int nt = 0; nt < 4; ++nt) {
        const int N = cb * 4 + nt, cg = N / 5;
        ST16(wsr, __builtin_bit_cast(u32x4, pf[nt]), lane * 16, WS_WP + ((cg * 68 + step) * 5 + N - cg * 5) * 1024);
    }
}

constexpr int OOB = 0x7ffffff0;

__device__ __forceinline__ void p1_compute(lds_ptr lds, int wid, int lane, int panel, int grp, int ra, int rb, rsrc_t wsr, const Pack& pk_ STAMP_ARG) {
    const int l15 = lane & 15, g4 = lane >> 4, q = (lane >> 2) & 3, wm = wid & 1, wn = wid >> 1, tt = panel * 4 + wm * 2;
    const LDS_AS float* rstd = (const LDS_AS float*)(lds + P1_STAT) + wm * 64;
    const int xo = P1_X0 + (wm * 64 + l15) * 128 + ((g4 ^ (l15 >> 1)) << 4);
    if (pk_.job < 680) pack_fetch(pk_, lds, wid, lane);
    {
        f32x4 acc[4][4];
#pragma unroll
        for (int i = 0; i < 16; ++i) acc[i >> 2][i & 3] = f32x4{};
        s16x8 pf[4];
        const LDS_AS float* colf = (const LDS_AS float*)(lds + P1_COLA) + wn * 64;
        const int sec = grp / 5, q0 = (grp - sec * 5) * 32 + wn * 16;
        lds_ptr wp[4];
#pragma unroll
        for (int t = 0; t < 4; ++t) wp[t] = lds + g4 * 2048 + wn * 1024 + q * 128 + (lane & 3) * 8 + ((t ^ (q >> 1) ^ ((g4 & 1) << 1)) << 5);
        auto mid = [&](auto N) __attribute__((always_inline)) { if constexpr (N.v == 5) pack_read(pf, lds, min(wid, 2), lane); };
        PSTAMP(0);
        LGKM_BAR();
        PSTAMP(1);
        f32x2 t[32];
        int vo[4], vs;
        if (sec < 2) {
            p1_pass<0>(acc, lds + xo + ra * P1_XSLOT, lds + (xo ^ 64) + ra * P1_XSLOT, wp, ra, mid);
            STAMP(1);
            f32x4 cv[4]; float rs[4], nm[4];
#pragma unroll
            for (int i = 0; i < 4; ++i) { cv[i] = *(const LDS_AS f32x4*)(colf + i * 16 + g4 * 4); nm[i] = rstd[128 + i * 16 + l15]; rs[i] = rstd[i * 16 + l15]; }
            const float qs = sec ? 1.0f : 0.22360679774997896f * 1.4426950408889634f;
#pragma unroll
            for (int c = 0; c < 4; ++c) { const int qd = q0 + 4 * c + g4, hd = (qd * 205) >> 10; vo[c] = (sec ? WS_KV + tt * KV_TILE + hd * KV_HEAD : WS_Q + tt * Q_TILE + hd * Q_HEAD) + (qd - hd * 5) * 512 + l15 * 16; }
            vs = 256;
#pragma unroll
            for (int k = 0; k < 32; ++k) { const int m = (k & 2) + ((k >> 2) & 1); t[k] = pfma(pk4(cv[k >> 3], (k & 1) * 2), f32x2{nm[m], nm[m]}, pk4(acc[m][k >> 3], (k & 1) * 2)); }
            SBAR();
#pragma unroll
            for (int k = 0; k < 32; ++k) { const float r = rs[(k & 2) + ((k >> 2) & 1)] * qs; t[k] *= f32x2{r, r}; }
        } else {
            if (sec == 2) p1_pass<1>(acc, lds + xo + ra * P1_XSLOT, lds + (xo ^ 64) + ra * P1_XSLOT, wp, ra, mid);
            else static_for<0, 10>([&](auto N) __attribute__((always_inline)) { mid(N); LGKM_BAR(); });
            STAMP(1);
            f32x4 rs[4], nm[4]; float cv[4];
#pragma unroll
            for (int i = 0; i < 4; ++i) { cv[i] = colf[i * 16 + l15]; nm[i] = *(const LDS_AS f32x4*)(rstd + 128 + i * 16 + g4 * 4); rs[i] = *(const LDS_AS f32x4*)(rstd + i * 16 + g4 * 4); }
            const int so = WS_KV + tt * KV_TILE + KV_V;
#pragma unroll
            for (int c = 0; c < 4; ++c) { const int qd = q0 + 4 * c + q, hd = (qd * 205) >> 10, pl = qd - hd * 5; vo[c] = sec == 2 ? so + hd * KV_HEAD + pl * 256 + ((g4 ^ (pl & 3)) << 6) + (lane & 3) * 16 : OOB - KV_VT; }
            vs = KV_VT;
#pragma unroll
            for (int k = 0; k < 32; ++k) { const int m = (k >> 1) & 3; t[k] = pfma(pk4(nm[m], (k & 1) * 2), f32x2{cv[k >> 3], cv[k >> 3]}, pk4(acc[m][k >> 3], (k & 1) * 2)); }
            SBAR();
#pragma unroll
            for (int k = 0; k < 32; ++k) t[k] *= pk4(rs[(k >> 1) & 3], (k & 1) * 2);
            const int hd = q0 / 5 + g4, oo = sec == 2 && hd * 5 + 4 < q0 + 16 ? so + hd * KV_HEAD + 5 * 256 + l15 * 16 : OOB - KV_VT;
            const unsigned ov = (lane & 3) ? 0u : 0x3f803f80u;
            ST16(wsr, (u32x4{ov, ov, ov, ov}), oo, 0); ST16(wsr, (u32x4{ov, ov, ov, ov}), oo, KV_VT);
        }
        SBAR();
#pragma unroll
        for (int i = 0; i < 8; ++i) ST16(wsr, (u32x4{cvt2(t[4 * i]), cvt2(t[4 * i + 1]), cvt2(t[4 * i + 2]), cvt2(t[4 * i + 3])}), vo[i >> 1], (i & 1) * vs);
        if (pk_.job < 680) pack_store(pk_, pf, lane, wsr);
    }
    STAMP(6);
    f32x4 acc[4][6];
#pragma unroll
    for (int i = 0; i < 24; ++i) acc[i / 6][i % 6] = f32x4{};
    {
        const int wo0 = P1_WSLOT + (g4 >> 1) * 6144 + (g4 & 1) * 512 + q * 64 + (lane & 3) * 8, T0 = wn * 3, T1 = T0 + 1;
        lds_ptr wp[2] = {lds + wo0 + (T0 >> 1) * 1024 + (((T0 ^ g4) & 1) << 5), lds + wo0 + (T1 >> 1) * 1024 + (((T1 ^ g4) & 1) << 5)};
        p1_pass<2>(acc, lds + xo + rb * P1_XSLOT, lds + (xo ^ 64) + rb * P1_XSLOT, wp, rb, [&](auto N) __attribute__((always_inline)) {
            if constexpr (N.v == FA_POS - 11) asm volatile("s_waitcnt vmcnt(0)" ::: "memory");
        });
    }
    STAMP(3);
    {
        const LDS_AS float* colf = (const LDS_AS float*)(lds + P1_COLB) + wn * 48 + g4 * 4;
        f32x4 cg[3], cu[3];
#pragma unroll
        for (int c = 0; c < 3; ++c) { cg[c] = *(const LDS_AS f32x4*)(colf + c * 16); cu[c] = *(const LDS_AS f32x4*)(colf + 96 + c * 16); }
        float nm[4], r2[4];
#pragma unroll
        for (int m = 0; m < 4; ++m) { const float r = rstd[m * 16 + l15]; nm[m] = rstd[128 + m * 16 + l15]; r2[m] = r * r; }
        const int vo = lane * 16 + WS_H16 + (tt * 96 + grp * 6 + wn * 3) * 1024;
#pragma unroll
        for (int mm = 0; mm < 2; ++mm)
#pragma unroll
            for (int c = 0; c < 3; ++c) {
                float g[8], u[8];
#pragma unroll
                for (int i = 0; i < 8; ++i) { const int m = mm * 2 + (i >> 2); g[i] = fma1(cg[c][i & 3], nm[m], acc[m][c][i & 3]); u[i] = fma1(cu[c][i & 3], nm[m], acc[m][3 + c][i & 3]); }
#pragma unroll
                for (int i = 0; i < 8; ++i) { g[i] = fmaxf(g[i], 0.f); u[i] = mul1(u[i], r2[mm * 2 + (i >> 2)]); }
#pragma unroll
                for (int i = 0; i < 8; ++i) g[i] = mul1(g[i], u[i]);
                ST16(wsr, (u32x4{cvtpk(g[0], g[1]), cvtpk(g[2], g[3]), cvtpk(g[4], g[5]), cvtpk(g[6], g[7])}), vo, (mm * 96 + c) * 1024);
            }
    }
}

__device__ __forceinline__ void glds_x4(const char* sbase, const unsigned (&v)[4], unsigned lds_base) {
    asm volatile("s_mov_b32 m0, %5\n\ts_nop 0\n\tglobal_load_lds_dwordx4 %0, %4\n\tglobal_load_lds_dwordx4 %1, %4 offset:1024\n\t"
                 "global_load_lds_dwordx4 %2, %4 offset:2048\n\tglobal_load_lds_dwordx4 %3, %4 offset:3072"
                 :: "v"(v[0]), "v"(v[1]), "v"(v[2]), "v"(v[3]), "s"(sbase), "s"(lds_base) : "memory");
}
__device__ __forceinline__ void glds_x3(const char* sbase, const unsigned* v, unsigned lds_base) {
    asm volatile("s_mov_b32 m0, %4\n\ts_nop 0\n\tglobal_load_lds_dwordx4 %0, %3\n\tglobal_load_lds_dwordx4 %1, %3 offset:1024\n\tglobal_load_lds_dwordx4 %2, %3 offset:2048"
                 :: "v"(v[0]), "v"(v[1]), "v"(v[2]), "s"(sbase), "s"(lds_base) : "memory");
}

__device__ __forceinline__ unsigned long long p1_helper(lds_ptr lds, int hw, int lane, const char* xp, const char* wq, const char* wgp, const char* wup, int ra, int rb,
                                          char* ws, const char* kv, const char* qsrc, const unsigned (&xv)[4], const unsigned (&wa)[4]) {
    const unsigned lds0 = (unsigned)(uintptr_t)lds;
    unsigned wb[3];
#pragma unroll
    for (int j = 0; j < 3; ++j) wb[j] = 4096 + (hw * 16 + (lane >> 2)) * (F_ * 2) + j * 64 + (((lane & 3) ^ ((lane >> 5) * 2)) << 4) - j * 1024;
    xp -= 4096; wq -= 4096; wgp -= 4096; wup -= 4096;
    int kb_ = 0, xs = ra, dws = 0, left = 5;
    auto issue1 = [&](bool pa) {
        if (pa) glds_x4(wq + (unsigned)((kb_ + xs) * 64 * 3 * D_ * 2), wa, lds0 + dws * P1_WSLOT + hw * 4096);
        else glds_x3(wgp + (unsigned)((kb_ + xs) * 64 * F_ * 2), wb, lds0 + dws * P1_WSLOT + hw * 6144);
        SBAR();
    };
    auto issue2 = [&](bool pa, bool xx) {
        SBAR();
        if (!pa) glds_x3(wup + (unsigned)((kb_ + xs) * 64 * F_ * 2), wb, lds0 + dws * P1_WSLOT + hw * 6144 + 3072);
        xs = xs == 4 ? 0 : xs + 1; dws = dws == 2 ? 0 : dws + 1; --left; kb_ = left == 0 ? 5 - kb_ : kb_; left = left == 0 ? 5 : left;
        if (xx) glds_x4(xp + (kb_ + xs) * 128, xv, lds0 + P1_X0 + xs * P1_XSLOT + hw * 4096);
    };
    issue2(true, true);
    __builtin_amdgcn_s_barrier();
    unsigned long long ep = 0;
    asm volatile("global_load_dwordx2 %0, %1, %2 sc1" : "+v"(ep) : "v"(0u), "s"(ws + WS_EPOCH) : "memory");
    __builtin_amdgcn_s_setprio(3);
    const int r16 = lane & 15, h = lane >> 5, blk = (lane >> 4) & 1, q = (lane >> 2) & 3;
    const unsigned olo = r16 < 2 ? 0x3f803f80u : 0u;
    const s16x8 ones2 = __builtin_bit_cast(s16x8, (u32x4{olo, olo, olo, olo}));
    const int idx = (r16 & 1) ? 0xEEEE : 0x4444;
    int ws3 = 0;
    {
        const int bo = (lane >> 4) * 2048 + (hw >> 1) * 1024 + q * 128 + (lane & 3) * 8 + ((((hw & 1) * 2) ^ (q >> 1) ^ (blk << 1)) << 5);
        lds_ptr b = lds + bo, b1 = lds + (bo ^ 32);
        const int so = P1_X0 + ra * P1_XSLOT + (hw * 32 + r16) * 128 + (((lane >> 4) ^ (r16 >> 1)) << 4);
        lds_ptr sp0 = lds + so, sp1 = lds + (so ^ 64);
        f32x4 cs = f32x4{}, cs1 = f32x4{}, s1[2] = {f32x4{}, f32x4{}}, s2[2] = {f32x4{}, f32x4{}};
        s16x16 wv0, wv1; s16x8 xa[2], xb[2];
        auto col_rd = [&]() {
            wv0 = CAT16(CAT8(trread(b), trread(b + 512)), CAT8(trread(b + 8192), trread(b + 8192 + 512)));
            wv1 = CAT16(CAT8(trread(b1), trread(b1 + 512)), CAT8(trread(b1 + 8192), trread(b1 + 8192 + 512)));
            const int dw = ws3 == 2 ? -2 * P1_WSLOT : P1_WSLOT;
            b += dw; b1 += dw; ws3 = ws3 == 2 ? 0 : ws3 + 1;
        };
        auto row_rd = [&](int dx) {
#pragma unroll
            for (int t = 0; t < 2; ++t) { xa[t] = *(const LDS_AS s16x8*)(sp0 + t * 2048); xb[t] = *(const LDS_AS s16x8*)(sp1 + t * 2048); }
            sp0 += dx; sp1 += dx;
        };
        auto row_mm = [&]() {
#pragma unroll
            for (int t = 0; t < 2; ++t) { s2[t] = MFMA16(xa[t], xa[t], s2[t]); s1[t] = SMF16(ones2, CAT16(xa[t], xb[t]), s1[t], idx); }
#pragma unroll
            for (int t = 0; t < 2; ++t) { s2[t] = MFMA16(xb[t], xb[t], s2[t]); PIN(s1[t]); PIN(s2[t]); }
        };
#pragma unroll
        for (int n = 0; n < 9; ++n) {
            PSTAMP(2 * n);
            if (n == 0) WAIT_BAR(5); else if (n < 8) WAIT_BAR(12); else WAIT_BAR(8);
            PSTAMP(2 * n + 1);
            col_rd(); row_rd(n % 5 == 4 - ra ? -4 * P1_XSLOT : P1_XSLOT);
            if (n == 0) { issue1(true); issue2(true, true); }
            if (n == 8) { kb_ = 5; xs = rb; }
            issue1(n < 8);
            cs = SMF16(ones2, wv0, cs, idx);
            row_mm();
            cs1 = SMF16(ones2, wv1, cs1, idx); PIN(cs); PIN(cs1);
            issue2(n < 8, n < 7);
        }
        MEMFENCE(); __builtin_amdgcn_s_waitcnt(0xF70 | 10); MEMFENCE();
        row_rd(0); row_mm();
        {
            const int t = blk;
            const float d0 = (lane & 2) ? ((lane & 1) ? s2[0][3] : s2[0][2]) : ((lane & 1) ? s2[0][1] : s2[0][0]);
            const float d1 = (lane & 2) ? ((lane & 1) ? s2[1][3] : s2[1][2]) : ((lane & 1) ? s2[1][1] : s2[1][0]);
            const float a0 = __shfl(s1[0][0] + s1[0][1], r16), a1 = __shfl(s1[1][0] + s1[1][1], r16), c0 = __shfl(d0, r16 + 16 * q), c1 = __shfl(d1, r16 + 16 * q);
            const float a = t ? a1 : a0, c = t ? c1 : c0;
            const float mean = a * (1.0f / D_), var = fmaxf(c * (1.0f / D_) - mean * mean, 0.f);
            const float rstd = __builtin_amdgcn_rcpf(rbf(__builtin_amdgcn_sqrtf(rbf(rbf(var) + 1e-5f))));
            ((LDS_AS float*)(lds + P1_STAT))[h * 128 + hw * 32 + (lane & 31)] = h ? -rbf(mean) : rstd;
        }
        PSTAMP(18);
        WAIT_BAR(6);
        PSTAMP(19);
        col_rd();
        issue1(false);
        cs = SMF16(ones2, wv0, cs, idx);
        cs1 = SMF16(ones2, wv1, cs1, idx); PIN(cs); PIN(cs1);
        issue2(false, false);
        LDS_AS float* colf = (LDS_AS float*)(lds + P1_COLA) + hw * 32;
        if (lane < 16) { colf[lane] = cs[0] + cs[1]; colf[16 + lane] = cs1[0] + cs1[1]; }
    }
    asm volatile("" : "+v"(ep));
    const unsigned long long tag = (((unsigned long long)(unsigned)__builtin_amdgcn_readfirstlane((unsigned)(ep >> 32)) << 32) | (unsigned)__builtin_amdgcn_readfirstlane((unsigned)ep)) * 6364136223846793005ull + 1442695040888963407ull;
    if (hw == 0 && lane == 0) *(LDS_AS unsigned long long*)(lds + P1_TAG) = tag;
    {
        const int g4 = lane >> 4, ro = P1_WSLOT + (g4 >> 1) * 6144 + (g4 & 1) * 512 + q * 64 + (lane & 3) * 8, T0 = hw * 3, T1 = T0 + 1;
        lds_ptr b = lds + ro + (T0 >> 1) * 1024 + (((T0 ^ g4) & 1) << 5), c = lds + ro + (T1 >> 1) * 1024 + (((T1 ^ g4) & 1) << 5);
        f32x4 cs = f32x4{}, ct = f32x4{}, cu = f32x4{};
        Poll pl = {};
        bool early = false;
#pragma unroll
        for (int n = 10; n < 20; ++n) {
            PSTAMP(2 * n);
            if (n < 13 || n == 19) WAIT_BAR(6); else if (n == 13) WAIT_BAR(10); else if (n == 18) WAIT_BAR(12); else WAIT_BAR(14);
            PSTAMP(2 * n + 1);
            const s16x16 wv0 = CAT16(CAT8(trread(b), trread(b + 256)), CAT8(trread(b + 12288), trread(b + 12288 + 256)));
            const s16x16 wv1 = CAT16(CAT8(trread(c), trread(c + 256)), CAT8(trread(c + 12288), trread(c + 12288 + 256)));
            const s16x16 wv2 = CAT16(CAT8(trread(b + 1024), trread(b + 1024 + 256)), CAT8(trread(b + 13312), trread(b + 13312 + 256)));
            if (n == FA_POS && hw == 0 && lane == 0) raise_flag(ws, WS_FA, tag);
            if (n < 18) issue1(false);
            cs = SMF16(ones2, wv0, cs, idx);
            ct = SMF16(ones2, wv1, ct, idx);
            cu = SMF16(ones2, wv2, cu, idx); PIN(cs); PIN(ct); PIN(cu);
            if (n < 18) issue2(false, n >= 12 && n < 17);
            if (n == 17) poll_request(pl, ws + WS_FA, lane * 16);
            if (n == 18) { early = poll_check(pl, tag); if (!early) poll_request(pl, ws + WS_FA, lane * 16); }
            if (n == 19 && !early) poll_wait(pl, ws + WS_FA, lane * 16, tag);
            if (n >= 18 && early == (n == 18)) {
#pragma unroll
                for (int j = 0; j < 6; ++j) glds16c(kv + (hw + 4 * j) * 1024, lane * 16, lds0 + K2_C0 + (hw + 4 * j) * 1024);
            }
            if (n == 19) {
#pragma unroll
                for (int j = 0; j < 3; ++j) glds16c(qsrc + (hw + 4 * j) * 1024, lane * 16, lds0 + K2_Q0 + (hw + 4 * j) * 1024);
            }
            const int dw = ws3 == 2 ? -2 * P1_WSLOT : P1_WSLOT;
            b += dw; c += dw; ws3 = ws3 == 2 ? 0 : ws3 + 1;
        }
        LDS_AS float* colf = (LDS_AS float*)(lds + P1_COLB) + hw * 48;
        if (lane < 16) { colf[lane] = cs[0] + cs[1]; colf[16 + lane] = ct[0] + ct[1]; colf[32 + lane] = cu[0] + cu[1]; }
        WAIT_BAR(0);
        if (blockIdx.x == 0 && hw == 0 && lane == 0) __hip_atomic_store((gu64*)(ws + WS_EPOCH), tag, __ATOMIC_RELAXED, __HIP_MEMORY_SCOPE_AGENT);
#pragma unroll
        for (int j = 0; j < 6; ++j) glds16c(kv + K2_CHUNK + (hw + 4 * j) * 1024, lane * 16, lds0 + K2_KV0 + K2_SLOTB + (hw + 4 * j) * 1024);
    }
    return tag;
}

constexpr int K2_XCH = 2 * K2_SLOTB;

#define SMF32(a, b, c) __builtin_amdgcn_smfmac_f32_32x32x32_bf16((bfx8)(a), __builtin_bit_cast(bfx16, (b)), (c), 0xDDDD, 0, 0)
#define MFMA8(a, b, c) __builtin_amdgcn_mfma_f32_32x32x8bf16_1k((a), (b), (c), 0, 0, 0)
__device__ __forceinline__ f32x8 lo8(const f32x16& v) { return __builtin_shufflevector(v, v, 0, 1, 2, 3, 4, 5, 6, 7); }
__device__ __forceinline__ f32x8 hi8(const f32x16& v) { return __builtin_shufflevector(v, v, 8, 9, 10, 11, 12, 13, 14, 15); }
__device__ __forceinline__ float max16(const f32x16& s) {
    float m = fmaxf(fmaxf(s[0], s[1]), s[2]);
#pragma unroll
    for (int r = 3; r < 15; r += 2) m = fmaxf(fmaxf(m, s[r]), s[r + 1]);
    return swap32_max(fmaxf(m, s[15]));
}

struct K2B { lds_ptr r0, r1, r2; const char *wsrc, *wsa, *wsb; unsigned wdst; int lane, osoff; rsrc_t wsr; };
constexpr int k2_slot(int st) { return st < 3 ? st : st == 3 ? 0 : st == 4 ? 3 : st == 5 ? 1 : st == 6 ? 2 : st == 7 ? 0 : 3; }
__device__ __forceinline__ void k2_wpiece(const K2B& c, int st, int i) {
    glds16c((st < 3 ? c.wsa : st < 6 ? c.wsb : c.wsrc) + st * 40960 + i * 8192, c.lane * 16, c.wdst + k2_slot(st) * 40960 + i * 8192);
}
template <int I, int D, int NP, int OF>
__device__ __forceinline__ void k2_bstep(const K2B& c, f32x4 (&acc)[10], const s16x8 (&w)[5], s16x8 (&n)[5], const s16x8& a0, const s16x8& a1, s16x8 (&of)[10]) {
    constexpr int O = k2_slot((I + 1) >> 1) * 40960 + ((I + 1) & 1) * 20480;
#pragma unroll
    for (int i = 0; i < 10; ++i) {
        if (I > 0 && I < 16 && i < 5) n[i] = *(const LDS_AS s16x8*)((O < 61440 ? c.r0 : O < 122880 ? c.r1 : c.r2) + O % 61440 + i * 1024);
        acc[i] = MFMA16(w[i >> 1], (i & 1) ? a1 : a0, acc[i]);
        PIN(acc[i]);
        if (OF >= 0 && !(i & 1)) { const int k = (OF < 0 ? 0 : OF) + (i >> 1); of[k] = LD16(c.wsr, c.lane * 16, c.osoff + ((k & 1) * 20 + (k >> 1) * 4) * 1024); MEMFENCE(); }
        if ((i & 1) && (i >> 1) < NP) k2_wpiece(c, D, (i >> 1) + (D == 4));
        SBAR();
    }
}

__global__ __launch_bounds__(512, 2) void fused_block(const char* __restrict__ x, const char* __restrict__ wqkv, const char* __restrict__ wg, const char* __restrict__ wu,
                                                      const char* __restrict__ wd, const char* __restrict__ wo, char* __restrict__ ws, char* __restrict__ out) {
    extern __shared__ __attribute__((aligned(16))) char smem[];
    if (threadIdx.x < 64) {
        unsigned off = ((blockIdx.x >> 3) * 13 + min(threadIdx.x & 63u, 12u)) * 128;
        asm volatile("s_getpc_b64 vcc\n.Lfb_begin:\n\tv_min_u32 %0, .Lfb_end-.Lfb_begin, %0\n\ts_mov_b32 m0, %1\n\ts_nop 0\n\tglobal_load_lds_dword %0, vcc"
                         : "+v"(off) : "s"((unsigned)(uintptr_t)(lds_ptr)smem + LDS_JUNK) : "vcc", "memory");
    }
    SBAR();
    const int wid = __builtin_amdgcn_readfirstlane(threadIdx.x >> 6), bid = blockIdx.x;
    const int bi = bid >> 3, panel = (bid & 1) * 8 + (bi & 7), grp = ((bid & 7) >> 1) * 4 + (bi >> 3), l0 = threadIdx.x & 63;
    const int rb = (5 * (bi & 7)) >> 3, ra = ((bi ^ (bi >> 3)) & 1) * 2;
    unsigned xv[4], wa[4];
    {
        const int hw = wid & 3, r = l0 >> 3, pos = l0 & 7;
#pragma unroll
        for (int j = 0; j < 4; ++j) {
            xv[j] = 4096 + (hw * 32 + j * 8 + r) * (D_ * 2) + ((pos ^ ((j & 1) * 4 + (r >> 1))) << 4) - j * 1024;
            wa[j] = 4096 + (hw * 16 + (j >> 1) * 8 + r) * (3 * D_ * 2) + (j & 1) * 128 + ((pos ^ (((r >> 1) & 1) * 2 + (j >> 1) * 4)) << 4) - j * 1024;
            PIN(xv[j]); PIN(wa[j]);
        }
        int xo = panel * 128 * (D_ * 2) + ra * 128 - 4096, wo_ = min(grp, 14) * 256 + ra * 64 * 3 * D_ * 2 - 4096;
        unsigned dx = (unsigned)(uintptr_t)(lds_ptr)smem + P1_X0 + ra * P1_XSLOT + hw * 4096, dw = (unsigned)(uintptr_t)(lds_ptr)smem + hw * 4096;
        asm volatile("" : "+s"(xo), "+s"(wo_), "+s"(dx), "+s"(dw));
        SBAR();
        asm volatile("" :: "s"(x), "s"(wqkv), "s"(wg), "s"(wu), "s"(wd), "s"(wo), "s"(ws), "s"(out));
        if (wid >= 4) { glds_x4(x + xo, xv, dx); glds_x4(wqkv + wo_, wa, dw); }
        SBAR();
    }
    STAMP_DECL; STAMP(0); PSTAMP(63);
    unsigned long long tag = 0;
    if (wid < 4) __builtin_amdgcn_s_barrier();
    const rsrc_t wsr = __builtin_amdgcn_make_buffer_rsrc(ws, 0, (int)WS_END, 0x00020000);
    const int head = (bid & 7) * 4 + ((bid >> 3) & 3), par = wid >> 2, qt0 = (bid >> 5) * 8 + (wid & 3) * 2;
    const char* kv = ws + WS_KV + head * KV_HEAD;
    {
        if (wid < 4) {
            p1_compute((lds_ptr)smem, wid, l0, panel, grp, ra, rb, wsr, Pack{wd, wo, wid * 256 + bid} STAMP_PASS);
            const u32x2 e = *(const LDS_AS u32x2*)((lds_ptr)smem + P1_TAG);
            tag = ((unsigned long long)(unsigned)__builtin_amdgcn_readfirstlane(e[1]) << 32) | (unsigned)__builtin_amdgcn_readfirstlane(e[0]);
        } else {
            tag = p1_helper((lds_ptr)smem, wid - 4, l0, x + (size_t)panel * 128 * (D_ * 2), wqkv + min(grp, 14) * 256, wg + grp * 192, wu + grp * 192, ra, rb,
                            ws, kv, ws + WS_Q + head * Q_HEAD + (bid >> 5) * 8 * Q_TILE, xv, wa);
        }
    }
    int lane = __builtin_amdgcn_mbcnt_hi(~0u, __builtin_amdgcn_mbcnt_lo(~0u, 0u));
    PIN(lane);
    if (wid < 4) __builtin_amdgcn_s_setprio(3); else __builtin_amdgcn_s_setprio(0);
    STAMP(4);
    lds_ptr lds = (lds_ptr)smem + K2_KV0;
    const unsigned lds0 = (unsigned)(uintptr_t)lds;
    const int tp = ((blockIdx.x & 7) >> 1) * 8 + ((blockIdx.x >> 3) & 7), cg = (blockIdx.x & 1) * 4 + (blockIdx.x >> 6);
    K2B kb;
    kb.wsrc = ws + WS_WP + cg * 68 * 5120 + wid * 1024; kb.wdst = (unsigned)(uintptr_t)(lds_ptr)smem + wid * 1024; kb.lane = lane;
    const int sw3 = tp & 1;
    kb.wsa = kb.wsrc + sw3 * 122880; kb.wsb = kb.wsrc - sw3 * 122880;
    const int hoff = WS_H16 + ((tp * 2 + (wid & 1)) * 96 + (wid >> 1) * 2 + sw3 * 48) * 1024, hoff1 = hoff + (1 - 2 * sw3) * 49152;
    s16x8 hf[24];
    const int h = lane >> 5, l31 = lane & 31, l16 = lane * 16;
    const char* src = kv + wid * 1024 + 3 * K2_CHUNK;
    const int kr = ((l31 & 4) << 2) | ((l31 >> 1) & 12) | (l31 & 3);
    const int k1off = h * 1024 + kr * 16, k2off = h ? KV_V + 5 * 256 : 2048 + kr * 16;
    const int vq = min(l31 >> 2, 5), voff = KV_V + vq * 256 + ((h ^ (vq & 3)) << 6) + (l31 < 24 ? l31 & 3 : 1) * 16;

    f32x16 oa[2] = {f32x16{}, f32x16{}}, sc[2];
    s16x8 k1[2], v0[2], v1[2]; s16x4 k2[2];
    const lds_ptr c0 = (lds_ptr)smem + K2_C0;
    lds_ptr pk1 = c0 + k1off + par * 8, pk2 = c0 + k2off + par * 8, pv = c0 + voff + par * KV_VT, pw = c0 + (voff ^ 128) + par * KV_VT;
    lds_ptr pk3 = pk1 + 512;
    asm volatile("" : "+v"(pk3));
#define LDS8(p) (*(const LDS_AS s16x4*)(p))
#define RD_K(t, j) do { k1[(t) & 1] = CAT8(LDS8(pk1 + (j) * 2 * KV_TILE), LDS8(pk3 + (j) * 2 * KV_TILE)); k2[(t) & 1] = LDS8(pk2 + (j) * 2 * KV_TILE); } while (0)
#define RD_V(t, j) do { v0[(t) & 1] = *(const LDS_AS s16x8*)(pv + (j) * 2 * KV_TILE); v1[(t) & 1] = *(const LDS_AS s16x8*)(pw + (j) * 2 * KV_TILE); } while (0)
    s16x8 q1[2]; s16x4 q2[2];
#pragma unroll
    for (int b = 0; b < 2; ++b) {
        const lds_ptr qp = (lds_ptr)smem + K2_Q0 + (wid & 3) * 2 * Q_TILE + b * 8 + l31 * 16;
        q1[b] = CAT8(LDS8(qp + h * 1024), LDS8(qp + h * 1024 + 512)); q2[b] = LDS8(qp + 2048);
    }
    {
        const s16x8 ka = CAT8(LDS8(c0 + k1off), LDS8(c0 + k1off + 512));
        const s16x4 kb = LDS8(c0 + k2off);
        RD_K(0, 0); RD_V(0, 0);
#pragma unroll
        for (int b = 0; b < 2; ++b) {
            if (h) q2[b] = s16x4{0, 0, 0, 0};
            f32x16 s = MFMA32(ka, q1[b], f32x16{});
            s = MFMA8(kb, q2[b], s);
            const float m = rbf(-max16(s));
            if (h) q2[b][0] = (short)(__float_as_uint(m) >> 16);
        }
        sc[0] = MFMA32(k1[0], q1[0], f32x16{});
        sc[0] = MFMA8(k2[0], q2[0], sc[0]);
    }
    STAMP(5); PSTAMP(42); PSTAMP(43);
#define EXP4(S, i) do { S[i] = __builtin_amdgcn_exp2f(S[i]); S[i + 1] = __builtin_amdgcn_exp2f(S[i + 1]); S[i + 2] = __builtin_amdgcn_exp2f(S[i + 2]); S[i + 3] = __builtin_amdgcn_exp2f(S[i + 3]); PIN(S); SBAR(); } while (0)
    Poll pl = {};
    s16x8 wfa[5], wfb[5];
    kb.r0 = (lds_ptr)smem + (wid >> 1) * 5120 + l16;
    auto request = [&](auto HS) __attribute__((always_inline)) {
        constexpr int hs = HS.v, p = (hs - 6) >> 3, r = (hs - 6) & 7;
        constexpr int w = (hs >= 9 && hs < 14) ? hs - 9 : (hs >= 17 && hs < 22) ? hs - 12 : (hs >= 54 && hs < 57) ? hs - 44 : hs >= 62 ? hs - 49 : -1;
        constexpr int e = (p >= 2 && p <= 4 && r >= 3) ? (p - 2) * 5 + r - 3 : p == 5 ? 15 + r : hs == 57 ? 23 : -1;
        if constexpr (hs < 3) glds16c(src - K2_CHUNK + hs * 8192, l16, lds0 + 2 * K2_SLOTB + wid * 1024 + hs * 8192);
        else if constexpr (hs >= 6 && p < 5 && r < 3) glds16c(src + p * K2_CHUNK + r * 8192, l16, lds0 + (p % 3) * K2_SLOTB + wid * 1024 + r * 8192);
        else if constexpr (w >= 0) k2_wpiece(kb, w / 5, w % 5);
        else if constexpr (e >= 0) { hf[e] = LD16(wsr, l16, (e < 12 ? hoff : hoff1) + ((e % 12) >> 1) * 8192 + (e & 1) * 1024); MEMFENCE(); }
        if constexpr (hs == 58) { if (DIAG_PF2) { PFLUSH(); } k2_wpiece(kb, 4, 4); }
        if constexpr (hs == 63) k2_wpiece(kb, 4, 0);
        if constexpr (hs == 3) {
            const unsigned wo_ = 6 * 40960 + min(((bi & 7) * 8 + wid) * 13 + min(lane, 12), 799) * 128;
            asm volatile("s_mov_b32 m0, %2\n\ts_nop 0\n\tglobal_load_lds_dword %0, %1 sc1" :: "v"(wo_), "s"(kb.wsrc - wid * 1024), "s"(kb.wdst - wid * 768 + LDS_JUNK) : "memory");
        }
        if (hs == 6 && wid == 4 && lane == 0) raise_flag(ws, WS_FB, tag);
        if (hs == 16 && wid == 4) poll_request(pl, ws + WS_FB, l16);
        SBAR();
    };
    static_for<0, 32>([&](auto T) __attribute__((always_inline)) {
        {
            constexpr int c = T.v >> 2, j = T.v & 3;
            if (j == 3) {
                PSTAMP(44 + 2 * c);
                if (c == 2 && wid == 4) poll_wait(pl, ws + WS_FB, l16, tag);
                if (c == 0) { asm volatile("s_waitcnt vmcnt(0)" ::: "memory"); LGKM_BAR(); } else if (c == 7) WAIT_BAR(0); else WAIT_BAR(7);
                PSTAMP(45 + 2 * c);
                if (c == 7 && par) __builtin_amdgcn_s_setprio(3); else __builtin_amdgcn_s_setprio(2);
                const int dsl = c == 0 ? K2_KV0 + K2_SLOTB - K2_C0 : (c % 3 == 2) ? -2 * K2_SLOTB : K2_SLOTB;
                pk1 += dsl; pk2 += dsl; pk3 += dsl; pv += dsl; pw += dsl;
                asm volatile("" : "+v"(pk1), "+v"(pk2), "+v"(pk3), "+v"(pv), "+v"(pw));
                SBAR();
            }
            if constexpr (T.v < 31) RD_K(j + 1, (j + 1) & 3);
            SBAR();
            if constexpr (T.v > 0) { oa[1] = SMF32(v1[(j + 1) & 1], hi8(sc[1]), oa[1]); PIN(oa[1]); }
            SBAR();
            EXP4(sc[0], 0);
            sc[1] = MFMA32(k1[j & 1], q1[1], f32x16{}); PIN(sc[1]); SBAR();
            request(IC<8 * c + 2 * j>{});
            EXP4(sc[0], 4);
            sc[1] = MFMA8(k2[j & 1], q2[1], sc[1]); PIN(sc[1]); SBAR();
            EXP4(sc[0], 8);
            oa[0] = SMF32(v0[j & 1], lo8(sc[0]), oa[0]); PIN(oa[0]); SBAR();
            EXP4(sc[0], 12);
            if constexpr (T.v < 31) RD_V(j + 1, (j + 1) & 3);
            if (j == 1 && c > 0) __builtin_amdgcn_s_setprio(1);
            if (j == 2 && c > 0) __builtin_amdgcn_s_setprio(0);
            SBAR();
            oa[0] = SMF32(v1[j & 1], hi8(sc[0]), oa[0]); PIN(oa[0]); SBAR();
            EXP4(sc[1], 0);
            if constexpr (T.v < 31) { sc[0] = MFMA32(k1[(j + 1) & 1], q1[0], f32x16{}); PIN(sc[0]); }
            SBAR();
            request(IC<8 * c + 2 * j + 1>{});
            EXP4(sc[1], 4);
            if constexpr (T.v < 31) { sc[0] = MFMA8(k2[(j + 1) & 1], q2[0], sc[0]); PIN(sc[0]); }
            else {
#pragma unroll
                for (int i = 0; i < 5; ++i) { wfa[i] = *(const LDS_AS s16x8*)(kb.r0 + i * 1024); wfb[i] = *(const LDS_AS s16x8*)(kb.r0 + 20480 + i * 1024); }
                if (par) {
#pragma unroll
                    for (int i = 0; i < 3; ++i) *(LDS_AS f32x4*)(lds + K2_XCH + (wid & 3) * 6144 + l16 + i * 1024) = f32x4{oa[0][4 * i], oa[0][4 * i + 1], oa[0][4 * i + 2], oa[0][4 * i + 3]};
                }
            }
            SBAR();
            EXP4(sc[1], 8);
            oa[1] = SMF32(v0[j & 1], lo8(sc[1]), oa[1]); PIN(oa[1]); SBAR();
            EXP4(sc[1], 12);
        }
    });
    oa[1] = SMF32(v1[1], hi8(sc[1]), oa[1]);
    unsigned qs_ = 0; (void)qs_;
    STAMP(2);
    PSTAMP(60); QSTAMP(60);
    int ln = __builtin_amdgcn_mbcnt_hi(~0u, __builtin_amdgcn_mbcnt_lo(~0u, 0u));
    PIN(ln);
    const int h2 = ln >> 5, l2 = ln & 31;
    const int mt = wid & 1, kq = wid >> 1;
    kb.lane = ln; kb.r0 = (lds_ptr)smem + kq * 5120 + ln * 16; kb.r1 = kb.r0 + 61440; kb.r2 = kb.r0 + 122880;
    asm volatile("" : "+v"(kb.r1), "+v"(kb.r2));
    kb.wsr = wsr; kb.osoff = WS_O16 + ((tp * 4 + mt * 2) * 20 + kq) * 1024;
    {
        const lds_ptr xp_ = lds + K2_XCH + (wid & 3) * 6144 + ln * 16;
        if (par) {
#pragma unroll
            for (int i = 3; i < 6; ++i) { const f32x16& g = oa[i / 3]; const int r = 4 * (i % 3); *(LDS_AS f32x4*)(xp_ + i * 1024) = f32x4{g[r], g[r + 1], g[r + 2], g[r + 3]}; }
        }
        LGKM_BAR();
        QSTAMP(21);
        if (!par) {
            __builtin_amdgcn_s_setprio(3);
            f32x4 tt[6];
#pragma unroll
            for (int i = 0; i < 6; ++i) tt[i] = *(const LDS_AS f32x4*)(xp_ + i * 1024);
            SBAR();
#pragma unroll
            for (int i = 0; i < 6; ++i) { const f32x4 t = tt[i]; f32x16& g = oa[i / 3]; const int r = 4 * (i % 3); g[r] = add1(g[r], t[0]); g[r + 1] = add1(g[r + 1], t[1]); g[r + 2] = add1(g[r + 2], t[2]); g[r + 3] = add1(g[r + 3], t[3]); }
            const int ob = (l2 >> 4) * 20 * 1024 + (l2 & 15) * 16, os = WS_O16 + qt0 * 2 * 20 * 1024;
            const int o1 = ob + h2 * 256 + os + (head >> 1) * 1024 + (head & 1) * 512, o2 = os + (16 + (head >> 3)) * 1024 + ((head >> 1) & 3) * 256 + (head & 1) * 8;
            auto put = [&](const f32x16& o, int b, u32x2& e) {
                const float l = __uint_as_float(__builtin_amdgcn_permlane32_swap(__float_as_uint(o[8]), __float_as_uint(o[8]), false, false)[1]);
                const float rl = __builtin_amdgcn_rcpf(l);
                unsigned w[6];
#pragma unroll
                for (int i = 0; i < 6; ++i) w[i] = cvtpk(mul1(o[2 * i], rl), mul1(o[2 * i + 1], rl));
                ST16(wsr, (u32x4{w[0], w[1], w[2], w[3]}), o1, b * 40 * 1024);
                e = u32x2{w[4], w[5]};
                return l;
            };
            u32x2 e0, e1;
            QSTAMP(22);
            const float l0 = put(oa[0], 0, e0), l1 = put(oa[1], 1, e1);
            ST8(wsr, (u32x2{__builtin_amdgcn_permlane32_swap(e0[0], e1[0], false, false)[0], __builtin_amdgcn_permlane32_swap(e0[1], e1[1], false, false)[0]}), ob + h2 * 40 * 1024, o2);
            MEMFENCE(); SBAR();
            QSTAMP(23);
            float big = 0.f;
#pragma unroll
            for (int r = 0; r < 12; ++r) big = add1(big, add1(fabsf(oa[0][r]), fabsf(oa[1][r])));
            if (__builtin_expect(__any(!(big < 1e30f) | !(fminf(l0, l1) > 1e-30f)), 0)) {
                const int gr = ((l2 & 4) << 2) | ((l2 >> 1) & 12) | (l2 & 3), g1 = h2 * 1024 + gr * 16, g2 = 2048 + gr * 16, gq = min(l2 >> 2, 5), gv = KV_V + gq * 256 + ((h2 ^ (gq & 3)) << 6) + (l2 & 3) * 16;
                asm volatile("s_waitcnt vmcnt(0)" ::: "memory");
#pragma unroll 1
                for (int b = 0; b < 2; ++b) {
                    const s16x8 qf1 = b ? q1[1] : q1[0];
                    s16x4 qf2 = b ? q2[1] : q2[0];
                    if (h2) qf2[0] = 0;
                    float m = -INFINITY;
                    f32x16 negm, o = f32x16{};
#pragma unroll 1
                    for (int t = 0; t < 64; ++t) {
                        const int g = WS_KV + head * KV_HEAD + (t >> 1) * 2 * KV_TILE + (t & 1) * 8;
                        f32x16 s = MFMA32(CAT8(LD8(wsr, g1, g), LD8(wsr, g1 + 512, g)), qf1, f32x16{});
                        s = MFMA8(LD8(wsr, g2, g), qf2, s);
                        m = fmaxf(m, max16(s));
                    }
#pragma unroll
                    for (int r = 0; r < 16; ++r) negm[r] = -m;
#pragma unroll 1
                    for (int t = 0; t < 64; ++t) {
                        const int g = WS_KV + head * KV_HEAD + (t >> 1) * 2 * KV_TILE + (t & 1) * 8;
                        f32x16 s = MFMA32(CAT8(LD8(wsr, g1, g), LD8(wsr, g1 + 512, g)), qf1, negm);
                        s = MFMA8(LD8(wsr, g2, g), qf2, s);
#pragma unroll
                        for (int r = 0; r < 16; ++r) s[r] = __builtin_amdgcn_exp2f(s[r]);
                        o = SMF32(LD16(wsr, gv, g + (t & 1) * (KV_VT - 8)), lo8(s), o);
                        o = SMF32(LD16(wsr, gv ^ 128, g + (t & 1) * (KV_VT - 8)), hi8(s), o);
                    }
                    u32x2 e;
                    put(o, b, e);
                    if (!h2) ST8(wsr, e, ob + b * 40 * 1024, o2);
                }
            }
        }
    }
    PSTAMP(61); QSTAMP(61);
    __builtin_amdgcn_s_setprio(0);
    const int ch0 = wid * 128 + ln, ch1 = ch0 + 64, er0 = ch0 / 10, er1 = ch1 / 10, ec0 = ch0 - er0 * 10, ec1 = ch1 - er1 * 10;
    const size_t eb = (size_t)tp * 64 * (D_ * 2) + cg * 160;
    const unsigned eo0 = er0 * (D_ * 2) + ec0 * 16, eo1 = er1 * (D_ * 2) + ec1 * 16;
    f32x4 acc[10];
#pragma unroll
    for (int i = 0; i < 10; ++i) acc[i] = f32x4{};
    s16x8 of[10];
#define HLO(i) __builtin_shufflevector(hf[2 * (i)], hf[2 * (i) + 1], 0, 1, 2, 3, 8, 9, 10, 11)
#define HHI(i) __builtin_shufflevector(hf[2 * (i)], hf[2 * (i) + 1], 4, 5, 6, 7, 12, 13, 14, 15)
    QSTAMP(0); k2_bstep<0, 0, 0, -1>(kb, acc, wfa, wfb, HLO(0), HHI(0), of);
    QSTAMP(1); k2_bstep<1, 0, 0, -1>(kb, acc, wfb, wfa, HLO(1), HHI(1), of);
    QSTAMP(2); k2_bstep<2, 0, 0, -1>(kb, acc, wfa, wfb, HLO(2), HHI(2), of);
    if (!par) {
        asm volatile("s_waitcnt vmcnt(0)" ::: "memory");
        if (ln == 0) __hip_atomic_store((gu64*)(ws + WS_FC) + (qt0 >> 1) * 32 + head, tag, __ATOMIC_RELAXED, __HIP_MEMORY_SCOPE_AGENT);
    } else {
#pragma unroll
        for (int i = 0; i < 10; ++i) glds16c(kb.wsb + 3 * 40960 - 4096 + i * 4096, ln * 16, kb.wdst - 4096 + i * 4096);
    }
    WAIT_BAR(10);
    QSTAMP(3); k2_bstep<3, 4, 3, -1>(kb, acc, wfb, wfa, HLO(3), HHI(3), of);
    QSTAMP(4); k2_bstep<4, 5, 5, -1>(kb, acc, wfa, wfb, HLO(4), HHI(4), of);
    WAIT_BAR(8);
    QSTAMP(5); k2_bstep<5, 6, 5, -1>(kb, acc, wfb, wfa, HLO(5), HHI(5), of);
    QSTAMP(6); k2_bstep<6, 0, 0, -1>(kb, acc, wfa, wfb, HLO(6), HHI(6), of);
    WAIT_BAR(10);
    QSTAMP(7); k2_bstep<7, 7, 5, -1>(kb, acc, wfb, wfa, HLO(7), HHI(7), of);
    unsigned long long seen = 0;
    const char* cw = ws + WS_FC + tp * 256;
    const unsigned cvo = l2 * 8;
    if (wid == 4) asm volatile("global_load_dwordx2 %0, %1, %2 sc1" : "+v"(seen) : "v"(cvo), "s"(cw) : "memory");
    SBAR();
    QSTAMP(8); k2_bstep<8, 0, 0, -1>(kb, acc, wfa, wfb, HLO(8), HHI(8), of);
    if (wid == 4) {
        asm volatile("s_waitcnt vmcnt(0)" : "+v"(seen) :: "memory");
#pragma unroll 1
        for (unsigned spin = 0; spin < (1u << 18) && !__all(seen == tag); ++spin) {
            __builtin_amdgcn_s_sleep(1);
            asm volatile("global_load_dwordx2 %0, %1, %2 sc1\n\ts_waitcnt vmcnt(0)" : "=&v"(seen) : "v"(cvo), "s"(cw) : "memory");
        }
    }
    WAIT_BAR(10);
    QSTAMP(9); k2_bstep<9, 0, 0, 0>(kb, acc, wfb, wfa, HLO(9), HHI(9), of);
    QSTAMP(10); k2_bstep<10, 0, 0, 5>(kb, acc, wfa, wfb, HLO(10), HHI(10), of);
    WAIT_BAR(10);
    QSTAMP(11); k2_bstep<11, 8, 3, -1>(kb, acc, wfb, wfa, HLO(11), HHI(11), of);
    u32x4 xv0 = u32x4{}, xv1 = u32x4{};
    if (wid < 5) { xv0 = *(const u32x4*)(x + eb + eo0); xv1 = *(const u32x4*)(x + eb + eo1); }
    MEMFENCE(); SBAR();
    QSTAMP(12); k2_bstep<12, 0, 0, -1>(kb, acc, wfa, wfb, of[0], of[1], of);
    QSTAMP(13); k2_bstep<13, 0, 0, -1>(kb, acc, wfb, wfa, of[2], of[3], of);
    QSTAMP(14); k2_bstep<14, 0, 0, -1>(kb, acc, wfa, wfb, of[4], of[5], of);
    WAIT_BAR(0);
    QSTAMP(15); k2_bstep<15, 0, 0, -1>(kb, acc, wfb, wfa, of[6], of[7], of);
    QSTAMP(16); k2_bstep<16, 0, 0, -1>(kb, acc, wfa, wfb, of[8], of[9], of);
    QSTAMP(17);
    {
        const int tk = ln & 15;
        const lds_ptr pp = (lds_ptr)smem + kq * 20480 + (mt * 32 + tk) * 320 + (((ln >> 4) ^ ((tk >> 1) & 3)) << 4);
#pragma unroll
        for (int i = 0; i < 10; ++i) *(LDS_AS f32x4*)(pp + (i & 1) * 16 * 320 + (i >> 1) * 64) = acc[i];
        QSTAMP(18);
        LGKM_BAR();
        QSTAMP(19);
        if (wid < 5) {
#pragma unroll
            for (int e = 0; e < 2; ++e) {
                const int er = e ? er1 : er0, ec = e ? ec1 : ec0, sw = (er >> 1) & 3;
                const lds_ptr p0 = (lds_ptr)smem + er * 320 + (((2 * ec) ^ sw) << 4), p1 = (lds_ptr)smem + er * 320 + (((2 * ec + 1) ^ sw) << 4);
                f32x4 a = *(const LDS_AS f32x4*)p0, b = *(const LDS_AS f32x4*)p1;
#pragma unroll
                for (int k = 1; k < 4; ++k) { a += *(const LDS_AS f32x4*)(p0 + k * 20480); b += *(const LDS_AS f32x4*)(p1 + k * 20480); }
                const u32x4 xr = e ? xv1 : xv0;
                const u32x4 ov = u32x4{cvtpk(a[0] + bf_lo(xr[0]), a[1] + bf_hi(xr[0])), cvtpk(a[2] + bf_lo(xr[1]), a[3] + bf_hi(xr[1])),
                                       cvtpk(b[0] + bf_lo(xr[2]), b[1] + bf_hi(xr[2])), cvtpk(b[2] + bf_lo(xr[3]), b[3] + bf_hi(xr[3]))};
                __builtin_amdgcn_raw_buffer_store_b128(ov, __builtin_amdgcn_make_buffer_rsrc(out, 0, S_ * D_ * 2, 0x00020000), (int)eb + (e ? eo1 : eo0), 0, 16);
            }
        }
    }
    QSTAMP(20);
    asm volatile(".Lfb_end:");
    STAMP(7); STAMP_FLUSH(0); QFLUSH();
}

extern "C" void kernel_launch(void* const* d_in, const int* in_sizes, int n_in, void* d_out, int out_size, void* d_ws, size_t ws_size,
                              hipStream_t stream) {
    const char* x = (const char*)d_in[0];
    const char* wqkv = (const char*)d_in[1];
    const char* wo = (const char*)d_in[2];
    const char* wg = (const char*)d_in[4];
    const char* wu = (const char*)d_in[5];
    const char* wd = (const char*)d_in[6];
    char* ws = (char*)d_ws;
    (void)hipFuncSetAttribute((const void*)fused_block, hipFuncAttributeMaxDynamicSharedMemorySize, LDS_BYTES);
    fused_block<<<256, 512, LDS_BYTES, stream>>>(x, wqkv, wg, wu, wd, wo, ws, (char*)d_out);
}
```

```cpp
#include <hip/hip_runtime.h>
#include <hip/hip_bf16.h>
#include <stdint.h>

typedef __bf16 bfx8 __attribute__((ext_vector_type(8)));
typedef __bf16 bfx16 __attribute__((ext_vector_type(16)));
typedef short s16x4 __attribute__((ext_vector_type(4)));
typedef short s16x8 __attribute__((ext_vector_type(8)));
typedef short s16x16 __attribute__((ext_vector_type(16)));
typedef float f32x2 __attribute__((ext_vector_type(2)));
typedef float f32x4 __attribute__((ext_vector_type(4)));
typedef float f32x8 __attribute__((ext_vector_type(8)));
typedef float f32x16 __attribute__((ext_vector_type(16)));
typedef unsigned u32x2 __attribute__((ext_vector_type(2)));
typedef unsigned u32x4 __attribute__((ext_vector_type(4)));
#define LDS_AS __attribute__((address_space(3)))
typedef LDS_AS char* lds_ptr;

constexpr int S_ = 2048, D_ = 640, H_ = 32, F_ = 1536;

constexpr int Q_TILE = 1280, Q_HEAD = 64 * Q_TILE, KV_TILE = 2816, KV_V = 2560, KV_VT = 1536, KV_HEAD = 64 * KV_TILE;
constexpr int WS_Q = 0;
constexpr int WS_KV = WS_Q + H_ * Q_HEAD;
constexpr int WS_O16 = WS_KV + H_ * KV_HEAD + 8192;
constexpr int WS_H16 = WS_O16 + 128 * 20 * 1024;
constexpr int WS_WP = WS_H16 + 128 * 48 * 1024;
constexpr int WS_EPOCH = WS_WP + 8 * 68 * 5120 + 8192;
constexpr int WS_FA = WS_EPOCH + 256, WS_FB = WS_FA + 2048, WS_FC = WS_FB + 2048, WS_END = WS_FC + 8192;

#define SBAR() __builtin_amdgcn_sched_barrier(0)
template <int V> struct IC { static constexpr int v = V; };
template <int I, int N, class F> __device__ __forceinline__ void static_for(F&& f) { if constexpr (I < N) { f(IC<I>{}); static_for<I + 1, N>(f); } }
#define MFMA32(a, b, c) __builtin_amdgcn_mfma_f32_32x32x16_bf16((bfx8)(a), (bfx8)(b), (c), 0, 0, 0)

__device__ __forceinline__ unsigned cvtpk(float lo, float hi) {
    typedef __bf16 bfx2 __attribute__((ext_vector_type(2)));
    bfx2 r = {(__bf16)lo, (__bf16)hi};
    return __builtin_bit_cast(unsigned, r);
}
__device__ __forceinline__ s16x4 trread(const LDS_AS char* p) {
    return __builtin_amdgcn_ds_read_tr16_b64_v4i16((LDS_AS s16x4*)p);
}
__device__ __forceinline__ float rbf(float v) { return (float)(__bf16)v; }
__device__ __forceinline__ float bf_lo(unsigned u) { return __uint_as_float(u << 16); }
__device__ __forceinline__ float bf_hi(unsigned u) { return __uint_as_float(u & 0xffff0000u); }
__device__ __forceinline__ float swap32_max(float v) {
    auto r = __builtin_amdgcn_permlane32_swap(__float_as_uint(v), __float_as_uint(v), false, false);
    return fmaxf(__uint_as_float(r[0]), __uint_as_float(r[1]));
}

constexpr int FA_POS = 14;
constexpr int P1_WSLOT = 24576, P1_X0 = 3 * P1_WSLOT, P1_XSLOT = 16384, P1_STAT = P1_X0 + 5 * P1_XSLOT;
constexpr int P1_COLA = P1_STAT + 1024, P1_COLB = P1_COLA + 512, P1_TAG = P1_COLB + 1024, LDS_JUNK = 161792, LDS_BYTES = 163840;
constexpr int K2_CHUNK = 8 * KV_TILE, K2_SLOTB = 24576, K2_KV0 = 81920, K2_C0 = 2 * P1_WSLOT, K2_Q0 = 0;
typedef __attribute__((address_space(1))) unsigned long long gu64;

#define PIN(x) asm volatile("" : "+v"(x))
#define MEMFENCE() asm volatile("" ::: "memory")
#define WAIT_BAR(N) do { MEMFENCE(); __builtin_amdgcn_s_waitcnt(0x70 | ((N) & 15) | (((N) >> 4) << 14)); __builtin_amdgcn_s_barrier(); MEMFENCE(); SBAR(); } while (0)
#define LGKM_BAR() do { MEMFENCE(); __builtin_amdgcn_s_waitcnt(0xC07F); __builtin_amdgcn_s_barrier(); MEMFENCE(); SBAR(); } while (0)
#define MFMA16(a, b, c) __builtin_amdgcn_mfma_f32_16x16x32_bf16((bfx8)(a), (bfx8)(b), (c), 0, 0, 0)
#define SMF16(a, b, c, i) __builtin_amdgcn_smfmac_f32_16x16x64_bf16((bfx8)(a), (bfx16)(b), (c), (i), 0, 0)
#define CAT8(a, b) __builtin_shufflevector(a, b, 0, 1, 2, 3, 4, 5, 6, 7)
#define CAT16(a, b) __builtin_shufflevector(a, b, 0, 1, 2, 3, 4, 5, 6, 7, 8, 9, 10, 11, 12, 13, 14, 15)

__device__ __forceinline__ void glds16s(const char* sbase, unsigned voff, unsigned lds_base) {
    asm volatile("s_mov_b32 m0, %2\n\ts_nop 0\n\tglobal_load_lds_dwordx4 %0, %1" :: "v"(voff), "s"(sbase), "s"(lds_base) : "memory");
}
__device__ __forceinline__ void glds16c(const char* sbase, unsigned voff, unsigned lds_base) {
    asm volatile("s_mov_b32 m0, %2\n\ts_nop 0\n\tglobal_load_lds_dwordx4 %0, %1 sc1" :: "v"(voff), "s"(sbase), "s"(lds_base) : "memory");
}
typedef __amdgpu_buffer_rsrc_t rsrc_t;
#define ST16(rs, v, voff, soff) __builtin_amdgcn_raw_buffer_store_b128((v), (rs), (voff) + (soff), 0, 16)
#define ST8(rs, v, voff, soff) __builtin_amdgcn_raw_buffer_store_b64((v), (rs), (voff), (soff), 16)
#define LD16(rs, voff, soff) __builtin_bit_cast(s16x8, __builtin_amdgcn_raw_buffer_load_b128((rs), (voff), (soff), 16))
#define LD8(rs, voff, soff) __builtin_bit_cast(s16x4, __builtin_amdgcn_raw_buffer_load_b64((rs), (voff), (soff), 16))
__device__ __forceinline__ f32x2 pk4(const f32x4& a, int i) { return f32x2{a[i], a[i + 1]}; }
__device__ __forceinline__ f32x2 pfma(f32x2 a, f32x2 b, f32x2 c) { return __builtin_elementwise_fma(a, b, c); }
__device__ __forceinline__ unsigned cvt2(f32x2 v) { return cvtpk(v[0], v[1]); }
__device__ __forceinline__ float add1(float a, float b) { float r = a + b; asm volatile("" : "+v"(r)); return r; }
__device__ __forceinline__ float fma1(float a, float b, float c) { float r = fmaf(a, b, c); asm volatile("" : "+v"(r)); return r; }
__device__ __forceinline__ float mul1(float a, float b) { float r = a * b; asm volatile("" : "+v"(r)); return r; }

__device__ __forceinline__ void raise_flag(char* ws, int arr, unsigned long long tag) {
    __hip_atomic_store((gu64*)(ws + arr) + blockIdx.x, tag, __ATOMIC_RELAXED, __HIP_MEMORY_SCOPE_AGENT);
}
struct Poll { u32x4 a, b; };
__device__ __forceinline__ void poll_request(Poll& p, const char* arr, unsigned voff) {
    asm volatile("global_load_dwordx4 %0, %2, %3 sc1\n\tglobal_load_dwordx4 %1, %2, %3 offset:1024 sc1" : "+v"(p.a), "+v"(p.b) : "v"(voff), "s"(arr) : "memory");
}
__device__ __forceinline__ bool poll_check(Poll& p, unsigned long long tag) {
    const unsigned tl = (unsigned)tag, th = (unsigned)(tag >> 32);
    asm volatile("s_waitcnt vmcnt(0)" : "+v"(p.a), "+v"(p.b) :: "memory");
    return __all(p.a[0] == tl && p.a[1] == th && p.a[2] == tl && p.a[3] == th && p.b[0] == tl && p.b[1] == th && p.b[2] == tl && p.b[3] == th);
}
__device__ __forceinline__ void poll_wait(Poll& p, const char* arr, unsigned voff, unsigned long long tag) {
#pragma unroll 1
    for (unsigned spin = 0; spin < (1u << 18) && !poll_check(p, tag); ++spin) {
        __builtin_amdgcn_s_sleep(4);
        poll_request(p, arr, voff);
    }
    asm volatile("s_waitcnt vmcnt(0)" : "+v"(p.a), "+v"(p.b) :: "memory");
}

template <int MODE, class FM>
__device__ __forceinline__ void p1_pass(f32x4 (&acc)[4][MODE == 2 ? 6 : 4], lds_ptr x0, lds_ptr x1, lds_ptr (&wp)[MODE == 2 ? 2 : 4], int start, FM&& mid) {
    constexpr int NC = MODE == 2 ? 6 : 4, NR = 4 + 2 * NC, WS0 = MODE == 2 ? 1 : 0;
    struct F { s16x8 x[4]; s16x4 w[NC][2]; } f[2];
    auto rd = [&](F& n, int ks, int i) __attribute__((always_inline)) {
        if (i == 0 || i > 2 * NC) { const int m = i == 0 ? 0 : i - 2 * NC; n.x[m] = *(const LDS_AS s16x8*)((ks ? x1 : x0) + m * 2048); }
        else {
            const int t = (i - 1) >> 1, e = (i - 1) & 1, c = t % 3;
            n.w[t][e] = trread(MODE == 2 ? wp[c & 1] + ks * 12288 + (t / 3) * 3072 + (c >> 1) * 1024 + e * 256 : wp[MODE == 2 ? 0 : t] + ks * 8192 + e * 512);
        }
    };
    auto step = [&](const F& c, F& n, int ks, bool more) __attribute__((always_inline)) {
#pragma unroll
        for (int i = 0; i < 4 * NC; ++i) {
            const s16x8 w = CAT8(c.w[i % NC][0], c.w[i % NC][1]);
            f32x4& a = acc[i / NC][i % NC];
            a = MODE == 1 ? MFMA16(c.x[i / NC], w, a) : MFMA16(w, c.x[i / NC], a);
            PIN(a); SBAR();
            if (more && i < NR) { rd(n, ks, i); SBAR(); }
        }
    };
#pragma unroll
    for (int i = 0; i < NR; ++i) rd(f[0], 0, i);
    SBAR();
    static_for<0, 10>([&](auto N) __attribute__((always_inline)) {
        constexpr int n = N.v, dw = (WS0 + n) % 3 == 2 ? -2 * P1_WSLOT : P1_WSLOT;
        const int dx = n % 5 == 4 - start ? -4 * P1_XSLOT : P1_XSLOT;
        step(f[0], f[1], 1, true);
        x0 += dx; x1 += dx; PIN(x0); PIN(x1);
#pragma unroll
        for (int i = 0; i < (MODE == 2 ? 2 : 4); ++i) { wp[i] += dw; PIN(wp[i]); }
        mid(N);
        LGKM_BAR();
        step(f[1], f[0], 0, n < 9);
    });
}

struct Pack { const char *wd, *wo; int job; };
__device__ __forceinline__ void pack_fetch(const Pack& pk_, lds_ptr lds, int wid, int lane) {
    const int step = pk_.job / 10, cb = pk_.job - step * 10, r = lane >> 3;
    const unsigned va = ((r & 3) + (step < 48 ? 16 : 8) * (r >> 2)) * (D_ * 2) + (lane & 7) * 16, vb = ((r >> 2) * 20 + 16 + (r & 3)) * (D_ * 2) + (lane & 7) * 16;
#pragma unroll
    for (int g = 0; g < 4; ++g) {
        const int ks = (step - 48) * 2 + (g >> 1), hh = g & 1;
        const char* a = step < 48 ? pk_.wd + (step * 32 + g * 4) * (D_ * 2) : ks < 32 ? pk_.wo + (ks * 20 + hh * 4) * (D_ * 2) : pk_.wo + ((ks - 32) * 4 + hh * 2) * 20 * (D_ * 2);
        glds16s(a + cb * 128, step < 64 ? va : vb, (unsigned)(uintptr_t)lds + wid * P1_WSLOT + 16384 + g * 1024);
    }
}
__device__ __forceinline__ void pack_read(s16x8 (&pf)[4], lds_ptr lds, int wid, int lane) {
    asm volatile("s_waitcnt vmcnt(0)" ::: "memory");
    const lds_ptr pr = lds + wid * P1_WSLOT + 16384 + (lane >> 4) * 1024 + ((lane >> 2) & 3) * 128 + (lane & 3) * 8;
#pragma unroll
    for (int nt = 0; nt < 4; ++nt) pf[nt] = CAT8(trread(pr + nt * 32), trread(pr + nt * 32 + 512));
}
__device__ __forceinline__ void pack_store(const Pack& pk_, const s16x8 (&pf)[4], int lane, rsrc_t wsr) {
    const int step = pk_.job / 10, cb = pk_.job - step * 10;
#pragma unroll
    for (int nt = 0; nt < 4; ++nt) {
        const int N = cb * 4 + nt, cg = N / 5;
        ST16(wsr, __builtin_bit_cast(u32x4, pf[nt]), lane * 16, WS_WP + ((cg * 68 + step) * 5 + N - cg * 5) * 1024);
    }
}

constexpr int OOB = 0x7ffffff0;

__device__ __forceinline__ void p1_compute(lds_ptr lds, int wid, int lane, int panel, int grp, int ra, int rb, rsrc_t wsr, const Pack& pk_) {
    const int l15 = lane & 15, g4 = lane >> 4, q = (lane >> 2) & 3, wm = wid & 1, wn = wid >> 1, tt = panel * 4 + wm * 2;
    const LDS_AS float* rstd = (const LDS_AS float*)(lds + P1_STAT) + wm * 64;
    const int xo = P1_X0 + (wm * 64 + l15) * 128 + ((g4 ^ (l15 >> 1)) << 4);
    if (pk_.job < 680) pack_fetch(pk_, lds, wid, lane);
    {
        f32x4 acc[4][4];
#pragma unroll
        for (int i = 0; i < 16; ++i) acc[i >> 2][i & 3] = f32x4{};
        s16x8 pf[4];
        const LDS_AS float* colf = (const LDS_AS float*)(lds + P1_COLA) + wn * 64;
        const int sec = grp / 5, q0 = (grp - sec * 5) * 32 + wn * 16;
        lds_ptr wp[4];
#pragma unroll
        for (int t = 0; t < 4; ++t) wp[t] = lds + g4 * 2048 + wn * 1024 + q * 128 + (lane & 3) * 8 + ((t ^ (q >> 1) ^ ((g4 & 1) << 1)) << 5);
        auto mid = [&](auto N) __attribute__((always_inline)) { if constexpr (N.v == 5) pack_read(pf, lds, min(wid, 2), lane); };
        LGKM_BAR();
        f32x2 t[32];
        int vo[4], vs;
        if (sec < 2) {
            p1_pass<0>(acc, lds + xo + ra * P1_XSLOT, lds + (xo ^ 64) + ra * P1_XSLOT, wp, ra, mid);
            f32x4 cv[4]; float rs[4], nm[4];
#pragma unroll
            for (int i = 0; i < 4; ++i) { cv[i] = *(const LDS_AS f32x4*)(colf + i * 16 + g4 * 4); nm[i] = rstd[128 + i * 16 + l15]; rs[i] = rstd[i * 16 + l15]; }
            const float qs = sec ? 1.0f : 0.22360679774997896f * 1.4426950408889634f;
#pragma unroll
            for (int c = 0; c < 4; ++c) { const int qd = q0 + 4 * c + g4, hd = (qd * 205) >> 10; vo[c] = (sec ? WS_KV + tt * KV_TILE + hd * KV_HEAD : WS_Q + tt * Q_TILE + hd * Q_HEAD) + (qd - hd * 5) * 512 + l15 * 16; }
            vs = 256;
#pragma unroll
            for (int k = 0; k < 32; ++k) { const int m = (k & 2) + ((k >> 2) & 1); t[k] = pfma(pk4(cv[k >> 3], (k & 1) * 2), f32x2{nm[m], nm[m]}, pk4(acc[m][k >> 3], (k & 1) * 2)); }
            SBAR();
#pragma unroll
            for (int k = 0; k < 32; ++k) { const float r = rs[(k & 2) + ((k >> 2) & 1)] * qs; t[k] *= f32x2{r, r}; }
        } else {
            if (sec == 2) p1_pass<1>(acc, lds + xo + ra * P1_XSLOT, lds + (xo ^ 64) + ra * P1_XSLOT, wp, ra, mid);
            else static_for<0, 10>([&](auto N) __attribute__((always_inline)) { mid(N); LGKM_BAR(); });
            f32x4 rs[4], nm[4]; float cv[4];
#pragma unroll
            for (int i = 0; i < 4; ++i) { cv[i] = colf[i * 16 + l15]; nm[i] = *(const LDS_AS f32x4*)(rstd + 128 + i * 16 + g4 * 4); rs[i] = *(const LDS_AS f32x4*)(rstd + i * 16 + g4 * 4); }
            const int so = WS_KV + tt * KV_TILE + KV_V;
#pragma unroll
            for (int c = 0; c < 4; ++c) { const int qd = q0 + 4 * c + q, hd = (qd * 205) >> 10, pl = qd - hd * 5; vo[c] = sec == 2 ? so + hd * KV_HEAD + pl * 256 + ((g4 ^ (pl & 3)) << 6) + (lane & 3) * 16 : OOB - KV_VT; }
            vs = KV_VT;
#pragma unroll
            for (int k = 0; k < 32; ++k) { const int m = (k >> 1) & 3; t[k] = pfma(pk4(nm[m], (k & 1) * 2), f32x2{cv[k >> 3], cv[k >> 3]}, pk4(acc[m][k >> 3], (k & 1) * 2)); }
            SBAR();
#pragma unroll
            for (int k = 0; k < 32; ++k) t[k] *= pk4(rs[(k >> 1) & 3], (k & 1) * 2);
            const int hd = q0 / 5 + g4, oo = sec == 2 && hd * 5 + 4 < q0 + 16 ? so + hd * KV_HEAD + 5 * 256 + l15 * 16 : OOB - KV_VT;
            const unsigned ov = (lane & 3) ? 0u : 0x3f803f80u;
            ST16(wsr, (u32x4{ov, ov, ov, ov}), oo, 0); ST16(wsr, (u32x4{ov, ov, ov, ov}), oo, KV_VT);
        }
        SBAR();
#pragma unroll
        for (int i = 0; i < 8; ++i) ST16(wsr, (u32x4{cvt2(t[4 * i]), cvt2(t[4 * i + 1]), cvt2(t[4 * i + 2]), cvt2(t[4 * i + 3])}), vo[i >> 1], (i & 1) * vs);
        if (pk_.job < 680) pack_store(pk_, pf, lane, wsr);
    }
    f32x4 acc[4][6];
#pragma unroll
    for (int i = 0; i < 24; ++i) acc[i / 6][i % 6] = f32x4{};
    {
        const int wo0 = P1_WSLOT + (g4 >> 1) * 6144 + (g4 & 1) * 512 + q * 64 + (lane & 3) * 8, T0 = wn * 3, T1 = T0 + 1;
        lds_ptr wp[2] = {lds + wo0 + (T0 >> 1) * 1024 + (((T0 ^ g4) & 1) << 5), lds + wo0 + (T1 >> 1) * 1024 + (((T1 ^ g4) & 1) << 5)};
        p1_pass<2>(acc, lds + xo + rb * P1_XSLOT, lds + (xo ^ 64) + rb * P1_XSLOT, wp, rb, [&](auto N) __attribute__((always_inline)) {
            if constexpr (N.v == FA_POS - 11) asm volatile("s_waitcnt vmcnt(0)" ::: "memory");
        });
    }
    {
        const LDS_AS float* colf = (const LDS_AS float*)(lds + P1_COLB) + wn * 48 + g4 * 4;
        f32x4 cg[3], cu[3];
#pragma unroll
        for (int c = 0; c < 3; ++c) { cg[c] = *(const LDS_AS f32x4*)(colf + c * 16); cu[c] = *(const LDS_AS f32x4*)(colf + 96 + c * 16); }
        float nm[4], r2[4];
#pragma unroll
        for (int m = 0; m < 4; ++m) { const float r = rstd[m * 16 + l15]; nm[m] = rstd[128 + m * 16 + l15]; r2[m] = r * r; }
        const int vo = lane * 16 + WS_H16 + (tt * 96 + grp * 6 + wn * 3) * 1024;
#pragma unroll
        for (int mm = 0; mm < 2; ++mm)
#pragma unroll
            for (int c = 0; c < 3; ++c) {
                float g[8], u[8];
#pragma unroll
                for (int i = 0; i < 8; ++i) { const int m = mm * 2 + (i >> 2); g[i] = fma1(cg[c][i & 3], nm[m], acc[m][c][i & 3]); u[i] = fma1(cu[c][i & 3], nm[m], acc[m][3 + c][i & 3]); }
#pragma unroll
                for (int i = 0; i < 8; ++i) { g[i] = fmaxf(g[i], 0.f); u[i] = mul1(u[i], r2[mm * 2 + (i >> 2)]); }
#pragma unroll
                for (int i = 0; i < 8; ++i) g[i] = mul1(g[i], u[i]);
                ST16(wsr, (u32x4{cvtpk(g[0], g[1]), cvtpk(g[2], g[3]), cvtpk(g[4], g[5]), cvtpk(g[6], g[7])}), vo, (mm * 96 + c) * 1024);
            }
    }
}

__device__ __forceinline__ void glds_x4(const char* sbase, const unsigned (&v)[4], unsigned lds_base) {
    asm volatile("s_mov_b32 m0, %5\n\ts_nop 0\n\tglobal_load_lds_dwordx4 %0, %4\n\tglobal_load_lds_dwordx4 %1, %4 offset:1024\n\t"
                 "global_load_lds_dwordx4 %2, %4 offset:2048\n\tglobal_load_lds_dwordx4 %3, %4 offset:3072"
                 :: "v"(v[0]), "v"(v[1]), "v"(v[2]), "v"(v[3]), "s"(sbase), "s"(lds_base) : "memory");
}
__device__ __forceinline__ void glds_x3(const char* sbase, const unsigned* v, unsigned lds_base) {
    asm volatile("s_mov_b32 m0, %4\n\ts_nop 0\n\tglobal_load_lds_dwordx4 %0, %3\n\tglobal_load_lds_dwordx4 %1, %3 offset:1024\n\tglobal_load_lds_dwordx4 %2, %3 offset:2048"
                 :: "v"(v[0]), "v"(v[1]), "v"(v[2]), "s"(sbase), "s"(lds_base) : "memory");
}

__device__ __forceinline__ unsigned long long p1_helper(lds_ptr lds, int hw, int lane, const char* xp, const char* wq, const char* wgp, const char* wup, int ra, int rb,
                                          char* ws, const char* kv, const char* qsrc, const unsigned (&xv)[4], const unsigned (&wa)[4]) {
    const unsigned lds0 = (unsigned)(uintptr_t)lds;
    unsigned wb[3];
#pragma unroll
    for (int j = 0; j < 3; ++j) wb[j] = 4096 + (hw * 16 + (lane >> 2)) * (F_ * 2) + j * 64 + (((lane & 3) ^ ((lane >> 5) * 2)) << 4) - j * 1024;
    xp -= 4096; wq -= 4096; wgp -= 4096; wup -= 4096;
    int kb_ = 0, xs = ra, dws = 0, left = 5;
    auto issue1 = [&](bool pa) {
        if (pa) glds_x4(wq + (unsigned)((kb_ + xs) * 64 * 3 * D_ * 2), wa, lds0 + dws * P1_WSLOT + hw * 4096);
        else glds_x3(wgp + (unsigned)((kb_ + xs) * 64 * F_ * 2), wb, lds0 + dws * P1_WSLOT + hw * 6144);
        SBAR();
    };
    auto issue2 = [&](bool pa, bool xx) {
        SBAR();
        if (!pa) glds_x3(wup + (unsigned)((kb_ + xs) * 64 * F_ * 2), wb, lds0 + dws * P1_WSLOT + hw * 6144 + 3072);
        xs = xs == 4 ? 0 : xs + 1; dws = dws == 2 ? 0 : dws + 1; --left; kb_ = left == 0 ? 5 - kb_ : kb_; left = left == 0 ? 5 : left;
        if (xx) glds_x4(xp + (kb_ + xs) * 128, xv, lds0 + P1_X0 + xs * P1_XSLOT + hw * 4096);
    };
    issue2(true, true);
    __builtin_amdgcn_s_barrier();
    unsigned long long ep = 0;
    asm volatile("global_load_dwordx2 %0, %1, %2 sc1" : "+v"(ep) : "v"(0u), "s"(ws + WS_EPOCH) : "memory");
    __builtin_amdgcn_s_setprio(3);
    const int r16 = lane & 15, h = lane >> 5, blk = (lane >> 4) & 1, q = (lane >> 2) & 3;
    const unsigned olo = r16 < 2 ? 0x3f803f80u : 0u;
    const s16x8 ones2 = __builtin_bit_cast(s16x8, (u32x4{olo, olo, olo, olo}));
    const int idx = (r16 & 1) ? 0xEEEE : 0x4444;
    int ws3 = 0;
    {
        const int bo = (lane >> 4) * 2048 + (hw >> 1) * 1024 + q * 128 + (lane & 3) * 8 + ((((hw & 1) * 2) ^ (q >> 1) ^ (blk << 1)) << 5);
        lds_ptr b = lds + bo, b1 = lds + (bo ^ 32);
        const int so = P1_X0 + ra * P1_XSLOT + (hw * 32 + r16) * 128 + (((lane >> 4) ^ (r16 >> 1)) << 4);
        lds_ptr sp0 = lds + so, sp1 = lds + (so ^ 64);
        f32x4 cs = f32x4{}, cs1 = f32x4{}, s1[2] = {f32x4{}, f32x4{}}, s2[2] = {f32x4{}, f32x4{}};
        s16x16 wv0, wv1; s16x8 xa[2], xb[2];
        auto col_rd = [&]() {
            wv0 = CAT16(CAT8(trread(b), trread(b + 512)), CAT8(trread(b + 8192), trread(b + 8192 + 512)));
            wv1 = CAT16(CAT8(trread(b1), trread(b1 + 512)), CAT8(trread(b1 + 8192), trread(b1 + 8192 + 512)));
            const int dw = ws3 == 2 ? -2 * P1_WSLOT : P1_WSLOT;
            b += dw; b1 += dw; ws3 = ws3 == 2 ? 0 : ws3 + 1;
        };
        auto row_rd = [&](int dx) {
#pragma unroll
            for (int t = 0; t < 2; ++t) { xa[t] = *(const LDS_AS s16x8*)(sp0 + t * 2048); xb[t] = *(const LDS_AS s16x8*)(sp1 + t * 2048); }
            sp0 += dx; sp1 += dx;
        };
        auto row_mm = [&]() {
#pragma unroll
            for (int t = 0; t < 2; ++t) { s2[t] = MFMA16(xa[t], xa[t], s2[t]); s1[t] = SMF16(ones2, CAT16(xa[t], xb[t]), s1[t], idx); }
#pragma unroll
            for (int t = 0; t < 2; ++t) { s2[t] = MFMA16(xb[t], xb[t], s2[t]); PIN(s1[t]); PIN(s2[t]); }
        };
#pragma unroll
        for (int n = 0; n < 9; ++n) {
            if (n == 0) WAIT_BAR(5); else if (n < 8) WAIT_BAR(12); else WAIT_BAR(8);
            col_rd(); row_rd(n % 5 == 4 - ra ? -4 * P1_XSLOT : P1_XSLOT);
            if (n == 0) { issue1(true); issue2(true, true); }
            if (n == 8) { kb_ = 5; xs = rb; }
            issue1(n < 8);
            cs = SMF16(ones2, wv0, cs, idx);
            row_mm();
            cs1 = SMF16(ones2, wv1, cs1, idx); PIN(cs); PIN(cs1);
            issue2(n < 8, n < 7);
        }
        MEMFENCE(); __builtin_amdgcn_s_waitcnt(0xF70 | 10); MEMFENCE();
        row_rd(0); row_mm();
        {
            const int t = blk;
            const float d0 = (lane & 2) ? ((lane & 1) ? s2[0][3] : s2[0][2]) : ((lane & 1) ? s2[0][1] : s2[0][0]);
            const float d1 = (lane & 2) ? ((lane & 1) ? s2[1][3] : s2[1][2]) : ((lane & 1) ? s2[1][1] : s2[1][0]);
            const float a0 = __shfl(s1[0][0] + s1[0][1], r16), a1 = __shfl(s1[1][0] + s1[1][1], r16), c0 = __shfl(d0, r16 + 16 * q), c1 = __shfl(d1, r16 + 16 * q);
            const float a = t ? a1 : a0, c = t ? c1 : c0;
            const float mean = a * (1.0f / D_), var = fmaxf(c * (1.0f / D_) - mean * mean, 0.f);
            const float rstd = __builtin_amdgcn_rcpf(rbf(__builtin_amdgcn_sqrtf(rbf(rbf(var) + 1e-5f))));
            ((LDS_AS float*)(lds + P1_STAT))[h * 128 + hw * 32 + (lane & 31)] = h ? -rbf(mean) : rstd;
        }
        WAIT_BAR(6);
        col_rd();
        issue1(false);
        cs = SMF16(ones2, wv0, cs, idx);
        cs1 = SMF16(ones2, wv1, cs1, idx); PIN(cs); PIN(cs1);
        issue2(false, false);
        LDS_AS float* colf = (LDS_AS float*)(lds + P1_COLA) + hw * 32;
        if (lane < 16) { colf[lane] = cs[0] + cs[1]; colf[16 + lane] = cs1[0] + cs1[1]; }
    }
    asm volatile("" : "+v"(ep));
    const unsigned long long tag = (((unsigned long long)(unsigned)__builtin_amdgcn_readfirstlane((unsigned)(ep >> 32)) << 32) | (unsigned)__builtin_amdgcn_readfirstlane((unsigned)ep)) * 6364136223846793005ull + 1442695040888963407ull;
    if (hw == 0 && lane == 0) *(LDS_AS unsigned long long*)(lds + P1_TAG) = tag;
    {
        const int g4 = lane >> 4, ro = P1_WSLOT + (g4 >> 1) * 6144 + (g4 & 1) * 512 + q * 64 + (lane & 3) * 8, T0 = hw * 3, T1 = T0 + 1;
        lds_ptr b = lds + ro + (T0 >> 1) * 1024 + (((T0 ^ g4) & 1) << 5), c = lds + ro + (T1 >> 1) * 1024 + (((T1 ^ g4) & 1) << 5);
        f32x4 cs = f32x4{}, ct = f32x4{}, cu = f32x4{};
        Poll pl = {};
        bool early = false;
#pragma unroll
        for (int n = 10; n < 20; ++n) {
            if (n < 13 || n == 19) WAIT_BAR(6); else if (n == 13) WAIT_BAR(10); else if (n == 18) WAIT_BAR(12); else WAIT_BAR(14);
            const s16x16 wv0 = CAT16(CAT8(trread(b), trread(b + 256)), CAT8(trread(b + 12288), trread(b + 12288 + 256)));
            const s16x16 wv1 = CAT16(CAT8(trread(c), trread(c + 256)), CAT8(trread(c + 12288), trread(c + 12288 + 256)));
            const s16x16 wv2 = CAT16(CAT8(trread(b + 1024), trread(b + 1024 + 256)), CAT8(trread(b + 13312), trread(b + 13312 + 256)));
            if (n == FA_POS && hw == 0 && lane == 0) raise_flag(ws, WS_FA, tag);
            if (n < 18) issue1(false);
            cs = SMF16(ones2, wv0, cs, idx);
            ct = SMF16(ones2, wv1, ct, idx);
            cu = SMF16(ones2, wv2, cu, idx); PIN(cs); PIN(ct); PIN(cu);
            if (n < 18) issue2(false, n >= 12 && n < 17);
            if (n == 17) poll_request(pl, ws + WS_FA, lane * 16);
            if (n == 18) { early = poll_check(pl, tag); if (!early) poll_request(pl, ws + WS_FA, lane * 16); }
            if (n == 19 && !early) poll_wait(pl, ws + WS_FA, lane * 16, tag);
            if (n >= 18 && early == (n == 18)) {
#pragma unroll
                for (int j = 0; j < 6; ++j) glds16c(kv + (hw + 4 * j) * 1024, lane * 16, lds0 + K2_C0 + (hw + 4 * j) * 1024);
            }
            if (n == 19) {
#pragma unroll
                for (int j = 0; j < 3; ++j) glds16c(qsrc + (hw + 4 * j) * 1024, lane * 16, lds0 + K2_Q0 + (hw + 4 * j) * 1024);
            }
            const int dw = ws3 == 2 ? -2 * P1_WSLOT : P1_WSLOT;
            b += dw; c += dw; ws3 = ws3 == 2 ? 0 : ws3 + 1;
        }
        LDS_AS float* colf = (LDS_AS float*)(lds + P1_COLB) + hw * 48;
        if (lane < 16) { colf[lane] = cs[0] + cs[1]; colf[16 + lane] = ct[0] + ct[1]; colf[32 + lane] = cu[0] + cu[1]; }
        WAIT_BAR(0);
        if (blockIdx.x == 0 && hw == 0 && lane == 0) __hip_atomic_store((gu64*)(ws + WS_EPOCH), tag, __ATOMIC_RELAXED, __HIP_MEMORY_SCOPE_AGENT);
#pragma unroll
        for (int j = 0; j < 6; ++j) glds16c(kv + K2_CHUNK + (hw + 4 * j) * 1024, lane * 16, lds0 + K2_KV0 + K2_SLOTB + (hw + 4 * j) * 1024);
    }
    return tag;
}

constexpr int K2_XCH = 2 * K2_SLOTB;

#define SMF32(a, b, c) __builtin_amdgcn_smfmac_f32_32x32x32_bf16((bfx8)(a), __builtin_bit_cast(bfx16, (b)), (c), 0xDDDD, 0, 0)
#define MFMA8(a, b, c) __builtin_amdgcn_mfma_f32_32x32x8bf16_1k((a), (b), (c), 0, 0, 0)
__device__ __forceinline__ f32x8 lo8(const f32x16& v) { return __builtin_shufflevector(v, v, 0, 1, 2, 3, 4, 5, 6, 7); }
__device__ __forceinline__ f32x8 hi8(const f32x16& v) { return __builtin_shufflevector(v, v, 8, 9, 10, 11, 12, 13, 14, 15); }
__device__ __forceinline__ float max16(const f32x16& s) {
    float m = fmaxf(fmaxf(s[0], s[1]), s[2]);
#pragma unroll
    for (int r = 3; r < 15; r += 2) m = fmaxf(fmaxf(m, s[r]), s[r + 1]);
    return swap32_max(fmaxf(m, s[15]));
}


struct K2B { lds_ptr r0, r1, r2; const char *wsrc, *wsa, *wsb; unsigned wdst; int lane, osoff; rsrc_t wsr; };
constexpr int k2_slot(int st) { return st < 3 ? st : st == 3 ? 0 : st == 4 ? 3 : st == 5 ? 1 : st == 6 ? 2 : st == 7 ? 0 : 3; }
__device__ __forceinline__ void k2_wpiece(const K2B& c, int st, int i) {
    glds16c((st < 3 ? c.wsa : st < 6 ? c.wsb : c.wsrc) + st * 40960 + i * 8192, c.lane * 16, c.wdst + k2_slot(st) * 40960 + i * 8192);
}
template <int I, int D, int NP, int OF>
__device__ __forceinline__ void k2_bstep(const K2B& c, f32x4 (&acc)[10], const s16x8 (&w)[5], s16x8 (&n)[5], const s16x8& a0, const s16x8& a1, s16x8 (&of)[10]) {
    constexpr int O = k2_slot((I + 1) >> 1) * 40960 + ((I + 1) & 1) * 20480;
#pragma unroll
    for (int i = 0; i < 10; ++i) {
        if (I > 0 && I < 16 && i < 5) n[i] = *(const LDS_AS s16x8*)((O < 61440 ? c.r0 : O < 122880 ? c.r1 : c.r2) + O % 61440 + i * 1024);
        acc[i] = MFMA16(w[i >> 1], (i & 1) ? a1 : a0, acc[i]);
        PIN(acc[i]);
        if (OF >= 0 && !(i & 1)) { const int k = (OF < 0 ? 0 : OF) + (i >> 1); of[k] = LD16(c.wsr, c.lane * 16, c.osoff + ((k & 1) * 20 + (k >> 1) * 4) * 1024); MEMFENCE(); }
        if (NP > 0 && (i + 1) * NP / 10 != i * NP / 10 ) { const int k = D + i * NP / 10 + (D < 24 && D + i * NP / 10 >= 24); k2_wpiece(c, k / 5, k % 5); }
        SBAR();
    }
}

__global__ __launch_bounds__(512, 2) void fused_block(const char* __restrict__ x, const char* __restrict__ wqkv, const char* __restrict__ wg, const char* __restrict__ wu,
                                                      const char* __restrict__ wd, const char* __restrict__ wo, char* __restrict__ ws, char* __restrict__ out) {
    extern __shared__ __attribute__((aligned(16))) char smem[];
    const int wid = __builtin_amdgcn_readfirstlane(threadIdx.x >> 6), bid = blockIdx.x;
    const int bi = bid >> 3, panel = (bid & 1) * 8 + (bi & 7), grp = ((bid & 7) >> 1) * 4 + (bi >> 3), l0 = threadIdx.x & 63;
    const int rb = (5 * (bi & 7)) >> 3, ra = ((bi ^ (bi >> 3)) & 1) * 2;
    unsigned xv[4], wa[4];
    {
        const int hw = wid & 3, r = l0 >> 3, pos = l0 & 7;
#pragma unroll
        for (int j = 0; j < 4; ++j) {
            xv[j] = 4096 + (hw * 32 + j * 8 + r) * (D_ * 2) + ((pos ^ ((j & 1) * 4 + (r >> 1))) << 4) - j * 1024;
            wa[j] = 4096 + (hw * 16 + (j >> 1) * 8 + r) * (3 * D_ * 2) + (j & 1) * 128 + ((pos ^ (((r >> 1) & 1) * 2 + (j >> 1) * 4)) << 4) - j * 1024;
            PIN(xv[j]); PIN(wa[j]);
        }
        int xo = panel * 128 * (D_ * 2) + ra * 128 - 4096, wo_ = min(grp, 14) * 256 + ra * 64 * 3 * D_ * 2 - 4096;
        unsigned dx = (unsigned)(uintptr_t)(lds_ptr)smem + P1_X0 + ra * P1_XSLOT + hw * 4096, dw = (unsigned)(uintptr_t)(lds_ptr)smem + hw * 4096;
        asm volatile("" : "+s"(xo), "+s"(wo_), "+s"(dx), "+s"(dw));
        SBAR();
        asm volatile("" :: "s"(x), "s"(wqkv), "s"(wg), "s"(wu), "s"(wd), "s"(wo), "s"(ws), "s"(out));
        if (wid >= 4) { glds_x4(x + xo, xv, dx); glds_x4(wqkv + wo_, wa, dw); }
        SBAR();
    }
    unsigned long long tag = 0;
    if (wid < 4) {
        __builtin_amdgcn_s_barrier();
        if (wid == 3) {
            unsigned off = ((blockIdx.x >> 3) * 13 + min(threadIdx.x & 63u, 12u)) * 128;
            asm volatile("s_getpc_b64 vcc\n.Lfb_begin:\n\tv_min_u32 %0, .Lfb_end-.Lfb_begin, %0\n\ts_mov_b32 m0, %1\n\ts_nop 0\n\tglobal_load_lds_dword %0, vcc"
                         : "+v"(off) : "s"((unsigned)(uintptr_t)(lds_ptr)smem + LDS_JUNK) : "vcc", "memory");
        }
    }
    const rsrc_t wsr = __builtin_amdgcn_make_buffer_rsrc(ws, 0, (int)WS_END, 0x00020000);
    const int head = (bid & 7) * 4 + ((bid >> 3) & 3), par = wid >> 2, qt0 = (bid >> 5) * 8 + (wid & 3) * 2;
    const char* kv = ws + WS_KV + head * KV_HEAD;
    {
        if (wid < 4) {
            p1_compute((lds_ptr)smem, wid, l0, panel, grp, ra, rb, wsr, Pack{wd, wo, wid * 256 + bid});
            const u32x2 e = *(const LDS_AS u32x2*)((lds_ptr)smem + P1_TAG);
            tag = ((unsigned long long)(unsigned)__builtin_amdgcn_readfirstlane(e[1]) << 32) | (unsigned)__builtin_amdgcn_readfirstlane(e[0]);
        } else {
            tag = p1_helper((lds_ptr)smem, wid - 4, l0, x + (size_t)panel * 128 * (D_ * 2), wqkv + min(grp, 14) * 256, wg + grp * 192, wu + grp * 192, ra, rb,
                            ws, kv, ws + WS_Q + head * Q_HEAD + (bid >> 5) * 8 * Q_TILE, xv, wa);
        }
    }
    int lane = __builtin_amdgcn_mbcnt_hi(~0u, __builtin_amdgcn_mbcnt_lo(~0u, 0u));
    PIN(lane);
    if (wid < 4) __builtin_amdgcn_s_setprio(3); else __builtin_amdgcn_s_setprio(0);
    lds_ptr lds = (lds_ptr)smem + K2_KV0;
    const unsigned lds0 = (unsigned)(uintptr_t)lds;
    const int tp = ((blockIdx.x & 7) >> 1) * 8 + ((blockIdx.x >> 3) & 7), cg = (blockIdx.x & 1) * 4 + (blockIdx.x >> 6);
    K2B kb;
    kb.wsrc = ws + WS_WP + cg * 68 * 5120 + wid * 1024; kb.wdst = (unsigned)(uintptr_t)(lds_ptr)smem + wid * 1024; kb.lane = lane;
    const int sw3 = tp & 1;
    kb.wsa = kb.wsrc + sw3 * 122880; kb.wsb = kb.wsrc - sw3 * 122880;
    const int hoff = WS_H16 + ((tp * 2 + (wid & 1)) * 96 + (wid >> 1) * 2 + sw3 * 48) * 1024, hoff1 = hoff + (1 - 2 * sw3) * 49152;
    s16x8 hf[24];
    const int h = lane >> 5, l31 = lane & 31, l16 = lane * 16;
    const char* src = kv + wid * 1024 + 3 * K2_CHUNK;
    const int kr = ((l31 & 4) << 2) | ((l31 >> 1) & 12) | (l31 & 3);
    const int k1off = h * 1024 + kr * 16, k2off = h ? KV_V + 5 * 256 : 2048 + kr * 16;
    const int vq = min(l31 >> 2, 5), voff = KV_V + vq * 256 + ((h ^ (vq & 3)) << 6) + (l31 < 24 ? l31 & 3 : 1) * 16;

    f32x16 oa[2] = {f32x16{}, f32x16{}}, sc[2];
    s16x8 k1[2], v0[2], v1[2]; s16x4 k2[2];
    const lds_ptr c0 = (lds_ptr)smem + K2_C0;
    lds_ptr pk1 = c0 + k1off + par * 8, pk2 = c0 + k2off + par * 8, pv = c0 + voff + par * KV_VT, pw = c0 + (voff ^ 128) + par * KV_VT;
    lds_ptr pk3 = pk1 + 512;
    asm volatile("" : "+v"(pk3));
#define LDS8(p) (*(const LDS_AS s16x4*)(p))
#define RD_K(t, j) do { k1[(t) & 1] = CAT8(LDS8(pk1 + (j) * 2 * KV_TILE), LDS8(pk3 + (j) * 2 * KV_TILE)); k2[(t) & 1] = LDS8(pk2 + (j) * 2 * KV_TILE); } while (0)
#define RD_V(t, j) do { v0[(t) & 1] = *(const LDS_AS s16x8*)(pv + (j) * 2 * KV_TILE); v1[(t) & 1] = *(const LDS_AS s16x8*)(pw + (j) * 2 * KV_TILE); } while (0)
    s16x8 q1[2]; s16x4 q2[2];
#pragma unroll
    for (int b = 0; b < 2; ++b) {
        const lds_ptr qp = (lds_ptr)smem + K2_Q0 + (wid & 3) * 2 * Q_TILE + b * 8 + l31 * 16;
        q1[b] = CAT8(LDS8(qp + h * 1024), LDS8(qp + h * 1024 + 512)); q2[b] = LDS8(qp + 2048);
    }
    {
        const s16x8 ka = CAT8(LDS8(c0 + k1off), LDS8(c0 + k1off + 512));
        const s16x4 kb = LDS8(c0 + k2off);
        RD_K(0, 0); RD_V(0, 0);
#pragma unroll
        for (int b = 0; b < 2; ++b) {
            if (h) q2[b] = s16x4{0, 0, 0, 0};
            f32x16 s = MFMA32(ka, q1[b], f32x16{});
            s = MFMA8(kb, q2[b], s);
            const float m = rbf(-max16(s));
            if (h) q2[b][0] = (short)(__float_as_uint(m) >> 16);
        }
        sc[0] = MFMA32(k1[0], q1[0], f32x16{});
        sc[0] = MFMA8(k2[0], q2[0], sc[0]);
    }
#define EXP4(S, i) do { S[i] = __builtin_amdgcn_exp2f(S[i]); S[i + 1] = __builtin_amdgcn_exp2f(S[i + 1]); S[i + 2] = __builtin_amdgcn_exp2f(S[i + 2]); S[i + 3] = __builtin_amdgcn_exp2f(S[i + 3]); PIN(S); SBAR(); } while (0)
    Poll pl = {};
    s16x8 wfa[5], wfb[5];
    kb.r0 = (lds_ptr)smem + (wid >> 1) * 5120 + l16;
    auto request = [&](auto HS) __attribute__((always_inline)) {
        constexpr int hs = HS.v, p = (hs - 6) >> 3, r = (hs - 6) & 7;
        constexpr int w = (hs >= 9 && hs < 14) ? hs - 9 : (hs >= 17 && hs < 22) ? hs - 12 : (hs >= 54 && hs < 57) ? hs - 44 : hs >= 62 ? hs - 49 : -1;
        constexpr int e = (p >= 2 && p <= 4 && r >= 3) ? (p - 2) * 5 + r - 3 : p == 5 ? 15 + r : hs == 57 ? 23 : -1;
        if constexpr (hs < 3) glds16c(src - K2_CHUNK + hs * 8192, l16, lds0 + 2 * K2_SLOTB + wid * 1024 + hs * 8192);
        else if constexpr (hs >= 6 && p < 5 && r < 3) glds16c(src + p * K2_CHUNK + r * 8192, l16, lds0 + (p % 3) * K2_SLOTB + wid * 1024 + r * 8192);
        else if constexpr (w >= 0) k2_wpiece(kb, w / 5, w % 5);
        else if constexpr (e >= 0) { hf[e] = LD16(wsr, l16, (e < 12 ? hoff : hoff1) + ((e % 12) >> 1) * 8192 + (e & 1) * 1024); MEMFENCE(); }
        if constexpr (hs == 58) k2_wpiece(kb, 4, 4);
        if constexpr (hs == 63) k2_wpiece(kb, 4, 0);
        if constexpr (hs == 3) {
            const unsigned wo_ = 6 * 40960 + min(((bi & 7) * 8 + wid) * 13 + min(lane, 12), 799) * 128;
            asm volatile("s_mov_b32 m0, %2\n\ts_nop 0\n\tglobal_load_lds_dword %0, %1 sc1" :: "v"(wo_), "s"(kb.wsrc - wid * 1024), "s"(kb.wdst - wid * 768 + LDS_JUNK) : "memory");
        }
        if (hs == 6 && wid == 4 && lane == 0) raise_flag(ws, WS_FB, tag);
        if (hs == 16 && wid == 4) poll_request(pl, ws + WS_FB, l16);
        SBAR();
    };
    static_for<0, 32>([&](auto T) __attribute__((always_inline)) {
        {
            constexpr int c = T.v >> 2, j = T.v & 3;
            if (j == 3) {
                if (c == 2 && wid == 4) poll_wait(pl, ws + WS_FB, l16, tag);
                if (c == 0) { asm volatile("s_waitcnt vmcnt(0)" ::: "memory"); LGKM_BAR(); } else if (c == 7) WAIT_BAR(0); else WAIT_BAR(7);
                if (c == 7 && par) __builtin_amdgcn_s_setprio(3); else __builtin_amdgcn_s_setprio(2);
                const int dsl = c == 0 ? K2_KV0 + K2_SLOTB - K2_C0 : (c % 3 == 2) ? -2 * K2_SLOTB : K2_SLOTB;
                pk1 += dsl; pk2 += dsl; pk3 += dsl; pv += dsl; pw += dsl;
                asm volatile("" : "+v"(pk1), "+v"(pk2), "+v"(pk3), "+v"(pv), "+v"(pw));
                SBAR();
            }
            if constexpr (T.v < 31) RD_K(j + 1, (j + 1) & 3);
            SBAR();
            if constexpr (T.v > 0) { oa[1] = SMF32(v1[(j + 1) & 1], hi8(sc[1]), oa[1]); PIN(oa[1]); }
            SBAR();
            EXP4(sc[0], 0);
            sc[1] = MFMA32(k1[j & 1], q1[1], f32x16{}); PIN(sc[1]); SBAR();
            request(IC<8 * c + 2 * j>{});
            EXP4(sc[0], 4);
            sc[1] = MFMA8(k2[j & 1], q2[1], sc[1]); PIN(sc[1]); SBAR();
            EXP4(sc[0], 8);
            oa[0] = SMF32(v0[j & 1], lo8(sc[0]), oa[0]); PIN(oa[0]); SBAR();
            EXP4(sc[0], 12);
            if constexpr (T.v < 31) RD_V(j + 1, (j + 1) & 3);
            if (j == 1 && c > 0) __builtin_amdgcn_s_setprio(1);
            if (j == 2 && c > 0) __builtin_amdgcn_s_setprio(0);
            SBAR();
            oa[0] = SMF32(v1[j & 1], hi8(sc[0]), oa[0]); PIN(oa[0]); SBAR();
            EXP4(sc[1], 0);
            if constexpr (T.v < 31) { sc[0] = MFMA32(k1[(j + 1) & 1], q1[0], f32x16{}); PIN(sc[0]); }
            SBAR();
            request(IC<8 * c + 2 * j + 1>{});
            EXP4(sc[1], 4);
            if constexpr (T.v < 31) { sc[0] = MFMA8(k2[(j + 1) & 1], q2[0], sc[0]); PIN(sc[0]); }
            else {
#pragma unroll
                for (int i = 0; i < 5; ++i) { wfa[i] = *(const LDS_AS s16x8*)(kb.r0 + i * 1024); wfb[i] = *(const LDS_AS s16x8*)(kb.r0 + 20480 + i * 1024); }
                if (par) {
#pragma unroll
                    for (int i = 0; i < 3; ++i) *(LDS_AS f32x4*)(lds + K2_XCH + (wid & 3) * 6144 + l16 + i * 1024) = f32x4{oa[0][4 * i], oa[0][4 * i + 1], oa[0][4 * i + 2], oa[0][4 * i + 3]};
                }
            }
            SBAR();
            EXP4(sc[1], 8);
            oa[1] = SMF32(v0[j & 1], lo8(sc[1]), oa[1]); PIN(oa[1]); SBAR();
            EXP4(sc[1], 12);
        }
    });
    oa[1] = SMF32(v1[1], hi8(sc[1]), oa[1]);
    int ln = __builtin_amdgcn_mbcnt_hi(~0u, __builtin_amdgcn_mbcnt_lo(~0u, 0u));
    PIN(ln);
    const int h2 = ln >> 5, l2 = ln & 31;
    const int mt = wid & 1, kq = wid >> 1;
    kb.lane = ln; kb.r0 = (lds_ptr)smem + kq * 5120 + ln * 16; kb.r1 = kb.r0 + 61440; kb.r2 = kb.r0 + 122880;
    asm volatile("" : "+v"(kb.r1), "+v"(kb.r2));
    kb.wsr = wsr; kb.osoff = WS_O16 + ((tp * 4 + mt * 2) * 20 + kq) * 1024;
    {
        const lds_ptr xp_ = lds + K2_XCH + (wid & 3) * 6144 + ln * 16;
        if (par) {
#pragma unroll
            for (int i = 3; i < 6; ++i) { const f32x16& g = oa[i / 3]; const int r = 4 * (i % 3); *(LDS_AS f32x4*)(xp_ + i * 1024) = f32x4{g[r], g[r + 1], g[r + 2], g[r + 3]}; }
        }
        LGKM_BAR();
        if (!par) {
            __builtin_amdgcn_s_setprio(3);
            f32x4 tt[6];
#pragma unroll
            for (int i = 0; i < 6; ++i) tt[i] = *(const LDS_AS f32x4*)(xp_ + i * 1024);
            SBAR();
#pragma unroll
            for (int i = 0; i < 6; ++i) { const f32x4 t = tt[i]; f32x16& g = oa[i / 3]; const int r = 4 * (i % 3); g[r] = add1(g[r], t[0]); g[r + 1] = add1(g[r + 1], t[1]); g[r + 2] = add1(g[r + 2], t[2]); g[r + 3] = add1(g[r + 3], t[3]); }
            const int ob = (l2 >> 4) * 20 * 1024 + (l2 & 15) * 16, os = WS_O16 + qt0 * 2 * 20 * 1024;
            const int o1 = ob + h2 * 256 + os + (head >> 1) * 1024 + (head & 1) * 512, o2 = os + (16 + (head >> 3)) * 1024 + ((head >> 1) & 3) * 256 + (head & 1) * 8;
            auto put = [&](const f32x16& o, int b, u32x2& e) {
                const float l = __uint_as_float(__builtin_amdgcn_permlane32_swap(__float_as_uint(o[8]), __float_as_uint(o[8]), false, false)[1]);
                const float rl = __builtin_amdgcn_rcpf(l);
                unsigned w[6];
#pragma unroll
                for (int i = 0; i < 6; ++i) w[i] = cvtpk(mul1(o[2 * i], rl), mul1(o[2 * i + 1], rl));
                ST16(wsr, (u32x4{w[0], w[1], w[2], w[3]}), o1, b * 40 * 1024);
                e = u32x2{w[4], w[5]};
                return l;
            };
            u32x2 e0, e1;
            const float l0 = put(oa[0], 0, e0), l1 = put(oa[1], 1, e1);
            ST8(wsr, (u32x2{__builtin_amdgcn_permlane32_swap(e0[0], e1[0], false, false)[0], __builtin_amdgcn_permlane32_swap(e0[1], e1[1], false, false)[0]}), ob + h2 * 40 * 1024, o2);
            MEMFENCE(); SBAR();
            float big = 0.f;
#pragma unroll
            for (int r = 0; r < 12; ++r) big = add1(big, add1(fabsf(oa[0][r]), fabsf(oa[1][r])));
            if (__builtin_expect(__any(!(big < 1e30f) | !(fminf(l0, l1) > 1e-30f)), 0)) {
                const int gr = ((l2 & 4) << 2) | ((l2 >> 1) & 12) | (l2 & 3), g1 = h2 * 1024 + gr * 16, g2 = 2048 + gr * 16, gq = min(l2 >> 2, 5), gv = KV_V + gq * 256 + ((h2 ^ (gq & 3)) << 6) + (l2 & 3) * 16;
                asm volatile("s_waitcnt vmcnt(0)" ::: "memory");
#pragma unroll 1
                for (int b = 0; b < 2; ++b) {
                    const s16x8 qf1 = b ? q1[1] : q1[0];
                    s16x4 qf2 = b ? q2[1] : q2[0];
                    if (h2) qf2[0] = 0;
                    float m = -INFINITY;
                    f32x16 negm, o = f32x16{};
#pragma unroll 1
                    for (int t = 0; t < 64; ++t) {
                        const int g = WS_KV + head * KV_HEAD + (t >> 1) * 2 * KV_TILE + (t & 1) * 8;
                        f32x16 s = MFMA32(CAT8(LD8(wsr, g1, g), LD8(wsr, g1 + 512, g)), qf1, f32x16{});
                        s = MFMA8(LD8(wsr, g2, g), qf2, s);
                        m = fmaxf(m, max16(s));
                    }
#pragma unroll
                    for (int r = 0; r < 16; ++r) negm[r] = -m;
#pragma unroll 1
                    for (int t = 0; t < 64; ++t) {
                        const int g = WS_KV + head * KV_HEAD + (t >> 1) * 2 * KV_TILE + (t & 1) * 8;
                        f32x16 s = MFMA32(CAT8(LD8(wsr, g1, g), LD8(wsr, g1 + 512, g)), qf1, negm);
                        s = MFMA8(LD8(wsr, g2, g), qf2, s);
#pragma unroll
                        for (int r = 0; r < 16; ++r) s[r] = __builtin_amdgcn_exp2f(s[r]);
                        o = SMF32(LD16(wsr, gv, g + (t & 1) * (KV_VT - 8)), lo8(s), o);
                        o = SMF32(LD16(wsr, gv ^ 128, g + (t & 1) * (KV_VT - 8)), hi8(s), o);
                    }
                    u32x2 e;
                    put(o, b, e);
                    if (!h2) ST8(wsr, e, ob + b * 40 * 1024, o2);
                }
            }
        }
    }
    __builtin_amdgcn_s_setprio(0);
    const int ch0 = min(wid, 4) * 128 + ln, ch1 = ch0 + 64, er0 = ch0 / 10, er1 = ch1 / 10, ec0 = ch0 - er0 * 10, ec1 = ch1 - er1 * 10;
    const size_t eb = (size_t)tp * 64 * (D_ * 2) + cg * 160;
    const unsigned eo0 = er0 * (D_ * 2) + ec0 * 16, eo1 = er1 * (D_ * 2) + ec1 * 16;
    f32x4 acc[10];
#pragma unroll
    for (int i = 0; i < 10; ++i) acc[i] = f32x4{};
    s16x8 of[10];
#define HLO(i) __builtin_shufflevector(hf[2 * (i)], hf[2 * (i) + 1], 0, 1, 2, 3, 8, 9, 10, 11)
#define HHI(i) __builtin_shufflevector(hf[2 * (i)], hf[2 * (i) + 1], 4, 5, 6, 7, 12, 13, 14, 15)
    k2_bstep<0, 0, 0, -1>(kb, acc, wfa, wfb, HLO(0), HHI(0), of);
    k2_bstep<1, 0, 0, -1>(kb, acc, wfb, wfa, HLO(1), HHI(1), of);
    k2_bstep<2, 0, 0, -1>(kb, acc, wfa, wfb, HLO(2), HHI(2), of);
    if (!par) {
        asm volatile("s_waitcnt vmcnt(0)" ::: "memory");
        if (ln == 0) __hip_atomic_store((gu64*)(ws + WS_FC) + (qt0 >> 1) * 32 + head, tag, __ATOMIC_RELAXED, __HIP_MEMORY_SCOPE_AGENT);
    } else {
#pragma unroll
        for (int i = 0; i < 10; ++i) glds16c(kb.wsb + 3 * 40960 - 4096 + i * 4096, ln * 16, kb.wdst - 4096 + i * 4096);
    }
    WAIT_BAR(10);
    k2_bstep<3, 21, 4, -1>(kb, acc, wfb, wfa, HLO(3), HHI(3), of);
    k2_bstep<4, 26, 4, -1>(kb, acc, wfa, wfb, HLO(4), HHI(4), of);
    WAIT_BAR(8);
    k2_bstep<5, 30, 3, -1>(kb, acc, wfb, wfa, HLO(5), HHI(5), of);
    k2_bstep<6, 33, 2, -1>(kb, acc, wfa, wfb, HLO(6), HHI(6), of);
    WAIT_BAR(10);
    k2_bstep<7, 35, 3, -1>(kb, acc, wfb, wfa, HLO(7), HHI(7), of);
    unsigned long long seen = 0;
    const char* cw = ws + WS_FC + tp * 256;
    const unsigned cvo = l2 * 8;
    if (wid == 4) asm volatile("global_load_dwordx2 %0, %1, %2 sc1" : "+v"(seen) : "v"(cvo), "s"(cw) : "memory");
    SBAR();
    k2_bstep<8, 38, 2, -1>(kb, acc, wfa, wfb, HLO(8), HHI(8), of);
    if (wid == 4) {
        asm volatile("s_waitcnt vmcnt(2)" : "+v"(seen) :: "memory");
#pragma unroll 1
        for (unsigned spin = 0; spin < (1u << 18) && !__all(seen == tag); ++spin) {
            __builtin_amdgcn_s_sleep(1);
            asm volatile("global_load_dwordx2 %0, %1, %2 sc1\n\ts_waitcnt vmcnt(0)" : "=&v"(seen) : "v"(cvo), "s"(cw) : "memory");
        }
    }
    WAIT_BAR(10);
    k2_bstep<9, 0, 0, 0>(kb, acc, wfb, wfa, HLO(9), HHI(9), of);
    k2_bstep<10, 0, 0, 5>(kb, acc, wfa, wfb, HLO(10), HHI(10), of);
    WAIT_BAR(10);
    k2_bstep<11, 40, 3, -1>(kb, acc, wfb, wfa, HLO(11), HHI(11), of);
    const rsrc_t xrs = __builtin_amdgcn_make_buffer_rsrc((void*)x, 0, S_ * D_ * 2, 0x00020000);
    const u32x4 xv0 = __builtin_amdgcn_raw_buffer_load_b128(xrs, eo0, (int)eb, 0), xv1 = __builtin_amdgcn_raw_buffer_load_b128(xrs, eo1, (int)eb, 0);
    MEMFENCE(); SBAR();
    k2_bstep<12, 0, 0, -1>(kb, acc, wfa, wfb, of[0], of[1], of);
    k2_bstep<13, 0, 0, -1>(kb, acc, wfb, wfa, of[2], of[3], of);
    k2_bstep<14, 0, 0, -1>(kb, acc, wfa, wfb, of[4], of[5], of);
    WAIT_BAR(2);
    k2_bstep<15, 0, 0, -1>(kb, acc, wfb, wfa, of[6], of[7], of);
    {
        const int tk = ln & 15;
        const lds_ptr pp = (lds_ptr)smem + kq * 20480 + (mt * 32 + tk) * 320 + (((ln >> 4) ^ ((tk >> 1) & 3)) << 4);
#pragma unroll
        for (int i = 0; i < 13; ++i) {
            if (i < 10) { acc[i] = MFMA16(wfa[i >> 1], (i & 1) ? of[9] : of[8], acc[i]); PIN(acc[i]); }
            if (i >= 3) *(LDS_AS f32x4*)(pp + ((i - 3) & 1) * 16 * 320 + ((i - 3) >> 1) * 64) = acc[i - 3];
            SBAR();
        }
        LGKM_BAR();
        if (wid < 5) {
#pragma unroll
            for (int e = 0; e < 2; ++e) {
                const int er = e ? er1 : er0, ec = e ? ec1 : ec0, sw = (er >> 1) & 3;
                const lds_ptr p0 = (lds_ptr)smem + er * 320 + (((2 * ec) ^ sw) << 4), p1 = (lds_ptr)smem + er * 320 + (((2 * ec + 1) ^ sw) << 4);
                f32x4 a = *(const LDS_AS f32x4*)p0, b = *(const LDS_AS f32x4*)p1;
#pragma unroll
                for (int k = 1; k < 4; ++k) { a += *(const LDS_AS f32x4*)(p0 + k * 20480); b += *(const LDS_AS f32x4*)(p1 + k * 20480); }
                const u32x4 xr = e ? xv1 : xv0;
                const u32x4 ov = u32x4{cvtpk(a[0] + bf_lo(xr[0]), a[1] + bf_hi(xr[0])), cvtpk(a[2] + bf_lo(xr[1]), a[3] + bf_hi(xr[1])),
                                       cvtpk(b[0] + bf_lo(xr[2]), b[1] + bf_hi(xr[2])), cvtpk(b[2] + bf_lo(xr[3]), b[3] + bf_hi(xr[3]))};
                __builtin_amdgcn_raw_buffer_store_b128(ov, __builtin_amdgcn_make_buffer_rsrc(out, 0, S_ * D_ * 2, 0x00020000), (int)eb + (e ? eo1 : eo0), 0, 16);
            }
        }
    }
    asm volatile(".Lfb_end:");
}

extern "C" void kernel_launch(void* const* d_in, const int* in_sizes, int n_in, void* d_out, int out_size, void* d_ws, size_t ws_size,
                              hipStream_t stream) {
    const char* x = (const char*)d_in[0];
    const char* wqkv = (const char*)d_in[1];
    const char* wo = (const char*)d_in[2];
    const char* wg = (const char*)d_in[4];
    const char* wu = (const char*)d_in[5];
    const char* wd = (const char*)d_in[6];
    char* ws = (char*)d_ws;
    (void)hipFuncSetAttribute((const void*)fused_block, hipFuncAttributeMaxDynamicSharedMemorySize, LDS_BYTES);
    fused_block<<<256, 512, LDS_BYTES, stream>>>(x, wqkv, wg, wu, wd, wo, ws, (char*)d_out);
}
```
